```python
import torch

def _softmax(x, axis=-1):
    return torch.softmax(x, dim=axis)

def _gelu(x):
    return torch.nn.functional.gelu(x, approximate='tanh')

def _silu(x):
    return torch.nn.functional.silu(x)

def _sigmoid(x):
    return torch.sigmoid(x)

def _layernorm(x, axis=-1, eps=1e-05):
    m = x.mean(dim=axis, keepdim=True)
    v = x.var(dim=axis, keepdim=True, unbiased=False)
    return (x - m) / torch.sqrt(v + eps)

def _scatter_add(tgt, idx, src, axis=0):
    return tgt.clone().index_add_(axis, idx, src)

def _segment_sum(src, seg, n):
    out = torch.zeros((n,) + src.shape[1:], dtype=src.dtype)
    return out.index_add_(0, seg, src)

def _pad(x, pw):
    flat = tuple((v for pair in reversed(pw) for v in pair))
    return torch.nn.functional.pad(x, flat)
kernel_name = 'adaLN_cond__mha_block_cached__glu_gated_reglu__prompt'

def setup_inputs():

    _w = lambda a: (lambda t: t.to(torch.bfloat16) if t.is_floating_point() else t)(torch.as_tensor(a))

    def _A_setup():
        B, Sq, D = (1, 2048, 576)
        H, Sk, Dh = (32, 2048, 128)
        return {'x': _w((lambda *s: torch.randn(*s, dtype=torch.bfloat16) * 0.05)(B, Sq, D)), 'k_cache': _w((lambda *s: torch.randn(*s, dtype=torch.bfloat16) * 1.6)(B, H, Sk, Dh)), 'v_cache': _w((lambda *s: torch.randn(*s, dtype=torch.bfloat16) * 0.8)(B, H, Sk, Dh)), 'wq': _w((lambda *s: torch.randn(*s, dtype=torch.bfloat16) * 1.6)(D, H * Dh) * D ** (-0.5)), 'wo': _w((lambda *s: torch.randn(*s, dtype=torch.bfloat16) * 0.8)(H * Dh, D) * (H * Dh) ** (-0.5))}
    _A_in = _A_setup()

    def _B_setup():
        B, S, D, F = (1, 2048, 576, 4608)
        return {'x': _w((lambda *s: torch.randn(*s, dtype=torch.bfloat16) * 0.05)(B, S, D)), 'W_gate': _w((lambda *s: torch.randn(*s, dtype=torch.bfloat16) * 0.4)(D, F) * (2.0 / D) ** 0.5), 'W_up': _w((lambda *s: torch.randn(*s, dtype=torch.bfloat16) * 0.4)(D, F) * (2.0 / D) ** 0.5), 'W_down': _w((lambda *s: torch.randn(*s, dtype=torch.bfloat16) * 0.4)(F, D) * (1.0 / F) ** 0.5)}
    _B_in = _B_setup()
    return {'A_x': _A_in['x'], 'A_k_cache': _A_in['k_cache'], 'A_v_cache': _A_in['v_cache'], 'A_wq': _A_in['wq'], 'A_wo': _A_in['wo'], 'B_x': _B_in['x'], 'B_W_gate': _B_in['W_gate'], 'B_W_up': _B_in['W_up'], 'B_W_down': _B_in['W_down'], 'c': _w((lambda *s: torch.randn(*s, dtype=torch.bfloat16) * 0.1)(1, 576) * 8.0), 'w_mod': _w((lambda *s: torch.randn(*s, dtype=torch.bfloat16) * 0.1)(576, 6 * 576) * 576 ** (-0.5) * 8.0)}

def reference(A_x, A_k_cache, A_v_cache, A_wq, A_wo, B_x, B_W_gate, B_W_up, B_W_down, c, w_mod):

    def _A_ref(x, k_cache, v_cache, wq, wo):
        B, Sq, D = x.shape
        H, Dh = (32, 128)
        q = (lambda x, *d: x.permute(*d))((x @ wq).reshape(B, Sq, H, Dh), 0, 2, 1, 3)
        scores = torch.einsum('bhqd,bhkd->bhqk', q, k_cache) * Dh ** (-0.5)
        p = _softmax(scores, axis=-1)
        o = torch.einsum('bhqk,bhkd->bhqd', p, v_cache)
        return (lambda x, *d: x.permute(*d))(o, 0, 2, 1, 3).reshape(B, Sq, H * Dh) @ wo

    def _B_ref(x, W_gate, W_up, W_down):
        gate = torch.einsum('bsd,df->bsf', x, W_gate)
        up = torch.einsum('bsd,df->bsf', x, W_up)
        gate = gate * (lambda x, dt: x.to(dt))(gate > 0, gate.dtype)
        h = gate * up
        return torch.einsum('bsf,fd->bsd', h, W_down)
    x = A_x
    mod = (_silu(c) @ w_mod).reshape(c.shape[0], 6, 1, -1)
    sa, ca, ga, sb, cb, gb = (mod[:, 0].clone(), mod[:, 1].clone(), mod[:, 2].clone(), mod[:, 3].clone(), mod[:, 4].clone(), mod[:, 5].clone())
    h = _layernorm(x, axis=-1) * (1 + ca) + sa
    x = x + ga * _A_ref(h, A_k_cache, A_v_cache, A_wq, A_wo)
    h = _layernorm(x, axis=-1) * (1 + cb) + sb
    x = x + gb * _B_ref(h, B_W_gate, B_W_up, B_W_down)
    return x
```

```cpp
#include <hip/hip_runtime.h>
#include <hip/hip_bf16.h>
#include <utility>
#include <cstdint>

typedef float    f32x16 __attribute__((ext_vector_type(16)));
typedef float    f32x4  __attribute__((ext_vector_type(4)));
typedef float    f32x2  __attribute__((ext_vector_type(2)));
typedef __bf16   bf16x8 __attribute__((ext_vector_type(8)));
typedef __bf16   bf16x4 __attribute__((ext_vector_type(4)));
typedef __bf16   bf16x2 __attribute__((ext_vector_type(2)));
typedef unsigned u32x4  __attribute__((ext_vector_type(4)));
typedef unsigned u32x2  __attribute__((ext_vector_type(2)));
typedef unsigned short u16;
template <int... Is> using iseq = std::integer_sequence<int, Is...>;
template <int N> using make_iseq = std::make_integer_sequence<int, N>;
#define DI __device__ __forceinline__
#define LAS __attribute__((address_space(3)))
#define GAP() __builtin_amdgcn_sched_barrier(0)
#define SRD_WORD3 0x00020000

constexpr int NTOK = 2048, DM = 576, NH = 32, DH = 128, SKV = 2048, FF = 4608;

constexpr size_t WS_MOD  = 0;
constexpr size_t WS_H1F  = 65536;
constexpr size_t WS_OF   = WS_H1F + (size_t)NTOK * DM * 2;
constexpr size_t WS_SLAB = WS_OF + (size_t)NTOK * NH * DH * 2;
constexpr size_t WS_X1   = WS_SLAB + (size_t)4 * NTOK * DM * 4;
constexpr size_t WS_H2F  = WS_X1 + (size_t)NTOK * DM * 2;
constexpr size_t WS_HHF  = WS_H2F + (size_t)NTOK * DM * 2;
constexpr size_t WS_END  = WS_HHF + (size_t)NTOK * FF * 2;

#define ACLOB_LO "a0","a1","a2","a3","a4","a5","a6","a7","a8","a9","a10","a11","a12","a13","a14","a15","a16","a17","a18","a19","a20","a21","a22","a23","a24","a25","a26","a27","a28","a29","a30","a31","a32","a33","a34","a35","a36","a37","a38","a39","a40","a41","a42","a43","a44","a45","a46","a47","a48","a49","a50","a51","a52","a53","a54","a55","a56","a57","a58","a59","a60","a61","a62","a63","a64","a65","a66","a67","a68","a69","a70","a71","a72","a73","a74","a75","a76","a77","a78","a79","a80","a81","a82","a83","a84","a85","a86","a87","a88","a89","a90","a91","a92","a93","a94","a95","a96","a97","a98","a99","a100","a101","a102","a103","a104","a105","a106","a107","a108","a109","a110","a111","a112","a113","a114","a115","a116","a117","a118","a119","a120","a121","a122","a123","a124","a125","a126","a127"
#define ACLOB_HI "a128","a129","a130","a131","a132","a133","a134","a135","a136","a137","a138","a139","a140","a141","a142","a143","a144","a145","a146","a147","a148","a149","a150","a151","a152","a153","a154","a155","a156","a157","a158","a159","a160","a161","a162","a163","a164","a165","a166","a167","a168","a169","a170","a171","a172","a173","a174","a175","a176","a177","a178","a179","a180","a181","a182","a183","a184","a185","a186","a187","a188","a189","a190","a191","a192","a193","a194","a195","a196","a197","a198","a199","a200","a201","a202","a203","a204","a205","a206","a207","a208","a209","a210","a211","a212","a213","a214","a215","a216","a217","a218","a219","a220","a221","a222","a223","a224","a225","a226","a227","a228","a229","a230","a231","a232","a233","a234","a235","a236","a237","a238","a239","a240","a241","a242","a243","a244","a245","a246","a247","a248","a249","a250","a251","a252","a253","a254","a255"


DI float bf2f(u16 u) { return __uint_as_float((unsigned)u << 16); }
DI float bflo(unsigned u) { return __uint_as_float(u << 16); }
DI float bfhi(unsigned u) { return __uint_as_float(u & 0xffff0000u); }
DI float rbf(float x) { return (float)(__bf16)x; }
DI unsigned cvtpk(float lo, float hi) { f32x2 v = {lo, hi}; bf16x2 b = __builtin_convertvector(v, bf16x2); return __builtin_bit_cast(unsigned, b); }
DI unsigned rfl(unsigned x) { return __builtin_amdgcn_readfirstlane(x); }

__global__ __launch_bounds__(256) void k_mod(const u16* __restrict__ c, const u16* __restrict__ wmod, float* __restrict__ mod) {
  __shared__ float sc[DM];
  __shared__ float red[32][65];
  const int tid = threadIdx.x, b = blockIdx.x;
  const int c8 = tid & 7, kg = tid >> 3;
  u32x4 w[18];
#pragma unroll
  for (int i = 0; i < 18; ++i) w[i] = *(const u32x4*)(wmod + (size_t)(kg + 32 * i) * (6 * DM) + 64 * b + 8 * c8);
  for (int i = tid; i < DM; i += 256) { float x = bf2f(c[i]); sc[i] = rbf(x / (1.f + __expf(-x))); }
  __syncthreads();
  float acc[8] = {0, 0, 0, 0, 0, 0, 0, 0};
#pragma unroll
  for (int i = 0; i < 18; ++i) {
    const float s = sc[kg + 32 * i];
#pragma unroll
    for (int j = 0; j < 4; ++j) { acc[2 * j] += s * bflo(w[i][j]); acc[2 * j + 1] += s * bfhi(w[i][j]); }
  }
#pragma unroll
  for (int j = 0; j < 8; ++j) red[kg][8 * c8 + j] = acc[j];
  __syncthreads();
  if (tid < 64) {
    float s = 0;
#pragma unroll
    for (int g = 0; g < 32; ++g) s += red[g][tid];
    mod[64 * b + tid] = rbf(s);
  }
}

DI void ln_mod_row(float (&v)[72], const float* __restrict__ shift, const float* __restrict__ scale, int sub) {
  float s = 0;
#pragma unroll
  for (int i = 0; i < 72; ++i) s += v[i];
  s += __shfl_xor(s, 1); s += __shfl_xor(s, 2); s += __shfl_xor(s, 4);
  const float mean = s * (1.f / DM);
  float q = 0;
#pragma unroll
  for (int i = 0; i < 72; ++i) { const float d = v[i] - mean; q += d * d; }
  q += __shfl_xor(q, 1); q += __shfl_xor(q, 2); q += __shfl_xor(q, 4);
  const float mb = rbf(mean), vb = rbf(q * (1.f / DM));
  const float den = rbf(sqrtf(rbf(vb + 1e-5f)));
#pragma unroll
  for (int i = 0; i < 9; ++i) {
    const int k0 = 8 * (sub + 8 * i);
    const f32x4 s0 = *(const f32x4*)(shift + k0), s1 = *(const f32x4*)(shift + k0 + 4);
    const f32x4 c0 = *(const f32x4*)(scale + k0), c1 = *(const f32x4*)(scale + k0 + 4);
#pragma unroll
    for (int e = 0; e < 8; ++e) {
      const float xn = rbf(rbf(v[8 * i + e] - mb) / den);
      const float cc = rbf(1.f + (e < 4 ? c0[e & 3] : c1[e & 3]));
      const float ss = e < 4 ? s0[e & 3] : s1[e & 3];
      v[8 * i + e] = rbf(rbf(xn * cc) + ss);
    }
  }
}

__global__ __launch_bounds__(64) void k_ln1(const u16* __restrict__ x, const float* __restrict__ mod, char* __restrict__ h1f) {
  const int lane = threadIdx.x, sub = lane & 7, row = blockIdx.x * 8 + (lane >> 3);
  float v[72];
#pragma unroll
  for (int i = 0; i < 9; ++i) {
    const u32x4 w = *(const u32x4*)(x + (size_t)row * DM + 8 * (sub + 8 * i));
#pragma unroll
    for (int j = 0; j < 4; ++j) { v[8 * i + 2 * j] = bflo(w[j]); v[8 * i + 2 * j + 1] = bfhi(w[j]); }
  }
  ln_mod_row(v, mod + 0 * DM, mod + 1 * DM, sub);
  const int rb = row >> 5, r = row & 31;
#pragma unroll
  for (int i = 0; i < 9; ++i) {
    const int c = sub + 8 * i, ks = c >> 1, a = c & 1;
#pragma unroll
    for (int hh = 0; hh < 2; ++hh) {
      u32x2 w = {cvtpk(v[8 * i + 4 * hh], v[8 * i + 4 * hh + 1]), cvtpk(v[8 * i + 4 * hh + 2], v[8 * i + 4 * hh + 3])};
      *(u32x2*)(h1f + ((size_t)((rb * 36 + ks) * 64 + 32 * hh + r)) * 16 + 8 * a) = w;
    }
  }
}

constexpr float ATT_THR = 8.0f;
constexpr int NT = SKV / 64;
constexpr unsigned KRS = 256u, KTILEB = 64u * KRS, WQRS = NH * DH * 2u;
constexpr size_t SHM_ATTN = 163840;
#define KRING 0x0000u
#define VRING 0x8000u
#define WRING 0x10000u

DI float max3f(float a, float b, float c) { float r; asm("v_max3_f32 %0, %1, %2, %3" : "=v"(r) : "v"(a), "v"(b), "v"(c)); return r; }
DI float max2f(float a, float b) { float r; asm("v_max_f32_e32 %0, %1, %2" : "=v"(r) : "v"(a), "v"(b)); return r; }
DI float fadd_s(float a, float b) { float r; asm("v_add_f32_e32 %0, %1, %2" : "=v"(r) : "v"(a), "v"(b)); return r; }
DI float fmul_s(float a, float b) { float r; asm("v_mul_f32_e32 %0, %1, %2" : "=v"(r) : "v"(a), "v"(b)); return r; }
DI float fsub_s(float a, float b) { float r; asm("v_sub_f32_e32 %0, %1, %2" : "=v"(r) : "v"(a), "v"(b)); return r; }
DI float w4_aone() { unsigned l; asm volatile("v_mbcnt_lo_u32_b32 %0, -1, 0\n\tv_mbcnt_hi_u32_b32 %0, -1, %0" : "=v"(l)); return l < 32u ? 1.f : 0.f; }
DI void negm_bcast(f32x16& negm, float aone, float neg_mhat) {
  asm volatile("s_nop 1\n\tv_mfma_f32_32x32x2_f32 %0, %1, %2, 0" : "+v"(negm) : "v"(aone), "v"(neg_mhat));
}

struct W4C {
  unsigned vkb[4], vvb, vksrc[2], vvsrc, m0k, m0v, sofk, sofv, ldsb;
  __amdgpu_buffer_rsrc_t srdK, srdV;
  f32x16 S[2][2][2];
  f32x16 NEGM[2];
  bf16x8 Vf[4][4];
  bf16x8 Pk[2][4];
  bf16x4 vlo[4][4], vup[4][4];
  float mhat[2], lsum[2], fct[2]; bool pend;
  float rs0[2], rs1[2], pmx0[2], pmx1[2], rmax[2], delta[2];
  unsigned pm0[8], psf[8];
};

constexpr int AO_(int db, int qb) { return 16 * (2 * db + qb); }
constexpr int AQ_(int qb, int d0) { return 128 + 32 * qb + 4 * d0; }
constexpr int AK_(int kb, int d0) { return 192 + 32 * kb + 4 * d0; }

template <int I> DI void tr_read(W4C& c, unsigned vaddr) {
  constexpr int ks = I >> 3, db = (I >> 1) & 3;
  if constexpr ((I & 1) == 0) asm volatile("ds_read_b64_tr_b16 %0, %1 offset:%2" : "=v"(c.vlo[db][ks]) : "v"(vaddr), "i"(4096 * ks + 512 * db) : "memory");
  else                        asm volatile("ds_read_b64_tr_b16 %0, %1 offset:%2" : "=v"(c.vup[db][ks]) : "v"(vaddr), "i"(4096 * ks + 2048 + 512 * db) : "memory");
}
template <int I> DI void k_read(W4C& c, unsigned slot) {
  constexpr int kb = I >> 3, d0 = I & 7;
  asm volatile("ds_read_b128 a[%c1:%c2], %0 offset:%c3" :: "v"(c.vkb[d0 & 3] + slot), "i"(AK_(kb, d0)), "i"(AK_(kb, d0) + 3), "i"(8192 * kb + 128 * (d0 >> 2)) : "memory", ACLOB_HI);
}
template <int I> DI void q_write1(const u32x4& w) {
  constexpr int aq = AQ_(I >> 3, I & 7);
  asm volatile("v_accvgpr_write_b32 a[%c4], %0\n\tv_accvgpr_write_b32 a[%c5], %1\n\tv_accvgpr_write_b32 a[%c6], %2\n\tv_accvgpr_write_b32 a[%c7], %3"
               :: "v"(w[0]), "v"(w[1]), "v"(w[2]), "v"(w[3]), "i"(aq), "i"(aq + 1), "i"(aq + 2), "i"(aq + 3) : ACLOB_HI);
}
template <int A> DI void acc_read8(float (&o)[8]) {
  asm volatile("v_accvgpr_read_b32 %0, a[%c8]\n\tv_accvgpr_read_b32 %1, a[%c9]\n\tv_accvgpr_read_b32 %2, a[%c10]\n\tv_accvgpr_read_b32 %3, a[%c11]\n\t"
               "v_accvgpr_read_b32 %4, a[%c12]\n\tv_accvgpr_read_b32 %5, a[%c13]\n\tv_accvgpr_read_b32 %6, a[%c14]\n\tv_accvgpr_read_b32 %7, a[%c15]"
               : "=v"(o[0]), "=v"(o[1]), "=v"(o[2]), "=v"(o[3]), "=v"(o[4]), "=v"(o[5]), "=v"(o[6]), "=v"(o[7])
               : "i"(A), "i"(A + 1), "i"(A + 2), "i"(A + 3), "i"(A + 4), "i"(A + 5), "i"(A + 6), "i"(A + 7));
}
template <int A> DI void acc_scale4(float f) {
  float t0, t1;
  asm volatile("v_accvgpr_read_b32 %0, a[%c3]\n\tv_accvgpr_read_b32 %1, a[%c4]\n\tv_mul_f32 %0, %2, %0\n\tv_mul_f32 %1, %2, %1\n\t"
               "v_accvgpr_write_b32 a[%c3], %0\n\tv_accvgpr_write_b32 a[%c4], %1\n\t"
               "v_accvgpr_read_b32 %0, a[%c5]\n\tv_accvgpr_read_b32 %1, a[%c6]\n\tv_mul_f32 %0, %2, %0\n\tv_mul_f32 %1, %2, %1\n\t"
               "v_accvgpr_write_b32 a[%c5], %0\n\tv_accvgpr_write_b32 a[%c6], %1"
               : "=&v"(t0), "=&v"(t1) : "v"(f), "i"(A), "i"(A + 1), "i"(A + 2), "i"(A + 3) : ACLOB_LO);
}
template <int... Is> DI void acc_scale_all(float f0, float f1, iseq<Is...>) { (acc_scale4<4 * Is>(((Is >> 2) & 1) ? f1 : f0), ...); }

template <int X, bool FIRST, int M> DI void qk_mfma(W4C& c) {
  constexpr int d0 = M >> 2, kb = (M >> 1) & 1, qb = M & 1, ak = AK_(kb, d0), aq = AQ_(qb, d0);
  if constexpr (d0 == 0) {
    if constexpr (FIRST) asm volatile("v_mfma_f32_32x32x16_bf16 %0, a[%c1:%c2], a[%c3:%c4], 0" : "=&v"(c.S[X][kb][qb]) : "i"(ak), "i"(ak + 3), "i"(aq), "i"(aq + 3) : ACLOB_LO, ACLOB_HI);
    else                 asm volatile("v_mfma_f32_32x32x16_bf16 %0, a[%c2:%c3], a[%c4:%c5], %1" : "=&v"(c.S[X][kb][qb]) : "v"(c.NEGM[qb]), "i"(ak), "i"(ak + 3), "i"(aq), "i"(aq + 3) : ACLOB_LO, ACLOB_HI);
  } else asm volatile("v_mfma_f32_32x32x16_bf16 %0, a[%c1:%c2], a[%c3:%c4], %0" : "+v"(c.S[X][kb][qb]) : "i"(ak), "i"(ak + 3), "i"(aq), "i"(aq + 3) : ACLOB_LO, ACLOB_HI);
}
template <int M, bool ZERO> DI void pv_mfma(W4C& c) {
  constexpr int ks = M >> 3, db = (M >> 1) & 3, qb = M & 1, ao = AO_(db, qb);
  if constexpr (ZERO && ks == 0) asm volatile("v_mfma_f32_32x32x16_bf16 a[%c2:%c3], %0, %1, 0" :: "v"(c.Vf[db][ks]), "v"(c.Pk[qb][ks]), "i"(ao), "i"(ao + 15) : ACLOB_LO);
  else                         asm volatile("v_mfma_f32_32x32x16_bf16 a[%c2:%c3], %0, %1, a[%c2:%c3]" :: "v"(c.Vf[db][ks]), "v"(c.Pk[qb][ks]), "i"(ao), "i"(ao + 15) : ACLOB_LO);
}
template <int X, int QB, int KS, int P> DI void pack1(W4C& c) {
  u32x4 w = __builtin_bit_cast(u32x4, c.Pk[QB][KS]);
  w[P] = cvtpk(c.S[X][KS >> 1][QB][8 * (KS & 1) + 2 * P], c.S[X][KS >> 1][QB][8 * (KS & 1) + 2 * P + 1]);
  c.Pk[QB][KS] = __builtin_bit_cast(bf16x8, w);
}
template <int X, int J> DI void rowsum1(W4C& c) {
  constexpr int qb = J >> 4, ks = (J >> 2) & 3, p = J & 3;
  c.rs0[qb] = fadd_s(c.rs0[qb], c.S[X][ks >> 1][qb][8 * (ks & 1) + 2 * p]); c.rs1[qb] = fadd_s(c.rs1[qb], c.S[X][ks >> 1][qb][8 * (ks & 1) + 2 * p + 1]);
}

template <int X, bool FIRST, int M> DI void gapA(W4C& c, unsigned vslot, const int t) {
  constexpr int Xp = X ^ 1;
  qk_mfma<X, FIRST, M>(c);
  if constexpr (M >= 16 && M <= 31) {
    const int tk = t + 2, tv = t + 1;
    constexpr int q = (M - 16) >> 1;
    if constexpr (((M - 16) & 1) == 0) {
      if constexpr (q < 4) c.pm0[q] = c.m0k + (unsigned)((t + 2) & 1) * 0x4000u + 1024u * q; else c.pm0[q] = c.m0v + (unsigned)((t + 1) & 1) * 0x4000u + 1024u * (q - 4);
      asm volatile("" : "+s"(c.pm0[q]));
    } else {
      if constexpr (q < 4) c.psf[q] = KTILEB * (unsigned)tk + c.sofk + KRS * 4u * q; else c.psf[q] = KTILEB * (unsigned)tv + c.sofv + KRS * 8u * ((q - 4) >> 1) + 128u * ((q - 4) & 1);
      asm volatile("" : "+s"(c.psf[q]));
    }
  }
  if constexpr (M == 31) asm volatile("s_nop 4" : "+s"(c.pm0[0]), "+s"(c.pm0[1]), "+s"(c.pm0[2]), "+s"(c.pm0[3]), "+s"(c.pm0[4]), "+s"(c.pm0[5]), "+s"(c.pm0[6]), "+s"(c.pm0[7]),
                                                  "+s"(c.psf[0]), "+s"(c.psf[1]), "+s"(c.psf[2]), "+s"(c.psf[3]), "+s"(c.psf[4]), "+s"(c.psf[5]), "+s"(c.psf[6]), "+s"(c.psf[7]));
  if constexpr (!FIRST && M == 8) asm volatile("" :: "v"(c.NEGM[0]), "v"(c.NEGM[1]));
  if constexpr (!FIRST) {
    constexpr int base = M + (M >= 4) + (M >= 8) + (M >= 12) + (M >= 16) + (M >= 20) + (M >= 24);
    if constexpr (M <= 25) tr_read<base>(c, c.vvb + vslot);
    if constexpr (M == 3 || M == 7 || M == 11 || M == 15 || M == 19 || M == 23) tr_read<base + 1>(c, c.vvb + vslot);
    if constexpr (M < 16) {
      constexpr int e0 = 2 * M, e1 = 2 * M + 1;
      c.S[Xp][1][e0 >> 4][e0 & 15] = __builtin_amdgcn_exp2f(c.S[Xp][1][e0 >> 4][e0 & 15]);
      c.S[Xp][1][e1 >> 4][e1 & 15] = __builtin_amdgcn_exp2f(c.S[Xp][1][e1 >> 4][e1 & 15]);
      pack1<Xp, (M >> 3), ((M >> 2) & 1), (M & 3)>(c);
    } else {
      constexpr int cc = M - 16;
      pack1<Xp, (cc >> 3), 2 + ((cc >> 2) & 1), (cc & 3)>(c);
      rowsum1<Xp, 16 * (cc >> 3) + 8 + 4 * ((cc >> 2) & 1) + (cc & 3)>(c);
    }
  }
  GAP();
}
#define W4_VLO "+v"(c.vlo[0][0]), "+v"(c.vlo[0][1]), "+v"(c.vlo[0][2]), "+v"(c.vlo[0][3]), "+v"(c.vlo[1][0]), "+v"(c.vlo[1][1]), "+v"(c.vlo[1][2]), "+v"(c.vlo[1][3]), \
               "+v"(c.vlo[2][0]), "+v"(c.vlo[2][1]), "+v"(c.vlo[2][2]), "+v"(c.vlo[2][3]), "+v"(c.vlo[3][0]), "+v"(c.vlo[3][1]), "+v"(c.vlo[3][2]), "+v"(c.vlo[3][3])
#define W4_VUP "+v"(c.vup[0][0]), "+v"(c.vup[0][1]), "+v"(c.vup[0][2]), "+v"(c.vup[0][3]), "+v"(c.vup[1][0]), "+v"(c.vup[1][1]), "+v"(c.vup[1][2]), "+v"(c.vup[1][3]), \
               "+v"(c.vup[2][0]), "+v"(c.vup[2][1]), "+v"(c.vup[2][2]), "+v"(c.vup[2][3]), "+v"(c.vup[3][0]), "+v"(c.vup[3][1]), "+v"(c.vup[3][2]), "+v"(c.vup[3][3])
DI void v_concat(W4C& c) {
#pragma unroll
  for (int ks = 0; ks < 4; ++ks)
#pragma unroll
    for (int db = 0; db < 4; ++db) c.Vf[db][ks] = __builtin_shufflevector(c.vlo[db][ks], c.vup[db][ks], 0, 1, 2, 3, 4, 5, 6, 7);
}
template <int X, bool FIRST, int... Ms> DI void phaseA_(W4C& c, const int t, iseq<Ms...>) {
  const unsigned vslot = (unsigned)((t - 1) & 1) * 0x4000u;
  (gapA<X, FIRST, Ms>(c, vslot, t), ...);
  if constexpr (!FIRST) {
#pragma unroll
    for (int qb = 0; qb < 2; ++qb) {
      const float rs = fadd_s(c.rs0[qb], c.rs1[qb]);
      auto pr = __builtin_amdgcn_permlane32_swap(__float_as_uint(rs), __float_as_uint(rs), false, false);
      c.lsum[qb] = fadd_s(c.lsum[qb], fadd_s(__uint_as_float(pr[0]), __uint_as_float(pr[1])));
    }
  }
  if constexpr (FIRST) asm volatile("s_waitcnt vmcnt(0) lgkmcnt(0)\n\ts_barrier" ::: "memory");
  else { asm volatile("s_waitcnt vmcnt(0) lgkmcnt(0)" : W4_VLO :: "memory"); asm volatile("s_barrier" : W4_VUP :: "memory"); }
  GAP();
  if constexpr (!FIRST) v_concat(c);
}
template <int X, bool FIRST> DI void phaseA(W4C& c, const int t) { phaseA_<X, FIRST>(c, t, make_iseq<32>{}); }

template <int X, bool PEEL, bool ZERO1, bool DK, bool DV, bool KR, int M> DI void gapB(W4C& c, const unsigned kslot_rd) {
  if constexpr (!PEEL) pv_mfma<M, ZERO1>(c);
  if constexpr (M < 4 && DK)                asm volatile("s_mov_b32 m0, %0\n\ts_nop 0\n\tbuffer_load_dwordx4 %1, %2, %3 offen lds" :: "s"(c.pm0[M]), "v"(c.vksrc[M & 1]), "s"(c.srdK), "s"(c.psf[M]) : "m0", "memory");
  else if constexpr (M >= 4 && M < 8 && DV) asm volatile("s_mov_b32 m0, %0\n\ts_nop 0\n\tbuffer_load_dwordx4 %1, %2, %3 offen lds" :: "s"(c.pm0[M]), "v"(c.vvsrc), "s"(c.srdV), "s"(c.psf[M]) : "m0", "memory");
  if constexpr (M >= 1 && M <= 16 && KR) k_read<M - 1>(c, kslot_rd);
  if constexpr (M >= 8 && M < 16) {
    constexpr int i = M - 8, q = i >> 2, r = 4 * (i & 3);
    if constexpr ((i & 3) == 0) { c.pmx0[q] = max3f(c.S[X][0][q][0], c.S[X][0][q][1], c.S[X][1][q][0]); c.pmx1[q] = max3f(c.S[X][0][q][2], c.S[X][0][q][3], c.S[X][1][q][1]);
                                  c.pmx0[q] = max3f(c.pmx0[q], c.S[X][1][q][2], c.S[X][1][q][3]); }
    else                        { c.pmx0[q] = max3f(c.pmx0[q], c.S[X][0][q][r], c.S[X][0][q][r + 1]); c.pmx1[q] = max3f(c.pmx1[q], c.S[X][0][q][r + 2], c.S[X][0][q][r + 3]);
                                  c.pmx0[q] = max3f(c.pmx0[q], c.S[X][1][q][r], c.S[X][1][q][r + 1]); c.pmx1[q] = max3f(c.pmx1[q], c.S[X][1][q][r + 2], c.S[X][1][q][r + 3]); }
  }
  if constexpr (M == 16 || M == 17) {
    constexpr int q = M - 16; const float mm = max2f(c.pmx0[q], c.pmx1[q]);
    auto pr = __builtin_amdgcn_permlane32_swap(__float_as_uint(mm), __float_as_uint(mm), false, false);
    c.rmax[q] = max2f(__uint_as_float(pr[0]), __uint_as_float(pr[1]));
  }
  if constexpr (M == 18) {
    if constexpr (PEEL) {
#pragma unroll
      for (int q = 0; q < 2; ++q) { c.mhat[q] = c.rmax[q]; c.lsum[q] = 0.f; c.fct[q] = 1.f;
#pragma unroll
        for (int kb = 0; kb < 2; ++kb) { _Pragma("unroll") for (int e = 0; e < 16; ++e) c.S[X][kb][q][e] = fsub_s(c.S[X][kb][q][e], c.rmax[q]); }
        negm_bcast(c.NEGM[q], w4_aone(), -c.mhat[q]);
      }
    } else {
      const bool grew = (c.rmax[0] > ATT_THR) | (c.rmax[1] > ATT_THR);
      if (__builtin_expect(__any((int)grew), 0)) {
#pragma unroll
        for (int q = 0; q < 2; ++q) { const float dl = max2f(c.rmax[q], 0.f); c.delta[q] = dl; c.mhat[q] = fadd_s(c.mhat[q], dl); c.fct[q] = __builtin_amdgcn_exp2f(-dl);
#pragma unroll
          for (int kb = 0; kb < 2; ++kb) { _Pragma("unroll") for (int e = 0; e < 16; ++e) c.S[X][kb][q][e] = fsub_s(c.S[X][kb][q][e], dl); }
          negm_bcast(c.NEGM[q], w4_aone(), -c.mhat[q]);
        }
        c.pend = true;
      }
    }
  }
  if constexpr (M == 18) { c.rs0[0] = c.rs0[1] = c.rs1[0] = c.rs1[1] = 0.f; }
  if constexpr (M >= 19) {
    constexpr int g = M - 19, e_lo = (32 * g) / 13, e_hi = (32 * (g + 1)) / 13;
    {
      constexpr int p_lo = g ? (32 * (g - 1)) / 13 : 0, p_hi = g ? (32 * g) / 13 : 0;
      if constexpr (p_lo + 0 < p_hi) { constexpr int e = p_lo + 0; if constexpr (e & 1) c.rs1[e >> 4] = fadd_s(c.rs1[e >> 4], c.S[X][0][e >> 4][e & 15]); else c.rs0[e >> 4] = fadd_s(c.rs0[e >> 4], c.S[X][0][e >> 4][e & 15]); }
      if constexpr (p_lo + 1 < p_hi) { constexpr int e = p_lo + 1; if constexpr (e & 1) c.rs1[e >> 4] = fadd_s(c.rs1[e >> 4], c.S[X][0][e >> 4][e & 15]); else c.rs0[e >> 4] = fadd_s(c.rs0[e >> 4], c.S[X][0][e >> 4][e & 15]); }
      if constexpr (p_lo + 2 < p_hi) { constexpr int e = p_lo + 2; if constexpr (e & 1) c.rs1[e >> 4] = fadd_s(c.rs1[e >> 4], c.S[X][0][e >> 4][e & 15]); else c.rs0[e >> 4] = fadd_s(c.rs0[e >> 4], c.S[X][0][e >> 4][e & 15]); }
    }
    if constexpr (e_lo + 0 < e_hi) c.S[X][0][(e_lo + 0) >> 4][(e_lo + 0) & 15] = __builtin_amdgcn_exp2f(c.S[X][0][(e_lo + 0) >> 4][(e_lo + 0) & 15]);
    if constexpr (e_lo + 1 < e_hi) c.S[X][0][(e_lo + 1) >> 4][(e_lo + 1) & 15] = __builtin_amdgcn_exp2f(c.S[X][0][(e_lo + 1) >> 4][(e_lo + 1) & 15]);
    if constexpr (e_lo + 2 < e_hi) c.S[X][0][(e_lo + 2) >> 4][(e_lo + 2) & 15] = __builtin_amdgcn_exp2f(c.S[X][0][(e_lo + 2) >> 4][(e_lo + 2) & 15]);
  }
  if constexpr (M == 31) asm volatile("" :: "v"(c.S[X][0][0]), "v"(c.S[X][0][1]), "v"(c.NEGM[0]), "v"(c.NEGM[1]), "v"(c.rs0[0]), "v"(c.rs0[1]), "v"(c.rs1[0]), "v"(c.rs1[1]));
  GAP();
}
template <int X, bool PEEL, bool ZERO1, bool DK, bool DV, bool KR, int... Ms> DI void phaseB_(W4C& c, const int t, iseq<Ms...>) {
  const unsigned kslot_rd = (unsigned)((t + 1) & 1) * 0x4000u;
  if constexpr (!PEEL) asm volatile("s_nop 1" ::: "memory");
  GAP();
  (gapB<X, PEEL, ZERO1, DK, DV, KR, Ms>(c, kslot_rd), ...);
  {
#pragma unroll
    for (int e = (32 * 12) / 13; e < 32; ++e) { if (e & 1) c.rs1[e >> 4] = fadd_s(c.rs1[e >> 4], c.S[X][0][e >> 4][e & 15]); else c.rs0[e >> 4] = fadd_s(c.rs0[e >> 4], c.S[X][0][e >> 4][e & 15]); }
  }
  if constexpr (!PEEL) {
    if (__builtin_expect(c.pend, 0)) {
      asm volatile("s_nop 7\n\ts_nop 7" ::: "memory");
      acc_scale_all(c.fct[0], c.fct[1], make_iseq<32>{});
      asm volatile("s_nop 1" ::: "memory");
      c.lsum[0] = fmul_s(c.lsum[0], c.fct[0]); c.lsum[1] = fmul_s(c.lsum[1], c.fct[1]); c.pend = false;
    }
  }
  asm volatile("s_waitcnt lgkmcnt(0)" ::: "memory");
  GAP();
}
template <int X, bool PEEL, bool ZERO1, bool DK = true, bool DV = true, bool KR = true> DI void phaseB(W4C& c, const int t) { phaseB_<X, PEEL, ZERO1, DK, DV, KR>(c, t, make_iseq<32>{}); }

template <int... Is> DI void k_reads(W4C& c, unsigned slot, iseq<Is...>) { (k_read<Is>(c, slot), ...); }
template <int... Is> DI void tr_reads(W4C& c, unsigned vaddr, iseq<Is...>) { (tr_read<Is>(c, vaddr), ...); }
template <int X, int... Cs> DI void packs(W4C& c, iseq<Cs...>) { (pack1<X, ((Cs >> 3) & 1), 2 * (Cs >> 4) + ((Cs >> 2) & 1), (Cs & 3)>(c), ...); }
template <int X, int J> DI void rowsum1_padded(W4C& c) { rowsum1<X, J>(c); asm volatile("s_nop 1"); }
template <int X, int... Js> DI void rowsums(W4C& c, iseq<Js...>) { (rowsum1_padded<X, Js>(c), ...); }
template <bool Z, int... Ms> DI void pv_all(W4C& c, iseq<Ms...>) { (pv_mfma<Ms, Z>(c), ...); }

template <int I> DI void q_convert1(const float C) {
  constexpr int db = I >> 2, qb = (I >> 1) & 1, s = I & 1;
  float o[8];
  acc_read8<AO_(db, qb) + 8 * s>(o);
  u32x4 w;
#pragma unroll
  for (int e = 0; e < 4; ++e) w[e] = cvtpk(fmul_s(rbf(o[2 * e]), C), fmul_s(rbf(o[2 * e + 1]), C));
  q_write1<8 * qb + 2 * db + s>(w);
}
template <int... Is> DI void q_convert(const float C, iseq<Is...>) { (q_convert1<Is>(C), ...); }

template <int I> DI void o_store1(char* __restrict__ obase, const float il0, const float il1) {
  constexpr int qb = I >> 2, db = I & 3;
  const float il = qb ? il1 : il0;
  float o0[8], o1[8];
  acc_read8<AO_(db, qb)>(o0); acc_read8<AO_(db, qb) + 8>(o1);
  u32x4 w0 = {cvtpk(fmul_s(o0[0], il), fmul_s(o0[1], il)), cvtpk(fmul_s(o0[2], il), fmul_s(o0[3], il)), cvtpk(fmul_s(o1[0], il), fmul_s(o1[1], il)), cvtpk(fmul_s(o1[2], il), fmul_s(o1[3], il))};
  u32x4 w1 = {cvtpk(fmul_s(o0[4], il), fmul_s(o0[5], il)), cvtpk(fmul_s(o0[6], il), fmul_s(o0[7], il)), cvtpk(fmul_s(o1[4], il), fmul_s(o1[5], il)), cvtpk(fmul_s(o1[6], il), fmul_s(o1[7], il))};
  char* p = obase + (size_t)qb * (2 * 128 * 1024) + db * 1024;
  *(u32x4*)(p) = w0;
  *(u32x4*)(p + 512) = w1;
}
template <int... Is> DI void o_store(char* obase, float il0, float il1, iseq<Is...>) { (o_store1<Is>(obase, il0, il1), ...); }

__global__ __launch_bounds__(256, 1)
void k_attn(const char* __restrict__ h1f, const u16* __restrict__ wq, const u16* __restrict__ Kc, const u16* __restrict__ Vc, char* __restrict__ of) {
  extern __shared__ __attribute__((aligned(16))) char lds[];
  const unsigned tid = threadIdx.x, lane = tid & 63u, wid = rfl(tid >> 6), l32 = lane & 31u, hi = lane >> 5;
  constexpr int T = NT;
  const float C = 0.12751743082f;
  W4C c;
  c.ldsb = (unsigned)(size_t)lds;
#pragma unroll
  for (int j = 0; j < 4; ++j) c.vkb[j] = c.ldsb + KRING + 256u * l32 + 16u * ((2u * j + hi) ^ (l32 & 7u));
  c.vvb = c.ldsb + VRING + (((lane & 3u) << 3) | (((lane >> 2) & 3u) << 6) | (((lane >> 4) & 1u) << 5) | (hi << 8));
#pragma unroll
  for (int par = 0; par < 2; ++par) c.vksrc[par] = KRS * (lane >> 4) + 16u * ((lane & 15u) ^ (4u * par + (lane >> 4)));
  c.vvsrc = KRS * ((lane >> 2) & 7u) + 64u * hi + 16u * (lane & 3u);
  c.m0k = rfl(c.ldsb + KRING + 4096u * wid); c.m0v = rfl(c.ldsb + VRING + 4096u * wid);
  c.sofk = rfl(KRS * 16u * wid); c.sofv = rfl(KRS * 16u * wid);

  const unsigned blk = blockIdx.x;
  const unsigned head = (blk & 7u) + 8u * ((blk >> 3) & 3u), y = blk >> 5;
  const unsigned row0 = 256u * y + 64u * wid;
  c.srdK = __builtin_amdgcn_make_buffer_rsrc((void*)((const char*)Kc + (size_t)head * SKV * 256), (short)0, SKV * 256, SRD_WORD3);
  c.srdV = __builtin_amdgcn_make_buffer_rsrc((void*)((const char*)Vc + (size_t)head * SKV * 256), (short)0, SKV * 256, SRD_WORD3);
  __amdgpu_buffer_rsrc_t srdW = __builtin_amdgcn_make_buffer_rsrc((void*)((const char*)wq + (size_t)head * 256), (short)0, (int)(DM * WQRS - head * 256), SRD_WORD3);
  __amdgpu_buffer_rsrc_t srdH = __builtin_amdgcn_make_buffer_rsrc((void*)(h1f + (size_t)(row0 >> 5) * 36 * 1024), (short)0, 2 * 36 * 1024, SRD_WORD3);

  auto dma_k = [&](int tk) {
    const unsigned slot = (unsigned)(tk & 1) * 0x4000u, skv = KTILEB * (unsigned)tk;
#pragma unroll
    for (int q = 0; q < 4; ++q)
      asm volatile("s_nop 4\n\ts_mov_b32 m0, %0\n\ts_nop 0\n\tbuffer_load_dwordx4 %1, %2, %3 offen lds"
                   :: "s"(rfl(c.m0k + slot + 1024u * q)), "v"(c.vksrc[q & 1]), "s"(c.srdK), "s"(rfl(skv + c.sofk + KRS * 4u * q)) : "m0", "memory");
  };
  auto dma_v = [&](int tv) {
    const unsigned slot = (unsigned)(tv & 1) * 0x4000u, skv = KTILEB * (unsigned)tv;
#pragma unroll
    for (int q = 0; q < 4; ++q)
      asm volatile("s_nop 4\n\ts_mov_b32 m0, %0\n\ts_nop 0\n\tbuffer_load_dwordx4 %1, %2, %3 offen lds"
                   :: "s"(rfl(c.m0v + slot + 1024u * q)), "v"(c.vvsrc), "s"(c.srdV), "s"(rfl(skv + c.sofv + KRS * 8u * (q >> 1) + 128u * (q & 1))) : "m0", "memory");
  };
  {
    const unsigned vwsrc = WQRS * ((lane >> 2) & 7u) + 64u * hi + 16u * (lane & 3u);
    const unsigned p = lane & 3u, pp = ((p & 1u) << 1) | (p >> 1);
    const unsigned vwb = c.ldsb + WRING + ((pp << 3) | (((lane >> 2) & 3u) << 6) | (((lane >> 4) & 1u) << 5) | (hi << 8));
    const unsigned m0w = rfl(c.ldsb + WRING + 4096u * wid), sofw = rfl(WQRS * 16u * wid);
    const unsigned vh = lane * 16u;
    auto dma_w = [&](int kt) {
      const unsigned slot = (unsigned)(kt % 6) * 0x4000u, sk = 64u * WQRS * (unsigned)kt;
#pragma unroll
      for (int q = 0; q < 4; ++q)
        asm volatile("s_nop 4\n\ts_mov_b32 m0, %0\n\ts_nop 0\n\tbuffer_load_dwordx4 %1, %2, %3 offen lds"
                     :: "s"(rfl(m0w + slot + 1024u * q)), "v"(vwsrc), "s"(srdW), "s"(rfl(sk + sofw + WQRS * 8u * (q >> 1) + 128u * (q & 1))) : "m0", "memory");
    };
#pragma unroll
    for (int kt = 0; kt < 6; ++kt) dma_w(kt);
    bf16x8 hn[2][4];
    auto load_h = [&](int kt) {
#pragma unroll
      for (int qb = 0; qb < 2; ++qb)
#pragma unroll
        for (int ks = 0; ks < 4; ++ks)
          asm volatile("buffer_load_dwordx4 %0, %1, %2, %3 offen" : "=v"(hn[qb][ks]) : "v"(vh), "s"(srdH), "s"(rfl((unsigned)((qb * 36 + 4 * kt + ks) * 1024))) : "memory");
    };
    load_h(0);
#pragma unroll
    for (int kt = 0; kt < 9; ++kt) {
      asm volatile("s_waitcnt vmcnt(0)" : "+v"(hn[0][0]), "+v"(hn[0][1]), "+v"(hn[0][2]), "+v"(hn[0][3]), "+v"(hn[1][0]), "+v"(hn[1][1]), "+v"(hn[1][2]), "+v"(hn[1][3]) :: "memory");
      asm volatile("s_barrier" ::: "memory");
#pragma unroll
      for (int qb = 0; qb < 2; ++qb)
#pragma unroll
        for (int ks = 0; ks < 4; ++ks) c.Pk[qb][ks] = hn[qb][ks];
      GAP();
      if (kt >= 1 && kt + 5 < 9) dma_w(kt + 5);
      if (kt + 1 < 9) load_h(kt + 1);
      if (kt == 6) { dma_k(0); dma_v(0); dma_k(1); }
      tr_reads(c, vwb + (unsigned)(kt % 6) * 0x4000u, make_iseq<32>{});
      asm volatile("s_waitcnt lgkmcnt(0)" : W4_VLO :: "memory"); asm volatile("" : W4_VUP :: "memory");
      GAP();
      v_concat(c);
      asm volatile("s_nop 1" ::: "memory");
      if (kt == 0) pv_all<true>(c, make_iseq<32>{}); else pv_all<false>(c, make_iseq<32>{});
      GAP();
    }
    asm volatile("s_nop 7\n\ts_nop 7" ::: "memory");
    q_convert(C, make_iseq<16>{});
  }
  asm volatile("s_waitcnt vmcnt(0)\n\ts_barrier" ::: "memory");
  k_reads(c, 0u, make_iseq<16>{});
  asm volatile("s_waitcnt lgkmcnt(0)\n\ts_nop 1" ::: "memory");

  c.mhat[0] = c.mhat[1] = 0.f; c.lsum[0] = c.lsum[1] = 0.f; c.fct[0] = c.fct[1] = 1.f; c.pend = false;

  phaseA<0, true>(c, 0);  phaseB<0, true,  false>(c, 0);
  phaseA<1, false>(c, 1); phaseB<1, false, true>(c, 1);
  for (int t = 2; t < T - 2; t += 2) {
    phaseA<0, false>(c, t);     phaseB<0, false, false>(c, t);
    phaseA<1, false>(c, t + 1); phaseB<1, false, false>(c, t + 1);
  }
  phaseA<0, false>(c, T - 2); phaseB<0, false, false, false, true, true>(c, T - 2);
  phaseA<1, false>(c, T - 1); phaseB<1, false, false, false, false, false>(c, T - 1);
  {
    const unsigned vslot = (unsigned)((T - 1) & 1) * 0x4000u;
    tr_reads(c, c.vvb + vslot, make_iseq<32>{});
#pragma unroll
    for (int e = 0; e < 32; ++e) c.S[1][1][e >> 4][e & 15] = __builtin_amdgcn_exp2f(c.S[1][1][e >> 4][e & 15]);
    packs<1>(c, make_iseq<32>{});
    rowsums<1>(c, iseq<8, 9, 10, 11, 12, 13, 14, 15, 24, 25, 26, 27, 28, 29, 30, 31>{});
#pragma unroll
    for (int qb = 0; qb < 2; ++qb) { const float rs = fadd_s(c.rs0[qb], c.rs1[qb]);
      auto pr = __builtin_amdgcn_permlane32_swap(__float_as_uint(rs), __float_as_uint(rs), false, false);
      c.lsum[qb] = fadd_s(c.lsum[qb], fadd_s(__uint_as_float(pr[0]), __uint_as_float(pr[1]))); }
    asm volatile("s_waitcnt lgkmcnt(0)" : W4_VLO :: "memory"); asm volatile("" : W4_VUP :: "memory");
    GAP();
    v_concat(c);
    asm volatile("s_nop 1" ::: "memory");
    pv_all<false>(c, make_iseq<32>{});
    asm volatile("s_nop 7\n\ts_nop 7" ::: "memory");
  }
  {
    unsigned lb; asm volatile("v_mbcnt_lo_u32_b32 %0, -1, 0\n\tv_mbcnt_hi_u32_b32 %0, -1, %0" : "=v"(lb));
    const unsigned r32 = lb & 31u, hh = lb >> 5;
    const unsigned row = row0 + r32;
    char* obase = of + ((size_t)(row >> 4) * 128 + head * 4) * 1024 + (16u * hh + (row & 15u)) * 16u;
    const float il0 = 1.f / c.lsum[0], il1 = 1.f / c.lsum[1];
    o_store(obase, il0, il1, make_iseq<8>{});
  }
}

constexpr int G_SLAB = 9216, G_XOFF = 2 * G_SLAB, G_STAGE = G_XOFF + 16384, G_NS = 4;
constexpr size_t SHM_GEMM = (size_t)G_STAGE * G_NS;

template <int MODE>
__global__ __launch_bounds__(256, 1)
void k_gemm(const char* __restrict__ xf, const u16* __restrict__ wA, const u16* __restrict__ wB, char* __restrict__ outp) {
  extern __shared__ __attribute__((aligned(16))) char lds[];
  constexpr bool G1 = (MODE == 1);
  constexpr int NSTG = (MODE == 0) ? 16 : 18;
  constexpr int XK32 = (MODE == 0) ? 128 : (MODE == 1 ? 18 : 144);
  constexpr unsigned LDWB = (G1 ? FF : DM) * 2u;
  constexpr int KROWS = (MODE == 0) ? NH * DH : (MODE == 1 ? DM : FF);
  constexpr int TB = G1 ? 4 : 2, NB = G1 ? 18 : 9;
  const unsigned tid = threadIdx.x, lane = tid & 63u, wid = rfl(tid >> 6);
  const unsigned blk = blockIdx.x, xcd = blk & 7u, idx = blk >> 3;
  const unsigned split = xcd >> 1;
  unsigned tok16_0, n0, k32_0;
  if constexpr (G1) { const unsigned ft = split * 8u + (idx & 7u), rb = (xcd & 1u) * 4u + (idx >> 3); tok16_0 = rb * 16u + wid * 4u; n0 = ft * 144u; k32_0 = 0; }
  else              { const unsigned cb = idx & 3u, rb = (xcd & 1u) * 8u + (idx >> 2); tok16_0 = rb * 8u + wid * 2u; n0 = cb * 144u; k32_0 = split * (2u * NSTG); }
  const unsigned ldsb = (unsigned)(size_t)lds;
  const __amdgpu_buffer_rsrc_t srdA = __builtin_amdgcn_make_buffer_rsrc((void*)((const char*)wA + n0 * 2u), (short)0, (int)(KROWS * LDWB - n0 * 2u), SRD_WORD3);
  const __amdgpu_buffer_rsrc_t srdB = __builtin_amdgcn_make_buffer_rsrc((void*)((const char*)wB + n0 * 2u), (short)0, (int)(KROWS * LDWB - n0 * 2u), SRD_WORD3);
  const __amdgpu_buffer_rsrc_t srdHf = __builtin_amdgcn_make_buffer_rsrc((void*)((const char*)(wid < 2 ? wA : wB) + n0 * 2u), (short)0, (int)(KROWS * LDWB - n0 * 2u), SRD_WORD3);
  const __amdgpu_buffer_rsrc_t srdX = __builtin_amdgcn_make_buffer_rsrc((void*)(xf + (size_t)tok16_0 * XK32 * 1024), (short)0, TB * XK32 * 1024, SRD_WORD3);
  unsigned vw[3];
#pragma unroll
  for (int i = 0; i < 3; ++i) { const unsigned id = (i < 2 ? (2u * wid + i) : 8u) * 64u + lane, r = id / 18u, ch = id - r * 18u; vw[i] = r * LDWB + ch * 16u; }
  const unsigned vx = lane * 16u;
  const unsigned long long hmask = (wid & 1u) ? 0xffffffff00000000ull : 0x00000000ffffffffull;
  auto issue = [&](int s) {
    const unsigned slot = ldsb + (unsigned)(s & (G_NS - 1)) * G_STAGE;
    const unsigned kA = G1 ? 32u * s : 32u * (k32_0 + 2u * s), kB = G1 ? kA : kA + 32u;
    const unsigned sA = rfl(kA * LDWB), sB = rfl(kB * LDWB), sH = rfl((wid < 2 ? kA : kB) * LDWB);
#pragma unroll
    for (int i = 0; i < 2; ++i) {
      asm volatile("s_nop 4\n\ts_mov_b32 m0, %0\n\ts_nop 0\n\tbuffer_load_dwordx4 %1, %2, %3 offen lds" :: "s"(rfl(slot + (2u * wid + i) * 1024u)), "v"(vw[i]), "s"(srdA), "s"(sA) : "m0", "memory");
      asm volatile("s_nop 4\n\ts_mov_b32 m0, %0\n\ts_nop 0\n\tbuffer_load_dwordx4 %1, %2, %3 offen lds" :: "s"(rfl(slot + G_SLAB + (2u * wid + i) * 1024u)), "v"(vw[i]), "s"(srdB), "s"(sB) : "m0", "memory");
    }
    asm volatile("s_nop 4\n\ts_mov_b32 m0, %0\n\ts_mov_b64 exec, %4\n\tbuffer_load_dwordx4 %1, %2, %3 offen lds\n\ts_mov_b64 exec, -1"
                 :: "s"(rfl(slot + (wid < 2 ? 0u : (unsigned)G_SLAB) + 8u * 1024u)), "v"(vw[2]), "s"(srdHf), "s"(sH), "s"(hmask) : "m0", "memory");
#pragma unroll
    for (int i = 0; i < 4; ++i) {
      const unsigned tb = G1 ? i : (i >> 1), k32 = G1 ? (unsigned)s : (k32_0 + 2u * s + (i & 1));
      asm volatile("s_nop 4\n\ts_mov_b32 m0, %0\n\ts_nop 0\n\tbuffer_load_dwordx4 %1, %2, %3 offen lds" :: "s"(rfl(slot + G_XOFF + (4u * wid + i) * 1024u)), "v"(vx), "s"(srdX), "s"(rfl((tb * XK32 + k32) * 1024u)) : "m0", "memory");
    }
  };
  f32x4 acc[NB][TB];
#pragma unroll
  for (int nb = 0; nb < NB; ++nb)
#pragma unroll
    for (int tb = 0; tb < TB; ++tb) acc[nb][tb] = f32x4{0, 0, 0, 0};
  const unsigned g = lane >> 4, q = (lane >> 2) & 3u, p = lane & 3u;
  const unsigned wrd = (4u * g + q) * 288u + 8u * p;
  const unsigned xrd = G_XOFF + wid * 4096u + lane * 16u;
  issue(0); issue(1); issue(2);
  for (int s = 0; s < NSTG; ++s) {
    if (s + 2 < NSTG)      asm volatile("s_waitcnt vmcnt(18) lgkmcnt(0)\n\ts_barrier" ::: "memory");
    else if (s + 1 < NSTG) asm volatile("s_waitcnt vmcnt(9) lgkmcnt(0)\n\ts_barrier" ::: "memory");
    else                   asm volatile("s_waitcnt vmcnt(0) lgkmcnt(0)\n\ts_barrier" ::: "memory");
    if (s + 3 < NSTG) issue(s + 3);
    const LAS char* sb = (const LAS char*)(uintptr_t)(ldsb + (unsigned)(s & (G_NS - 1)) * G_STAGE);
    bf16x8 xfr[4];
#pragma unroll
    for (int i = 0; i < 4; ++i) xfr[i] = *(const LAS bf16x8*)(sb + xrd + i * 1024);
#pragma unroll
    for (int h = 0; h < 2; ++h)
#pragma unroll
      for (int nb = 0; nb < 9; ++nb) {
        const bf16x4 lo = __builtin_amdgcn_ds_read_tr16_b64_v4bf16((LAS bf16x4*)(sb + wrd + h * G_SLAB + nb * 32));
        const bf16x4 up = __builtin_amdgcn_ds_read_tr16_b64_v4bf16((LAS bf16x4*)(sb + wrd + h * G_SLAB + 16 * 288 + nb * 32));
        const bf16x8 wf = __builtin_shufflevector(lo, up, 0, 1, 2, 3, 4, 5, 6, 7);
        if constexpr (G1) {
#pragma unroll
          for (int tb = 0; tb < 4; ++tb) acc[9 * h + nb][tb] = __builtin_amdgcn_mfma_f32_16x16x32_bf16(wf, xfr[tb], acc[9 * h + nb][tb], 0, 0, 0);
        } else {
#pragma unroll
          for (int tb = 0; tb < 2; ++tb) acc[nb][tb] = __builtin_amdgcn_mfma_f32_16x16x32_bf16(wf, xfr[2 * tb + h], acc[nb][tb], 0, 0, 0);
        }
      }
  }
  const unsigned r = lane & 15u;
  if constexpr (G1) {
#pragma unroll
    for (int tb = 0; tb < 4; ++tb)
#pragma unroll
      for (int nb = 0; nb < 9; ++nb) {
        float hv[4];
#pragma unroll
        for (int e = 0; e < 4; ++e) { const float gt = rbf(acc[nb][tb][e]), up = rbf(acc[9 + nb][tb][e]); hv[e] = (gt > 0.f ? gt : 0.f) * up; }
        const unsigned b16 = n0 / 16u + nb;
        u32x2 w = {cvtpk(hv[0], hv[1]), cvtpk(hv[2], hv[3])};
        *(u32x2*)(outp + ((size_t)((tok16_0 + tb) * 144u + (b16 >> 1)) * 64u + lane) * 16u + 8u * (b16 & 1u)) = w;
      }
  } else {
    float* slab = (float*)outp + (size_t)split * NTOK * DM;
#pragma unroll
    for (int tb = 0; tb < 2; ++tb)
#pragma unroll
      for (int nb = 0; nb < 9; ++nb)
        *(f32x4*)(slab + (size_t)((tok16_0 + tb) * 16u + r) * DM + n0 + 16 * nb + 4u * g) = acc[nb][tb];
  }
}

template <bool FINAL>
__global__ __launch_bounds__(64) void k_red(const float* __restrict__ slab, const u16* __restrict__ xin, const float* __restrict__ mod,
                                            u16* __restrict__ xout, char* __restrict__ h2f) {
  const int lane = threadIdx.x, sub = lane & 7, row = blockIdx.x * 8 + (lane >> 3);
  const float* gate = mod + (FINAL ? 5 : 2) * DM;
  float v[72];
#pragma unroll
  for (int i = 0; i < 9; ++i) {
    const int k0 = 8 * (sub + 8 * i);
    f32x4 a0 = {0, 0, 0, 0}, a1 = {0, 0, 0, 0};
#pragma unroll
    for (int sp = 0; sp < 4; ++sp) {
      const float* ps = slab + ((size_t)sp * NTOK + row) * DM + k0;
      a0 += *(const f32x4*)ps; a1 += *(const f32x4*)(ps + 4);
    }
    const u32x4 w = *(const u32x4*)(xin + (size_t)row * DM + k0);
    const f32x4 g0 = *(const f32x4*)(gate + k0), g1 = *(const f32x4*)(gate + k0 + 4);
#pragma unroll
    for (int e = 0; e < 8; ++e) {
      const float xv = (e & 1) ? bfhi(w[e >> 1]) : bflo(w[e >> 1]);
      const float av = rbf(e < 4 ? a0[e & 3] : a1[e & 3]);
      const float gv = e < 4 ? g0[e & 3] : g1[e & 3];
      v[8 * i + e] = rbf(xv + rbf(gv * av));
    }
    u32x4 o = {cvtpk(v[8 * i], v[8 * i + 1]), cvtpk(v[8 * i + 2], v[8 * i + 3]), cvtpk(v[8 * i + 4], v[8 * i + 5]), cvtpk(v[8 * i + 6], v[8 * i + 7])};
    *(u32x4*)(xout + (size_t)row * DM + k0) = o;
  }
  if constexpr (!FINAL) {
    ln_mod_row(v, mod + 3 * DM, mod + 4 * DM, sub);
    const int tok16 = row >> 4, r = row & 15;
#pragma unroll
    for (int i = 0; i < 9; ++i) {
      const int c = sub + 8 * i, k32 = c >> 2, t = (c >> 1) & 1;
#pragma unroll
      for (int qd = 0; qd < 2; ++qd) {
        const int g = 2 * (c & 1) + qd;
        u32x2 w = {cvtpk(v[8 * i + 4 * qd], v[8 * i + 4 * qd + 1]), cvtpk(v[8 * i + 4 * qd + 2], v[8 * i + 4 * qd + 3])};
        *(u32x2*)(h2f + ((size_t)((tok16 * 18 + k32) * 64 + 16 * g + r)) * 16 + 8 * t) = w;
      }
    }
  }
}

extern "C" void kernel_launch(void* const* d_in, const int* in_sizes, int n_in, void* d_out, int out_size, void* d_ws, size_t ws_size, hipStream_t stream) {
  const u16* x    = (const u16*)d_in[0];
  const u16* kc   = (const u16*)d_in[1];
  const u16* vc   = (const u16*)d_in[2];
  const u16* wq   = (const u16*)d_in[3];
  const u16* wo   = (const u16*)d_in[4];
  const u16* wg   = (const u16*)d_in[6];
  const u16* wu   = (const u16*)d_in[7];
  const u16* wd   = (const u16*)d_in[8];
  const u16* cc   = (const u16*)d_in[9];
  const u16* wmod = (const u16*)d_in[10];
  char* ws = (char*)d_ws;
  float* mod = (float*)(ws + WS_MOD);
  (void)hipFuncSetAttribute((const void*)k_attn, hipFuncAttributeMaxDynamicSharedMemorySize, (int)SHM_ATTN);
  (void)hipFuncSetAttribute((const void*)k_gemm<0>, hipFuncAttributeMaxDynamicSharedMemorySize, (int)SHM_GEMM);
  (void)hipFuncSetAttribute((const void*)k_gemm<1>, hipFuncAttributeMaxDynamicSharedMemorySize, (int)SHM_GEMM);
  (void)hipFuncSetAttribute((const void*)k_gemm<2>, hipFuncAttributeMaxDynamicSharedMemorySize, (int)SHM_GEMM);
  k_mod<<<54, 256, 0, stream>>>(cc, wmod, mod);
  k_ln1<<<256, 64, 0, stream>>>(x, mod, ws + WS_H1F);
  k_attn<<<256, 256, SHM_ATTN, stream>>>(ws + WS_H1F, wq, kc, vc, ws + WS_OF);
  k_gemm<0><<<256, 256, SHM_GEMM, stream>>>(ws + WS_OF, wo, wo, ws + WS_SLAB);
  k_red<false><<<256, 64, 0, stream>>>((const float*)(ws + WS_SLAB), x, mod, (u16*)(ws + WS_X1), ws + WS_H2F);
  k_gemm<1><<<256, 256, SHM_GEMM, stream>>>(ws + WS_H2F, wg, wu, ws + WS_HHF);
  k_gemm<2><<<256, 256, SHM_GEMM, stream>>>(ws + WS_HHF, wd, wd, ws + WS_SLAB);
  k_red<true><<<256, 64, 0, stream>>>((const float*)(ws + WS_SLAB), (const u16*)(ws + WS_X1), mod, (u16*)d_out, nullptr);
}
```

```cpp
#include <hip/hip_runtime.h>
#include <hip/hip_bf16.h>
#include <utility>
#include <cstdint>

typedef float    f32x16 __attribute__((ext_vector_type(16)));
typedef float    f32x4  __attribute__((ext_vector_type(4)));
typedef float    f32x2  __attribute__((ext_vector_type(2)));
typedef __bf16   bf16x8 __attribute__((ext_vector_type(8)));
typedef __bf16   bf16x4 __attribute__((ext_vector_type(4)));
typedef __bf16   bf16x2 __attribute__((ext_vector_type(2)));
typedef unsigned u32x4  __attribute__((ext_vector_type(4)));
typedef unsigned u32x2  __attribute__((ext_vector_type(2)));
typedef unsigned short u16;
template <int... Is> using iseq = std::integer_sequence<int, Is...>;
template <int N> using make_iseq = std::make_integer_sequence<int, N>;
#define DI __device__ __forceinline__
#define LAS __attribute__((address_space(3)))
#define GAP() __builtin_amdgcn_sched_barrier(0)
#define SRD_WORD3 0x00020000

constexpr int NTOK = 2048, DM = 576, NH = 32, DH = 128, SKV = 2048, FF = 4608;

constexpr size_t WS_MOD  = 0;
constexpr size_t WS_H1F  = 131072;
constexpr size_t WS_OF   = WS_H1F + (size_t)NTOK * DM * 2;
constexpr size_t WS_SLAB = WS_OF + (size_t)NTOK * NH * DH * 2;
constexpr size_t WS_X1   = WS_SLAB + (size_t)4 * NTOK * DM * 4;
constexpr size_t WS_H2F  = WS_X1 + (size_t)NTOK * DM * 4;
constexpr size_t WS_HHF  = WS_H2F + (size_t)NTOK * DM * 2;
constexpr size_t WS_WOT  = WS_HHF + (size_t)NTOK * FF * 2;
constexpr size_t WS_WGT  = WS_WOT + (size_t)NH * DH * DM * 2;
constexpr size_t WT_UP   = (size_t)DM * FF * 2;
constexpr size_t WS_WDT  = WS_WGT + 2 * WT_UP;
constexpr size_t WS_SLAB2 = WS_WDT + (size_t)FF * DM * 2;
constexpr size_t WS_END  = WS_SLAB2 + (size_t)4 * NTOK * DM * 4;
constexpr size_t WS_CS   = 98304;
constexpr size_t WS_CNT  = 16384;
constexpr unsigned CNT_B = 0, CNT_C = 16, CNT_D = 24, CNT_E = 56, CNT_A = 120, CNT_W = 152, CNT_C2 = 160, CNT_N = 176;

DI float bf2f(u16 u) { return __uint_as_float((unsigned)u << 16); }
DI float bflo(unsigned u) { return __uint_as_float(u << 16); }
DI float bfhi(unsigned u) { return __uint_as_float(u & 0xffff0000u); }
DI float rbf(float x) { return (float)(__bf16)x; }
DI unsigned cvtpk(float lo, float hi) { f32x2 v = {lo, hi}; bf16x2 b = __builtin_convertvector(v, bf16x2); return __builtin_bit_cast(unsigned, b); }
DI unsigned rfl(unsigned x) { return __builtin_amdgcn_readfirstlane(x); }

constexpr float QSCALE = 0.12751743082f;
DI float wave_sum(float x) {
  x += __builtin_bit_cast(float, __builtin_amdgcn_update_dpp(0, __builtin_bit_cast(int, x), 0x121, 0xf, 0xf, false));
  x += __builtin_bit_cast(float, __builtin_amdgcn_update_dpp(0, __builtin_bit_cast(int, x), 0x122, 0xf, 0xf, false));
  x += __builtin_bit_cast(float, __builtin_amdgcn_update_dpp(0, __builtin_bit_cast(int, x), 0x124, 0xf, 0xf, false));
  x += __builtin_bit_cast(float, __builtin_amdgcn_update_dpp(0, __builtin_bit_cast(int, x), 0x128, 0xf, 0xf, false));
  const auto a = __builtin_amdgcn_permlane16_swap(__float_as_uint(x), __float_as_uint(x), false, false);
  x = __uint_as_float(a[0]) + __uint_as_float(a[1]);
  const auto b = __builtin_amdgcn_permlane32_swap(__float_as_uint(x), __float_as_uint(x), false, false);
  return __uint_as_float(b[0]) + __uint_as_float(b[1]);
}
typedef _Float16 f16x2 __attribute__((ext_vector_type(2)));
DI unsigned pkh(float a, float b) { f16x2 h = {(_Float16)a, (_Float16)b}; return __builtin_bit_cast(unsigned, h); }
constexpr int NMOD = 108;
__global__ __launch_bounds__(256) void k_prep(const u16* __restrict__ c, const u16* __restrict__ wmod, const u16* __restrict__ x, char* __restrict__ ws, const u16* __restrict__ wq) {
  __shared__ float sc[DM];
  __shared__ float red[64][33];
  const int tid = threadIdx.x, b = blockIdx.x;
  if (b < NMOD) {
    float* mod = (float*)(ws + WS_MOD);
    if (b == 0 && tid < (int)CNT_N) { ((unsigned*)(ws + WS_CNT))[tid * 64] = 0u; ((unsigned*)(ws + WS_CNT))[tid * 64 + 32] = 0u; }
    const int c8 = tid & 3, kg = tid >> 2;
    u32x4 w[9];
#pragma unroll
    for (int i = 0; i < 9; ++i) w[i] = *(const u32x4*)(wmod + (size_t)(kg + 64 * i) * (6 * DM) + 32 * b + 8 * c8);
    for (int i = tid; i < DM; i += 256) { float v = bf2f(c[i]); sc[i] = rbf(v / (1.f + __expf(-v))); }
    __syncthreads();
    float acc[8] = {0, 0, 0, 0, 0, 0, 0, 0};
#pragma unroll
    for (int i = 0; i < 9; ++i) {
      const float s = sc[kg + 64 * i];
#pragma unroll
      for (int j = 0; j < 4; ++j) { acc[2 * j] += s * bflo(w[i][j]); acc[2 * j + 1] += s * bfhi(w[i][j]); }
    }
#pragma unroll
    for (int j = 0; j < 8; ++j) red[kg][8 * c8 + j] = acc[j];
    __syncthreads();
    {
      const int col32 = tid >> 3, part = tid & 7;
      float s = 0;
#pragma unroll
      for (int g = 0; g < 8; ++g) s += red[8 * part + g][col32];
      s += __shfl_xor(s, 1); s += __shfl_xor(s, 2); s += __shfl_xor(s, 4);
      if (part == 0) {
        s = rbf(s);
        const int col = 32 * b + col32;
        mod[col] = s;
        float* cs = (float*)(ws + WS_CS);
        if (col < DM) cs[DM + col] = QSCALE * s;
        else if (col < 2 * DM) cs[col - DM] = QSCALE * rbf(1.f + s);
      }
    }
  } else if (b < NMOD + 512) {
    const int lane = tid & 63, row = (b - NMOD) * 4 + (tid >> 6);
    const bool two = lane < 8;
    float v[16];
#pragma unroll
    for (int i = 0; i < 2; ++i) {
      const u32x4 w = *(const u32x4*)(x + (size_t)row * DM + 8 * ((i && two) ? 64 + lane : lane));
#pragma unroll
      for (int j = 0; j < 4; ++j) { v[8 * i + 2 * j] = bflo(w[j]); v[8 * i + 2 * j + 1] = bfhi(w[j]); }
    }
    float s = 0, s2 = 0;
#pragma unroll
    for (int i = 0; i < 8; ++i) { s += v[i]; s2 += v[8 + i]; }
    const float mean = wave_sum(two ? s + s2 : s) * (1.f / DM);
    float q = 0, q2 = 0;
#pragma unroll
    for (int i = 0; i < 8; ++i) { const float d = v[i] - mean, d2 = v[8 + i] - mean; q += d * d; q2 += d2 * d2; }
    q = wave_sum(two ? q + q2 : q);
    const float mb = rbf(mean), vb = rbf(q * (1.f / DM));
    const float rden = 1.f / rbf(sqrtf(rbf(vb + 1e-5f)));
    char* h1f = ws + WS_H1F;
    const int tok16 = row >> 4, r = row & 15;
#pragma unroll
    for (int i = 0; i < 2; ++i) {
      const int cc = i ? 64 + lane : lane, k32 = cc >> 2, tt = (cc >> 1) & 1;
      if (i == 0 || two) {
#pragma unroll
        for (int qd = 0; qd < 2; ++qd) {
          float y[4];
#pragma unroll
          for (int e = 0; e < 4; ++e) y[e] = rbf(rbf(v[8 * i + 4 * qd + e] - mb) * rden);
          *(u32x2*)(h1f + ((size_t)((tok16 * 18 + k32) * 64 + 16 * (2 * (cc & 1) + qd) + r)) * 16 + 8 * tt) = u32x2{pkh(y[0], y[1]), pkh(y[2], y[3])};
        }
      }
    }
  } else {
    const char* src = (const char*)wq + (size_t)(b - (NMOD + 512)) * 32768;
    u32x4 acc = {0, 0, 0, 0};
#pragma unroll
    for (int i = 0; i < 8; ++i) acc ^= *(const u32x4*)(src + (i * 256 + tid) * 16);
    if ((acc[0] ^ acc[1] ^ acc[2] ^ acc[3]) == 0x9e3779b9u && acc[0] == 0x7f4a7c15u && acc[1] == 0x12345678u) ((unsigned*)(ws + WS_CS))[1200] = 1u;
  }
}

typedef float f32x32 __attribute__((ext_vector_type(32)));
typedef float f32x8  __attribute__((ext_vector_type(8)));
DI u32x4 make_srd(const void* p, unsigned bytes) { const uint64_t a = (uint64_t)p; return u32x4{rfl((unsigned)a), rfl((unsigned)(a >> 32) & 0xffffu), rfl(bytes), (unsigned)SRD_WORD3}; }
#define CODE_REF(NAME, pc) asm volatile("s_getpc_b64 %0\n" NAME ":" : "=s"(pc))
#define CODE_LABEL(NAME) asm volatile(NAME ":")
#define CODE_OFF(REF, NAME, off) asm volatile("s_mov_b32 %0, " NAME "-" REF : "=s"(off))
DI void code_touch(uint64_t pc, unsigned beg, unsigned end, unsigned ldsscratch, unsigned wid, unsigned lane, unsigned step = 32768u) {
  const u32x4 srd = {(unsigned)pc, (unsigned)(pc >> 32) & 0xffffu, end, (unsigned)SRD_WORD3};
  const unsigned vo = (wid * 64u + lane) * 128u;
#pragma nounroll
  for (unsigned o = beg; o < end; o += step)
    asm volatile("s_mov_b32 m0, %0\n\ts_nop 4\n\tbuffer_load_dword %1, %2, %3 offen lds" :: "s"(ldsscratch), "v"(vo), "s"(srd), "s"(o) : "memory");
}
constexpr unsigned WQRS = NH * DH * 2u;
constexpr size_t SHM_ATTN = 163840;
constexpr bool LSUM_ADD = false;
constexpr int G0S = 7, G01 = 3;

typedef __attribute__((address_space(1))) unsigned gu32;
#define RLX_AGENT __ATOMIC_RELAXED, __HIP_MEMORY_SCOPE_AGENT
DI __amdgpu_buffer_rsrc_t mk_rsrc(const void* p, unsigned bytes) {
  const uint64_t a = (uint64_t)p; const uint64_t u = ((uint64_t)rfl((unsigned)(a >> 32)) << 32) | rfl((unsigned)a);
  return __builtin_amdgcn_make_buffer_rsrc((void*)u, (short)0, (int)rfl(bytes), SRD_WORD3);
}
DI void st16_sc1(__amdgpu_buffer_rsrc_t r, unsigned off, u32x4 v) { __builtin_amdgcn_raw_buffer_store_b128(v, r, off, 0, 16); }
DI void st8_sc1(__amdgpu_buffer_rsrc_t r, unsigned off, u32x2 v) { __builtin_amdgcn_raw_buffer_store_b64(v, r, off, 0, 16); }
DI f32x4 ld16_sc1(__amdgpu_buffer_rsrc_t r, unsigned off) { return __builtin_bit_cast(f32x4, __builtin_amdgcn_raw_buffer_load_b128(r, off, 0, 16)); }
DI unsigned lane_id() { unsigned l; asm volatile("v_mbcnt_lo_u32_b32 %0, -1, 0\n\tv_mbcnt_hi_u32_b32 %0, -1, %0" : "=v"(l)); return l; }
template <bool DRAIN = true, unsigned REP = 1> DI void signal_cnt(unsigned* cnt, unsigned wid) {
  if (DRAIN) asm volatile("s_waitcnt vmcnt(0)\n\ts_barrier" ::: "memory"); else asm volatile("s_barrier" ::: "memory");
  const unsigned l = lane_id();
  if (wid == 0 && l < REP) __hip_atomic_fetch_add((gu32*)(cnt + 32u * l), 1u, RLX_AGENT);
}
DI void wait_cnt(unsigned* cnt, unsigned want, unsigned wid) {
  if (wid == 3) {
    unsigned a, b, c, n = 1u << 21, z = 0, sv;
    asm volatile(
      "s_nop 4\n\tglobal_load_dword %0, %5, %6 sc1\n\ts_sleep 3\n\tglobal_load_dword %1, %5, %6 sc1\n\ts_sleep 3\n\tglobal_load_dword %2, %5, %6 sc1\n"
      "Lw%=:\n\t"
      "s_waitcnt vmcnt(2)\n\tv_readfirstlane_b32 %4, %0\n\ts_cmp_ge_u32 %4, %7\n\ts_cbranch_scc1 Ld%=\n\tglobal_load_dword %0, %5, %6 sc1\n\t"
      "s_waitcnt vmcnt(2)\n\tv_readfirstlane_b32 %4, %1\n\ts_cmp_ge_u32 %4, %7\n\ts_cbranch_scc1 Ld%=\n\tglobal_load_dword %1, %5, %6 sc1\n\t"
      "s_waitcnt vmcnt(2)\n\tv_readfirstlane_b32 %4, %2\n\ts_cmp_ge_u32 %4, %7\n\ts_cbranch_scc1 Ld%=\n\tglobal_load_dword %2, %5, %6 sc1\n\t"
      "s_sub_u32 %3, %3, 1\n\ts_cmp_lg_u32 %3, 0\n\ts_cbranch_scc1 Lw%=\n"
      "Ld%=:\n\ts_barrier\n\ts_waitcnt vmcnt(0)"
      : "=&v"(a), "=&v"(b), "=&v"(c), "+s"(n), "=&s"(sv) : "v"(z), "s"(cnt), "s"(want) : "scc", "memory");
  } else asm volatile("s_waitcnt vmcnt(0)\n\ts_barrier" ::: "memory");
}
DI float red_r4(float x) {
  x += __builtin_bit_cast(float, __builtin_amdgcn_update_dpp(0, __builtin_bit_cast(int, x), 0x124, 0xf, 0xf, false));
  x += __builtin_bit_cast(float, __builtin_amdgcn_update_dpp(0, __builtin_bit_cast(int, x), 0x128, 0xf, 0xf, false));
  const auto a = __builtin_amdgcn_permlane16_swap(__float_as_uint(x), __float_as_uint(x), false, false);
  x = __uint_as_float(a[0]) + __uint_as_float(a[1]);
  const auto b = __builtin_amdgcn_permlane32_swap(__float_as_uint(x), __float_as_uint(x), false, false);
  return __uint_as_float(b[0]) + __uint_as_float(b[1]);
}

struct GemmArgs { u32x4 srdA, srdX, srdO, srdN; unsigned mA, vw, vx0, vx1, vx2, vx3, wrd, vo0, vo1, vo2, wid, want; const unsigned *sig, *poll; };
template <int MODE> DI GemmArgs gemm_args(const char* xf, const char* wtile, unsigned tok16_0, unsigned k32_0, unsigned ldsb, unsigned wid, unsigned lane) {
  constexpr bool G1 = (MODE == 1);
  constexpr unsigned XK32 = (MODE == 0) ? 128 : (MODE == 1 ? 18 : 144), NF = (MODE == 0) ? 288 : 324;
  GemmArgs a;
  a.srdA = make_srd(wtile, NF * 1024u);
  a.srdX = make_srd(xf + ((size_t)tok16_0 * XK32 + k32_0) * 1024, ((G1 ? 4u : 2u) * XK32 - k32_0) * 1024u);
  a.mA = rfl(ldsb + wid * 4096u); a.wid = wid; a.want = 8u; a.vo2 = 0;
  a.vw = wid * 4096u + lane * 16u;
  a.vx0 = lane * 16u; a.vx1 = a.vx0 + XK32 * 1024u; a.vx2 = a.vx0 + 2u * XK32 * 1024u; a.vx3 = a.vx0 + 3u * XK32 * 1024u;
  a.wrd = ldsb + (4u * (lane >> 4) + ((lane >> 2) & 3u)) * 32u + 8u * (lane & 3u);
  return a;
}
#define GEMM_IN(a) "{s[8:11]}"(a.srdA), "{s20}"(a.mA), "{v1}"(a.vw), "{s[12:15]}"(a.srdX), "{s[16:19]}"(a.srdO), "{s[24:27]}"(a.srdN), "{v2}"(a.vx0), "{v3}"(a.vx1), "{v4}"(a.vx2), "{v5}"(a.vx3), \
                   "{v6}"(a.wrd), "{v7}"(a.wrd + 65536u), "{v8}"(a.wrd + 131072u), "{v9}"(a.vo0), "{v10}"(a.vo1), "{v11}"(a.vo2), "{s23}"(a.wid), "{s[48:49]}"(a.sig), "{s[50:51]}"(a.poll), "{s52}"(a.want)
DI void unpack4(float (&d)[4], unsigned p0, unsigned p1) { d[0] += bflo(p0); d[1] += bfhi(p0); d[2] += bflo(p1); d[3] += bfhi(p1); }
DI u32x4 ldu16_sc1(__amdgpu_buffer_rsrc_t r, unsigned off) { return __builtin_amdgcn_raw_buffer_load_b128(r, off, 0, 16); }

DI void attn_slow(const char* __restrict__ h1f, const float* __restrict__ mod, const u16* __restrict__ wq, const u16* __restrict__ Kh, const u16* __restrict__ Vh,
                                       const __amdgpu_buffer_rsrc_t ro, unsigned row, unsigned head, unsigned lane) {
  float o[128];
#pragma unroll
  for (int d = 0; d < 128; ++d) o[d] = 0.f;
#pragma unroll 1
  for (unsigned k = 0; k < (unsigned)DM; ++k) {
    const float xn = (float)(*(const _Float16*)(h1f + ((size_t)(((row >> 4) * 18u + (k >> 5)) * 64u + 16u * ((k >> 2) & 3u) + (row & 15u))) * 16u + 8u * ((k >> 4) & 1u) + 2u * (k & 3u)));
    const float hv = rbf(xn * rbf(1.f + mod[DM + k]) + mod[k]);
    const u32x4* wr = (const u32x4*)(wq + (size_t)k * (NH * DH) + head * DH);
#pragma unroll
    for (int c = 0; c < 16; ++c) { const u32x4 w = wr[c];
#pragma unroll
      for (int j = 0; j < 4; ++j) { o[8 * c + 2 * j] += hv * bflo(w[j]); o[8 * c + 2 * j + 1] += hv * bfhi(w[j]); } }
  }
  unsigned qp[64];
#pragma unroll
  for (int d = 0; d < 64; ++d) { qp[d] = cvtpk(o[2 * d], o[2 * d + 1]); o[2 * d] = 0.f; o[2 * d + 1] = 0.f; }
  float m = -3.0e38f, l = 0.f;
#pragma unroll 1
  for (unsigned key = 0; key < (unsigned)SKV; ++key) {
    const u32x4* kr = (const u32x4*)(Kh + (size_t)key * DH);
    float s = 0.f;
#pragma unroll
    for (int c = 0; c < 16; ++c) { const u32x4 w = kr[c];
#pragma unroll
      for (int j = 0; j < 4; ++j) { s += bflo(qp[4 * c + j]) * bflo(w[j]); s += bfhi(qp[4 * c + j]) * bfhi(w[j]); } }
    s *= QSCALE;
    const float mn = fmaxf(m, s), al = __builtin_amdgcn_exp2f(m - mn), pv = __builtin_amdgcn_exp2f(s - mn);
    m = mn; l = l * al + pv;
    const u32x4* vr = (const u32x4*)(Vh + (size_t)key * DH);
#pragma unroll
    for (int c = 0; c < 16; ++c) { const u32x4 w = vr[c];
#pragma unroll
      for (int j = 0; j < 4; ++j) { o[8 * c + 2 * j] = o[8 * c + 2 * j] * al + pv * bflo(w[j]); o[8 * c + 2 * j + 1] = o[8 * c + 2 * j + 1] * al + pv * bfhi(w[j]); } }
  }
  const float il = 1.f / l;
  const unsigned ob = (lane >> 4) * 131072u + (lane & 15u) * 16u;
#pragma unroll
  for (int db = 0; db < 4; ++db)
#pragma unroll
    for (int g = 0; g < 4; ++g) {
      const int d0 = 32 * db + 4 * g;
      st16_sc1(ro, ob + ((db >> 1) * 16 + (db & 1)) * 1024 + g * 256, u32x4{cvtpk(o[d0] * il, o[d0 + 1] * il), cvtpk(o[d0 + 2] * il, o[d0 + 3] * il),
                                                   cvtpk(o[d0 + 16] * il, o[d0 + 17] * il), cvtpk(o[d0 + 18] * il, o[d0 + 19] * il)});
    }
}

__global__ __launch_bounds__(256, 1)
void k_main(const u16* __restrict__ wq, const u16* __restrict__ Kc, const u16* __restrict__ Vc,
            const u16* __restrict__ wo, const u16* __restrict__ wg, const u16* __restrict__ wu, const u16* __restrict__ wd, char* __restrict__ ws,
            const u16* __restrict__ x, u16* __restrict__ out) {
  extern __shared__ __attribute__((aligned(16))) char lds[];

  const unsigned wid = rfl(threadIdx.x >> 6);
  const unsigned ldsb = (unsigned)(size_t)lds;
  const unsigned blk = blockIdx.x;
  const char* h1f = ws + WS_H1F; char* of = ws + WS_OF;
  unsigned* cnt = (unsigned*)(ws + WS_CNT);
  uint64_t pc; unsigned oB, oC, oE, oZ;
  CODE_REF("Lm_ref", pc);
  CODE_OFF("Lm_ref", "Lm_B", oB); CODE_OFF("Lm_ref", "Lm_C", oC); CODE_OFF("Lm_ref", "Lm_E", oE); CODE_OFF("Lm_ref", "Lm_Z", oZ);
  const unsigned tsc = rfl(ldsb + 147456u + 1024u);
  const unsigned xcd = blk & 7u, idx = blk >> 3;
  const float* mod = (const float*)(ws + WS_MOD);
  const unsigned split = xcd >> 1, cb = idx & 3u, rb = (xcd & 1u) * 8u + (idx >> 2);
  const unsigned ft = split * 8u + (idx & 7u), rb2 = (xcd & 1u) * 4u + (idx >> 3);
  const bool three = wid == 0u;
  u32x2 xw[3][2]; f32x4 ga[3], sh[3], sc[3];
  const unsigned* sigA;
  {
  const unsigned lane = threadIdx.x & 63u;
  code_touch(pc, 0u, oB, tsc, wid, lane);
  const unsigned head = (blk & 7u) + 8u * ((blk >> 3) & 3u), y = blk >> 5;
  const unsigned row0 = 256u * y + 64u * wid;
  const u16* Kh = Kc + (size_t)head * SKV * DH;
  const u16* Vh = Vc + (size_t)head * SKV * DH;
  const u32x4 srdK = make_srd(Kh, SKV * 256u), srdV = make_srd(Vh, SKV * 256u);
  const u32x4 srdW = make_srd((const char*)wq + head * 256u, DM * WQRS - head * 256u);
  const u32x4 srdH = make_srd(h1f + (size_t)(row0 >> 4) * 18432u, 4u * 18432u);
  const unsigned gw = blk * 4u + wid;
  const char* rsrc; char* rdst; unsigned rL, rch0, rK, rrowb, rtyp;
  if (gw < 288u)      { rtyp = 0; rsrc = (const char*)wo; rdst = ws + WS_WOT; rL = gw >> 5;            rch0 = gw & 31u;           rK = 4096u; rrowb = DM * 2u; }
  else if (gw < 576u) { rtyp = 1; rsrc = (const char*)wd; rdst = ws + WS_WDT; rL = (gw - 288u) >> 5;   rch0 = (gw - 288u) & 31u;  rK = FF;    rrowb = DM * 2u; }
  else if (gw < 792u) { rtyp = 2; rsrc = (const char*)wg; rdst = ws + WS_WGT; rL = (gw - 576u) / 3u;   rch0 = (gw - 576u) % 3u;   rK = DM;    rrowb = FF * 2u; }
  else                { rtyp = 3; rsrc = (const char*)wu; rdst = ws + WS_WGT; rL = (gw - 792u) / 3u;   rch0 = (gw - 792u) % 3u;   rK = DM;    rrowb = FF * 2u; }
  const bool ract = gw < 1008u;
  if (!ract) { rL = 0; rch0 = 0; }
  const unsigned rsteps = !ract ? 0u : rtyp == 0 ? 16u : rtyp == 1 ? 18u : 24u, rrow0 = rch0 * 8u * (rtyp == 0 ? 16u : rtyp == 1 ? 18u : 24u);
  const u32x4 srdRs = make_srd(rsrc + (size_t)rrow0 * rrowb + rL * 128u, ract ? rsteps * 8u * rrowb - rL * 128u : 0u);
  const u32x4 srdRd = make_srd(rdst, !ract ? 0u : rtyp == 0 ? 16u * 294912u : rtyp == 1 ? 16u * 331776u : 32u * 331776u);
  const unsigned rcc = 8u * rL + (lane & 7u), rcb = rcc / 18u, rc = rcc - rcb * 18u, rnb = rc >> 1;
  unsigned rbase, rstride, rtile;
  if (rtyp < 2u) {
    const unsigned NK = rtyp == 0 ? 32u : 36u;
    rbase = rnb < (unsigned)G0S ? rnb : (unsigned)G0S * NK + (rnb - G0S) * NK; rstride = rnb < (unsigned)G0S ? (unsigned)G0S : 1u;
    rtile = (rcb * 4u + (rch0 >> 3)) * (9u * NK * 1024u);
  } else {
    const unsigned u = rtyp - 2u;
    rbase = rnb < (unsigned)G01 ? 2u * rnb + u : 2u * G01 * 18u + (rnb - G01) * 36u + u; rstride = rnb < (unsigned)G01 ? 2u * G01 : 2u;
    rtile = rcb * 331776u;
  }
  const unsigned vRs = (lane >> 3) * rrowb + (lane & 7u) * 16u;
  const unsigned vRb = rtile + rbase * 1024u + (lane >> 3) * 32u + (rc & 1u) * 16u, vRst = rstride * 1024u;
  unsigned tabK, tabR;
  if (rtyp == 1u) {
    const unsigned j = lane >> 1, par = rch0 & 1u, tp = (rch0 & 7u) >> 1, jp = (j - 1u) >> 1;
    tabK = j == 0u ? 7u * tp + 6u : jp < 3u ? 7u * tp + 3u * par + jp : 28u + 2u * tp + par; tabR = (16u * (j ? ((j - 1u) & 1u) : par) + 8u * (lane & 1u)) * 32u;
  } else {
    const unsigned k = (rtyp == 0u ? (rrow0 & 1023u) : rrow0) + 8u * lane, kk = k >> 5;
    tabK = rtyp == 0u ? ((kk >> 1) & 1u) * 16u + (kk >> 2) * 2u + (kk & 1u) : kk; tabR = (k & 31u) * 32u;
  }
  if (lane >= rsteps) { tabK = 0; tabR = 0x40000000u; }
  const unsigned sRstep = rfl(8u * rrowb);
  unsigned vk[4], ka[4];
  const unsigned l16 = lane & 15u, gq = lane >> 4, q4 = (lane >> 2) & 3u, p4 = lane & 3u;
#pragma unroll
  for (int q = 0; q < 4; ++q) vk[q] = 256u * gq + 16u * (l16 ^ (4u * q + gq));
#pragma unroll
  for (int ds = 0; ds < 4; ++ds) ka[ds] = ldsb + 256u * l16 + 16u * ((4u * ds + gq) ^ l16);
  const unsigned vv0 = q4 * 256u + gq * 64u + p4 * 16u, vv1 = q4 * 256u + gq * 64u + (p4 ^ 2u) * 16u;
  const unsigned vw = q4 * WQRS + gq * 64u + p4 * 16u;
  const unsigned vh = lane * 16u;
  const unsigned va0 = ldsb + 65536u + gq * 1024u + q4 * 64u + (gq & 1u) * 32u + p4 * 8u, va1 = va0 ^ 32u;
  const unsigned waddr = ldsb + gq * 1024u + q4 * 64u + p4 * 16u;
  const unsigned sM0 = rfl(ldsb + wid * 4096u), sKV0 = rfl(wid * 4096u), sW0 = rfl(wid * 16u * WQRS);
  f32x32 o0, o1, o2, o3; f32x16 lv;
  const u32x4 srdC = make_srd(ws + WS_CS, 4608u);
  const u32x4 srdT = make_srd((const char*)(pc + oB), oZ - oB);
  const unsigned* cntw0 = cnt + CNT_W * 64u;
  const unsigned* cntw = cntw0 + (blk & 7u) * 64u;
  const unsigned sig = wid == 0u ? 3u : 99u;
  const u32x4 srdWo = make_srd(ws + WS_WOT + (size_t)(((blk >> 3) & 3u) * 4u + ((blk & 7u) >> 1)) * 294912u, 294912u);
#define ATTN_IN "{s[8:11]}"(srdK), "{s[12:15]}"(srdV), "{s[16:19]}"(srdW), "{s[20:23]}"(srdH), "{s[24:27]}"(srdRs), "{s[28:31]}"(srdRd), \
    "{s44}"(sM0), "{s45}"(sKV0), "{s46}"(sW0), "{s47}"(sRstep), "{s[4:5]}"(cntw), "{s6}"(sig), "{s[2:3]}"(cntw0), "{s[64:67]}"(srdWo), "{s[60:63]}"(srdC), "{s[56:59]}"(srdT), "{v14}"(threadIdx.x * 128u), \
    "{v1}"(vk[0]), "{v2}"(vk[1]), "{v3}"(vk[2]), "{v4}"(vk[3]), "{v5}"(vv0), "{v12}"(vv1), "{v6}"(vw), "{v7}"(vh), \
    "{v8}"(ka[0]), "{v9}"(ka[1]), "{v10}"(ka[2]), "{v11}"(ka[3]), \
    "{v16}"(va0), "{v13}"(va1), "{v17}"(waddr), "{v18}"(vRs), "{v19}"(vRb), "{v28}"(vRst), "{v29}"(tabK), "{v30}"(tabR)
  asm volatile(
"s_mov_b32 s41, 0\n"
"s_mov_b32 s42, 0\n"
"s_nop 4\n"
"s_add_u32 m0, s44, 0\n"
"s_add_u32 s39, s46, 0\n"
"buffer_load_dwordx4 v6, s[16:19], s39 offen lds\n"
"s_add_u32 m0, s44, 1024\n"
"s_add_u32 s39, s46, 32768\n"
"buffer_load_dwordx4 v6, s[16:19], s39 offen lds\n"
"s_add_u32 m0, s44, 2048\n"
"s_add_u32 s39, s46, 65536\n"
"buffer_load_dwordx4 v6, s[16:19], s39 offen lds\n"
"s_add_u32 m0, s44, 3072\n"
"s_add_u32 s39, s46, 98304\n"
"buffer_load_dwordx4 v6, s[16:19], s39 offen lds\n"
"s_mov_b32 s39, 0\n"
"buffer_load_dwordx4 v[112:115], v7, s[20:23], s39 offen\n"
"buffer_load_dwordx4 v[128:131], v7, s[20:23], s39 offen offset:1024\n"
"s_mov_b32 s39, 18432\n"
"buffer_load_dwordx4 v[116:119], v7, s[20:23], s39 offen\n"
"buffer_load_dwordx4 v[132:135], v7, s[20:23], s39 offen offset:1024\n"
"s_mov_b32 s39, 36864\n"
"buffer_load_dwordx4 v[120:123], v7, s[20:23], s39 offen\n"
"buffer_load_dwordx4 v[136:139], v7, s[20:23], s39 offen offset:1024\n"
"s_mov_b32 s39, 55296\n"
"buffer_load_dwordx4 v[124:127], v7, s[20:23], s39 offen\n"
"buffer_load_dwordx4 v[140:143], v7, s[20:23], s39 offen offset:1024\n"
"s_lshr_b32 s39, s45, 2\n"
"s_add_u32 m0, s39, 149504\n"
"s_nop 0\n"
"buffer_load_dwordx4 v7, s[60:63], s39 offen lds\n"
"s_mov_b32 m0, 153600\n"
"s_mov_b32 s39, 4096\n"
"buffer_load_dwordx4 v7, s[60:63], s39 offen lds\n"
"s_add_u32 m0, s44, 65536\n"
"s_add_u32 s39, s46, 524288\n"
"buffer_load_dwordx4 v6, s[16:19], s39 offen lds\n"
"s_add_u32 m0, s44, 66560\n"
"s_add_u32 s39, s46, 557056\n"
"buffer_load_dwordx4 v6, s[16:19], s39 offen lds\n"
"s_add_u32 m0, s44, 67584\n"
"s_add_u32 s39, s46, 589824\n"
"buffer_load_dwordx4 v6, s[16:19], s39 offen lds\n"
"s_add_u32 m0, s44, 68608\n"
"s_add_u32 s39, s46, 622592\n"
"buffer_load_dwordx4 v6, s[16:19], s39 offen lds\n"
"s_mov_b32 s39, 2048\n"
"buffer_load_dwordx4 v[144:147], v7, s[20:23], s39 offen\n"
"buffer_load_dwordx4 v[160:163], v7, s[20:23], s39 offen offset:1024\n"
"s_mov_b32 s39, 20480\n"
"buffer_load_dwordx4 v[148:151], v7, s[20:23], s39 offen\n"
"buffer_load_dwordx4 v[164:167], v7, s[20:23], s39 offen offset:1024\n"
"s_mov_b32 s39, 38912\n"
"buffer_load_dwordx4 v[152:155], v7, s[20:23], s39 offen\n"
"buffer_load_dwordx4 v[168:171], v7, s[20:23], s39 offen offset:1024\n"
"s_mov_b32 s39, 57344\n"
"buffer_load_dwordx4 v[156:159], v7, s[20:23], s39 offen\n"
"buffer_load_dwordx4 v[172:175], v7, s[20:23], s39 offen offset:1024\n"
"s_add_u32 m0, s44, 16384\n"
"s_add_u32 s39, s46, 1048576\n"
"buffer_load_dwordx4 v6, s[16:19], s39 offen lds\n"
"s_add_u32 m0, s44, 17408\n"
"s_add_u32 s39, s46, 1081344\n"
"buffer_load_dwordx4 v6, s[16:19], s39 offen lds\n"
"s_add_u32 m0, s44, 18432\n"
"s_add_u32 s39, s46, 1114112\n"
"buffer_load_dwordx4 v6, s[16:19], s39 offen lds\n"
"s_add_u32 m0, s44, 19456\n"
"s_add_u32 s39, s46, 1146880\n"
"buffer_load_dwordx4 v6, s[16:19], s39 offen lds\n"
"s_mov_b32 s39, 4096\n"
"buffer_load_dwordx4 v[176:179], v7, s[20:23], s39 offen\n"
"buffer_load_dwordx4 v[192:195], v7, s[20:23], s39 offen offset:1024\n"
"s_mov_b32 s39, 22528\n"
"buffer_load_dwordx4 v[180:183], v7, s[20:23], s39 offen\n"
"buffer_load_dwordx4 v[196:199], v7, s[20:23], s39 offen offset:1024\n"
"s_mov_b32 s39, 40960\n"
"buffer_load_dwordx4 v[184:187], v7, s[20:23], s39 offen\n"
"buffer_load_dwordx4 v[200:203], v7, s[20:23], s39 offen offset:1024\n"
"s_mov_b32 s39, 59392\n"
"buffer_load_dwordx4 v[188:191], v7, s[20:23], s39 offen\n"
"buffer_load_dwordx4 v[204:207], v7, s[20:23], s39 offen offset:1024\n"
"s_mov_b32 s38, s45\n"
"v_add_u32_e32 v22, 0, v17\n"
"v_lshrrev_b32_e32 v108, 8, v7\n"
"v_lshlrev_b32_e32 v108, 4, v108\n"
"v_add_u32_e32 v108, 149504, v108\n"
"s_waitcnt vmcnt(24)\n"
"s_barrier\n"
"ds_read_b128 v[68:71], v108 offset:0\n"
"ds_read_b128 v[72:75], v108 offset:64\n"
"ds_read_b128 v[76:79], v108 offset:2304\n"
"ds_read_b128 v[80:83], v108 offset:2368\n"
"ds_read_b128 v[84:87], v108 offset:128\n"
"ds_read_b128 v[88:91], v108 offset:192\n"
"ds_read_b128 v[92:95], v108 offset:2432\n"
"ds_read_b128 v[96:99], v108 offset:2496\n"
"ds_read_b64_tr_b16 v[32:33], v22 offset:0\n"
"ds_read_b64_tr_b16 v[34:35], v22 offset:4096\n"
"ds_read_b64_tr_b16 v[36:37], v22 offset:8\n"
"ds_read_b64_tr_b16 v[38:39], v22 offset:4104\n"
"ds_read_b64_tr_b16 v[40:41], v22 offset:256\n"
"ds_read_b64_tr_b16 v[42:43], v22 offset:4352\n"
"ds_read_b64_tr_b16 v[44:45], v22 offset:264\n"
"ds_read_b64_tr_b16 v[46:47], v22 offset:4360\n"
"ds_read_b64_tr_b16 v[48:49], v22 offset:512\n"
"ds_read_b64_tr_b16 v[50:51], v22 offset:4608\n"
"ds_read_b64_tr_b16 v[52:53], v22 offset:520\n"
"ds_read_b64_tr_b16 v[54:55], v22 offset:4616\n"
"ds_read_b64_tr_b16 v[56:57], v22 offset:768\n"
"ds_read_b64_tr_b16 v[58:59], v22 offset:4864\n"
"ds_read_b64_tr_b16 v[60:61], v22 offset:776\n"
"ds_read_b64_tr_b16 v[62:63], v22 offset:4872\n"
"s_waitcnt lgkmcnt(15)\n"
"v_fma_mix_f32 v100, v112, v68, v76 op_sel:[0,0,0] op_sel_hi:[1,0,0]\n"
"v_fma_mix_f32 v101, v112, v69, v77 op_sel:[1,0,0] op_sel_hi:[1,0,0]\n"
"v_fma_mix_f32 v102, v113, v70, v78 op_sel:[0,0,0] op_sel_hi:[1,0,0]\n"
"v_fma_mix_f32 v103, v113, v71, v79 op_sel:[1,0,0] op_sel_hi:[1,0,0]\n"
"v_cvt_pk_bf16_f32 v112, v100, v101\n"
"v_cvt_pk_bf16_f32 v113, v102, v103\n"
"v_fma_mix_f32 v104, v114, v72, v80 op_sel:[0,0,0] op_sel_hi:[1,0,0]\n"
"v_fma_mix_f32 v105, v114, v73, v81 op_sel:[1,0,0] op_sel_hi:[1,0,0]\n"
"v_fma_mix_f32 v106, v115, v74, v82 op_sel:[0,0,0] op_sel_hi:[1,0,0]\n"
"v_fma_mix_f32 v107, v115, v75, v83 op_sel:[1,0,0] op_sel_hi:[1,0,0]\n"
"v_cvt_pk_bf16_f32 v114, v104, v105\n"
"v_cvt_pk_bf16_f32 v115, v106, v107\n"
"v_fma_mix_f32 v100, v116, v68, v76 op_sel:[0,0,0] op_sel_hi:[1,0,0]\n"
"v_fma_mix_f32 v101, v116, v69, v77 op_sel:[1,0,0] op_sel_hi:[1,0,0]\n"
"v_fma_mix_f32 v102, v117, v70, v78 op_sel:[0,0,0] op_sel_hi:[1,0,0]\n"
"v_fma_mix_f32 v103, v117, v71, v79 op_sel:[1,0,0] op_sel_hi:[1,0,0]\n"
"v_cvt_pk_bf16_f32 v116, v100, v101\n"
"v_cvt_pk_bf16_f32 v117, v102, v103\n"
"v_fma_mix_f32 v104, v118, v72, v80 op_sel:[0,0,0] op_sel_hi:[1,0,0]\n"
"v_fma_mix_f32 v105, v118, v73, v81 op_sel:[1,0,0] op_sel_hi:[1,0,0]\n"
"v_fma_mix_f32 v106, v119, v74, v82 op_sel:[0,0,0] op_sel_hi:[1,0,0]\n"
"v_fma_mix_f32 v107, v119, v75, v83 op_sel:[1,0,0] op_sel_hi:[1,0,0]\n"
"v_cvt_pk_bf16_f32 v118, v104, v105\n"
"v_cvt_pk_bf16_f32 v119, v106, v107\n"
"v_fma_mix_f32 v100, v120, v68, v76 op_sel:[0,0,0] op_sel_hi:[1,0,0]\n"
"v_fma_mix_f32 v101, v120, v69, v77 op_sel:[1,0,0] op_sel_hi:[1,0,0]\n"
"v_fma_mix_f32 v102, v121, v70, v78 op_sel:[0,0,0] op_sel_hi:[1,0,0]\n"
"v_fma_mix_f32 v103, v121, v71, v79 op_sel:[1,0,0] op_sel_hi:[1,0,0]\n"
"v_cvt_pk_bf16_f32 v120, v100, v101\n"
"v_cvt_pk_bf16_f32 v121, v102, v103\n"
"v_fma_mix_f32 v104, v122, v72, v80 op_sel:[0,0,0] op_sel_hi:[1,0,0]\n"
"v_fma_mix_f32 v105, v122, v73, v81 op_sel:[1,0,0] op_sel_hi:[1,0,0]\n"
"v_fma_mix_f32 v106, v123, v74, v82 op_sel:[0,0,0] op_sel_hi:[1,0,0]\n"
"v_fma_mix_f32 v107, v123, v75, v83 op_sel:[1,0,0] op_sel_hi:[1,0,0]\n"
"v_cvt_pk_bf16_f32 v122, v104, v105\n"
"v_cvt_pk_bf16_f32 v123, v106, v107\n"
"v_fma_mix_f32 v100, v124, v68, v76 op_sel:[0,0,0] op_sel_hi:[1,0,0]\n"
"v_fma_mix_f32 v101, v124, v69, v77 op_sel:[1,0,0] op_sel_hi:[1,0,0]\n"
"v_fma_mix_f32 v102, v125, v70, v78 op_sel:[0,0,0] op_sel_hi:[1,0,0]\n"
"v_fma_mix_f32 v103, v125, v71, v79 op_sel:[1,0,0] op_sel_hi:[1,0,0]\n"
"v_cvt_pk_bf16_f32 v124, v100, v101\n"
"v_cvt_pk_bf16_f32 v125, v102, v103\n"
"v_fma_mix_f32 v104, v126, v72, v80 op_sel:[0,0,0] op_sel_hi:[1,0,0]\n"
"v_fma_mix_f32 v105, v126, v73, v81 op_sel:[1,0,0] op_sel_hi:[1,0,0]\n"
"v_fma_mix_f32 v106, v127, v74, v82 op_sel:[0,0,0] op_sel_hi:[1,0,0]\n"
"v_fma_mix_f32 v107, v127, v75, v83 op_sel:[1,0,0] op_sel_hi:[1,0,0]\n"
"v_cvt_pk_bf16_f32 v126, v104, v105\n"
"v_cvt_pk_bf16_f32 v127, v106, v107\n"
"s_waitcnt lgkmcnt(8)\n"
"v_mfma_f32_16x16x32_bf16 a[0:3], v[32:35], v[112:115], 0\n"
"ds_read_b64_tr_b16 v[64:65], v22 offset:8192\n"
"v_fma_mix_f32 v100, v128, v84, v92 op_sel:[0,0,0] op_sel_hi:[1,0,0]\n"
"v_mfma_f32_16x16x32_bf16 a[4:7], v[32:35], v[116:119], 0\n"
"ds_read_b64_tr_b16 v[66:67], v22 offset:12288\n"
"v_fma_mix_f32 v101, v128, v85, v93 op_sel:[1,0,0] op_sel_hi:[1,0,0]\n"
"v_mfma_f32_16x16x32_bf16 a[8:11], v[32:35], v[120:123], 0\n"
"v_fma_mix_f32 v102, v129, v86, v94 op_sel:[0,0,0] op_sel_hi:[1,0,0]\n"
"v_fma_mix_f32 v103, v129, v87, v95 op_sel:[1,0,0] op_sel_hi:[1,0,0]\n"
"v_mfma_f32_16x16x32_bf16 a[12:15], v[32:35], v[124:127], 0\n"
"s_mov_b32 s39, 6144\n"
"buffer_load_dwordx4 v[208:211], v7, s[20:23], s39 offen\n"
"v_cvt_pk_bf16_f32 v128, v100, v101\n"
"v_cvt_pk_bf16_f32 v129, v102, v103\n"
"v_mfma_f32_16x16x32_bf16 a[16:19], v[36:39], v[112:115], 0\n"
"ds_read_b64_tr_b16 v[32:33], v22 offset:8200\n"
"v_fma_mix_f32 v104, v130, v88, v96 op_sel:[0,0,0] op_sel_hi:[1,0,0]\n"
"v_mfma_f32_16x16x32_bf16 a[20:23], v[36:39], v[116:119], 0\n"
"ds_read_b64_tr_b16 v[34:35], v22 offset:12296\n"
"v_fma_mix_f32 v105, v130, v89, v97 op_sel:[1,0,0] op_sel_hi:[1,0,0]\n"
"v_mfma_f32_16x16x32_bf16 a[24:27], v[36:39], v[120:123], 0\n"
"ds_read_b128 v[68:71], v108 offset:256\n"
"v_fma_mix_f32 v106, v131, v90, v98 op_sel:[0,0,0] op_sel_hi:[1,0,0]\n"
"v_fma_mix_f32 v107, v131, v91, v99 op_sel:[1,0,0] op_sel_hi:[1,0,0]\n"
"v_mfma_f32_16x16x32_bf16 a[28:31], v[36:39], v[124:127], 0\n"
"buffer_load_dwordx4 v[224:227], v7, s[20:23], s39 offen offset:1024\n"
"v_cvt_pk_bf16_f32 v130, v104, v105\n"
"v_cvt_pk_bf16_f32 v131, v106, v107\n"
"v_mfma_f32_16x16x32_bf16 a[32:35], v[40:43], v[112:115], 0\n"
"ds_read_b64_tr_b16 v[36:37], v22 offset:8448\n"
"v_fma_mix_f32 v100, v132, v84, v92 op_sel:[0,0,0] op_sel_hi:[1,0,0]\n"
"v_mfma_f32_16x16x32_bf16 a[36:39], v[40:43], v[116:119], 0\n"
"ds_read_b64_tr_b16 v[38:39], v22 offset:12544\n"
"v_fma_mix_f32 v101, v132, v85, v93 op_sel:[1,0,0] op_sel_hi:[1,0,0]\n"
"v_mfma_f32_16x16x32_bf16 a[40:43], v[40:43], v[120:123], 0\n"
"v_fma_mix_f32 v102, v133, v86, v94 op_sel:[0,0,0] op_sel_hi:[1,0,0]\n"
"v_fma_mix_f32 v103, v133, v87, v95 op_sel:[1,0,0] op_sel_hi:[1,0,0]\n"
"v_mfma_f32_16x16x32_bf16 a[44:47], v[40:43], v[124:127], 0\n"
"s_mov_b32 s39, 24576\n"
"buffer_load_dwordx4 v[212:215], v7, s[20:23], s39 offen\n"
"v_cvt_pk_bf16_f32 v132, v100, v101\n"
"v_cvt_pk_bf16_f32 v133, v102, v103\n"
"v_mfma_f32_16x16x32_bf16 a[48:51], v[44:47], v[112:115], 0\n"
"ds_read_b64_tr_b16 v[40:41], v22 offset:8456\n"
"v_fma_mix_f32 v104, v134, v88, v96 op_sel:[0,0,0] op_sel_hi:[1,0,0]\n"
"v_mfma_f32_16x16x32_bf16 a[52:55], v[44:47], v[116:119], 0\n"
"ds_read_b64_tr_b16 v[42:43], v22 offset:12552\n"
"v_fma_mix_f32 v105, v134, v89, v97 op_sel:[1,0,0] op_sel_hi:[1,0,0]\n"
"v_mfma_f32_16x16x32_bf16 a[56:59], v[44:47], v[120:123], 0\n"
"ds_read_b128 v[72:75], v108 offset:320\n"
"v_fma_mix_f32 v106, v135, v90, v98 op_sel:[0,0,0] op_sel_hi:[1,0,0]\n"
"v_fma_mix_f32 v107, v135, v91, v99 op_sel:[1,0,0] op_sel_hi:[1,0,0]\n"
"v_mfma_f32_16x16x32_bf16 a[60:63], v[44:47], v[124:127], 0\n"
"buffer_load_dwordx4 v[228:231], v7, s[20:23], s39 offen offset:1024\n"
"v_cvt_pk_bf16_f32 v134, v104, v105\n"
"v_cvt_pk_bf16_f32 v135, v106, v107\n"
"s_waitcnt lgkmcnt(10)\n"
"v_mfma_f32_16x16x32_bf16 a[64:67], v[48:51], v[112:115], 0\n"
"ds_read_b64_tr_b16 v[44:45], v22 offset:8704\n"
"v_fma_mix_f32 v100, v136, v84, v92 op_sel:[0,0,0] op_sel_hi:[1,0,0]\n"
"v_mfma_f32_16x16x32_bf16 a[68:71], v[48:51], v[116:119], 0\n"
"ds_read_b64_tr_b16 v[46:47], v22 offset:12800\n"
"v_fma_mix_f32 v101, v136, v85, v93 op_sel:[1,0,0] op_sel_hi:[1,0,0]\n"
"v_mfma_f32_16x16x32_bf16 a[72:75], v[48:51], v[120:123], 0\n"
"v_fma_mix_f32 v102, v137, v86, v94 op_sel:[0,0,0] op_sel_hi:[1,0,0]\n"
"v_fma_mix_f32 v103, v137, v87, v95 op_sel:[1,0,0] op_sel_hi:[1,0,0]\n"
"v_mfma_f32_16x16x32_bf16 a[76:79], v[48:51], v[124:127], 0\n"
"s_mov_b32 s39, 43008\n"
"buffer_load_dwordx4 v[216:219], v7, s[20:23], s39 offen\n"
"v_cvt_pk_bf16_f32 v136, v100, v101\n"
"v_cvt_pk_bf16_f32 v137, v102, v103\n"
"v_mfma_f32_16x16x32_bf16 a[80:83], v[52:55], v[112:115], 0\n"
"ds_read_b64_tr_b16 v[48:49], v22 offset:8712\n"
"v_fma_mix_f32 v104, v138, v88, v96 op_sel:[0,0,0] op_sel_hi:[1,0,0]\n"
"v_mfma_f32_16x16x32_bf16 a[84:87], v[52:55], v[116:119], 0\n"
"ds_read_b64_tr_b16 v[50:51], v22 offset:12808\n"
"v_fma_mix_f32 v105, v138, v89, v97 op_sel:[1,0,0] op_sel_hi:[1,0,0]\n"
"v_mfma_f32_16x16x32_bf16 a[88:91], v[52:55], v[120:123], 0\n"
"ds_read_b128 v[76:79], v108 offset:2560\n"
"v_fma_mix_f32 v106, v139, v90, v98 op_sel:[0,0,0] op_sel_hi:[1,0,0]\n"
"v_fma_mix_f32 v107, v139, v91, v99 op_sel:[1,0,0] op_sel_hi:[1,0,0]\n"
"v_mfma_f32_16x16x32_bf16 a[92:95], v[52:55], v[124:127], 0\n"
"buffer_load_dwordx4 v[232:235], v7, s[20:23], s39 offen offset:1024\n"
"v_cvt_pk_bf16_f32 v138, v104, v105\n"
"v_cvt_pk_bf16_f32 v139, v106, v107\n"
"v_mfma_f32_16x16x32_bf16 a[96:99], v[56:59], v[112:115], 0\n"
"ds_read_b64_tr_b16 v[52:53], v22 offset:8960\n"
"v_fma_mix_f32 v100, v140, v84, v92 op_sel:[0,0,0] op_sel_hi:[1,0,0]\n"
"v_mfma_f32_16x16x32_bf16 a[100:103], v[56:59], v[116:119], 0\n"
"ds_read_b64_tr_b16 v[54:55], v22 offset:13056\n"
"v_fma_mix_f32 v101, v140, v85, v93 op_sel:[1,0,0] op_sel_hi:[1,0,0]\n"
"v_mfma_f32_16x16x32_bf16 a[104:107], v[56:59], v[120:123], 0\n"
"v_fma_mix_f32 v102, v141, v86, v94 op_sel:[0,0,0] op_sel_hi:[1,0,0]\n"
"v_fma_mix_f32 v103, v141, v87, v95 op_sel:[1,0,0] op_sel_hi:[1,0,0]\n"
"v_mfma_f32_16x16x32_bf16 a[108:111], v[56:59], v[124:127], 0\n"
"s_mov_b32 s39, 61440\n"
"buffer_load_dwordx4 v[220:223], v7, s[20:23], s39 offen\n"
"v_cvt_pk_bf16_f32 v140, v100, v101\n"
"v_cvt_pk_bf16_f32 v141, v102, v103\n"
"v_mfma_f32_16x16x32_bf16 a[112:115], v[60:63], v[112:115], 0\n"
"ds_read_b64_tr_b16 v[56:57], v22 offset:8968\n"
"v_fma_mix_f32 v104, v142, v88, v96 op_sel:[0,0,0] op_sel_hi:[1,0,0]\n"
"v_mfma_f32_16x16x32_bf16 a[116:119], v[60:63], v[116:119], 0\n"
"ds_read_b64_tr_b16 v[58:59], v22 offset:13064\n"
"v_fma_mix_f32 v105, v142, v89, v97 op_sel:[1,0,0] op_sel_hi:[1,0,0]\n"
"v_mfma_f32_16x16x32_bf16 a[120:123], v[60:63], v[120:123], 0\n"
"ds_read_b128 v[80:83], v108 offset:2624\n"
"v_fma_mix_f32 v106, v143, v90, v98 op_sel:[0,0,0] op_sel_hi:[1,0,0]\n"
"v_fma_mix_f32 v107, v143, v91, v99 op_sel:[1,0,0] op_sel_hi:[1,0,0]\n"
"v_mfma_f32_16x16x32_bf16 a[124:127], v[60:63], v[124:127], 0\n"
"buffer_load_dwordx4 v[236:239], v7, s[20:23], s39 offen offset:1024\n"
"v_cvt_pk_bf16_f32 v142, v104, v105\n"
"v_cvt_pk_bf16_f32 v143, v106, v107\n"
"s_waitcnt vmcnt(20)\n"
"s_waitcnt lgkmcnt(0)\n"
"v_add_u32_e32 v23, 65536, v17\n"
"s_barrier\n"
"v_mfma_f32_16x16x32_bf16 a[0:3], v[64:67], v[128:131], a[0:3]\n"
"ds_read_b64_tr_b16 v[60:61], v23 offset:0\n"
"v_fma_mix_f32 v100, v144, v68, v76 op_sel:[0,0,0] op_sel_hi:[1,0,0]\n"
"v_mfma_f32_16x16x32_bf16 a[4:7], v[64:67], v[132:135], a[4:7]\n"
"ds_read_b64_tr_b16 v[62:63], v23 offset:4096\n"
"v_fma_mix_f32 v101, v144, v69, v77 op_sel:[1,0,0] op_sel_hi:[1,0,0]\n"
"v_mfma_f32_16x16x32_bf16 a[8:11], v[64:67], v[136:139], a[8:11]\n"
"v_fma_mix_f32 v102, v145, v70, v78 op_sel:[0,0,0] op_sel_hi:[1,0,0]\n"
"v_fma_mix_f32 v103, v145, v71, v79 op_sel:[1,0,0] op_sel_hi:[1,0,0]\n"
"v_mfma_f32_16x16x32_bf16 a[12:15], v[64:67], v[140:143], a[12:15]\n"
"s_add_u32 m0, s44, 81920\n"
"s_add_u32 s39, s46, 1572864\n"
"buffer_load_dwordx4 v6, s[16:19], s39 offen lds\n"
"v_cvt_pk_bf16_f32 v144, v100, v101\n"
"v_cvt_pk_bf16_f32 v145, v102, v103\n"
"v_mfma_f32_16x16x32_bf16 a[16:19], v[32:35], v[128:131], a[16:19]\n"
"ds_read_b64_tr_b16 v[64:65], v23 offset:8\n"
"v_fma_mix_f32 v104, v146, v72, v80 op_sel:[0,0,0] op_sel_hi:[1,0,0]\n"
"v_mfma_f32_16x16x32_bf16 a[20:23], v[32:35], v[132:135], a[20:23]\n"
"ds_read_b64_tr_b16 v[66:67], v23 offset:4104\n"
"v_fma_mix_f32 v105, v146, v73, v81 op_sel:[1,0,0] op_sel_hi:[1,0,0]\n"
"v_mfma_f32_16x16x32_bf16 a[24:27], v[32:35], v[136:139], a[24:27]\n"
"ds_read_b128 v[84:87], v108 offset:384\n"
"v_fma_mix_f32 v106, v147, v74, v82 op_sel:[0,0,0] op_sel_hi:[1,0,0]\n"
"v_fma_mix_f32 v107, v147, v75, v83 op_sel:[1,0,0] op_sel_hi:[1,0,0]\n"
"v_mfma_f32_16x16x32_bf16 a[28:31], v[32:35], v[140:143], a[28:31]\n"
"s_add_u32 m0, s44, 82944\n"
"s_add_u32 s39, s46, 1605632\n"
"buffer_load_dwordx4 v6, s[16:19], s39 offen lds\n"
"v_cvt_pk_bf16_f32 v146, v104, v105\n"
"v_cvt_pk_bf16_f32 v147, v106, v107\n"
"v_mfma_f32_16x16x32_bf16 a[32:35], v[36:39], v[128:131], a[32:35]\n"
"ds_read_b64_tr_b16 v[32:33], v23 offset:256\n"
"v_fma_mix_f32 v100, v148, v68, v76 op_sel:[0,0,0] op_sel_hi:[1,0,0]\n"
"v_mfma_f32_16x16x32_bf16 a[36:39], v[36:39], v[132:135], a[36:39]\n"
"ds_read_b64_tr_b16 v[34:35], v23 offset:4352\n"
"v_fma_mix_f32 v101, v148, v69, v77 op_sel:[1,0,0] op_sel_hi:[1,0,0]\n"
"v_mfma_f32_16x16x32_bf16 a[40:43], v[36:39], v[136:139], a[40:43]\n"
"v_fma_mix_f32 v102, v149, v70, v78 op_sel:[0,0,0] op_sel_hi:[1,0,0]\n"
"v_fma_mix_f32 v103, v149, v71, v79 op_sel:[1,0,0] op_sel_hi:[1,0,0]\n"
"v_mfma_f32_16x16x32_bf16 a[44:47], v[36:39], v[140:143], a[44:47]\n"
"s_add_u32 m0, s44, 83968\n"
"s_add_u32 s39, s46, 1638400\n"
"buffer_load_dwordx4 v6, s[16:19], s39 offen lds\n"
"v_cvt_pk_bf16_f32 v148, v100, v101\n"
"v_cvt_pk_bf16_f32 v149, v102, v103\n"
"v_mfma_f32_16x16x32_bf16 a[48:51], v[40:43], v[128:131], a[48:51]\n"
"ds_read_b64_tr_b16 v[36:37], v23 offset:264\n"
"v_fma_mix_f32 v104, v150, v72, v80 op_sel:[0,0,0] op_sel_hi:[1,0,0]\n"
"v_mfma_f32_16x16x32_bf16 a[52:55], v[40:43], v[132:135], a[52:55]\n"
"ds_read_b64_tr_b16 v[38:39], v23 offset:4360\n"
"v_fma_mix_f32 v105, v150, v73, v81 op_sel:[1,0,0] op_sel_hi:[1,0,0]\n"
"v_mfma_f32_16x16x32_bf16 a[56:59], v[40:43], v[136:139], a[56:59]\n"
"ds_read_b128 v[88:91], v108 offset:448\n"
"v_fma_mix_f32 v106, v151, v74, v82 op_sel:[0,0,0] op_sel_hi:[1,0,0]\n"
"v_fma_mix_f32 v107, v151, v75, v83 op_sel:[1,0,0] op_sel_hi:[1,0,0]\n"
"v_mfma_f32_16x16x32_bf16 a[60:63], v[40:43], v[140:143], a[60:63]\n"
"s_add_u32 m0, s44, 84992\n"
"s_add_u32 s39, s46, 1671168\n"
"buffer_load_dwordx4 v6, s[16:19], s39 offen lds\n"
"v_cvt_pk_bf16_f32 v150, v104, v105\n"
"v_cvt_pk_bf16_f32 v151, v106, v107\n"
"v_mfma_f32_16x16x32_bf16 a[64:67], v[44:47], v[128:131], a[64:67]\n"
"ds_read_b64_tr_b16 v[40:41], v23 offset:512\n"
"v_fma_mix_f32 v100, v152, v68, v76 op_sel:[0,0,0] op_sel_hi:[1,0,0]\n"
"v_mfma_f32_16x16x32_bf16 a[68:71], v[44:47], v[132:135], a[68:71]\n"
"ds_read_b64_tr_b16 v[42:43], v23 offset:4608\n"
"v_fma_mix_f32 v101, v152, v69, v77 op_sel:[1,0,0] op_sel_hi:[1,0,0]\n"
"v_mfma_f32_16x16x32_bf16 a[72:75], v[44:47], v[136:139], a[72:75]\n"
"v_fma_mix_f32 v102, v153, v70, v78 op_sel:[0,0,0] op_sel_hi:[1,0,0]\n"
"v_fma_mix_f32 v103, v153, v71, v79 op_sel:[1,0,0] op_sel_hi:[1,0,0]\n"
"v_mfma_f32_16x16x32_bf16 a[76:79], v[44:47], v[140:143], a[76:79]\n"
"s_add_u32 m0, s44, 32768\n"
"s_add_u32 s39, s46, 2097152\n"
"buffer_load_dwordx4 v6, s[16:19], s39 offen lds\n"
"v_cvt_pk_bf16_f32 v152, v100, v101\n"
"v_cvt_pk_bf16_f32 v153, v102, v103\n"
"v_mfma_f32_16x16x32_bf16 a[80:83], v[48:51], v[128:131], a[80:83]\n"
"ds_read_b64_tr_b16 v[44:45], v23 offset:520\n"
"v_fma_mix_f32 v104, v154, v72, v80 op_sel:[0,0,0] op_sel_hi:[1,0,0]\n"
"v_mfma_f32_16x16x32_bf16 a[84:87], v[48:51], v[132:135], a[84:87]\n"
"ds_read_b64_tr_b16 v[46:47], v23 offset:4616\n"
"v_fma_mix_f32 v105, v154, v73, v81 op_sel:[1,0,0] op_sel_hi:[1,0,0]\n"
"v_mfma_f32_16x16x32_bf16 a[88:91], v[48:51], v[136:139], a[88:91]\n"
"ds_read_b128 v[92:95], v108 offset:2688\n"
"v_fma_mix_f32 v106, v155, v74, v82 op_sel:[0,0,0] op_sel_hi:[1,0,0]\n"
"v_fma_mix_f32 v107, v155, v75, v83 op_sel:[1,0,0] op_sel_hi:[1,0,0]\n"
"v_mfma_f32_16x16x32_bf16 a[92:95], v[48:51], v[140:143], a[92:95]\n"
"s_add_u32 m0, s44, 33792\n"
"s_add_u32 s39, s46, 2129920\n"
"buffer_load_dwordx4 v6, s[16:19], s39 offen lds\n"
"v_cvt_pk_bf16_f32 v154, v104, v105\n"
"v_cvt_pk_bf16_f32 v155, v106, v107\n"
"v_mfma_f32_16x16x32_bf16 a[96:99], v[52:55], v[128:131], a[96:99]\n"
"ds_read_b64_tr_b16 v[48:49], v23 offset:768\n"
"v_fma_mix_f32 v100, v156, v68, v76 op_sel:[0,0,0] op_sel_hi:[1,0,0]\n"
"v_mfma_f32_16x16x32_bf16 a[100:103], v[52:55], v[132:135], a[100:103]\n"
"ds_read_b64_tr_b16 v[50:51], v23 offset:4864\n"
"v_fma_mix_f32 v101, v156, v69, v77 op_sel:[1,0,0] op_sel_hi:[1,0,0]\n"
"v_mfma_f32_16x16x32_bf16 a[104:107], v[52:55], v[136:139], a[104:107]\n"
"v_fma_mix_f32 v102, v157, v70, v78 op_sel:[0,0,0] op_sel_hi:[1,0,0]\n"
"v_fma_mix_f32 v103, v157, v71, v79 op_sel:[1,0,0] op_sel_hi:[1,0,0]\n"
"v_mfma_f32_16x16x32_bf16 a[108:111], v[52:55], v[140:143], a[108:111]\n"
"s_add_u32 m0, s44, 34816\n"
"s_add_u32 s39, s46, 2162688\n"
"buffer_load_dwordx4 v6, s[16:19], s39 offen lds\n"
"v_cvt_pk_bf16_f32 v156, v100, v101\n"
"v_cvt_pk_bf16_f32 v157, v102, v103\n"
"v_mfma_f32_16x16x32_bf16 a[112:115], v[56:59], v[128:131], a[112:115]\n"
"ds_read_b64_tr_b16 v[52:53], v23 offset:776\n"
"v_fma_mix_f32 v104, v158, v72, v80 op_sel:[0,0,0] op_sel_hi:[1,0,0]\n"
"v_mfma_f32_16x16x32_bf16 a[116:119], v[56:59], v[132:135], a[116:119]\n"
"ds_read_b64_tr_b16 v[54:55], v23 offset:4872\n"
"v_fma_mix_f32 v105, v158, v73, v81 op_sel:[1,0,0] op_sel_hi:[1,0,0]\n"
"v_mfma_f32_16x16x32_bf16 a[120:123], v[56:59], v[136:139], a[120:123]\n"
"ds_read_b128 v[96:99], v108 offset:2752\n"
"v_fma_mix_f32 v106, v159, v74, v82 op_sel:[0,0,0] op_sel_hi:[1,0,0]\n"
"v_fma_mix_f32 v107, v159, v75, v83 op_sel:[1,0,0] op_sel_hi:[1,0,0]\n"
"v_mfma_f32_16x16x32_bf16 a[124:127], v[56:59], v[140:143], a[124:127]\n"
"s_add_u32 m0, s44, 35840\n"
"s_add_u32 s39, s46, 2195456\n"
"buffer_load_dwordx4 v6, s[16:19], s39 offen lds\n"
"v_cvt_pk_bf16_f32 v158, v104, v105\n"
"v_cvt_pk_bf16_f32 v159, v106, v107\n"
"s_waitcnt lgkmcnt(0)\n"
"v_mfma_f32_16x16x32_bf16 a[0:3], v[60:63], v[144:147], a[0:3]\n"
"ds_read_b64_tr_b16 v[56:57], v23 offset:8192\n"
"v_fma_mix_f32 v100, v160, v84, v92 op_sel:[0,0,0] op_sel_hi:[1,0,0]\n"
"v_mfma_f32_16x16x32_bf16 a[4:7], v[60:63], v[148:151], a[4:7]\n"
"ds_read_b64_tr_b16 v[58:59], v23 offset:12288\n"
"v_fma_mix_f32 v101, v160, v85, v93 op_sel:[1,0,0] op_sel_hi:[1,0,0]\n"
"v_mfma_f32_16x16x32_bf16 a[8:11], v[60:63], v[152:155], a[8:11]\n"
"v_fma_mix_f32 v102, v161, v86, v94 op_sel:[0,0,0] op_sel_hi:[1,0,0]\n"
"v_fma_mix_f32 v103, v161, v87, v95 op_sel:[1,0,0] op_sel_hi:[1,0,0]\n"
"v_mfma_f32_16x16x32_bf16 a[12:15], v[60:63], v[156:159], a[12:15]\n"
"s_mov_b32 s39, 8192\n"
"buffer_load_dwordx4 v[112:115], v7, s[20:23], s39 offen\n"
"v_cvt_pk_bf16_f32 v160, v100, v101\n"
"v_cvt_pk_bf16_f32 v161, v102, v103\n"
"v_mfma_f32_16x16x32_bf16 a[16:19], v[64:67], v[144:147], a[16:19]\n"
"ds_read_b64_tr_b16 v[60:61], v23 offset:8200\n"
"v_fma_mix_f32 v104, v162, v88, v96 op_sel:[0,0,0] op_sel_hi:[1,0,0]\n"
"v_mfma_f32_16x16x32_bf16 a[20:23], v[64:67], v[148:151], a[20:23]\n"
"ds_read_b64_tr_b16 v[62:63], v23 offset:12296\n"
"v_fma_mix_f32 v105, v162, v89, v97 op_sel:[1,0,0] op_sel_hi:[1,0,0]\n"
"v_mfma_f32_16x16x32_bf16 a[24:27], v[64:67], v[152:155], a[24:27]\n"
"ds_read_b128 v[68:71], v108 offset:512\n"
"v_fma_mix_f32 v106, v163, v90, v98 op_sel:[0,0,0] op_sel_hi:[1,0,0]\n"
"v_fma_mix_f32 v107, v163, v91, v99 op_sel:[1,0,0] op_sel_hi:[1,0,0]\n"
"v_mfma_f32_16x16x32_bf16 a[28:31], v[64:67], v[156:159], a[28:31]\n"
"buffer_load_dwordx4 v[128:131], v7, s[20:23], s39 offen offset:1024\n"
"v_cvt_pk_bf16_f32 v162, v104, v105\n"
"v_cvt_pk_bf16_f32 v163, v106, v107\n"
"v_mfma_f32_16x16x32_bf16 a[32:35], v[32:35], v[144:147], a[32:35]\n"
"ds_read_b64_tr_b16 v[64:65], v23 offset:8448\n"
"v_fma_mix_f32 v100, v164, v84, v92 op_sel:[0,0,0] op_sel_hi:[1,0,0]\n"
"v_mfma_f32_16x16x32_bf16 a[36:39], v[32:35], v[148:151], a[36:39]\n"
"ds_read_b64_tr_b16 v[66:67], v23 offset:12544\n"
"v_fma_mix_f32 v101, v164, v85, v93 op_sel:[1,0,0] op_sel_hi:[1,0,0]\n"
"v_mfma_f32_16x16x32_bf16 a[40:43], v[32:35], v[152:155], a[40:43]\n"
"v_fma_mix_f32 v102, v165, v86, v94 op_sel:[0,0,0] op_sel_hi:[1,0,0]\n"
"v_fma_mix_f32 v103, v165, v87, v95 op_sel:[1,0,0] op_sel_hi:[1,0,0]\n"
"v_mfma_f32_16x16x32_bf16 a[44:47], v[32:35], v[156:159], a[44:47]\n"
"s_mov_b32 s39, 26624\n"
"buffer_load_dwordx4 v[116:119], v7, s[20:23], s39 offen\n"
"v_cvt_pk_bf16_f32 v164, v100, v101\n"
"v_cvt_pk_bf16_f32 v165, v102, v103\n"
"v_mfma_f32_16x16x32_bf16 a[48:51], v[36:39], v[144:147], a[48:51]\n"
"ds_read_b64_tr_b16 v[32:33], v23 offset:8456\n"
"v_fma_mix_f32 v104, v166, v88, v96 op_sel:[0,0,0] op_sel_hi:[1,0,0]\n"
"v_mfma_f32_16x16x32_bf16 a[52:55], v[36:39], v[148:151], a[52:55]\n"
"ds_read_b64_tr_b16 v[34:35], v23 offset:12552\n"
"v_fma_mix_f32 v105, v166, v89, v97 op_sel:[1,0,0] op_sel_hi:[1,0,0]\n"
"v_mfma_f32_16x16x32_bf16 a[56:59], v[36:39], v[152:155], a[56:59]\n"
"ds_read_b128 v[72:75], v108 offset:576\n"
"v_fma_mix_f32 v106, v167, v90, v98 op_sel:[0,0,0] op_sel_hi:[1,0,0]\n"
"v_fma_mix_f32 v107, v167, v91, v99 op_sel:[1,0,0] op_sel_hi:[1,0,0]\n"
"v_mfma_f32_16x16x32_bf16 a[60:63], v[36:39], v[156:159], a[60:63]\n"
"buffer_load_dwordx4 v[132:135], v7, s[20:23], s39 offen offset:1024\n"
"v_cvt_pk_bf16_f32 v166, v104, v105\n"
"v_cvt_pk_bf16_f32 v167, v106, v107\n"
"v_mfma_f32_16x16x32_bf16 a[64:67], v[40:43], v[144:147], a[64:67]\n"
"ds_read_b64_tr_b16 v[36:37], v23 offset:8704\n"
"v_fma_mix_f32 v100, v168, v84, v92 op_sel:[0,0,0] op_sel_hi:[1,0,0]\n"
"v_mfma_f32_16x16x32_bf16 a[68:71], v[40:43], v[148:151], a[68:71]\n"
"ds_read_b64_tr_b16 v[38:39], v23 offset:12800\n"
"v_fma_mix_f32 v101, v168, v85, v93 op_sel:[1,0,0] op_sel_hi:[1,0,0]\n"
"v_mfma_f32_16x16x32_bf16 a[72:75], v[40:43], v[152:155], a[72:75]\n"
"v_fma_mix_f32 v102, v169, v86, v94 op_sel:[0,0,0] op_sel_hi:[1,0,0]\n"
"v_fma_mix_f32 v103, v169, v87, v95 op_sel:[1,0,0] op_sel_hi:[1,0,0]\n"
"v_mfma_f32_16x16x32_bf16 a[76:79], v[40:43], v[156:159], a[76:79]\n"
"s_mov_b32 s39, 45056\n"
"buffer_load_dwordx4 v[120:123], v7, s[20:23], s39 offen\n"
"v_cvt_pk_bf16_f32 v168, v100, v101\n"
"v_cvt_pk_bf16_f32 v169, v102, v103\n"
"v_mfma_f32_16x16x32_bf16 a[80:83], v[44:47], v[144:147], a[80:83]\n"
"ds_read_b64_tr_b16 v[40:41], v23 offset:8712\n"
"v_fma_mix_f32 v104, v170, v88, v96 op_sel:[0,0,0] op_sel_hi:[1,0,0]\n"
"v_mfma_f32_16x16x32_bf16 a[84:87], v[44:47], v[148:151], a[84:87]\n"
"ds_read_b64_tr_b16 v[42:43], v23 offset:12808\n"
"v_fma_mix_f32 v105, v170, v89, v97 op_sel:[1,0,0] op_sel_hi:[1,0,0]\n"
"v_mfma_f32_16x16x32_bf16 a[88:91], v[44:47], v[152:155], a[88:91]\n"
"ds_read_b128 v[76:79], v108 offset:2816\n"
"v_fma_mix_f32 v106, v171, v90, v98 op_sel:[0,0,0] op_sel_hi:[1,0,0]\n"
"v_fma_mix_f32 v107, v171, v91, v99 op_sel:[1,0,0] op_sel_hi:[1,0,0]\n"
"v_mfma_f32_16x16x32_bf16 a[92:95], v[44:47], v[156:159], a[92:95]\n"
"buffer_load_dwordx4 v[136:139], v7, s[20:23], s39 offen offset:1024\n"
"v_cvt_pk_bf16_f32 v170, v104, v105\n"
"v_cvt_pk_bf16_f32 v171, v106, v107\n"
"v_mfma_f32_16x16x32_bf16 a[96:99], v[48:51], v[144:147], a[96:99]\n"
"ds_read_b64_tr_b16 v[44:45], v23 offset:8960\n"
"v_fma_mix_f32 v100, v172, v84, v92 op_sel:[0,0,0] op_sel_hi:[1,0,0]\n"
"v_mfma_f32_16x16x32_bf16 a[100:103], v[48:51], v[148:151], a[100:103]\n"
"ds_read_b64_tr_b16 v[46:47], v23 offset:13056\n"
"v_fma_mix_f32 v101, v172, v85, v93 op_sel:[1,0,0] op_sel_hi:[1,0,0]\n"
"v_mfma_f32_16x16x32_bf16 a[104:107], v[48:51], v[152:155], a[104:107]\n"
"v_fma_mix_f32 v102, v173, v86, v94 op_sel:[0,0,0] op_sel_hi:[1,0,0]\n"
"v_fma_mix_f32 v103, v173, v87, v95 op_sel:[1,0,0] op_sel_hi:[1,0,0]\n"
"v_mfma_f32_16x16x32_bf16 a[108:111], v[48:51], v[156:159], a[108:111]\n"
"s_mov_b32 s39, 63488\n"
"buffer_load_dwordx4 v[124:127], v7, s[20:23], s39 offen\n"
"v_cvt_pk_bf16_f32 v172, v100, v101\n"
"v_cvt_pk_bf16_f32 v173, v102, v103\n"
"v_mfma_f32_16x16x32_bf16 a[112:115], v[52:55], v[144:147], a[112:115]\n"
"ds_read_b64_tr_b16 v[48:49], v23 offset:8968\n"
"v_fma_mix_f32 v104, v174, v88, v96 op_sel:[0,0,0] op_sel_hi:[1,0,0]\n"
"v_mfma_f32_16x16x32_bf16 a[116:119], v[52:55], v[148:151], a[116:119]\n"
"ds_read_b64_tr_b16 v[50:51], v23 offset:13064\n"
"v_fma_mix_f32 v105, v174, v89, v97 op_sel:[1,0,0] op_sel_hi:[1,0,0]\n"
"v_mfma_f32_16x16x32_bf16 a[120:123], v[52:55], v[152:155], a[120:123]\n"
"ds_read_b128 v[80:83], v108 offset:2880\n"
"v_fma_mix_f32 v106, v175, v90, v98 op_sel:[0,0,0] op_sel_hi:[1,0,0]\n"
"v_fma_mix_f32 v107, v175, v91, v99 op_sel:[1,0,0] op_sel_hi:[1,0,0]\n"
"v_mfma_f32_16x16x32_bf16 a[124:127], v[52:55], v[156:159], a[124:127]\n"
"buffer_load_dwordx4 v[140:143], v7, s[20:23], s39 offen offset:1024\n"
"v_cvt_pk_bf16_f32 v174, v104, v105\n"
"v_cvt_pk_bf16_f32 v175, v106, v107\n"
"s_waitcnt vmcnt(24)\n"
"s_waitcnt lgkmcnt(0)\n"
"v_add_u32_e32 v22, 16384, v17\n"
"s_barrier\n"
"v_mfma_f32_16x16x32_bf16 a[0:3], v[56:59], v[160:163], a[0:3]\n"
"ds_read_b64_tr_b16 v[52:53], v22 offset:0\n"
"v_fma_mix_f32 v100, v176, v68, v76 op_sel:[0,0,0] op_sel_hi:[1,0,0]\n"
"v_mfma_f32_16x16x32_bf16 a[4:7], v[56:59], v[164:167], a[4:7]\n"
"ds_read_b64_tr_b16 v[54:55], v22 offset:4096\n"
"v_fma_mix_f32 v101, v176, v69, v77 op_sel:[1,0,0] op_sel_hi:[1,0,0]\n"
"v_mfma_f32_16x16x32_bf16 a[8:11], v[56:59], v[168:171], a[8:11]\n"
"v_fma_mix_f32 v102, v177, v70, v78 op_sel:[0,0,0] op_sel_hi:[1,0,0]\n"
"v_fma_mix_f32 v103, v177, v71, v79 op_sel:[1,0,0] op_sel_hi:[1,0,0]\n"
"v_mfma_f32_16x16x32_bf16 a[12:15], v[56:59], v[172:175], a[12:15]\n"
"s_add_u32 m0, s44, 98304\n"
"s_add_u32 s39, s46, 2621440\n"
"buffer_load_dwordx4 v6, s[16:19], s39 offen lds\n"
"v_cvt_pk_bf16_f32 v176, v100, v101\n"
"v_cvt_pk_bf16_f32 v177, v102, v103\n"
"v_mfma_f32_16x16x32_bf16 a[16:19], v[60:63], v[160:163], a[16:19]\n"
"ds_read_b64_tr_b16 v[56:57], v22 offset:8\n"
"v_fma_mix_f32 v104, v178, v72, v80 op_sel:[0,0,0] op_sel_hi:[1,0,0]\n"
"v_mfma_f32_16x16x32_bf16 a[20:23], v[60:63], v[164:167], a[20:23]\n"
"ds_read_b64_tr_b16 v[58:59], v22 offset:4104\n"
"v_fma_mix_f32 v105, v178, v73, v81 op_sel:[1,0,0] op_sel_hi:[1,0,0]\n"
"v_mfma_f32_16x16x32_bf16 a[24:27], v[60:63], v[168:171], a[24:27]\n"
"ds_read_b128 v[84:87], v108 offset:640\n"
"v_fma_mix_f32 v106, v179, v74, v82 op_sel:[0,0,0] op_sel_hi:[1,0,0]\n"
"v_fma_mix_f32 v107, v179, v75, v83 op_sel:[1,0,0] op_sel_hi:[1,0,0]\n"
"v_mfma_f32_16x16x32_bf16 a[28:31], v[60:63], v[172:175], a[28:31]\n"
"s_add_u32 m0, s44, 99328\n"
"s_add_u32 s39, s46, 2654208\n"
"buffer_load_dwordx4 v6, s[16:19], s39 offen lds\n"
"v_cvt_pk_bf16_f32 v178, v104, v105\n"
"v_cvt_pk_bf16_f32 v179, v106, v107\n"
"v_mfma_f32_16x16x32_bf16 a[32:35], v[64:67], v[160:163], a[32:35]\n"
"ds_read_b64_tr_b16 v[60:61], v22 offset:256\n"
"v_fma_mix_f32 v100, v180, v68, v76 op_sel:[0,0,0] op_sel_hi:[1,0,0]\n"
"v_mfma_f32_16x16x32_bf16 a[36:39], v[64:67], v[164:167], a[36:39]\n"
"ds_read_b64_tr_b16 v[62:63], v22 offset:4352\n"
"v_fma_mix_f32 v101, v180, v69, v77 op_sel:[1,0,0] op_sel_hi:[1,0,0]\n"
"v_mfma_f32_16x16x32_bf16 a[40:43], v[64:67], v[168:171], a[40:43]\n"
"v_fma_mix_f32 v102, v181, v70, v78 op_sel:[0,0,0] op_sel_hi:[1,0,0]\n"
"v_fma_mix_f32 v103, v181, v71, v79 op_sel:[1,0,0] op_sel_hi:[1,0,0]\n"
"v_mfma_f32_16x16x32_bf16 a[44:47], v[64:67], v[172:175], a[44:47]\n"
"s_add_u32 m0, s44, 100352\n"
"s_add_u32 s39, s46, 2686976\n"
"buffer_load_dwordx4 v6, s[16:19], s39 offen lds\n"
"v_cvt_pk_bf16_f32 v180, v100, v101\n"
"v_cvt_pk_bf16_f32 v181, v102, v103\n"
"v_mfma_f32_16x16x32_bf16 a[48:51], v[32:35], v[160:163], a[48:51]\n"
"ds_read_b64_tr_b16 v[64:65], v22 offset:264\n"
"v_fma_mix_f32 v104, v182, v72, v80 op_sel:[0,0,0] op_sel_hi:[1,0,0]\n"
"v_mfma_f32_16x16x32_bf16 a[52:55], v[32:35], v[164:167], a[52:55]\n"
"ds_read_b64_tr_b16 v[66:67], v22 offset:4360\n"
"v_fma_mix_f32 v105, v182, v73, v81 op_sel:[1,0,0] op_sel_hi:[1,0,0]\n"
"v_mfma_f32_16x16x32_bf16 a[56:59], v[32:35], v[168:171], a[56:59]\n"
"ds_read_b128 v[88:91], v108 offset:704\n"
"v_fma_mix_f32 v106, v183, v74, v82 op_sel:[0,0,0] op_sel_hi:[1,0,0]\n"
"v_fma_mix_f32 v107, v183, v75, v83 op_sel:[1,0,0] op_sel_hi:[1,0,0]\n"
"v_mfma_f32_16x16x32_bf16 a[60:63], v[32:35], v[172:175], a[60:63]\n"
"s_add_u32 m0, s44, 101376\n"
"s_add_u32 s39, s46, 2719744\n"
"buffer_load_dwordx4 v6, s[16:19], s39 offen lds\n"
"v_cvt_pk_bf16_f32 v182, v104, v105\n"
"v_cvt_pk_bf16_f32 v183, v106, v107\n"
"v_mfma_f32_16x16x32_bf16 a[64:67], v[36:39], v[160:163], a[64:67]\n"
"ds_read_b64_tr_b16 v[32:33], v22 offset:512\n"
"v_fma_mix_f32 v100, v184, v68, v76 op_sel:[0,0,0] op_sel_hi:[1,0,0]\n"
"v_mfma_f32_16x16x32_bf16 a[68:71], v[36:39], v[164:167], a[68:71]\n"
"ds_read_b64_tr_b16 v[34:35], v22 offset:4608\n"
"v_fma_mix_f32 v101, v184, v69, v77 op_sel:[1,0,0] op_sel_hi:[1,0,0]\n"
"v_mfma_f32_16x16x32_bf16 a[72:75], v[36:39], v[168:171], a[72:75]\n"
"v_fma_mix_f32 v102, v185, v70, v78 op_sel:[0,0,0] op_sel_hi:[1,0,0]\n"
"v_fma_mix_f32 v103, v185, v71, v79 op_sel:[1,0,0] op_sel_hi:[1,0,0]\n"
"v_mfma_f32_16x16x32_bf16 a[76:79], v[36:39], v[172:175], a[76:79]\n"
"s_add_u32 m0, s44, 49152\n"
"s_add_u32 s39, s46, 3145728\n"
"buffer_load_dwordx4 v6, s[16:19], s39 offen lds\n"
"v_cvt_pk_bf16_f32 v184, v100, v101\n"
"v_cvt_pk_bf16_f32 v185, v102, v103\n"
"v_mfma_f32_16x16x32_bf16 a[80:83], v[40:43], v[160:163], a[80:83]\n"
"ds_read_b64_tr_b16 v[36:37], v22 offset:520\n"
"v_fma_mix_f32 v104, v186, v72, v80 op_sel:[0,0,0] op_sel_hi:[1,0,0]\n"
"v_mfma_f32_16x16x32_bf16 a[84:87], v[40:43], v[164:167], a[84:87]\n"
"ds_read_b64_tr_b16 v[38:39], v22 offset:4616\n"
"v_fma_mix_f32 v105, v186, v73, v81 op_sel:[1,0,0] op_sel_hi:[1,0,0]\n"
"v_mfma_f32_16x16x32_bf16 a[88:91], v[40:43], v[168:171], a[88:91]\n"
"ds_read_b128 v[92:95], v108 offset:2944\n"
"v_fma_mix_f32 v106, v187, v74, v82 op_sel:[0,0,0] op_sel_hi:[1,0,0]\n"
"v_fma_mix_f32 v107, v187, v75, v83 op_sel:[1,0,0] op_sel_hi:[1,0,0]\n"
"v_mfma_f32_16x16x32_bf16 a[92:95], v[40:43], v[172:175], a[92:95]\n"
"s_add_u32 m0, s44, 50176\n"
"s_add_u32 s39, s46, 3178496\n"
"buffer_load_dwordx4 v6, s[16:19], s39 offen lds\n"
"v_cvt_pk_bf16_f32 v186, v104, v105\n"
"v_cvt_pk_bf16_f32 v187, v106, v107\n"
"v_mfma_f32_16x16x32_bf16 a[96:99], v[44:47], v[160:163], a[96:99]\n"
"ds_read_b64_tr_b16 v[40:41], v22 offset:768\n"
"v_fma_mix_f32 v100, v188, v68, v76 op_sel:[0,0,0] op_sel_hi:[1,0,0]\n"
"v_mfma_f32_16x16x32_bf16 a[100:103], v[44:47], v[164:167], a[100:103]\n"
"ds_read_b64_tr_b16 v[42:43], v22 offset:4864\n"
"v_fma_mix_f32 v101, v188, v69, v77 op_sel:[1,0,0] op_sel_hi:[1,0,0]\n"
"v_mfma_f32_16x16x32_bf16 a[104:107], v[44:47], v[168:171], a[104:107]\n"
"v_fma_mix_f32 v102, v189, v70, v78 op_sel:[0,0,0] op_sel_hi:[1,0,0]\n"
"v_fma_mix_f32 v103, v189, v71, v79 op_sel:[1,0,0] op_sel_hi:[1,0,0]\n"
"v_mfma_f32_16x16x32_bf16 a[108:111], v[44:47], v[172:175], a[108:111]\n"
"s_add_u32 m0, s44, 51200\n"
"s_add_u32 s39, s46, 3211264\n"
"buffer_load_dwordx4 v6, s[16:19], s39 offen lds\n"
"v_cvt_pk_bf16_f32 v188, v100, v101\n"
"v_cvt_pk_bf16_f32 v189, v102, v103\n"
"v_mfma_f32_16x16x32_bf16 a[112:115], v[48:51], v[160:163], a[112:115]\n"
"ds_read_b64_tr_b16 v[44:45], v22 offset:776\n"
"v_fma_mix_f32 v104, v190, v72, v80 op_sel:[0,0,0] op_sel_hi:[1,0,0]\n"
"v_mfma_f32_16x16x32_bf16 a[116:119], v[48:51], v[164:167], a[116:119]\n"
"ds_read_b64_tr_b16 v[46:47], v22 offset:4872\n"
"v_fma_mix_f32 v105, v190, v73, v81 op_sel:[1,0,0] op_sel_hi:[1,0,0]\n"
"v_mfma_f32_16x16x32_bf16 a[120:123], v[48:51], v[168:171], a[120:123]\n"
"ds_read_b128 v[96:99], v108 offset:3008\n"
"v_fma_mix_f32 v106, v191, v74, v82 op_sel:[0,0,0] op_sel_hi:[1,0,0]\n"
"v_fma_mix_f32 v107, v191, v75, v83 op_sel:[1,0,0] op_sel_hi:[1,0,0]\n"
"v_mfma_f32_16x16x32_bf16 a[124:127], v[48:51], v[172:175], a[124:127]\n"
"s_add_u32 m0, s44, 52224\n"
"s_add_u32 s39, s46, 3244032\n"
"buffer_load_dwordx4 v6, s[16:19], s39 offen lds\n"
"v_cvt_pk_bf16_f32 v190, v104, v105\n"
"v_cvt_pk_bf16_f32 v191, v106, v107\n"
"s_waitcnt lgkmcnt(0)\n"
"v_mfma_f32_16x16x32_bf16 a[0:3], v[52:55], v[176:179], a[0:3]\n"
"ds_read_b64_tr_b16 v[48:49], v22 offset:8192\n"
"v_fma_mix_f32 v100, v192, v84, v92 op_sel:[0,0,0] op_sel_hi:[1,0,0]\n"
"v_mfma_f32_16x16x32_bf16 a[4:7], v[52:55], v[180:183], a[4:7]\n"
"ds_read_b64_tr_b16 v[50:51], v22 offset:12288\n"
"v_fma_mix_f32 v101, v192, v85, v93 op_sel:[1,0,0] op_sel_hi:[1,0,0]\n"
"v_mfma_f32_16x16x32_bf16 a[8:11], v[52:55], v[184:187], a[8:11]\n"
"v_fma_mix_f32 v102, v193, v86, v94 op_sel:[0,0,0] op_sel_hi:[1,0,0]\n"
"v_fma_mix_f32 v103, v193, v87, v95 op_sel:[1,0,0] op_sel_hi:[1,0,0]\n"
"v_mfma_f32_16x16x32_bf16 a[12:15], v[52:55], v[188:191], a[12:15]\n"
"s_mov_b32 s39, 10240\n"
"buffer_load_dwordx4 v[144:147], v7, s[20:23], s39 offen\n"
"v_cvt_pk_bf16_f32 v192, v100, v101\n"
"v_cvt_pk_bf16_f32 v193, v102, v103\n"
"v_mfma_f32_16x16x32_bf16 a[16:19], v[56:59], v[176:179], a[16:19]\n"
"ds_read_b64_tr_b16 v[52:53], v22 offset:8200\n"
"v_fma_mix_f32 v104, v194, v88, v96 op_sel:[0,0,0] op_sel_hi:[1,0,0]\n"
"v_mfma_f32_16x16x32_bf16 a[20:23], v[56:59], v[180:183], a[20:23]\n"
"ds_read_b64_tr_b16 v[54:55], v22 offset:12296\n"
"v_fma_mix_f32 v105, v194, v89, v97 op_sel:[1,0,0] op_sel_hi:[1,0,0]\n"
"v_mfma_f32_16x16x32_bf16 a[24:27], v[56:59], v[184:187], a[24:27]\n"
"ds_read_b128 v[68:71], v108 offset:768\n"
"v_fma_mix_f32 v106, v195, v90, v98 op_sel:[0,0,0] op_sel_hi:[1,0,0]\n"
"v_fma_mix_f32 v107, v195, v91, v99 op_sel:[1,0,0] op_sel_hi:[1,0,0]\n"
"v_mfma_f32_16x16x32_bf16 a[28:31], v[56:59], v[188:191], a[28:31]\n"
"buffer_load_dwordx4 v[160:163], v7, s[20:23], s39 offen offset:1024\n"
"v_cvt_pk_bf16_f32 v194, v104, v105\n"
"v_cvt_pk_bf16_f32 v195, v106, v107\n"
"v_mfma_f32_16x16x32_bf16 a[32:35], v[60:63], v[176:179], a[32:35]\n"
"ds_read_b64_tr_b16 v[56:57], v22 offset:8448\n"
"v_fma_mix_f32 v100, v196, v84, v92 op_sel:[0,0,0] op_sel_hi:[1,0,0]\n"
"v_mfma_f32_16x16x32_bf16 a[36:39], v[60:63], v[180:183], a[36:39]\n"
"ds_read_b64_tr_b16 v[58:59], v22 offset:12544\n"
"v_fma_mix_f32 v101, v196, v85, v93 op_sel:[1,0,0] op_sel_hi:[1,0,0]\n"
"v_mfma_f32_16x16x32_bf16 a[40:43], v[60:63], v[184:187], a[40:43]\n"
"v_fma_mix_f32 v102, v197, v86, v94 op_sel:[0,0,0] op_sel_hi:[1,0,0]\n"
"v_fma_mix_f32 v103, v197, v87, v95 op_sel:[1,0,0] op_sel_hi:[1,0,0]\n"
"v_mfma_f32_16x16x32_bf16 a[44:47], v[60:63], v[188:191], a[44:47]\n"
"s_mov_b32 s39, 28672\n"
"buffer_load_dwordx4 v[148:151], v7, s[20:23], s39 offen\n"
"v_cvt_pk_bf16_f32 v196, v100, v101\n"
"v_cvt_pk_bf16_f32 v197, v102, v103\n"
"v_mfma_f32_16x16x32_bf16 a[48:51], v[64:67], v[176:179], a[48:51]\n"
"ds_read_b64_tr_b16 v[60:61], v22 offset:8456\n"
"v_fma_mix_f32 v104, v198, v88, v96 op_sel:[0,0,0] op_sel_hi:[1,0,0]\n"
"v_mfma_f32_16x16x32_bf16 a[52:55], v[64:67], v[180:183], a[52:55]\n"
"ds_read_b64_tr_b16 v[62:63], v22 offset:12552\n"
"v_fma_mix_f32 v105, v198, v89, v97 op_sel:[1,0,0] op_sel_hi:[1,0,0]\n"
"v_mfma_f32_16x16x32_bf16 a[56:59], v[64:67], v[184:187], a[56:59]\n"
"ds_read_b128 v[72:75], v108 offset:832\n"
"v_fma_mix_f32 v106, v199, v90, v98 op_sel:[0,0,0] op_sel_hi:[1,0,0]\n"
"v_fma_mix_f32 v107, v199, v91, v99 op_sel:[1,0,0] op_sel_hi:[1,0,0]\n"
"v_mfma_f32_16x16x32_bf16 a[60:63], v[64:67], v[188:191], a[60:63]\n"
"buffer_load_dwordx4 v[164:167], v7, s[20:23], s39 offen offset:1024\n"
"v_cvt_pk_bf16_f32 v198, v104, v105\n"
"v_cvt_pk_bf16_f32 v199, v106, v107\n"
"v_mfma_f32_16x16x32_bf16 a[64:67], v[32:35], v[176:179], a[64:67]\n"
"ds_read_b64_tr_b16 v[64:65], v22 offset:8704\n"
"v_fma_mix_f32 v100, v200, v84, v92 op_sel:[0,0,0] op_sel_hi:[1,0,0]\n"
"v_mfma_f32_16x16x32_bf16 a[68:71], v[32:35], v[180:183], a[68:71]\n"
"ds_read_b64_tr_b16 v[66:67], v22 offset:12800\n"
"v_fma_mix_f32 v101, v200, v85, v93 op_sel:[1,0,0] op_sel_hi:[1,0,0]\n"
"v_mfma_f32_16x16x32_bf16 a[72:75], v[32:35], v[184:187], a[72:75]\n"
"v_fma_mix_f32 v102, v201, v86, v94 op_sel:[0,0,0] op_sel_hi:[1,0,0]\n"
"v_fma_mix_f32 v103, v201, v87, v95 op_sel:[1,0,0] op_sel_hi:[1,0,0]\n"
"v_mfma_f32_16x16x32_bf16 a[76:79], v[32:35], v[188:191], a[76:79]\n"
"s_mov_b32 s39, 47104\n"
"buffer_load_dwordx4 v[152:155], v7, s[20:23], s39 offen\n"
"v_cvt_pk_bf16_f32 v200, v100, v101\n"
"v_cvt_pk_bf16_f32 v201, v102, v103\n"
"v_mfma_f32_16x16x32_bf16 a[80:83], v[36:39], v[176:179], a[80:83]\n"
"ds_read_b64_tr_b16 v[32:33], v22 offset:8712\n"
"v_fma_mix_f32 v104, v202, v88, v96 op_sel:[0,0,0] op_sel_hi:[1,0,0]\n"
"v_mfma_f32_16x16x32_bf16 a[84:87], v[36:39], v[180:183], a[84:87]\n"
"ds_read_b64_tr_b16 v[34:35], v22 offset:12808\n"
"v_fma_mix_f32 v105, v202, v89, v97 op_sel:[1,0,0] op_sel_hi:[1,0,0]\n"
"v_mfma_f32_16x16x32_bf16 a[88:91], v[36:39], v[184:187], a[88:91]\n"
"ds_read_b128 v[76:79], v108 offset:3072\n"
"v_fma_mix_f32 v106, v203, v90, v98 op_sel:[0,0,0] op_sel_hi:[1,0,0]\n"
"v_fma_mix_f32 v107, v203, v91, v99 op_sel:[1,0,0] op_sel_hi:[1,0,0]\n"
"v_mfma_f32_16x16x32_bf16 a[92:95], v[36:39], v[188:191], a[92:95]\n"
"buffer_load_dwordx4 v[168:171], v7, s[20:23], s39 offen offset:1024\n"
"v_cvt_pk_bf16_f32 v202, v104, v105\n"
"v_cvt_pk_bf16_f32 v203, v106, v107\n"
"v_mfma_f32_16x16x32_bf16 a[96:99], v[40:43], v[176:179], a[96:99]\n"
"ds_read_b64_tr_b16 v[36:37], v22 offset:8960\n"
"v_fma_mix_f32 v100, v204, v84, v92 op_sel:[0,0,0] op_sel_hi:[1,0,0]\n"
"v_mfma_f32_16x16x32_bf16 a[100:103], v[40:43], v[180:183], a[100:103]\n"
"ds_read_b64_tr_b16 v[38:39], v22 offset:13056\n"
"v_fma_mix_f32 v101, v204, v85, v93 op_sel:[1,0,0] op_sel_hi:[1,0,0]\n"
"v_mfma_f32_16x16x32_bf16 a[104:107], v[40:43], v[184:187], a[104:107]\n"
"v_fma_mix_f32 v102, v205, v86, v94 op_sel:[0,0,0] op_sel_hi:[1,0,0]\n"
"v_fma_mix_f32 v103, v205, v87, v95 op_sel:[1,0,0] op_sel_hi:[1,0,0]\n"
"v_mfma_f32_16x16x32_bf16 a[108:111], v[40:43], v[188:191], a[108:111]\n"
"s_mov_b32 s39, 65536\n"
"buffer_load_dwordx4 v[156:159], v7, s[20:23], s39 offen\n"
"v_cvt_pk_bf16_f32 v204, v100, v101\n"
"v_cvt_pk_bf16_f32 v205, v102, v103\n"
"v_mfma_f32_16x16x32_bf16 a[112:115], v[44:47], v[176:179], a[112:115]\n"
"ds_read_b64_tr_b16 v[40:41], v22 offset:8968\n"
"v_fma_mix_f32 v104, v206, v88, v96 op_sel:[0,0,0] op_sel_hi:[1,0,0]\n"
"v_mfma_f32_16x16x32_bf16 a[116:119], v[44:47], v[180:183], a[116:119]\n"
"ds_read_b64_tr_b16 v[42:43], v22 offset:13064\n"
"v_fma_mix_f32 v105, v206, v89, v97 op_sel:[1,0,0] op_sel_hi:[1,0,0]\n"
"v_mfma_f32_16x16x32_bf16 a[120:123], v[44:47], v[184:187], a[120:123]\n"
"ds_read_b128 v[80:83], v108 offset:3136\n"
"v_fma_mix_f32 v106, v207, v90, v98 op_sel:[0,0,0] op_sel_hi:[1,0,0]\n"
"v_fma_mix_f32 v107, v207, v91, v99 op_sel:[1,0,0] op_sel_hi:[1,0,0]\n"
"v_mfma_f32_16x16x32_bf16 a[124:127], v[44:47], v[188:191], a[124:127]\n"
"buffer_load_dwordx4 v[172:175], v7, s[20:23], s39 offen offset:1024\n"
"v_cvt_pk_bf16_f32 v206, v104, v105\n"
"v_cvt_pk_bf16_f32 v207, v106, v107\n"
"s_waitcnt vmcnt(32)\n"
"s_waitcnt lgkmcnt(0)\n"
"s_waitcnt vmcnt(28)\n"
"v_add_u32_e32 v23, 81920, v17\n"
"s_barrier\n"
"v_mfma_f32_16x16x32_bf16 a[0:3], v[48:51], v[192:195], a[0:3]\n"
"ds_read_b64_tr_b16 v[44:45], v23 offset:0\n"
"v_fma_mix_f32 v100, v208, v68, v76 op_sel:[0,0,0] op_sel_hi:[1,0,0]\n"
"v_mfma_f32_16x16x32_bf16 a[4:7], v[48:51], v[196:199], a[4:7]\n"
"ds_read_b64_tr_b16 v[46:47], v23 offset:4096\n"
"v_fma_mix_f32 v101, v208, v69, v77 op_sel:[1,0,0] op_sel_hi:[1,0,0]\n"
"v_mfma_f32_16x16x32_bf16 a[8:11], v[48:51], v[200:203], a[8:11]\n"
"v_fma_mix_f32 v102, v209, v70, v78 op_sel:[0,0,0] op_sel_hi:[1,0,0]\n"
"v_fma_mix_f32 v103, v209, v71, v79 op_sel:[1,0,0] op_sel_hi:[1,0,0]\n"
"v_mfma_f32_16x16x32_bf16 a[12:15], v[48:51], v[204:207], a[12:15]\n"
"s_add_u32 m0, s44, 114688\n"
"s_add_u32 s39, s46, 3670016\n"
"buffer_load_dwordx4 v6, s[16:19], s39 offen lds\n"
"v_cvt_pk_bf16_f32 v208, v100, v101\n"
"v_cvt_pk_bf16_f32 v209, v102, v103\n"
"v_mfma_f32_16x16x32_bf16 a[16:19], v[52:55], v[192:195], a[16:19]\n"
"ds_read_b64_tr_b16 v[48:49], v23 offset:8\n"
"v_fma_mix_f32 v104, v210, v72, v80 op_sel:[0,0,0] op_sel_hi:[1,0,0]\n"
"v_mfma_f32_16x16x32_bf16 a[20:23], v[52:55], v[196:199], a[20:23]\n"
"ds_read_b64_tr_b16 v[50:51], v23 offset:4104\n"
"v_fma_mix_f32 v105, v210, v73, v81 op_sel:[1,0,0] op_sel_hi:[1,0,0]\n"
"v_mfma_f32_16x16x32_bf16 a[24:27], v[52:55], v[200:203], a[24:27]\n"
"ds_read_b128 v[84:87], v108 offset:896\n"
"v_fma_mix_f32 v106, v211, v74, v82 op_sel:[0,0,0] op_sel_hi:[1,0,0]\n"
"v_fma_mix_f32 v107, v211, v75, v83 op_sel:[1,0,0] op_sel_hi:[1,0,0]\n"
"v_mfma_f32_16x16x32_bf16 a[28:31], v[52:55], v[204:207], a[28:31]\n"
"s_add_u32 m0, s44, 115712\n"
"s_add_u32 s39, s46, 3702784\n"
"buffer_load_dwordx4 v6, s[16:19], s39 offen lds\n"
"v_cvt_pk_bf16_f32 v210, v104, v105\n"
"v_cvt_pk_bf16_f32 v211, v106, v107\n"
"v_mfma_f32_16x16x32_bf16 a[32:35], v[56:59], v[192:195], a[32:35]\n"
"ds_read_b64_tr_b16 v[52:53], v23 offset:256\n"
"v_fma_mix_f32 v100, v212, v68, v76 op_sel:[0,0,0] op_sel_hi:[1,0,0]\n"
"v_mfma_f32_16x16x32_bf16 a[36:39], v[56:59], v[196:199], a[36:39]\n"
"ds_read_b64_tr_b16 v[54:55], v23 offset:4352\n"
"v_fma_mix_f32 v101, v212, v69, v77 op_sel:[1,0,0] op_sel_hi:[1,0,0]\n"
"v_mfma_f32_16x16x32_bf16 a[40:43], v[56:59], v[200:203], a[40:43]\n"
"v_fma_mix_f32 v102, v213, v70, v78 op_sel:[0,0,0] op_sel_hi:[1,0,0]\n"
"v_fma_mix_f32 v103, v213, v71, v79 op_sel:[1,0,0] op_sel_hi:[1,0,0]\n"
"v_mfma_f32_16x16x32_bf16 a[44:47], v[56:59], v[204:207], a[44:47]\n"
"s_add_u32 m0, s44, 116736\n"
"s_add_u32 s39, s46, 3735552\n"
"buffer_load_dwordx4 v6, s[16:19], s39 offen lds\n"
"v_cvt_pk_bf16_f32 v212, v100, v101\n"
"v_cvt_pk_bf16_f32 v213, v102, v103\n"
"v_mfma_f32_16x16x32_bf16 a[48:51], v[60:63], v[192:195], a[48:51]\n"
"ds_read_b64_tr_b16 v[56:57], v23 offset:264\n"
"v_fma_mix_f32 v104, v214, v72, v80 op_sel:[0,0,0] op_sel_hi:[1,0,0]\n"
"v_mfma_f32_16x16x32_bf16 a[52:55], v[60:63], v[196:199], a[52:55]\n"
"ds_read_b64_tr_b16 v[58:59], v23 offset:4360\n"
"v_fma_mix_f32 v105, v214, v73, v81 op_sel:[1,0,0] op_sel_hi:[1,0,0]\n"
"v_mfma_f32_16x16x32_bf16 a[56:59], v[60:63], v[200:203], a[56:59]\n"
"ds_read_b128 v[88:91], v108 offset:960\n"
"v_fma_mix_f32 v106, v215, v74, v82 op_sel:[0,0,0] op_sel_hi:[1,0,0]\n"
"v_fma_mix_f32 v107, v215, v75, v83 op_sel:[1,0,0] op_sel_hi:[1,0,0]\n"
"v_mfma_f32_16x16x32_bf16 a[60:63], v[60:63], v[204:207], a[60:63]\n"
"s_add_u32 m0, s44, 117760\n"
"s_add_u32 s39, s46, 3768320\n"
"buffer_load_dwordx4 v6, s[16:19], s39 offen lds\n"
"v_cvt_pk_bf16_f32 v214, v104, v105\n"
"v_cvt_pk_bf16_f32 v215, v106, v107\n"
"v_mfma_f32_16x16x32_bf16 a[64:67], v[64:67], v[192:195], a[64:67]\n"
"ds_read_b64_tr_b16 v[60:61], v23 offset:512\n"
"v_fma_mix_f32 v100, v216, v68, v76 op_sel:[0,0,0] op_sel_hi:[1,0,0]\n"
"v_mfma_f32_16x16x32_bf16 a[68:71], v[64:67], v[196:199], a[68:71]\n"
"ds_read_b64_tr_b16 v[62:63], v23 offset:4608\n"
"v_fma_mix_f32 v101, v216, v69, v77 op_sel:[1,0,0] op_sel_hi:[1,0,0]\n"
"v_mfma_f32_16x16x32_bf16 a[72:75], v[64:67], v[200:203], a[72:75]\n"
"v_fma_mix_f32 v102, v217, v70, v78 op_sel:[0,0,0] op_sel_hi:[1,0,0]\n"
"v_fma_mix_f32 v103, v217, v71, v79 op_sel:[1,0,0] op_sel_hi:[1,0,0]\n"
"v_mfma_f32_16x16x32_bf16 a[76:79], v[64:67], v[204:207], a[76:79]\n"
"s_add_u32 m0, s44, 131072\n"
"s_add_u32 s39, s46, 4194304\n"
"buffer_load_dwordx4 v6, s[16:19], s39 offen lds\n"
"v_cvt_pk_bf16_f32 v216, v100, v101\n"
"v_cvt_pk_bf16_f32 v217, v102, v103\n"
"v_mfma_f32_16x16x32_bf16 a[80:83], v[32:35], v[192:195], a[80:83]\n"
"ds_read_b64_tr_b16 v[64:65], v23 offset:520\n"
"v_fma_mix_f32 v104, v218, v72, v80 op_sel:[0,0,0] op_sel_hi:[1,0,0]\n"
"v_mfma_f32_16x16x32_bf16 a[84:87], v[32:35], v[196:199], a[84:87]\n"
"ds_read_b64_tr_b16 v[66:67], v23 offset:4616\n"
"v_fma_mix_f32 v105, v218, v73, v81 op_sel:[1,0,0] op_sel_hi:[1,0,0]\n"
"v_mfma_f32_16x16x32_bf16 a[88:91], v[32:35], v[200:203], a[88:91]\n"
"ds_read_b128 v[92:95], v108 offset:3200\n"
"v_fma_mix_f32 v106, v219, v74, v82 op_sel:[0,0,0] op_sel_hi:[1,0,0]\n"
"v_fma_mix_f32 v107, v219, v75, v83 op_sel:[1,0,0] op_sel_hi:[1,0,0]\n"
"v_mfma_f32_16x16x32_bf16 a[92:95], v[32:35], v[204:207], a[92:95]\n"
"s_add_u32 m0, s44, 132096\n"
"s_add_u32 s39, s46, 4227072\n"
"buffer_load_dwordx4 v6, s[16:19], s39 offen lds\n"
"v_cvt_pk_bf16_f32 v218, v104, v105\n"
"v_cvt_pk_bf16_f32 v219, v106, v107\n"
"v_mfma_f32_16x16x32_bf16 a[96:99], v[36:39], v[192:195], a[96:99]\n"
"ds_read_b64_tr_b16 v[32:33], v23 offset:768\n"
"v_fma_mix_f32 v100, v220, v68, v76 op_sel:[0,0,0] op_sel_hi:[1,0,0]\n"
"v_mfma_f32_16x16x32_bf16 a[100:103], v[36:39], v[196:199], a[100:103]\n"
"ds_read_b64_tr_b16 v[34:35], v23 offset:4864\n"
"v_fma_mix_f32 v101, v220, v69, v77 op_sel:[1,0,0] op_sel_hi:[1,0,0]\n"
"v_mfma_f32_16x16x32_bf16 a[104:107], v[36:39], v[200:203], a[104:107]\n"
"v_fma_mix_f32 v102, v221, v70, v78 op_sel:[0,0,0] op_sel_hi:[1,0,0]\n"
"v_fma_mix_f32 v103, v221, v71, v79 op_sel:[1,0,0] op_sel_hi:[1,0,0]\n"
"v_mfma_f32_16x16x32_bf16 a[108:111], v[36:39], v[204:207], a[108:111]\n"
"s_add_u32 m0, s44, 133120\n"
"s_add_u32 s39, s46, 4259840\n"
"buffer_load_dwordx4 v6, s[16:19], s39 offen lds\n"
"v_cvt_pk_bf16_f32 v220, v100, v101\n"
"v_cvt_pk_bf16_f32 v221, v102, v103\n"
"v_mfma_f32_16x16x32_bf16 a[112:115], v[40:43], v[192:195], a[112:115]\n"
"ds_read_b64_tr_b16 v[36:37], v23 offset:776\n"
"v_fma_mix_f32 v104, v222, v72, v80 op_sel:[0,0,0] op_sel_hi:[1,0,0]\n"
"v_mfma_f32_16x16x32_bf16 a[116:119], v[40:43], v[196:199], a[116:119]\n"
"ds_read_b64_tr_b16 v[38:39], v23 offset:4872\n"
"v_fma_mix_f32 v105, v222, v73, v81 op_sel:[1,0,0] op_sel_hi:[1,0,0]\n"
"v_mfma_f32_16x16x32_bf16 a[120:123], v[40:43], v[200:203], a[120:123]\n"
"ds_read_b128 v[96:99], v108 offset:3264\n"
"v_fma_mix_f32 v106, v223, v74, v82 op_sel:[0,0,0] op_sel_hi:[1,0,0]\n"
"v_fma_mix_f32 v107, v223, v75, v83 op_sel:[1,0,0] op_sel_hi:[1,0,0]\n"
"v_mfma_f32_16x16x32_bf16 a[124:127], v[40:43], v[204:207], a[124:127]\n"
"s_add_u32 m0, s44, 134144\n"
"s_add_u32 s39, s46, 4292608\n"
"buffer_load_dwordx4 v6, s[16:19], s39 offen lds\n"
"v_cvt_pk_bf16_f32 v222, v104, v105\n"
"v_cvt_pk_bf16_f32 v223, v106, v107\n"
"s_waitcnt lgkmcnt(0)\n"
"v_mfma_f32_16x16x32_bf16 a[0:3], v[44:47], v[208:211], a[0:3]\n"
"ds_read_b64_tr_b16 v[40:41], v23 offset:8192\n"
"v_fma_mix_f32 v100, v224, v84, v92 op_sel:[0,0,0] op_sel_hi:[1,0,0]\n"
"v_mfma_f32_16x16x32_bf16 a[4:7], v[44:47], v[212:215], a[4:7]\n"
"ds_read_b64_tr_b16 v[42:43], v23 offset:12288\n"
"v_fma_mix_f32 v101, v224, v85, v93 op_sel:[1,0,0] op_sel_hi:[1,0,0]\n"
"v_mfma_f32_16x16x32_bf16 a[8:11], v[44:47], v[216:219], a[8:11]\n"
"v_fma_mix_f32 v102, v225, v86, v94 op_sel:[0,0,0] op_sel_hi:[1,0,0]\n"
"v_fma_mix_f32 v103, v225, v87, v95 op_sel:[1,0,0] op_sel_hi:[1,0,0]\n"
"v_mfma_f32_16x16x32_bf16 a[12:15], v[44:47], v[220:223], a[12:15]\n"
"s_mov_b32 s39, 12288\n"
"buffer_load_dwordx4 v[176:179], v7, s[20:23], s39 offen\n"
"v_cvt_pk_bf16_f32 v224, v100, v101\n"
"v_cvt_pk_bf16_f32 v225, v102, v103\n"
"v_mfma_f32_16x16x32_bf16 a[16:19], v[48:51], v[208:211], a[16:19]\n"
"ds_read_b64_tr_b16 v[44:45], v23 offset:8200\n"
"v_fma_mix_f32 v104, v226, v88, v96 op_sel:[0,0,0] op_sel_hi:[1,0,0]\n"
"v_mfma_f32_16x16x32_bf16 a[20:23], v[48:51], v[212:215], a[20:23]\n"
"ds_read_b64_tr_b16 v[46:47], v23 offset:12296\n"
"v_fma_mix_f32 v105, v226, v89, v97 op_sel:[1,0,0] op_sel_hi:[1,0,0]\n"
"v_mfma_f32_16x16x32_bf16 a[24:27], v[48:51], v[216:219], a[24:27]\n"
"ds_read_b128 v[68:71], v108 offset:1024\n"
"v_fma_mix_f32 v106, v227, v90, v98 op_sel:[0,0,0] op_sel_hi:[1,0,0]\n"
"v_fma_mix_f32 v107, v227, v91, v99 op_sel:[1,0,0] op_sel_hi:[1,0,0]\n"
"v_mfma_f32_16x16x32_bf16 a[28:31], v[48:51], v[220:223], a[28:31]\n"
"buffer_load_dwordx4 v[192:195], v7, s[20:23], s39 offen offset:1024\n"
"v_cvt_pk_bf16_f32 v226, v104, v105\n"
"v_cvt_pk_bf16_f32 v227, v106, v107\n"
"v_mfma_f32_16x16x32_bf16 a[32:35], v[52:55], v[208:211], a[32:35]\n"
"ds_read_b64_tr_b16 v[48:49], v23 offset:8448\n"
"v_fma_mix_f32 v100, v228, v84, v92 op_sel:[0,0,0] op_sel_hi:[1,0,0]\n"
"v_mfma_f32_16x16x32_bf16 a[36:39], v[52:55], v[212:215], a[36:39]\n"
"ds_read_b64_tr_b16 v[50:51], v23 offset:12544\n"
"v_fma_mix_f32 v101, v228, v85, v93 op_sel:[1,0,0] op_sel_hi:[1,0,0]\n"
"v_mfma_f32_16x16x32_bf16 a[40:43], v[52:55], v[216:219], a[40:43]\n"
"v_fma_mix_f32 v102, v229, v86, v94 op_sel:[0,0,0] op_sel_hi:[1,0,0]\n"
"v_fma_mix_f32 v103, v229, v87, v95 op_sel:[1,0,0] op_sel_hi:[1,0,0]\n"
"v_mfma_f32_16x16x32_bf16 a[44:47], v[52:55], v[220:223], a[44:47]\n"
"s_mov_b32 s39, 30720\n"
"buffer_load_dwordx4 v[180:183], v7, s[20:23], s39 offen\n"
"v_cvt_pk_bf16_f32 v228, v100, v101\n"
"v_cvt_pk_bf16_f32 v229, v102, v103\n"
"v_mfma_f32_16x16x32_bf16 a[48:51], v[56:59], v[208:211], a[48:51]\n"
"ds_read_b64_tr_b16 v[52:53], v23 offset:8456\n"
"v_fma_mix_f32 v104, v230, v88, v96 op_sel:[0,0,0] op_sel_hi:[1,0,0]\n"
"v_mfma_f32_16x16x32_bf16 a[52:55], v[56:59], v[212:215], a[52:55]\n"
"ds_read_b64_tr_b16 v[54:55], v23 offset:12552\n"
"v_fma_mix_f32 v105, v230, v89, v97 op_sel:[1,0,0] op_sel_hi:[1,0,0]\n"
"v_mfma_f32_16x16x32_bf16 a[56:59], v[56:59], v[216:219], a[56:59]\n"
"ds_read_b128 v[72:75], v108 offset:1088\n"
"v_fma_mix_f32 v106, v231, v90, v98 op_sel:[0,0,0] op_sel_hi:[1,0,0]\n"
"v_fma_mix_f32 v107, v231, v91, v99 op_sel:[1,0,0] op_sel_hi:[1,0,0]\n"
"v_mfma_f32_16x16x32_bf16 a[60:63], v[56:59], v[220:223], a[60:63]\n"
"v_cvt_pk_bf16_f32 v230, v104, v105\n"
"v_cvt_pk_bf16_f32 v231, v106, v107\n"
"v_mfma_f32_16x16x32_bf16 a[64:67], v[60:63], v[208:211], a[64:67]\n"
"ds_read_b64_tr_b16 v[56:57], v23 offset:8704\n"
"v_fma_mix_f32 v100, v232, v84, v92 op_sel:[0,0,0] op_sel_hi:[1,0,0]\n"
"v_mfma_f32_16x16x32_bf16 a[68:71], v[60:63], v[212:215], a[68:71]\n"
"ds_read_b64_tr_b16 v[58:59], v23 offset:12800\n"
"v_fma_mix_f32 v101, v232, v85, v93 op_sel:[1,0,0] op_sel_hi:[1,0,0]\n"
"v_mfma_f32_16x16x32_bf16 a[72:75], v[60:63], v[216:219], a[72:75]\n"
"v_fma_mix_f32 v102, v233, v86, v94 op_sel:[0,0,0] op_sel_hi:[1,0,0]\n"
"v_fma_mix_f32 v103, v233, v87, v95 op_sel:[1,0,0] op_sel_hi:[1,0,0]\n"
"v_mfma_f32_16x16x32_bf16 a[76:79], v[60:63], v[220:223], a[76:79]\n"
"buffer_load_dwordx4 v[196:199], v7, s[20:23], s39 offen offset:1024\n"
"v_cvt_pk_bf16_f32 v232, v100, v101\n"
"v_cvt_pk_bf16_f32 v233, v102, v103\n"
"v_mfma_f32_16x16x32_bf16 a[80:83], v[64:67], v[208:211], a[80:83]\n"
"ds_read_b64_tr_b16 v[60:61], v23 offset:8712\n"
"v_fma_mix_f32 v104, v234, v88, v96 op_sel:[0,0,0] op_sel_hi:[1,0,0]\n"
"v_mfma_f32_16x16x32_bf16 a[84:87], v[64:67], v[212:215], a[84:87]\n"
"ds_read_b64_tr_b16 v[62:63], v23 offset:12808\n"
"v_fma_mix_f32 v105, v234, v89, v97 op_sel:[1,0,0] op_sel_hi:[1,0,0]\n"
"v_mfma_f32_16x16x32_bf16 a[88:91], v[64:67], v[216:219], a[88:91]\n"
"ds_read_b128 v[76:79], v108 offset:3328\n"
"v_fma_mix_f32 v106, v235, v90, v98 op_sel:[0,0,0] op_sel_hi:[1,0,0]\n"
"v_fma_mix_f32 v107, v235, v91, v99 op_sel:[1,0,0] op_sel_hi:[1,0,0]\n"
"v_mfma_f32_16x16x32_bf16 a[92:95], v[64:67], v[220:223], a[92:95]\n"
"s_mov_b32 s39, 49152\n"
"buffer_load_dwordx4 v[184:187], v7, s[20:23], s39 offen\n"
"v_cvt_pk_bf16_f32 v234, v104, v105\n"
"v_cvt_pk_bf16_f32 v235, v106, v107\n"
"v_mfma_f32_16x16x32_bf16 a[96:99], v[32:35], v[208:211], a[96:99]\n"
"ds_read_b64_tr_b16 v[64:65], v23 offset:8960\n"
"v_fma_mix_f32 v100, v236, v84, v92 op_sel:[0,0,0] op_sel_hi:[1,0,0]\n"
"v_mfma_f32_16x16x32_bf16 a[100:103], v[32:35], v[212:215], a[100:103]\n"
"ds_read_b64_tr_b16 v[66:67], v23 offset:13056\n"
"v_fma_mix_f32 v101, v236, v85, v93 op_sel:[1,0,0] op_sel_hi:[1,0,0]\n"
"v_mfma_f32_16x16x32_bf16 a[104:107], v[32:35], v[216:219], a[104:107]\n"
"v_fma_mix_f32 v102, v237, v86, v94 op_sel:[0,0,0] op_sel_hi:[1,0,0]\n"
"v_fma_mix_f32 v103, v237, v87, v95 op_sel:[1,0,0] op_sel_hi:[1,0,0]\n"
"v_mfma_f32_16x16x32_bf16 a[108:111], v[32:35], v[220:223], a[108:111]\n"
"buffer_load_dwordx4 v[200:203], v7, s[20:23], s39 offen offset:1024\n"
"v_cvt_pk_bf16_f32 v236, v100, v101\n"
"v_cvt_pk_bf16_f32 v237, v102, v103\n"
"v_mfma_f32_16x16x32_bf16 a[112:115], v[36:39], v[208:211], a[112:115]\n"
"ds_read_b64_tr_b16 v[32:33], v23 offset:8968\n"
"v_fma_mix_f32 v104, v238, v88, v96 op_sel:[0,0,0] op_sel_hi:[1,0,0]\n"
"v_mfma_f32_16x16x32_bf16 a[116:119], v[36:39], v[212:215], a[116:119]\n"
"ds_read_b64_tr_b16 v[34:35], v23 offset:13064\n"
"v_fma_mix_f32 v105, v238, v89, v97 op_sel:[1,0,0] op_sel_hi:[1,0,0]\n"
"v_mfma_f32_16x16x32_bf16 a[120:123], v[36:39], v[216:219], a[120:123]\n"
"ds_read_b128 v[80:83], v108 offset:3392\n"
"v_fma_mix_f32 v106, v239, v90, v98 op_sel:[0,0,0] op_sel_hi:[1,0,0]\n"
"v_fma_mix_f32 v107, v239, v91, v99 op_sel:[1,0,0] op_sel_hi:[1,0,0]\n"
"v_mfma_f32_16x16x32_bf16 a[124:127], v[36:39], v[220:223], a[124:127]\n"
"v_cvt_pk_bf16_f32 v238, v104, v105\n"
"v_cvt_pk_bf16_f32 v239, v106, v107\n"
"s_waitcnt vmcnt(30)\n"
"s_waitcnt lgkmcnt(0)\n"
"v_add_u32_e32 v22, 32768, v17\n"
"s_barrier\n"
"v_mfma_f32_16x16x32_bf16 a[0:3], v[40:43], v[224:227], a[0:3]\n"
"ds_read_b64_tr_b16 v[36:37], v22 offset:0\n"
"v_fma_mix_f32 v100, v112, v68, v76 op_sel:[0,0,0] op_sel_hi:[1,0,0]\n"
"v_mfma_f32_16x16x32_bf16 a[4:7], v[40:43], v[228:231], a[4:7]\n"
"ds_read_b64_tr_b16 v[38:39], v22 offset:4096\n"
"v_fma_mix_f32 v101, v112, v69, v77 op_sel:[1,0,0] op_sel_hi:[1,0,0]\n"
"v_mfma_f32_16x16x32_bf16 a[8:11], v[40:43], v[232:235], a[8:11]\n"
"v_fma_mix_f32 v102, v113, v70, v78 op_sel:[0,0,0] op_sel_hi:[1,0,0]\n"
"v_fma_mix_f32 v103, v113, v71, v79 op_sel:[1,0,0] op_sel_hi:[1,0,0]\n"
"v_mfma_f32_16x16x32_bf16 a[12:15], v[40:43], v[236:239], a[12:15]\n"
"s_mov_b32 s39, 67584\n"
"buffer_load_dwordx4 v[188:191], v7, s[20:23], s39 offen\n"
"v_cvt_pk_bf16_f32 v112, v100, v101\n"
"v_cvt_pk_bf16_f32 v113, v102, v103\n"
"v_mfma_f32_16x16x32_bf16 a[16:19], v[44:47], v[224:227], a[16:19]\n"
"ds_read_b64_tr_b16 v[40:41], v22 offset:8\n"
"v_fma_mix_f32 v104, v114, v72, v80 op_sel:[0,0,0] op_sel_hi:[1,0,0]\n"
"v_mfma_f32_16x16x32_bf16 a[20:23], v[44:47], v[228:231], a[20:23]\n"
"ds_read_b64_tr_b16 v[42:43], v22 offset:4104\n"
"v_fma_mix_f32 v105, v114, v73, v81 op_sel:[1,0,0] op_sel_hi:[1,0,0]\n"
"v_mfma_f32_16x16x32_bf16 a[24:27], v[44:47], v[232:235], a[24:27]\n"
"ds_read_b128 v[84:87], v108 offset:1152\n"
"v_fma_mix_f32 v106, v115, v74, v82 op_sel:[0,0,0] op_sel_hi:[1,0,0]\n"
"v_fma_mix_f32 v107, v115, v75, v83 op_sel:[1,0,0] op_sel_hi:[1,0,0]\n"
"v_mfma_f32_16x16x32_bf16 a[28:31], v[44:47], v[236:239], a[28:31]\n"
"buffer_load_dwordx4 v[204:207], v7, s[20:23], s39 offen offset:1024\n"
"v_cvt_pk_bf16_f32 v114, v104, v105\n"
"v_cvt_pk_bf16_f32 v115, v106, v107\n"
"v_mfma_f32_16x16x32_bf16 a[32:35], v[48:51], v[224:227], a[32:35]\n"
"ds_read_b64_tr_b16 v[44:45], v22 offset:256\n"
"v_fma_mix_f32 v100, v116, v68, v76 op_sel:[0,0,0] op_sel_hi:[1,0,0]\n"
"v_mfma_f32_16x16x32_bf16 a[36:39], v[48:51], v[228:231], a[36:39]\n"
"ds_read_b64_tr_b16 v[46:47], v22 offset:4352\n"
"v_fma_mix_f32 v101, v116, v69, v77 op_sel:[1,0,0] op_sel_hi:[1,0,0]\n"
"v_mfma_f32_16x16x32_bf16 a[40:43], v[48:51], v[232:235], a[40:43]\n"
"v_fma_mix_f32 v102, v117, v70, v78 op_sel:[0,0,0] op_sel_hi:[1,0,0]\n"
"v_fma_mix_f32 v103, v117, v71, v79 op_sel:[1,0,0] op_sel_hi:[1,0,0]\n"
"v_mfma_f32_16x16x32_bf16 a[44:47], v[48:51], v[236:239], a[44:47]\n"
"s_add_u32 s38, s45, 0\n"
"s_add_u32 m0, s44, 0\n"
"s_nop 0\n"
"buffer_load_dwordx4 v1, s[8:11], s38 offen lds\n"
"v_cvt_pk_bf16_f32 v116, v100, v101\n"
"v_cvt_pk_bf16_f32 v117, v102, v103\n"
"v_mfma_f32_16x16x32_bf16 a[48:51], v[52:55], v[224:227], a[48:51]\n"
"ds_read_b64_tr_b16 v[48:49], v22 offset:264\n"
"v_fma_mix_f32 v104, v118, v72, v80 op_sel:[0,0,0] op_sel_hi:[1,0,0]\n"
"v_mfma_f32_16x16x32_bf16 a[52:55], v[52:55], v[228:231], a[52:55]\n"
"ds_read_b64_tr_b16 v[50:51], v22 offset:4360\n"
"v_fma_mix_f32 v105, v118, v73, v81 op_sel:[1,0,0] op_sel_hi:[1,0,0]\n"
"v_mfma_f32_16x16x32_bf16 a[56:59], v[52:55], v[232:235], a[56:59]\n"
"ds_read_b128 v[88:91], v108 offset:1216\n"
"v_fma_mix_f32 v106, v119, v74, v82 op_sel:[0,0,0] op_sel_hi:[1,0,0]\n"
"v_fma_mix_f32 v107, v119, v75, v83 op_sel:[1,0,0] op_sel_hi:[1,0,0]\n"
"v_mfma_f32_16x16x32_bf16 a[60:63], v[52:55], v[236:239], a[60:63]\n"
"v_cvt_pk_bf16_f32 v118, v104, v105\n"
"v_cvt_pk_bf16_f32 v119, v106, v107\n"
"v_mfma_f32_16x16x32_bf16 a[64:67], v[56:59], v[224:227], a[64:67]\n"
"ds_read_b64_tr_b16 v[52:53], v22 offset:512\n"
"v_fma_mix_f32 v100, v120, v68, v76 op_sel:[0,0,0] op_sel_hi:[1,0,0]\n"
"v_mfma_f32_16x16x32_bf16 a[68:71], v[56:59], v[228:231], a[68:71]\n"
"ds_read_b64_tr_b16 v[54:55], v22 offset:4608\n"
"v_fma_mix_f32 v101, v120, v69, v77 op_sel:[1,0,0] op_sel_hi:[1,0,0]\n"
"v_mfma_f32_16x16x32_bf16 a[72:75], v[56:59], v[232:235], a[72:75]\n"
"v_fma_mix_f32 v102, v121, v70, v78 op_sel:[0,0,0] op_sel_hi:[1,0,0]\n"
"v_fma_mix_f32 v103, v121, v71, v79 op_sel:[1,0,0] op_sel_hi:[1,0,0]\n"
"v_mfma_f32_16x16x32_bf16 a[76:79], v[56:59], v[236:239], a[76:79]\n"
"buffer_load_dwordx4 v2, s[8:11], s38 offen offset:1024 lds\n"
"v_cvt_pk_bf16_f32 v120, v100, v101\n"
"v_cvt_pk_bf16_f32 v121, v102, v103\n"
"v_mfma_f32_16x16x32_bf16 a[80:83], v[60:63], v[224:227], a[80:83]\n"
"ds_read_b64_tr_b16 v[56:57], v22 offset:520\n"
"v_fma_mix_f32 v104, v122, v72, v80 op_sel:[0,0,0] op_sel_hi:[1,0,0]\n"
"v_mfma_f32_16x16x32_bf16 a[84:87], v[60:63], v[228:231], a[84:87]\n"
"ds_read_b64_tr_b16 v[58:59], v22 offset:4616\n"
"v_fma_mix_f32 v105, v122, v73, v81 op_sel:[1,0,0] op_sel_hi:[1,0,0]\n"
"v_mfma_f32_16x16x32_bf16 a[88:91], v[60:63], v[232:235], a[88:91]\n"
"ds_read_b128 v[92:95], v108 offset:3456\n"
"v_fma_mix_f32 v106, v123, v74, v82 op_sel:[0,0,0] op_sel_hi:[1,0,0]\n"
"v_fma_mix_f32 v107, v123, v75, v83 op_sel:[1,0,0] op_sel_hi:[1,0,0]\n"
"v_mfma_f32_16x16x32_bf16 a[92:95], v[60:63], v[236:239], a[92:95]\n"
"buffer_load_dwordx4 v3, s[8:11], s38 offen offset:2048 lds\n"
"v_cvt_pk_bf16_f32 v122, v104, v105\n"
"v_cvt_pk_bf16_f32 v123, v106, v107\n"
"v_mfma_f32_16x16x32_bf16 a[96:99], v[64:67], v[224:227], a[96:99]\n"
"ds_read_b64_tr_b16 v[60:61], v22 offset:768\n"
"v_fma_mix_f32 v100, v124, v68, v76 op_sel:[0,0,0] op_sel_hi:[1,0,0]\n"
"v_mfma_f32_16x16x32_bf16 a[100:103], v[64:67], v[228:231], a[100:103]\n"
"ds_read_b64_tr_b16 v[62:63], v22 offset:4864\n"
"v_fma_mix_f32 v101, v124, v69, v77 op_sel:[1,0,0] op_sel_hi:[1,0,0]\n"
"v_mfma_f32_16x16x32_bf16 a[104:107], v[64:67], v[232:235], a[104:107]\n"
"v_fma_mix_f32 v102, v125, v70, v78 op_sel:[0,0,0] op_sel_hi:[1,0,0]\n"
"v_fma_mix_f32 v103, v125, v71, v79 op_sel:[1,0,0] op_sel_hi:[1,0,0]\n"
"v_mfma_f32_16x16x32_bf16 a[108:111], v[64:67], v[236:239], a[108:111]\n"
"buffer_load_dwordx4 v4, s[8:11], s38 offen offset:3072 lds\n"
"v_cvt_pk_bf16_f32 v124, v100, v101\n"
"v_cvt_pk_bf16_f32 v125, v102, v103\n"
"v_mfma_f32_16x16x32_bf16 a[112:115], v[32:35], v[224:227], a[112:115]\n"
"ds_read_b64_tr_b16 v[64:65], v22 offset:776\n"
"v_fma_mix_f32 v104, v126, v72, v80 op_sel:[0,0,0] op_sel_hi:[1,0,0]\n"
"v_mfma_f32_16x16x32_bf16 a[116:119], v[32:35], v[228:231], a[116:119]\n"
"ds_read_b64_tr_b16 v[66:67], v22 offset:4872\n"
"v_fma_mix_f32 v105, v126, v73, v81 op_sel:[1,0,0] op_sel_hi:[1,0,0]\n"
"v_mfma_f32_16x16x32_bf16 a[120:123], v[32:35], v[232:235], a[120:123]\n"
"ds_read_b128 v[96:99], v108 offset:3520\n"
"v_fma_mix_f32 v106, v127, v74, v82 op_sel:[0,0,0] op_sel_hi:[1,0,0]\n"
"v_fma_mix_f32 v107, v127, v75, v83 op_sel:[1,0,0] op_sel_hi:[1,0,0]\n"
"v_mfma_f32_16x16x32_bf16 a[124:127], v[32:35], v[236:239], a[124:127]\n"
"v_cvt_pk_bf16_f32 v126, v104, v105\n"
"v_cvt_pk_bf16_f32 v127, v106, v107\n"
"s_waitcnt lgkmcnt(0)\n"
"v_mfma_f32_16x16x32_bf16 a[0:3], v[36:39], v[112:115], a[0:3]\n"
"ds_read_b64_tr_b16 v[32:33], v22 offset:8192\n"
"v_fma_mix_f32 v100, v128, v84, v92 op_sel:[0,0,0] op_sel_hi:[1,0,0]\n"
"v_mfma_f32_16x16x32_bf16 a[4:7], v[36:39], v[116:119], a[4:7]\n"
"ds_read_b64_tr_b16 v[34:35], v22 offset:12288\n"
"v_fma_mix_f32 v101, v128, v85, v93 op_sel:[1,0,0] op_sel_hi:[1,0,0]\n"
"v_mfma_f32_16x16x32_bf16 a[8:11], v[36:39], v[120:123], a[8:11]\n"
"v_fma_mix_f32 v102, v129, v86, v94 op_sel:[0,0,0] op_sel_hi:[1,0,0]\n"
"v_fma_mix_f32 v103, v129, v87, v95 op_sel:[1,0,0] op_sel_hi:[1,0,0]\n"
"v_mfma_f32_16x16x32_bf16 a[12:15], v[36:39], v[124:127], a[12:15]\n"
"s_mov_b32 s39, 14336\n"
"buffer_load_dwordx4 v[208:211], v7, s[20:23], s39 offen\n"
"v_cvt_pk_bf16_f32 v128, v100, v101\n"
"v_cvt_pk_bf16_f32 v129, v102, v103\n"
"v_mfma_f32_16x16x32_bf16 a[16:19], v[40:43], v[112:115], a[16:19]\n"
"ds_read_b64_tr_b16 v[36:37], v22 offset:8200\n"
"v_fma_mix_f32 v104, v130, v88, v96 op_sel:[0,0,0] op_sel_hi:[1,0,0]\n"
"v_mfma_f32_16x16x32_bf16 a[20:23], v[40:43], v[116:119], a[20:23]\n"
"ds_read_b64_tr_b16 v[38:39], v22 offset:12296\n"
"v_fma_mix_f32 v105, v130, v89, v97 op_sel:[1,0,0] op_sel_hi:[1,0,0]\n"
"v_mfma_f32_16x16x32_bf16 a[24:27], v[40:43], v[120:123], a[24:27]\n"
"ds_read_b128 v[68:71], v108 offset:1280\n"
"v_fma_mix_f32 v106, v131, v90, v98 op_sel:[0,0,0] op_sel_hi:[1,0,0]\n"
"v_fma_mix_f32 v107, v131, v91, v99 op_sel:[1,0,0] op_sel_hi:[1,0,0]\n"
"v_mfma_f32_16x16x32_bf16 a[28:31], v[40:43], v[124:127], a[28:31]\n"
"buffer_load_dwordx4 v[224:227], v7, s[20:23], s39 offen offset:1024\n"
"v_cvt_pk_bf16_f32 v130, v104, v105\n"
"v_cvt_pk_bf16_f32 v131, v106, v107\n"
"v_mfma_f32_16x16x32_bf16 a[32:35], v[44:47], v[112:115], a[32:35]\n"
"ds_read_b64_tr_b16 v[40:41], v22 offset:8448\n"
"v_fma_mix_f32 v100, v132, v84, v92 op_sel:[0,0,0] op_sel_hi:[1,0,0]\n"
"v_mfma_f32_16x16x32_bf16 a[36:39], v[44:47], v[116:119], a[36:39]\n"
"ds_read_b64_tr_b16 v[42:43], v22 offset:12544\n"
"v_fma_mix_f32 v101, v132, v85, v93 op_sel:[1,0,0] op_sel_hi:[1,0,0]\n"
"v_mfma_f32_16x16x32_bf16 a[40:43], v[44:47], v[120:123], a[40:43]\n"
"v_fma_mix_f32 v102, v133, v86, v94 op_sel:[0,0,0] op_sel_hi:[1,0,0]\n"
"v_fma_mix_f32 v103, v133, v87, v95 op_sel:[1,0,0] op_sel_hi:[1,0,0]\n"
"v_mfma_f32_16x16x32_bf16 a[44:47], v[44:47], v[124:127], a[44:47]\n"
"s_mov_b32 s39, 32768\n"
"buffer_load_dwordx4 v[212:215], v7, s[20:23], s39 offen\n"
"v_cvt_pk_bf16_f32 v132, v100, v101\n"
"v_cvt_pk_bf16_f32 v133, v102, v103\n"
"v_mfma_f32_16x16x32_bf16 a[48:51], v[48:51], v[112:115], a[48:51]\n"
"ds_read_b64_tr_b16 v[44:45], v22 offset:8456\n"
"v_fma_mix_f32 v104, v134, v88, v96 op_sel:[0,0,0] op_sel_hi:[1,0,0]\n"
"v_mfma_f32_16x16x32_bf16 a[52:55], v[48:51], v[116:119], a[52:55]\n"
"ds_read_b64_tr_b16 v[46:47], v22 offset:12552\n"
"v_fma_mix_f32 v105, v134, v89, v97 op_sel:[1,0,0] op_sel_hi:[1,0,0]\n"
"v_mfma_f32_16x16x32_bf16 a[56:59], v[48:51], v[120:123], a[56:59]\n"
"ds_read_b128 v[72:75], v108 offset:1344\n"
"v_fma_mix_f32 v106, v135, v90, v98 op_sel:[0,0,0] op_sel_hi:[1,0,0]\n"
"v_fma_mix_f32 v107, v135, v91, v99 op_sel:[1,0,0] op_sel_hi:[1,0,0]\n"
"v_mfma_f32_16x16x32_bf16 a[60:63], v[48:51], v[124:127], a[60:63]\n"
"v_cvt_pk_bf16_f32 v134, v104, v105\n"
"v_cvt_pk_bf16_f32 v135, v106, v107\n"
"v_mfma_f32_16x16x32_bf16 a[64:67], v[52:55], v[112:115], a[64:67]\n"
"ds_read_b64_tr_b16 v[48:49], v22 offset:8704\n"
"v_fma_mix_f32 v100, v136, v84, v92 op_sel:[0,0,0] op_sel_hi:[1,0,0]\n"
"v_mfma_f32_16x16x32_bf16 a[68:71], v[52:55], v[116:119], a[68:71]\n"
"ds_read_b64_tr_b16 v[50:51], v22 offset:12800\n"
"v_fma_mix_f32 v101, v136, v85, v93 op_sel:[1,0,0] op_sel_hi:[1,0,0]\n"
"v_mfma_f32_16x16x32_bf16 a[72:75], v[52:55], v[120:123], a[72:75]\n"
"v_fma_mix_f32 v102, v137, v86, v94 op_sel:[0,0,0] op_sel_hi:[1,0,0]\n"
"v_fma_mix_f32 v103, v137, v87, v95 op_sel:[1,0,0] op_sel_hi:[1,0,0]\n"
"v_mfma_f32_16x16x32_bf16 a[76:79], v[52:55], v[124:127], a[76:79]\n"
"buffer_load_dwordx4 v[228:231], v7, s[20:23], s39 offen offset:1024\n"
"v_cvt_pk_bf16_f32 v136, v100, v101\n"
"v_cvt_pk_bf16_f32 v137, v102, v103\n"
"v_mfma_f32_16x16x32_bf16 a[80:83], v[56:59], v[112:115], a[80:83]\n"
"ds_read_b64_tr_b16 v[52:53], v22 offset:8712\n"
"v_fma_mix_f32 v104, v138, v88, v96 op_sel:[0,0,0] op_sel_hi:[1,0,0]\n"
"v_mfma_f32_16x16x32_bf16 a[84:87], v[56:59], v[116:119], a[84:87]\n"
"ds_read_b64_tr_b16 v[54:55], v22 offset:12808\n"
"v_fma_mix_f32 v105, v138, v89, v97 op_sel:[1,0,0] op_sel_hi:[1,0,0]\n"
"v_mfma_f32_16x16x32_bf16 a[88:91], v[56:59], v[120:123], a[88:91]\n"
"ds_read_b128 v[76:79], v108 offset:3584\n"
"v_fma_mix_f32 v106, v139, v90, v98 op_sel:[0,0,0] op_sel_hi:[1,0,0]\n"
"v_fma_mix_f32 v107, v139, v91, v99 op_sel:[1,0,0] op_sel_hi:[1,0,0]\n"
"v_mfma_f32_16x16x32_bf16 a[92:95], v[56:59], v[124:127], a[92:95]\n"
"s_mov_b32 s39, 51200\n"
"buffer_load_dwordx4 v[216:219], v7, s[20:23], s39 offen\n"
"v_cvt_pk_bf16_f32 v138, v104, v105\n"
"v_cvt_pk_bf16_f32 v139, v106, v107\n"
"v_mfma_f32_16x16x32_bf16 a[96:99], v[60:63], v[112:115], a[96:99]\n"
"ds_read_b64_tr_b16 v[56:57], v22 offset:8960\n"
"v_fma_mix_f32 v100, v140, v84, v92 op_sel:[0,0,0] op_sel_hi:[1,0,0]\n"
"v_mfma_f32_16x16x32_bf16 a[100:103], v[60:63], v[116:119], a[100:103]\n"
"ds_read_b64_tr_b16 v[58:59], v22 offset:13056\n"
"v_fma_mix_f32 v101, v140, v85, v93 op_sel:[1,0,0] op_sel_hi:[1,0,0]\n"
"v_mfma_f32_16x16x32_bf16 a[104:107], v[60:63], v[120:123], a[104:107]\n"
"v_fma_mix_f32 v102, v141, v86, v94 op_sel:[0,0,0] op_sel_hi:[1,0,0]\n"
"v_fma_mix_f32 v103, v141, v87, v95 op_sel:[1,0,0] op_sel_hi:[1,0,0]\n"
"v_mfma_f32_16x16x32_bf16 a[108:111], v[60:63], v[124:127], a[108:111]\n"
"buffer_load_dwordx4 v[232:235], v7, s[20:23], s39 offen offset:1024\n"
"v_cvt_pk_bf16_f32 v140, v100, v101\n"
"v_cvt_pk_bf16_f32 v141, v102, v103\n"
"v_mfma_f32_16x16x32_bf16 a[112:115], v[64:67], v[112:115], a[112:115]\n"
"ds_read_b64_tr_b16 v[60:61], v22 offset:8968\n"
"v_fma_mix_f32 v104, v142, v88, v96 op_sel:[0,0,0] op_sel_hi:[1,0,0]\n"
"v_mfma_f32_16x16x32_bf16 a[116:119], v[64:67], v[116:119], a[116:119]\n"
"ds_read_b64_tr_b16 v[62:63], v22 offset:13064\n"
"v_fma_mix_f32 v105, v142, v89, v97 op_sel:[1,0,0] op_sel_hi:[1,0,0]\n"
"v_mfma_f32_16x16x32_bf16 a[120:123], v[64:67], v[120:123], a[120:123]\n"
"ds_read_b128 v[80:83], v108 offset:3648\n"
"v_fma_mix_f32 v106, v143, v90, v98 op_sel:[0,0,0] op_sel_hi:[1,0,0]\n"
"v_fma_mix_f32 v107, v143, v91, v99 op_sel:[1,0,0] op_sel_hi:[1,0,0]\n"
"v_mfma_f32_16x16x32_bf16 a[124:127], v[64:67], v[124:127], a[124:127]\n"
"v_cvt_pk_bf16_f32 v142, v104, v105\n"
"v_cvt_pk_bf16_f32 v143, v106, v107\n"
"s_waitcnt vmcnt(26)\n"
"s_waitcnt lgkmcnt(0)\n"
"v_add_u32_e32 v23, 98304, v17\n"
"s_barrier\n"
"v_mfma_f32_16x16x32_bf16 a[0:3], v[32:35], v[128:131], a[0:3]\n"
"ds_read_b64_tr_b16 v[64:65], v23 offset:0\n"
"v_fma_mix_f32 v100, v144, v68, v76 op_sel:[0,0,0] op_sel_hi:[1,0,0]\n"
"v_mfma_f32_16x16x32_bf16 a[4:7], v[32:35], v[132:135], a[4:7]\n"
"ds_read_b64_tr_b16 v[66:67], v23 offset:4096\n"
"v_fma_mix_f32 v101, v144, v69, v77 op_sel:[1,0,0] op_sel_hi:[1,0,0]\n"
"v_mfma_f32_16x16x32_bf16 a[8:11], v[32:35], v[136:139], a[8:11]\n"
"v_fma_mix_f32 v102, v145, v70, v78 op_sel:[0,0,0] op_sel_hi:[1,0,0]\n"
"v_fma_mix_f32 v103, v145, v71, v79 op_sel:[1,0,0] op_sel_hi:[1,0,0]\n"
"v_mfma_f32_16x16x32_bf16 a[12:15], v[32:35], v[140:143], a[12:15]\n"
"s_mov_b32 s39, 69632\n"
"buffer_load_dwordx4 v[220:223], v7, s[20:23], s39 offen\n"
"v_cvt_pk_bf16_f32 v144, v100, v101\n"
"v_cvt_pk_bf16_f32 v145, v102, v103\n"
"v_mfma_f32_16x16x32_bf16 a[16:19], v[36:39], v[128:131], a[16:19]\n"
"ds_read_b64_tr_b16 v[32:33], v23 offset:8\n"
"v_fma_mix_f32 v104, v146, v72, v80 op_sel:[0,0,0] op_sel_hi:[1,0,0]\n"
"v_mfma_f32_16x16x32_bf16 a[20:23], v[36:39], v[132:135], a[20:23]\n"
"ds_read_b64_tr_b16 v[34:35], v23 offset:4104\n"
"v_fma_mix_f32 v105, v146, v73, v81 op_sel:[1,0,0] op_sel_hi:[1,0,0]\n"
"v_mfma_f32_16x16x32_bf16 a[24:27], v[36:39], v[136:139], a[24:27]\n"
"ds_read_b128 v[84:87], v108 offset:1408\n"
"v_fma_mix_f32 v106, v147, v74, v82 op_sel:[0,0,0] op_sel_hi:[1,0,0]\n"
"v_fma_mix_f32 v107, v147, v75, v83 op_sel:[1,0,0] op_sel_hi:[1,0,0]\n"
"v_mfma_f32_16x16x32_bf16 a[28:31], v[36:39], v[140:143], a[28:31]\n"
"buffer_load_dwordx4 v[236:239], v7, s[20:23], s39 offen offset:1024\n"
"v_cvt_pk_bf16_f32 v146, v104, v105\n"
"v_cvt_pk_bf16_f32 v147, v106, v107\n"
"v_mfma_f32_16x16x32_bf16 a[32:35], v[40:43], v[128:131], a[32:35]\n"
"ds_read_b64_tr_b16 v[36:37], v23 offset:256\n"
"v_fma_mix_f32 v100, v148, v68, v76 op_sel:[0,0,0] op_sel_hi:[1,0,0]\n"
"v_mfma_f32_16x16x32_bf16 a[36:39], v[40:43], v[132:135], a[36:39]\n"
"ds_read_b64_tr_b16 v[38:39], v23 offset:4352\n"
"v_fma_mix_f32 v101, v148, v69, v77 op_sel:[1,0,0] op_sel_hi:[1,0,0]\n"
"v_mfma_f32_16x16x32_bf16 a[40:43], v[40:43], v[136:139], a[40:43]\n"
"v_fma_mix_f32 v102, v149, v70, v78 op_sel:[0,0,0] op_sel_hi:[1,0,0]\n"
"v_fma_mix_f32 v103, v149, v71, v79 op_sel:[1,0,0] op_sel_hi:[1,0,0]\n"
"v_mfma_f32_16x16x32_bf16 a[44:47], v[40:43], v[140:143], a[44:47]\n"
"s_add_u32 s38, s45, 0\n"
"s_add_u32 m0, s44, 65536\n"
"s_nop 0\n"
"buffer_load_dwordx4 v5, s[12:15], s38 offen lds\n"
"v_cvt_pk_bf16_f32 v148, v100, v101\n"
"v_cvt_pk_bf16_f32 v149, v102, v103\n"
"v_mfma_f32_16x16x32_bf16 a[48:51], v[44:47], v[128:131], a[48:51]\n"
"ds_read_b64_tr_b16 v[40:41], v23 offset:264\n"
"v_fma_mix_f32 v104, v150, v72, v80 op_sel:[0,0,0] op_sel_hi:[1,0,0]\n"
"v_mfma_f32_16x16x32_bf16 a[52:55], v[44:47], v[132:135], a[52:55]\n"
"ds_read_b64_tr_b16 v[42:43], v23 offset:4360\n"
"v_fma_mix_f32 v105, v150, v73, v81 op_sel:[1,0,0] op_sel_hi:[1,0,0]\n"
"v_mfma_f32_16x16x32_bf16 a[56:59], v[44:47], v[136:139], a[56:59]\n"
"ds_read_b128 v[88:91], v108 offset:1472\n"
"v_fma_mix_f32 v106, v151, v74, v82 op_sel:[0,0,0] op_sel_hi:[1,0,0]\n"
"v_fma_mix_f32 v107, v151, v75, v83 op_sel:[1,0,0] op_sel_hi:[1,0,0]\n"
"v_mfma_f32_16x16x32_bf16 a[60:63], v[44:47], v[140:143], a[60:63]\n"
"v_cvt_pk_bf16_f32 v150, v104, v105\n"
"v_cvt_pk_bf16_f32 v151, v106, v107\n"
"v_mfma_f32_16x16x32_bf16 a[64:67], v[48:51], v[128:131], a[64:67]\n"
"ds_read_b64_tr_b16 v[44:45], v23 offset:512\n"
"v_fma_mix_f32 v100, v152, v68, v76 op_sel:[0,0,0] op_sel_hi:[1,0,0]\n"
"v_mfma_f32_16x16x32_bf16 a[68:71], v[48:51], v[132:135], a[68:71]\n"
"ds_read_b64_tr_b16 v[46:47], v23 offset:4608\n"
"v_fma_mix_f32 v101, v152, v69, v77 op_sel:[1,0,0] op_sel_hi:[1,0,0]\n"
"v_mfma_f32_16x16x32_bf16 a[72:75], v[48:51], v[136:139], a[72:75]\n"
"v_fma_mix_f32 v102, v153, v70, v78 op_sel:[0,0,0] op_sel_hi:[1,0,0]\n"
"v_fma_mix_f32 v103, v153, v71, v79 op_sel:[1,0,0] op_sel_hi:[1,0,0]\n"
"v_mfma_f32_16x16x32_bf16 a[76:79], v[48:51], v[140:143], a[76:79]\n"
"buffer_load_dwordx4 v12, s[12:15], s38 offen offset:1024 lds\n"
"v_cvt_pk_bf16_f32 v152, v100, v101\n"
"v_cvt_pk_bf16_f32 v153, v102, v103\n"
"v_mfma_f32_16x16x32_bf16 a[80:83], v[52:55], v[128:131], a[80:83]\n"
"ds_read_b64_tr_b16 v[48:49], v23 offset:520\n"
"v_fma_mix_f32 v104, v154, v72, v80 op_sel:[0,0,0] op_sel_hi:[1,0,0]\n"
"v_mfma_f32_16x16x32_bf16 a[84:87], v[52:55], v[132:135], a[84:87]\n"
"ds_read_b64_tr_b16 v[50:51], v23 offset:4616\n"
"v_fma_mix_f32 v105, v154, v73, v81 op_sel:[1,0,0] op_sel_hi:[1,0,0]\n"
"v_mfma_f32_16x16x32_bf16 a[88:91], v[52:55], v[136:139], a[88:91]\n"
"ds_read_b128 v[92:95], v108 offset:3712\n"
"v_fma_mix_f32 v106, v155, v74, v82 op_sel:[0,0,0] op_sel_hi:[1,0,0]\n"
"v_fma_mix_f32 v107, v155, v75, v83 op_sel:[1,0,0] op_sel_hi:[1,0,0]\n"
"v_mfma_f32_16x16x32_bf16 a[92:95], v[52:55], v[140:143], a[92:95]\n"
"buffer_load_dwordx4 v5, s[12:15], s38 offen offset:2048 lds\n"
"v_cvt_pk_bf16_f32 v154, v104, v105\n"
"v_cvt_pk_bf16_f32 v155, v106, v107\n"
"v_mfma_f32_16x16x32_bf16 a[96:99], v[56:59], v[128:131], a[96:99]\n"
"ds_read_b64_tr_b16 v[52:53], v23 offset:768\n"
"v_fma_mix_f32 v100, v156, v68, v76 op_sel:[0,0,0] op_sel_hi:[1,0,0]\n"
"v_mfma_f32_16x16x32_bf16 a[100:103], v[56:59], v[132:135], a[100:103]\n"
"ds_read_b64_tr_b16 v[54:55], v23 offset:4864\n"
"v_fma_mix_f32 v101, v156, v69, v77 op_sel:[1,0,0] op_sel_hi:[1,0,0]\n"
"v_mfma_f32_16x16x32_bf16 a[104:107], v[56:59], v[136:139], a[104:107]\n"
"v_fma_mix_f32 v102, v157, v70, v78 op_sel:[0,0,0] op_sel_hi:[1,0,0]\n"
"v_fma_mix_f32 v103, v157, v71, v79 op_sel:[1,0,0] op_sel_hi:[1,0,0]\n"
"v_mfma_f32_16x16x32_bf16 a[108:111], v[56:59], v[140:143], a[108:111]\n"
"buffer_load_dwordx4 v12, s[12:15], s38 offen offset:3072 lds\n"
"v_cvt_pk_bf16_f32 v156, v100, v101\n"
"v_cvt_pk_bf16_f32 v157, v102, v103\n"
"v_mfma_f32_16x16x32_bf16 a[112:115], v[60:63], v[128:131], a[112:115]\n"
"ds_read_b64_tr_b16 v[56:57], v23 offset:776\n"
"v_fma_mix_f32 v104, v158, v72, v80 op_sel:[0,0,0] op_sel_hi:[1,0,0]\n"
"v_mfma_f32_16x16x32_bf16 a[116:119], v[60:63], v[132:135], a[116:119]\n"
"ds_read_b64_tr_b16 v[58:59], v23 offset:4872\n"
"v_fma_mix_f32 v105, v158, v73, v81 op_sel:[1,0,0] op_sel_hi:[1,0,0]\n"
"v_mfma_f32_16x16x32_bf16 a[120:123], v[60:63], v[136:139], a[120:123]\n"
"ds_read_b128 v[96:99], v108 offset:3776\n"
"v_fma_mix_f32 v106, v159, v74, v82 op_sel:[0,0,0] op_sel_hi:[1,0,0]\n"
"v_fma_mix_f32 v107, v159, v75, v83 op_sel:[1,0,0] op_sel_hi:[1,0,0]\n"
"v_mfma_f32_16x16x32_bf16 a[124:127], v[60:63], v[140:143], a[124:127]\n"
"v_cvt_pk_bf16_f32 v158, v104, v105\n"
"v_cvt_pk_bf16_f32 v159, v106, v107\n"
"s_waitcnt lgkmcnt(0)\n"
"v_mfma_f32_16x16x32_bf16 a[0:3], v[64:67], v[144:147], a[0:3]\n"
"ds_read_b64_tr_b16 v[60:61], v23 offset:8192\n"
"v_fma_mix_f32 v100, v160, v84, v92 op_sel:[0,0,0] op_sel_hi:[1,0,0]\n"
"v_mfma_f32_16x16x32_bf16 a[4:7], v[64:67], v[148:151], a[4:7]\n"
"ds_read_b64_tr_b16 v[62:63], v23 offset:12288\n"
"v_fma_mix_f32 v101, v160, v85, v93 op_sel:[1,0,0] op_sel_hi:[1,0,0]\n"
"v_mfma_f32_16x16x32_bf16 a[8:11], v[64:67], v[152:155], a[8:11]\n"
"v_fma_mix_f32 v102, v161, v86, v94 op_sel:[0,0,0] op_sel_hi:[1,0,0]\n"
"v_fma_mix_f32 v103, v161, v87, v95 op_sel:[1,0,0] op_sel_hi:[1,0,0]\n"
"v_mfma_f32_16x16x32_bf16 a[12:15], v[64:67], v[156:159], a[12:15]\n"
"s_mov_b32 s39, 16384\n"
"buffer_load_dwordx4 v[112:115], v7, s[20:23], s39 offen\n"
"v_cvt_pk_bf16_f32 v160, v100, v101\n"
"v_cvt_pk_bf16_f32 v161, v102, v103\n"
"v_mfma_f32_16x16x32_bf16 a[16:19], v[32:35], v[144:147], a[16:19]\n"
"ds_read_b64_tr_b16 v[64:65], v23 offset:8200\n"
"v_fma_mix_f32 v104, v162, v88, v96 op_sel:[0,0,0] op_sel_hi:[1,0,0]\n"
"v_mfma_f32_16x16x32_bf16 a[20:23], v[32:35], v[148:151], a[20:23]\n"
"ds_read_b64_tr_b16 v[66:67], v23 offset:12296\n"
"v_fma_mix_f32 v105, v162, v89, v97 op_sel:[1,0,0] op_sel_hi:[1,0,0]\n"
"v_mfma_f32_16x16x32_bf16 a[24:27], v[32:35], v[152:155], a[24:27]\n"
"ds_read_b128 v[68:71], v108 offset:1536\n"
"v_fma_mix_f32 v106, v163, v90, v98 op_sel:[0,0,0] op_sel_hi:[1,0,0]\n"
"v_fma_mix_f32 v107, v163, v91, v99 op_sel:[1,0,0] op_sel_hi:[1,0,0]\n"
"v_mfma_f32_16x16x32_bf16 a[28:31], v[32:35], v[156:159], a[28:31]\n"
"buffer_load_dwordx4 v[128:131], v7, s[20:23], s39 offen offset:1024\n"
"v_cvt_pk_bf16_f32 v162, v104, v105\n"
"v_cvt_pk_bf16_f32 v163, v106, v107\n"
"v_mfma_f32_16x16x32_bf16 a[32:35], v[36:39], v[144:147], a[32:35]\n"
"ds_read_b64_tr_b16 v[32:33], v23 offset:8448\n"
"v_fma_mix_f32 v100, v164, v84, v92 op_sel:[0,0,0] op_sel_hi:[1,0,0]\n"
"v_mfma_f32_16x16x32_bf16 a[36:39], v[36:39], v[148:151], a[36:39]\n"
"ds_read_b64_tr_b16 v[34:35], v23 offset:12544\n"
"v_fma_mix_f32 v101, v164, v85, v93 op_sel:[1,0,0] op_sel_hi:[1,0,0]\n"
"v_mfma_f32_16x16x32_bf16 a[40:43], v[36:39], v[152:155], a[40:43]\n"
"v_fma_mix_f32 v102, v165, v86, v94 op_sel:[0,0,0] op_sel_hi:[1,0,0]\n"
"v_fma_mix_f32 v103, v165, v87, v95 op_sel:[1,0,0] op_sel_hi:[1,0,0]\n"
"v_mfma_f32_16x16x32_bf16 a[44:47], v[36:39], v[156:159], a[44:47]\n"
"s_mov_b32 s39, 34816\n"
"buffer_load_dwordx4 v[116:119], v7, s[20:23], s39 offen\n"
"v_cvt_pk_bf16_f32 v164, v100, v101\n"
"v_cvt_pk_bf16_f32 v165, v102, v103\n"
"v_mfma_f32_16x16x32_bf16 a[48:51], v[40:43], v[144:147], a[48:51]\n"
"ds_read_b64_tr_b16 v[36:37], v23 offset:8456\n"
"v_fma_mix_f32 v104, v166, v88, v96 op_sel:[0,0,0] op_sel_hi:[1,0,0]\n"
"v_mfma_f32_16x16x32_bf16 a[52:55], v[40:43], v[148:151], a[52:55]\n"
"ds_read_b64_tr_b16 v[38:39], v23 offset:12552\n"
"v_fma_mix_f32 v105, v166, v89, v97 op_sel:[1,0,0] op_sel_hi:[1,0,0]\n"
"v_mfma_f32_16x16x32_bf16 a[56:59], v[40:43], v[152:155], a[56:59]\n"
"ds_read_b128 v[72:75], v108 offset:1600\n"
"v_fma_mix_f32 v106, v167, v90, v98 op_sel:[0,0,0] op_sel_hi:[1,0,0]\n"
"v_fma_mix_f32 v107, v167, v91, v99 op_sel:[1,0,0] op_sel_hi:[1,0,0]\n"
"v_mfma_f32_16x16x32_bf16 a[60:63], v[40:43], v[156:159], a[60:63]\n"
"v_cvt_pk_bf16_f32 v166, v104, v105\n"
"v_cvt_pk_bf16_f32 v167, v106, v107\n"
"v_mfma_f32_16x16x32_bf16 a[64:67], v[44:47], v[144:147], a[64:67]\n"
"ds_read_b64_tr_b16 v[40:41], v23 offset:8704\n"
"v_fma_mix_f32 v100, v168, v84, v92 op_sel:[0,0,0] op_sel_hi:[1,0,0]\n"
"v_mfma_f32_16x16x32_bf16 a[68:71], v[44:47], v[148:151], a[68:71]\n"
"ds_read_b64_tr_b16 v[42:43], v23 offset:12800\n"
"v_fma_mix_f32 v101, v168, v85, v93 op_sel:[1,0,0] op_sel_hi:[1,0,0]\n"
"v_mfma_f32_16x16x32_bf16 a[72:75], v[44:47], v[152:155], a[72:75]\n"
"v_fma_mix_f32 v102, v169, v86, v94 op_sel:[0,0,0] op_sel_hi:[1,0,0]\n"
"v_fma_mix_f32 v103, v169, v87, v95 op_sel:[1,0,0] op_sel_hi:[1,0,0]\n"
"v_mfma_f32_16x16x32_bf16 a[76:79], v[44:47], v[156:159], a[76:79]\n"
"buffer_load_dwordx4 v[132:135], v7, s[20:23], s39 offen offset:1024\n"
"v_cvt_pk_bf16_f32 v168, v100, v101\n"
"v_cvt_pk_bf16_f32 v169, v102, v103\n"
"v_mfma_f32_16x16x32_bf16 a[80:83], v[48:51], v[144:147], a[80:83]\n"
"ds_read_b64_tr_b16 v[44:45], v23 offset:8712\n"
"v_fma_mix_f32 v104, v170, v88, v96 op_sel:[0,0,0] op_sel_hi:[1,0,0]\n"
"v_mfma_f32_16x16x32_bf16 a[84:87], v[48:51], v[148:151], a[84:87]\n"
"ds_read_b64_tr_b16 v[46:47], v23 offset:12808\n"
"v_fma_mix_f32 v105, v170, v89, v97 op_sel:[1,0,0] op_sel_hi:[1,0,0]\n"
"v_mfma_f32_16x16x32_bf16 a[88:91], v[48:51], v[152:155], a[88:91]\n"
"ds_read_b128 v[76:79], v108 offset:3840\n"
"v_fma_mix_f32 v106, v171, v90, v98 op_sel:[0,0,0] op_sel_hi:[1,0,0]\n"
"v_fma_mix_f32 v107, v171, v91, v99 op_sel:[1,0,0] op_sel_hi:[1,0,0]\n"
"v_mfma_f32_16x16x32_bf16 a[92:95], v[48:51], v[156:159], a[92:95]\n"
"s_mov_b32 s39, 53248\n"
"buffer_load_dwordx4 v[120:123], v7, s[20:23], s39 offen\n"
"v_cvt_pk_bf16_f32 v170, v104, v105\n"
"v_cvt_pk_bf16_f32 v171, v106, v107\n"
"v_mfma_f32_16x16x32_bf16 a[96:99], v[52:55], v[144:147], a[96:99]\n"
"ds_read_b64_tr_b16 v[48:49], v23 offset:8960\n"
"v_fma_mix_f32 v100, v172, v84, v92 op_sel:[0,0,0] op_sel_hi:[1,0,0]\n"
"v_mfma_f32_16x16x32_bf16 a[100:103], v[52:55], v[148:151], a[100:103]\n"
"ds_read_b64_tr_b16 v[50:51], v23 offset:13056\n"
"v_fma_mix_f32 v101, v172, v85, v93 op_sel:[1,0,0] op_sel_hi:[1,0,0]\n"
"v_mfma_f32_16x16x32_bf16 a[104:107], v[52:55], v[152:155], a[104:107]\n"
"v_fma_mix_f32 v102, v173, v86, v94 op_sel:[0,0,0] op_sel_hi:[1,0,0]\n"
"v_fma_mix_f32 v103, v173, v87, v95 op_sel:[1,0,0] op_sel_hi:[1,0,0]\n"
"v_mfma_f32_16x16x32_bf16 a[108:111], v[52:55], v[156:159], a[108:111]\n"
"buffer_load_dwordx4 v[136:139], v7, s[20:23], s39 offen offset:1024\n"
"v_cvt_pk_bf16_f32 v172, v100, v101\n"
"v_cvt_pk_bf16_f32 v173, v102, v103\n"
"v_mfma_f32_16x16x32_bf16 a[112:115], v[56:59], v[144:147], a[112:115]\n"
"ds_read_b64_tr_b16 v[52:53], v23 offset:8968\n"
"v_fma_mix_f32 v104, v174, v88, v96 op_sel:[0,0,0] op_sel_hi:[1,0,0]\n"
"v_mfma_f32_16x16x32_bf16 a[116:119], v[56:59], v[148:151], a[116:119]\n"
"ds_read_b64_tr_b16 v[54:55], v23 offset:13064\n"
"v_fma_mix_f32 v105, v174, v89, v97 op_sel:[1,0,0] op_sel_hi:[1,0,0]\n"
"v_mfma_f32_16x16x32_bf16 a[120:123], v[56:59], v[152:155], a[120:123]\n"
"ds_read_b128 v[80:83], v108 offset:3904\n"
"v_fma_mix_f32 v106, v175, v90, v98 op_sel:[0,0,0] op_sel_hi:[1,0,0]\n"
"v_fma_mix_f32 v107, v175, v91, v99 op_sel:[1,0,0] op_sel_hi:[1,0,0]\n"
"v_mfma_f32_16x16x32_bf16 a[124:127], v[56:59], v[156:159], a[124:127]\n"
"v_cvt_pk_bf16_f32 v174, v104, v105\n"
"v_cvt_pk_bf16_f32 v175, v106, v107\n"
"s_waitcnt vmcnt(22)\n"
"s_waitcnt lgkmcnt(0)\n"
"v_add_u32_e32 v22, 49152, v17\n"
"s_barrier\n"
"v_mfma_f32_16x16x32_bf16 a[0:3], v[60:63], v[160:163], a[0:3]\n"
"ds_read_b64_tr_b16 v[56:57], v22 offset:0\n"
"v_fma_mix_f32 v100, v176, v68, v76 op_sel:[0,0,0] op_sel_hi:[1,0,0]\n"
"v_mfma_f32_16x16x32_bf16 a[4:7], v[60:63], v[164:167], a[4:7]\n"
"ds_read_b64_tr_b16 v[58:59], v22 offset:4096\n"
"v_fma_mix_f32 v101, v176, v69, v77 op_sel:[1,0,0] op_sel_hi:[1,0,0]\n"
"v_mfma_f32_16x16x32_bf16 a[8:11], v[60:63], v[168:171], a[8:11]\n"
"v_fma_mix_f32 v102, v177, v70, v78 op_sel:[0,0,0] op_sel_hi:[1,0,0]\n"
"v_fma_mix_f32 v103, v177, v71, v79 op_sel:[1,0,0] op_sel_hi:[1,0,0]\n"
"v_mfma_f32_16x16x32_bf16 a[12:15], v[60:63], v[172:175], a[12:15]\n"
"s_mov_b32 s39, 71680\n"
"buffer_load_dwordx4 v[124:127], v7, s[20:23], s39 offen\n"
"v_cvt_pk_bf16_f32 v176, v100, v101\n"
"v_cvt_pk_bf16_f32 v177, v102, v103\n"
"v_mfma_f32_16x16x32_bf16 a[16:19], v[64:67], v[160:163], a[16:19]\n"
"ds_read_b64_tr_b16 v[60:61], v22 offset:8\n"
"v_fma_mix_f32 v104, v178, v72, v80 op_sel:[0,0,0] op_sel_hi:[1,0,0]\n"
"v_mfma_f32_16x16x32_bf16 a[20:23], v[64:67], v[164:167], a[20:23]\n"
"ds_read_b64_tr_b16 v[62:63], v22 offset:4104\n"
"v_fma_mix_f32 v105, v178, v73, v81 op_sel:[1,0,0] op_sel_hi:[1,0,0]\n"
"v_mfma_f32_16x16x32_bf16 a[24:27], v[64:67], v[168:171], a[24:27]\n"
"ds_read_b128 v[84:87], v108 offset:1664\n"
"v_fma_mix_f32 v106, v179, v74, v82 op_sel:[0,0,0] op_sel_hi:[1,0,0]\n"
"v_fma_mix_f32 v107, v179, v75, v83 op_sel:[1,0,0] op_sel_hi:[1,0,0]\n"
"v_mfma_f32_16x16x32_bf16 a[28:31], v[64:67], v[172:175], a[28:31]\n"
"buffer_load_dwordx4 v[140:143], v7, s[20:23], s39 offen offset:1024\n"
"v_cvt_pk_bf16_f32 v178, v104, v105\n"
"v_cvt_pk_bf16_f32 v179, v106, v107\n"
"v_mfma_f32_16x16x32_bf16 a[32:35], v[32:35], v[160:163], a[32:35]\n"
"ds_read_b64_tr_b16 v[64:65], v22 offset:256\n"
"v_fma_mix_f32 v100, v180, v68, v76 op_sel:[0,0,0] op_sel_hi:[1,0,0]\n"
"v_mfma_f32_16x16x32_bf16 a[36:39], v[32:35], v[164:167], a[36:39]\n"
"ds_read_b64_tr_b16 v[66:67], v22 offset:4352\n"
"v_fma_mix_f32 v101, v180, v69, v77 op_sel:[1,0,0] op_sel_hi:[1,0,0]\n"
"v_mfma_f32_16x16x32_bf16 a[40:43], v[32:35], v[168:171], a[40:43]\n"
"v_fma_mix_f32 v102, v181, v70, v78 op_sel:[0,0,0] op_sel_hi:[1,0,0]\n"
"v_fma_mix_f32 v103, v181, v71, v79 op_sel:[1,0,0] op_sel_hi:[1,0,0]\n"
"v_mfma_f32_16x16x32_bf16 a[44:47], v[32:35], v[172:175], a[44:47]\n"
"s_add_u32 s38, s45, 16384\n"
"s_add_u32 m0, s44, 16384\n"
"s_nop 0\n"
"buffer_load_dwordx4 v1, s[8:11], s38 offen lds\n"
"v_cvt_pk_bf16_f32 v180, v100, v101\n"
"v_cvt_pk_bf16_f32 v181, v102, v103\n"
"v_mfma_f32_16x16x32_bf16 a[48:51], v[36:39], v[160:163], a[48:51]\n"
"ds_read_b64_tr_b16 v[32:33], v22 offset:264\n"
"v_fma_mix_f32 v104, v182, v72, v80 op_sel:[0,0,0] op_sel_hi:[1,0,0]\n"
"v_mfma_f32_16x16x32_bf16 a[52:55], v[36:39], v[164:167], a[52:55]\n"
"ds_read_b64_tr_b16 v[34:35], v22 offset:4360\n"
"v_fma_mix_f32 v105, v182, v73, v81 op_sel:[1,0,0] op_sel_hi:[1,0,0]\n"
"v_mfma_f32_16x16x32_bf16 a[56:59], v[36:39], v[168:171], a[56:59]\n"
"ds_read_b128 v[88:91], v108 offset:1728\n"
"v_fma_mix_f32 v106, v183, v74, v82 op_sel:[0,0,0] op_sel_hi:[1,0,0]\n"
"v_fma_mix_f32 v107, v183, v75, v83 op_sel:[1,0,0] op_sel_hi:[1,0,0]\n"
"v_mfma_f32_16x16x32_bf16 a[60:63], v[36:39], v[172:175], a[60:63]\n"
"v_cvt_pk_bf16_f32 v182, v104, v105\n"
"v_cvt_pk_bf16_f32 v183, v106, v107\n"
"v_mfma_f32_16x16x32_bf16 a[64:67], v[40:43], v[160:163], a[64:67]\n"
"ds_read_b64_tr_b16 v[36:37], v22 offset:512\n"
"v_fma_mix_f32 v100, v184, v68, v76 op_sel:[0,0,0] op_sel_hi:[1,0,0]\n"
"v_mfma_f32_16x16x32_bf16 a[68:71], v[40:43], v[164:167], a[68:71]\n"
"ds_read_b64_tr_b16 v[38:39], v22 offset:4608\n"
"v_fma_mix_f32 v101, v184, v69, v77 op_sel:[1,0,0] op_sel_hi:[1,0,0]\n"
"v_mfma_f32_16x16x32_bf16 a[72:75], v[40:43], v[168:171], a[72:75]\n"
"v_fma_mix_f32 v102, v185, v70, v78 op_sel:[0,0,0] op_sel_hi:[1,0,0]\n"
"v_fma_mix_f32 v103, v185, v71, v79 op_sel:[1,0,0] op_sel_hi:[1,0,0]\n"
"v_mfma_f32_16x16x32_bf16 a[76:79], v[40:43], v[172:175], a[76:79]\n"
"buffer_load_dwordx4 v2, s[8:11], s38 offen offset:1024 lds\n"
"v_cvt_pk_bf16_f32 v184, v100, v101\n"
"v_cvt_pk_bf16_f32 v185, v102, v103\n"
"v_mfma_f32_16x16x32_bf16 a[80:83], v[44:47], v[160:163], a[80:83]\n"
"ds_read_b64_tr_b16 v[40:41], v22 offset:520\n"
"v_fma_mix_f32 v104, v186, v72, v80 op_sel:[0,0,0] op_sel_hi:[1,0,0]\n"
"v_mfma_f32_16x16x32_bf16 a[84:87], v[44:47], v[164:167], a[84:87]\n"
"ds_read_b64_tr_b16 v[42:43], v22 offset:4616\n"
"v_fma_mix_f32 v105, v186, v73, v81 op_sel:[1,0,0] op_sel_hi:[1,0,0]\n"
"v_mfma_f32_16x16x32_bf16 a[88:91], v[44:47], v[168:171], a[88:91]\n"
"ds_read_b128 v[92:95], v108 offset:3968\n"
"v_fma_mix_f32 v106, v187, v74, v82 op_sel:[0,0,0] op_sel_hi:[1,0,0]\n"
"v_fma_mix_f32 v107, v187, v75, v83 op_sel:[1,0,0] op_sel_hi:[1,0,0]\n"
"v_mfma_f32_16x16x32_bf16 a[92:95], v[44:47], v[172:175], a[92:95]\n"
"buffer_load_dwordx4 v3, s[8:11], s38 offen offset:2048 lds\n"
"v_cvt_pk_bf16_f32 v186, v104, v105\n"
"v_cvt_pk_bf16_f32 v187, v106, v107\n"
"v_mfma_f32_16x16x32_bf16 a[96:99], v[48:51], v[160:163], a[96:99]\n"
"ds_read_b64_tr_b16 v[44:45], v22 offset:768\n"
"v_fma_mix_f32 v100, v188, v68, v76 op_sel:[0,0,0] op_sel_hi:[1,0,0]\n"
"v_mfma_f32_16x16x32_bf16 a[100:103], v[48:51], v[164:167], a[100:103]\n"
"ds_read_b64_tr_b16 v[46:47], v22 offset:4864\n"
"v_fma_mix_f32 v101, v188, v69, v77 op_sel:[1,0,0] op_sel_hi:[1,0,0]\n"
"v_mfma_f32_16x16x32_bf16 a[104:107], v[48:51], v[168:171], a[104:107]\n"
"v_fma_mix_f32 v102, v189, v70, v78 op_sel:[0,0,0] op_sel_hi:[1,0,0]\n"
"v_fma_mix_f32 v103, v189, v71, v79 op_sel:[1,0,0] op_sel_hi:[1,0,0]\n"
"v_mfma_f32_16x16x32_bf16 a[108:111], v[48:51], v[172:175], a[108:111]\n"
"buffer_load_dwordx4 v4, s[8:11], s38 offen offset:3072 lds\n"
"v_cvt_pk_bf16_f32 v188, v100, v101\n"
"v_cvt_pk_bf16_f32 v189, v102, v103\n"
"v_mfma_f32_16x16x32_bf16 a[112:115], v[52:55], v[160:163], a[112:115]\n"
"ds_read_b64_tr_b16 v[48:49], v22 offset:776\n"
"v_fma_mix_f32 v104, v190, v72, v80 op_sel:[0,0,0] op_sel_hi:[1,0,0]\n"
"v_mfma_f32_16x16x32_bf16 a[116:119], v[52:55], v[164:167], a[116:119]\n"
"ds_read_b64_tr_b16 v[50:51], v22 offset:4872\n"
"v_fma_mix_f32 v105, v190, v73, v81 op_sel:[1,0,0] op_sel_hi:[1,0,0]\n"
"v_mfma_f32_16x16x32_bf16 a[120:123], v[52:55], v[168:171], a[120:123]\n"
"ds_read_b128 v[96:99], v108 offset:4032\n"
"v_fma_mix_f32 v106, v191, v74, v82 op_sel:[0,0,0] op_sel_hi:[1,0,0]\n"
"v_fma_mix_f32 v107, v191, v75, v83 op_sel:[1,0,0] op_sel_hi:[1,0,0]\n"
"v_mfma_f32_16x16x32_bf16 a[124:127], v[52:55], v[172:175], a[124:127]\n"
"v_cvt_pk_bf16_f32 v190, v104, v105\n"
"v_cvt_pk_bf16_f32 v191, v106, v107\n"
"s_waitcnt lgkmcnt(0)\n"
"v_mfma_f32_16x16x32_bf16 a[0:3], v[56:59], v[176:179], a[0:3]\n"
"ds_read_b64_tr_b16 v[52:53], v22 offset:8192\n"
"v_fma_mix_f32 v100, v192, v84, v92 op_sel:[0,0,0] op_sel_hi:[1,0,0]\n"
"v_mfma_f32_16x16x32_bf16 a[4:7], v[56:59], v[180:183], a[4:7]\n"
"ds_read_b64_tr_b16 v[54:55], v22 offset:12288\n"
"v_fma_mix_f32 v101, v192, v85, v93 op_sel:[1,0,0] op_sel_hi:[1,0,0]\n"
"v_mfma_f32_16x16x32_bf16 a[8:11], v[56:59], v[184:187], a[8:11]\n"
"v_fma_mix_f32 v102, v193, v86, v94 op_sel:[0,0,0] op_sel_hi:[1,0,0]\n"
"v_fma_mix_f32 v103, v193, v87, v95 op_sel:[1,0,0] op_sel_hi:[1,0,0]\n"
"v_mfma_f32_16x16x32_bf16 a[12:15], v[56:59], v[188:191], a[12:15]\n"
"s_add_u32 s38, s45, 16384\n"
"s_add_u32 m0, s44, 81920\n"
"s_nop 0\n"
"buffer_load_dwordx4 v5, s[12:15], s38 offen lds\n"
"v_cvt_pk_bf16_f32 v192, v100, v101\n"
"v_cvt_pk_bf16_f32 v193, v102, v103\n"
"v_mfma_f32_16x16x32_bf16 a[16:19], v[60:63], v[176:179], a[16:19]\n"
"ds_read_b64_tr_b16 v[56:57], v22 offset:8200\n"
"v_fma_mix_f32 v104, v194, v88, v96 op_sel:[0,0,0] op_sel_hi:[1,0,0]\n"
"v_mfma_f32_16x16x32_bf16 a[20:23], v[60:63], v[180:183], a[20:23]\n"
"ds_read_b64_tr_b16 v[58:59], v22 offset:12296\n"
"v_fma_mix_f32 v105, v194, v89, v97 op_sel:[1,0,0] op_sel_hi:[1,0,0]\n"
"v_mfma_f32_16x16x32_bf16 a[24:27], v[60:63], v[184:187], a[24:27]\n"
"ds_read_b128 v[68:71], v108 offset:1792\n"
"v_fma_mix_f32 v106, v195, v90, v98 op_sel:[0,0,0] op_sel_hi:[1,0,0]\n"
"v_fma_mix_f32 v107, v195, v91, v99 op_sel:[1,0,0] op_sel_hi:[1,0,0]\n"
"v_mfma_f32_16x16x32_bf16 a[28:31], v[60:63], v[188:191], a[28:31]\n"
"v_cvt_pk_bf16_f32 v194, v104, v105\n"
"v_cvt_pk_bf16_f32 v195, v106, v107\n"
"v_mfma_f32_16x16x32_bf16 a[32:35], v[64:67], v[176:179], a[32:35]\n"
"ds_read_b64_tr_b16 v[60:61], v22 offset:8448\n"
"v_fma_mix_f32 v100, v196, v84, v92 op_sel:[0,0,0] op_sel_hi:[1,0,0]\n"
"v_mfma_f32_16x16x32_bf16 a[36:39], v[64:67], v[180:183], a[36:39]\n"
"ds_read_b64_tr_b16 v[62:63], v22 offset:12544\n"
"v_fma_mix_f32 v101, v196, v85, v93 op_sel:[1,0,0] op_sel_hi:[1,0,0]\n"
"v_mfma_f32_16x16x32_bf16 a[40:43], v[64:67], v[184:187], a[40:43]\n"
"v_fma_mix_f32 v102, v197, v86, v94 op_sel:[0,0,0] op_sel_hi:[1,0,0]\n"
"v_fma_mix_f32 v103, v197, v87, v95 op_sel:[1,0,0] op_sel_hi:[1,0,0]\n"
"v_mfma_f32_16x16x32_bf16 a[44:47], v[64:67], v[188:191], a[44:47]\n"
"buffer_load_dwordx4 v12, s[12:15], s38 offen offset:1024 lds\n"
"v_cvt_pk_bf16_f32 v196, v100, v101\n"
"v_cvt_pk_bf16_f32 v197, v102, v103\n"
"v_mfma_f32_16x16x32_bf16 a[48:51], v[32:35], v[176:179], a[48:51]\n"
"ds_read_b64_tr_b16 v[64:65], v22 offset:8456\n"
"v_fma_mix_f32 v104, v198, v88, v96 op_sel:[0,0,0] op_sel_hi:[1,0,0]\n"
"v_mfma_f32_16x16x32_bf16 a[52:55], v[32:35], v[180:183], a[52:55]\n"
"ds_read_b64_tr_b16 v[66:67], v22 offset:12552\n"
"v_fma_mix_f32 v105, v198, v89, v97 op_sel:[1,0,0] op_sel_hi:[1,0,0]\n"
"v_mfma_f32_16x16x32_bf16 a[56:59], v[32:35], v[184:187], a[56:59]\n"
"ds_read_b128 v[72:75], v108 offset:1856\n"
"v_fma_mix_f32 v106, v199, v90, v98 op_sel:[0,0,0] op_sel_hi:[1,0,0]\n"
"v_fma_mix_f32 v107, v199, v91, v99 op_sel:[1,0,0] op_sel_hi:[1,0,0]\n"
"v_mfma_f32_16x16x32_bf16 a[60:63], v[32:35], v[188:191], a[60:63]\n"
"v_cvt_pk_bf16_f32 v198, v104, v105\n"
"v_cvt_pk_bf16_f32 v199, v106, v107\n"
"v_mfma_f32_16x16x32_bf16 a[64:67], v[36:39], v[176:179], a[64:67]\n"
"ds_read_b64_tr_b16 v[32:33], v22 offset:8704\n"
"v_fma_mix_f32 v100, v200, v84, v92 op_sel:[0,0,0] op_sel_hi:[1,0,0]\n"
"v_mfma_f32_16x16x32_bf16 a[68:71], v[36:39], v[180:183], a[68:71]\n"
"ds_read_b64_tr_b16 v[34:35], v22 offset:12800\n"
"v_fma_mix_f32 v101, v200, v85, v93 op_sel:[1,0,0] op_sel_hi:[1,0,0]\n"
"v_mfma_f32_16x16x32_bf16 a[72:75], v[36:39], v[184:187], a[72:75]\n"
"v_fma_mix_f32 v102, v201, v86, v94 op_sel:[0,0,0] op_sel_hi:[1,0,0]\n"
"v_fma_mix_f32 v103, v201, v87, v95 op_sel:[1,0,0] op_sel_hi:[1,0,0]\n"
"v_mfma_f32_16x16x32_bf16 a[76:79], v[36:39], v[188:191], a[76:79]\n"
"buffer_load_dwordx4 v5, s[12:15], s38 offen offset:2048 lds\n"
"v_cvt_pk_bf16_f32 v200, v100, v101\n"
"v_cvt_pk_bf16_f32 v201, v102, v103\n"
"v_mfma_f32_16x16x32_bf16 a[80:83], v[40:43], v[176:179], a[80:83]\n"
"ds_read_b64_tr_b16 v[36:37], v22 offset:8712\n"
"v_fma_mix_f32 v104, v202, v88, v96 op_sel:[0,0,0] op_sel_hi:[1,0,0]\n"
"v_mfma_f32_16x16x32_bf16 a[84:87], v[40:43], v[180:183], a[84:87]\n"
"ds_read_b64_tr_b16 v[38:39], v22 offset:12808\n"
"v_fma_mix_f32 v105, v202, v89, v97 op_sel:[1,0,0] op_sel_hi:[1,0,0]\n"
"v_mfma_f32_16x16x32_bf16 a[88:91], v[40:43], v[184:187], a[88:91]\n"
"ds_read_b128 v[76:79], v108 offset:4096\n"
"v_fma_mix_f32 v106, v203, v90, v98 op_sel:[0,0,0] op_sel_hi:[1,0,0]\n"
"v_fma_mix_f32 v107, v203, v91, v99 op_sel:[1,0,0] op_sel_hi:[1,0,0]\n"
"v_mfma_f32_16x16x32_bf16 a[92:95], v[40:43], v[188:191], a[92:95]\n"
"v_cvt_pk_bf16_f32 v202, v104, v105\n"
"v_cvt_pk_bf16_f32 v203, v106, v107\n"
"v_mfma_f32_16x16x32_bf16 a[96:99], v[44:47], v[176:179], a[96:99]\n"
"ds_read_b64_tr_b16 v[40:41], v22 offset:8960\n"
"v_fma_mix_f32 v100, v204, v84, v92 op_sel:[0,0,0] op_sel_hi:[1,0,0]\n"
"v_mfma_f32_16x16x32_bf16 a[100:103], v[44:47], v[180:183], a[100:103]\n"
"ds_read_b64_tr_b16 v[42:43], v22 offset:13056\n"
"v_fma_mix_f32 v101, v204, v85, v93 op_sel:[1,0,0] op_sel_hi:[1,0,0]\n"
"v_mfma_f32_16x16x32_bf16 a[104:107], v[44:47], v[184:187], a[104:107]\n"
"v_fma_mix_f32 v102, v205, v86, v94 op_sel:[0,0,0] op_sel_hi:[1,0,0]\n"
"v_fma_mix_f32 v103, v205, v87, v95 op_sel:[1,0,0] op_sel_hi:[1,0,0]\n"
"v_mfma_f32_16x16x32_bf16 a[108:111], v[44:47], v[188:191], a[108:111]\n"
"buffer_load_dwordx4 v12, s[12:15], s38 offen offset:3072 lds\n"
"v_cvt_pk_bf16_f32 v204, v100, v101\n"
"v_cvt_pk_bf16_f32 v205, v102, v103\n"
"v_mfma_f32_16x16x32_bf16 a[112:115], v[48:51], v[176:179], a[112:115]\n"
"ds_read_b64_tr_b16 v[44:45], v22 offset:8968\n"
"v_fma_mix_f32 v104, v206, v88, v96 op_sel:[0,0,0] op_sel_hi:[1,0,0]\n"
"v_mfma_f32_16x16x32_bf16 a[116:119], v[48:51], v[180:183], a[116:119]\n"
"ds_read_b64_tr_b16 v[46:47], v22 offset:13064\n"
"v_fma_mix_f32 v105, v206, v89, v97 op_sel:[1,0,0] op_sel_hi:[1,0,0]\n"
"v_mfma_f32_16x16x32_bf16 a[120:123], v[48:51], v[184:187], a[120:123]\n"
"ds_read_b128 v[80:83], v108 offset:4160\n"
"v_fma_mix_f32 v106, v207, v90, v98 op_sel:[0,0,0] op_sel_hi:[1,0,0]\n"
"v_fma_mix_f32 v107, v207, v91, v99 op_sel:[1,0,0] op_sel_hi:[1,0,0]\n"
"v_mfma_f32_16x16x32_bf16 a[124:127], v[48:51], v[188:191], a[124:127]\n"
"v_cvt_pk_bf16_f32 v206, v104, v105\n"
"v_cvt_pk_bf16_f32 v207, v106, v107\n"
"s_waitcnt vmcnt(20)\n"
"s_waitcnt lgkmcnt(0)\n"
"v_add_u32_e32 v23, 114688, v17\n"
"s_barrier\n"
"v_mfma_f32_16x16x32_bf16 a[0:3], v[52:55], v[192:195], a[0:3]\n"
"ds_read_b64_tr_b16 v[48:49], v23 offset:0\n"
"v_fma_mix_f32 v100, v208, v68, v76 op_sel:[0,0,0] op_sel_hi:[1,0,0]\n"
"v_mfma_f32_16x16x32_bf16 a[4:7], v[52:55], v[196:199], a[4:7]\n"
"ds_read_b64_tr_b16 v[50:51], v23 offset:4096\n"
"v_fma_mix_f32 v101, v208, v69, v77 op_sel:[1,0,0] op_sel_hi:[1,0,0]\n"
"v_mfma_f32_16x16x32_bf16 a[8:11], v[52:55], v[200:203], a[8:11]\n"
"v_fma_mix_f32 v102, v209, v70, v78 op_sel:[0,0,0] op_sel_hi:[1,0,0]\n"
"v_fma_mix_f32 v103, v209, v71, v79 op_sel:[1,0,0] op_sel_hi:[1,0,0]\n"
"v_mfma_f32_16x16x32_bf16 a[12:15], v[52:55], v[204:207], a[12:15]\n"
"s_add_u32 s38, s45, 32768\n"
"s_add_u32 m0, s44, 32768\n"
"s_nop 0\n"
"buffer_load_dwordx4 v1, s[8:11], s38 offen lds\n"
"v_cvt_pk_bf16_f32 v208, v100, v101\n"
"v_cvt_pk_bf16_f32 v209, v102, v103\n"
"v_mfma_f32_16x16x32_bf16 a[16:19], v[56:59], v[192:195], a[16:19]\n"
"ds_read_b64_tr_b16 v[52:53], v23 offset:8\n"
"v_fma_mix_f32 v104, v210, v72, v80 op_sel:[0,0,0] op_sel_hi:[1,0,0]\n"
"v_mfma_f32_16x16x32_bf16 a[20:23], v[56:59], v[196:199], a[20:23]\n"
"ds_read_b64_tr_b16 v[54:55], v23 offset:4104\n"
"v_fma_mix_f32 v105, v210, v73, v81 op_sel:[1,0,0] op_sel_hi:[1,0,0]\n"
"v_mfma_f32_16x16x32_bf16 a[24:27], v[56:59], v[200:203], a[24:27]\n"
"ds_read_b128 v[84:87], v108 offset:1920\n"
"v_fma_mix_f32 v106, v211, v74, v82 op_sel:[0,0,0] op_sel_hi:[1,0,0]\n"
"v_fma_mix_f32 v107, v211, v75, v83 op_sel:[1,0,0] op_sel_hi:[1,0,0]\n"
"v_mfma_f32_16x16x32_bf16 a[28:31], v[56:59], v[204:207], a[28:31]\n"
"v_cvt_pk_bf16_f32 v210, v104, v105\n"
"v_cvt_pk_bf16_f32 v211, v106, v107\n"
"v_mfma_f32_16x16x32_bf16 a[32:35], v[60:63], v[192:195], a[32:35]\n"
"ds_read_b64_tr_b16 v[56:57], v23 offset:256\n"
"v_fma_mix_f32 v100, v212, v68, v76 op_sel:[0,0,0] op_sel_hi:[1,0,0]\n"
"v_mfma_f32_16x16x32_bf16 a[36:39], v[60:63], v[196:199], a[36:39]\n"
"ds_read_b64_tr_b16 v[58:59], v23 offset:4352\n"
"v_fma_mix_f32 v101, v212, v69, v77 op_sel:[1,0,0] op_sel_hi:[1,0,0]\n"
"v_mfma_f32_16x16x32_bf16 a[40:43], v[60:63], v[200:203], a[40:43]\n"
"v_fma_mix_f32 v102, v213, v70, v78 op_sel:[0,0,0] op_sel_hi:[1,0,0]\n"
"v_fma_mix_f32 v103, v213, v71, v79 op_sel:[1,0,0] op_sel_hi:[1,0,0]\n"
"v_mfma_f32_16x16x32_bf16 a[44:47], v[60:63], v[204:207], a[44:47]\n"
"buffer_load_dwordx4 v2, s[8:11], s38 offen offset:1024 lds\n"
"v_cvt_pk_bf16_f32 v212, v100, v101\n"
"v_cvt_pk_bf16_f32 v213, v102, v103\n"
"v_mfma_f32_16x16x32_bf16 a[48:51], v[64:67], v[192:195], a[48:51]\n"
"ds_read_b64_tr_b16 v[60:61], v23 offset:264\n"
"v_fma_mix_f32 v104, v214, v72, v80 op_sel:[0,0,0] op_sel_hi:[1,0,0]\n"
"v_mfma_f32_16x16x32_bf16 a[52:55], v[64:67], v[196:199], a[52:55]\n"
"ds_read_b64_tr_b16 v[62:63], v23 offset:4360\n"
"v_fma_mix_f32 v105, v214, v73, v81 op_sel:[1,0,0] op_sel_hi:[1,0,0]\n"
"v_mfma_f32_16x16x32_bf16 a[56:59], v[64:67], v[200:203], a[56:59]\n"
"ds_read_b128 v[88:91], v108 offset:1984\n"
"v_fma_mix_f32 v106, v215, v74, v82 op_sel:[0,0,0] op_sel_hi:[1,0,0]\n"
"v_fma_mix_f32 v107, v215, v75, v83 op_sel:[1,0,0] op_sel_hi:[1,0,0]\n"
"v_mfma_f32_16x16x32_bf16 a[60:63], v[64:67], v[204:207], a[60:63]\n"
"v_cvt_pk_bf16_f32 v214, v104, v105\n"
"v_cvt_pk_bf16_f32 v215, v106, v107\n"
"v_mfma_f32_16x16x32_bf16 a[64:67], v[32:35], v[192:195], a[64:67]\n"
"ds_read_b64_tr_b16 v[64:65], v23 offset:512\n"
"v_fma_mix_f32 v100, v216, v68, v76 op_sel:[0,0,0] op_sel_hi:[1,0,0]\n"
"v_mfma_f32_16x16x32_bf16 a[68:71], v[32:35], v[196:199], a[68:71]\n"
"ds_read_b64_tr_b16 v[66:67], v23 offset:4608\n"
"v_fma_mix_f32 v101, v216, v69, v77 op_sel:[1,0,0] op_sel_hi:[1,0,0]\n"
"v_mfma_f32_16x16x32_bf16 a[72:75], v[32:35], v[200:203], a[72:75]\n"
"v_fma_mix_f32 v102, v217, v70, v78 op_sel:[0,0,0] op_sel_hi:[1,0,0]\n"
"v_fma_mix_f32 v103, v217, v71, v79 op_sel:[1,0,0] op_sel_hi:[1,0,0]\n"
"v_mfma_f32_16x16x32_bf16 a[76:79], v[32:35], v[204:207], a[76:79]\n"
"buffer_load_dwordx4 v3, s[8:11], s38 offen offset:2048 lds\n"
"v_cvt_pk_bf16_f32 v216, v100, v101\n"
"v_cvt_pk_bf16_f32 v217, v102, v103\n"
"v_mfma_f32_16x16x32_bf16 a[80:83], v[36:39], v[192:195], a[80:83]\n"
"ds_read_b64_tr_b16 v[32:33], v23 offset:520\n"
"v_fma_mix_f32 v104, v218, v72, v80 op_sel:[0,0,0] op_sel_hi:[1,0,0]\n"
"v_mfma_f32_16x16x32_bf16 a[84:87], v[36:39], v[196:199], a[84:87]\n"
"ds_read_b64_tr_b16 v[34:35], v23 offset:4616\n"
"v_fma_mix_f32 v105, v218, v73, v81 op_sel:[1,0,0] op_sel_hi:[1,0,0]\n"
"v_mfma_f32_16x16x32_bf16 a[88:91], v[36:39], v[200:203], a[88:91]\n"
"ds_read_b128 v[92:95], v108 offset:4224\n"
"v_fma_mix_f32 v106, v219, v74, v82 op_sel:[0,0,0] op_sel_hi:[1,0,0]\n"
"v_fma_mix_f32 v107, v219, v75, v83 op_sel:[1,0,0] op_sel_hi:[1,0,0]\n"
"v_mfma_f32_16x16x32_bf16 a[92:95], v[36:39], v[204:207], a[92:95]\n"
"v_cvt_pk_bf16_f32 v218, v104, v105\n"
"v_cvt_pk_bf16_f32 v219, v106, v107\n"
"v_mfma_f32_16x16x32_bf16 a[96:99], v[40:43], v[192:195], a[96:99]\n"
"ds_read_b64_tr_b16 v[36:37], v23 offset:768\n"
"v_fma_mix_f32 v100, v220, v68, v76 op_sel:[0,0,0] op_sel_hi:[1,0,0]\n"
"v_mfma_f32_16x16x32_bf16 a[100:103], v[40:43], v[196:199], a[100:103]\n"
"ds_read_b64_tr_b16 v[38:39], v23 offset:4864\n"
"v_fma_mix_f32 v101, v220, v69, v77 op_sel:[1,0,0] op_sel_hi:[1,0,0]\n"
"v_mfma_f32_16x16x32_bf16 a[104:107], v[40:43], v[200:203], a[104:107]\n"
"v_fma_mix_f32 v102, v221, v70, v78 op_sel:[0,0,0] op_sel_hi:[1,0,0]\n"
"v_fma_mix_f32 v103, v221, v71, v79 op_sel:[1,0,0] op_sel_hi:[1,0,0]\n"
"v_mfma_f32_16x16x32_bf16 a[108:111], v[40:43], v[204:207], a[108:111]\n"
"buffer_load_dwordx4 v4, s[8:11], s38 offen offset:3072 lds\n"
"v_cvt_pk_bf16_f32 v220, v100, v101\n"
"v_cvt_pk_bf16_f32 v221, v102, v103\n"
"v_mfma_f32_16x16x32_bf16 a[112:115], v[44:47], v[192:195], a[112:115]\n"
"ds_read_b64_tr_b16 v[40:41], v23 offset:776\n"
"v_fma_mix_f32 v104, v222, v72, v80 op_sel:[0,0,0] op_sel_hi:[1,0,0]\n"
"v_mfma_f32_16x16x32_bf16 a[116:119], v[44:47], v[196:199], a[116:119]\n"
"ds_read_b64_tr_b16 v[42:43], v23 offset:4872\n"
"v_fma_mix_f32 v105, v222, v73, v81 op_sel:[1,0,0] op_sel_hi:[1,0,0]\n"
"v_mfma_f32_16x16x32_bf16 a[120:123], v[44:47], v[200:203], a[120:123]\n"
"ds_read_b128 v[96:99], v108 offset:4288\n"
"v_fma_mix_f32 v106, v223, v74, v82 op_sel:[0,0,0] op_sel_hi:[1,0,0]\n"
"v_fma_mix_f32 v107, v223, v75, v83 op_sel:[1,0,0] op_sel_hi:[1,0,0]\n"
"v_mfma_f32_16x16x32_bf16 a[124:127], v[44:47], v[204:207], a[124:127]\n"
"v_cvt_pk_bf16_f32 v222, v104, v105\n"
"v_cvt_pk_bf16_f32 v223, v106, v107\n"
"s_waitcnt lgkmcnt(0)\n"
"v_mfma_f32_16x16x32_bf16 a[0:3], v[48:51], v[208:211], a[0:3]\n"
"ds_read_b64_tr_b16 v[44:45], v23 offset:8192\n"
"v_fma_mix_f32 v100, v224, v84, v92 op_sel:[0,0,0] op_sel_hi:[1,0,0]\n"
"v_mfma_f32_16x16x32_bf16 a[4:7], v[48:51], v[212:215], a[4:7]\n"
"ds_read_b64_tr_b16 v[46:47], v23 offset:12288\n"
"v_fma_mix_f32 v101, v224, v85, v93 op_sel:[1,0,0] op_sel_hi:[1,0,0]\n"
"v_mfma_f32_16x16x32_bf16 a[8:11], v[48:51], v[216:219], a[8:11]\n"
"v_fma_mix_f32 v102, v225, v86, v94 op_sel:[0,0,0] op_sel_hi:[1,0,0]\n"
"v_fma_mix_f32 v103, v225, v87, v95 op_sel:[1,0,0] op_sel_hi:[1,0,0]\n"
"v_mfma_f32_16x16x32_bf16 a[12:15], v[48:51], v[220:223], a[12:15]\n"
"s_add_u32 s38, s45, 32768\n"
"s_add_u32 m0, s44, 98304\n"
"s_nop 0\n"
"buffer_load_dwordx4 v5, s[12:15], s38 offen lds\n"
"v_cvt_pk_bf16_f32 v224, v100, v101\n"
"v_cvt_pk_bf16_f32 v225, v102, v103\n"
"v_mfma_f32_16x16x32_bf16 a[16:19], v[52:55], v[208:211], a[16:19]\n"
"ds_read_b64_tr_b16 v[48:49], v23 offset:8200\n"
"v_fma_mix_f32 v104, v226, v88, v96 op_sel:[0,0,0] op_sel_hi:[1,0,0]\n"
"v_mfma_f32_16x16x32_bf16 a[20:23], v[52:55], v[212:215], a[20:23]\n"
"ds_read_b64_tr_b16 v[50:51], v23 offset:12296\n"
"v_fma_mix_f32 v105, v226, v89, v97 op_sel:[1,0,0] op_sel_hi:[1,0,0]\n"
"v_mfma_f32_16x16x32_bf16 a[24:27], v[52:55], v[216:219], a[24:27]\n"
"ds_read_b128 v[68:71], v108 offset:2048\n"
"v_fma_mix_f32 v106, v227, v90, v98 op_sel:[0,0,0] op_sel_hi:[1,0,0]\n"
"v_fma_mix_f32 v107, v227, v91, v99 op_sel:[1,0,0] op_sel_hi:[1,0,0]\n"
"v_mfma_f32_16x16x32_bf16 a[28:31], v[52:55], v[220:223], a[28:31]\n"
"v_cvt_pk_bf16_f32 v226, v104, v105\n"
"v_cvt_pk_bf16_f32 v227, v106, v107\n"
"v_mfma_f32_16x16x32_bf16 a[32:35], v[56:59], v[208:211], a[32:35]\n"
"ds_read_b64_tr_b16 v[52:53], v23 offset:8448\n"
"v_fma_mix_f32 v100, v228, v84, v92 op_sel:[0,0,0] op_sel_hi:[1,0,0]\n"
"v_mfma_f32_16x16x32_bf16 a[36:39], v[56:59], v[212:215], a[36:39]\n"
"ds_read_b64_tr_b16 v[54:55], v23 offset:12544\n"
"v_fma_mix_f32 v101, v228, v85, v93 op_sel:[1,0,0] op_sel_hi:[1,0,0]\n"
"v_mfma_f32_16x16x32_bf16 a[40:43], v[56:59], v[216:219], a[40:43]\n"
"v_fma_mix_f32 v102, v229, v86, v94 op_sel:[0,0,0] op_sel_hi:[1,0,0]\n"
"v_fma_mix_f32 v103, v229, v87, v95 op_sel:[1,0,0] op_sel_hi:[1,0,0]\n"
"v_mfma_f32_16x16x32_bf16 a[44:47], v[56:59], v[220:223], a[44:47]\n"
"v_cvt_pk_bf16_f32 v228, v100, v101\n"
"v_cvt_pk_bf16_f32 v229, v102, v103\n"
"v_mfma_f32_16x16x32_bf16 a[48:51], v[60:63], v[208:211], a[48:51]\n"
"ds_read_b64_tr_b16 v[56:57], v23 offset:8456\n"
"v_fma_mix_f32 v104, v230, v88, v96 op_sel:[0,0,0] op_sel_hi:[1,0,0]\n"
"v_mfma_f32_16x16x32_bf16 a[52:55], v[60:63], v[212:215], a[52:55]\n"
"ds_read_b64_tr_b16 v[58:59], v23 offset:12552\n"
"v_fma_mix_f32 v105, v230, v89, v97 op_sel:[1,0,0] op_sel_hi:[1,0,0]\n"
"v_mfma_f32_16x16x32_bf16 a[56:59], v[60:63], v[216:219], a[56:59]\n"
"ds_read_b128 v[72:75], v108 offset:2112\n"
"v_fma_mix_f32 v106, v231, v90, v98 op_sel:[0,0,0] op_sel_hi:[1,0,0]\n"
"v_fma_mix_f32 v107, v231, v91, v99 op_sel:[1,0,0] op_sel_hi:[1,0,0]\n"
"v_mfma_f32_16x16x32_bf16 a[60:63], v[60:63], v[220:223], a[60:63]\n"
"v_cvt_pk_bf16_f32 v230, v104, v105\n"
"v_cvt_pk_bf16_f32 v231, v106, v107\n"
"v_mfma_f32_16x16x32_bf16 a[64:67], v[64:67], v[208:211], a[64:67]\n"
"ds_read_b64_tr_b16 v[60:61], v23 offset:8704\n"
"v_fma_mix_f32 v100, v232, v84, v92 op_sel:[0,0,0] op_sel_hi:[1,0,0]\n"
"v_mfma_f32_16x16x32_bf16 a[68:71], v[64:67], v[212:215], a[68:71]\n"
"ds_read_b64_tr_b16 v[62:63], v23 offset:12800\n"
"v_fma_mix_f32 v101, v232, v85, v93 op_sel:[1,0,0] op_sel_hi:[1,0,0]\n"
"v_mfma_f32_16x16x32_bf16 a[72:75], v[64:67], v[216:219], a[72:75]\n"
"v_fma_mix_f32 v102, v233, v86, v94 op_sel:[0,0,0] op_sel_hi:[1,0,0]\n"
"v_fma_mix_f32 v103, v233, v87, v95 op_sel:[1,0,0] op_sel_hi:[1,0,0]\n"
"v_mfma_f32_16x16x32_bf16 a[76:79], v[64:67], v[220:223], a[76:79]\n"
"buffer_load_dwordx4 v12, s[12:15], s38 offen offset:1024 lds\n"
"v_cvt_pk_bf16_f32 v232, v100, v101\n"
"v_cvt_pk_bf16_f32 v233, v102, v103\n"
"v_mfma_f32_16x16x32_bf16 a[80:83], v[32:35], v[208:211], a[80:83]\n"
"ds_read_b64_tr_b16 v[64:65], v23 offset:8712\n"
"v_fma_mix_f32 v104, v234, v88, v96 op_sel:[0,0,0] op_sel_hi:[1,0,0]\n"
"v_mfma_f32_16x16x32_bf16 a[84:87], v[32:35], v[212:215], a[84:87]\n"
"ds_read_b64_tr_b16 v[66:67], v23 offset:12808\n"
"v_fma_mix_f32 v105, v234, v89, v97 op_sel:[1,0,0] op_sel_hi:[1,0,0]\n"
"v_mfma_f32_16x16x32_bf16 a[88:91], v[32:35], v[216:219], a[88:91]\n"
"ds_read_b128 v[76:79], v108 offset:4352\n"
"v_fma_mix_f32 v106, v235, v90, v98 op_sel:[0,0,0] op_sel_hi:[1,0,0]\n"
"v_fma_mix_f32 v107, v235, v91, v99 op_sel:[1,0,0] op_sel_hi:[1,0,0]\n"
"v_mfma_f32_16x16x32_bf16 a[92:95], v[32:35], v[220:223], a[92:95]\n"
"v_cvt_pk_bf16_f32 v234, v104, v105\n"
"v_cvt_pk_bf16_f32 v235, v106, v107\n"
"v_mfma_f32_16x16x32_bf16 a[96:99], v[36:39], v[208:211], a[96:99]\n"
"ds_read_b64_tr_b16 v[32:33], v23 offset:8960\n"
"v_fma_mix_f32 v100, v236, v84, v92 op_sel:[0,0,0] op_sel_hi:[1,0,0]\n"
"v_mfma_f32_16x16x32_bf16 a[100:103], v[36:39], v[212:215], a[100:103]\n"
"ds_read_b64_tr_b16 v[34:35], v23 offset:13056\n"
"v_fma_mix_f32 v101, v236, v85, v93 op_sel:[1,0,0] op_sel_hi:[1,0,0]\n"
"v_mfma_f32_16x16x32_bf16 a[104:107], v[36:39], v[216:219], a[104:107]\n"
"v_fma_mix_f32 v102, v237, v86, v94 op_sel:[0,0,0] op_sel_hi:[1,0,0]\n"
"v_fma_mix_f32 v103, v237, v87, v95 op_sel:[1,0,0] op_sel_hi:[1,0,0]\n"
"v_mfma_f32_16x16x32_bf16 a[108:111], v[36:39], v[220:223], a[108:111]\n"
"v_cvt_pk_bf16_f32 v236, v100, v101\n"
"v_cvt_pk_bf16_f32 v237, v102, v103\n"
"v_mfma_f32_16x16x32_bf16 a[112:115], v[40:43], v[208:211], a[112:115]\n"
"ds_read_b64_tr_b16 v[36:37], v23 offset:8968\n"
"v_fma_mix_f32 v104, v238, v88, v96 op_sel:[0,0,0] op_sel_hi:[1,0,0]\n"
"v_mfma_f32_16x16x32_bf16 a[116:119], v[40:43], v[212:215], a[116:119]\n"
"ds_read_b64_tr_b16 v[38:39], v23 offset:13064\n"
"v_fma_mix_f32 v105, v238, v89, v97 op_sel:[1,0,0] op_sel_hi:[1,0,0]\n"
"v_mfma_f32_16x16x32_bf16 a[120:123], v[40:43], v[216:219], a[120:123]\n"
"ds_read_b128 v[80:83], v108 offset:4416\n"
"v_fma_mix_f32 v106, v239, v90, v98 op_sel:[0,0,0] op_sel_hi:[1,0,0]\n"
"v_fma_mix_f32 v107, v239, v91, v99 op_sel:[1,0,0] op_sel_hi:[1,0,0]\n"
"v_mfma_f32_16x16x32_bf16 a[124:127], v[40:43], v[220:223], a[124:127]\n"
"v_cvt_pk_bf16_f32 v238, v104, v105\n"
"v_cvt_pk_bf16_f32 v239, v106, v107\n"
"s_waitcnt vmcnt(14)\n"
"s_waitcnt lgkmcnt(0)\n"
"v_add_u32_e32 v22, 131072, v17\n"
"s_barrier\n"
"v_mfma_f32_16x16x32_bf16 a[0:3], v[44:47], v[224:227], a[0:3]\n"
"ds_read_b64_tr_b16 v[40:41], v22 offset:0\n"
"v_fma_mix_f32 v100, v112, v68, v76 op_sel:[0,0,0] op_sel_hi:[1,0,0]\n"
"v_mfma_f32_16x16x32_bf16 a[4:7], v[44:47], v[228:231], a[4:7]\n"
"ds_read_b64_tr_b16 v[42:43], v22 offset:4096\n"
"v_fma_mix_f32 v101, v112, v69, v77 op_sel:[1,0,0] op_sel_hi:[1,0,0]\n"
"v_mfma_f32_16x16x32_bf16 a[8:11], v[44:47], v[232:235], a[8:11]\n"
"v_fma_mix_f32 v102, v113, v70, v78 op_sel:[0,0,0] op_sel_hi:[1,0,0]\n"
"v_fma_mix_f32 v103, v113, v71, v79 op_sel:[1,0,0] op_sel_hi:[1,0,0]\n"
"v_mfma_f32_16x16x32_bf16 a[12:15], v[44:47], v[236:239], a[12:15]\n"
"buffer_load_dwordx4 v5, s[12:15], s38 offen offset:2048 lds\n"
"v_cvt_pk_bf16_f32 v112, v100, v101\n"
"v_cvt_pk_bf16_f32 v113, v102, v103\n"
"v_mfma_f32_16x16x32_bf16 a[16:19], v[48:51], v[224:227], a[16:19]\n"
"ds_read_b64_tr_b16 v[44:45], v22 offset:8\n"
"v_fma_mix_f32 v104, v114, v72, v80 op_sel:[0,0,0] op_sel_hi:[1,0,0]\n"
"v_mfma_f32_16x16x32_bf16 a[20:23], v[48:51], v[228:231], a[20:23]\n"
"ds_read_b64_tr_b16 v[46:47], v22 offset:4104\n"
"v_fma_mix_f32 v105, v114, v73, v81 op_sel:[1,0,0] op_sel_hi:[1,0,0]\n"
"v_mfma_f32_16x16x32_bf16 a[24:27], v[48:51], v[232:235], a[24:27]\n"
"ds_read_b128 v[84:87], v108 offset:2176\n"
"v_fma_mix_f32 v106, v115, v74, v82 op_sel:[0,0,0] op_sel_hi:[1,0,0]\n"
"v_fma_mix_f32 v107, v115, v75, v83 op_sel:[1,0,0] op_sel_hi:[1,0,0]\n"
"v_mfma_f32_16x16x32_bf16 a[28:31], v[48:51], v[236:239], a[28:31]\n"
"v_cvt_pk_bf16_f32 v114, v104, v105\n"
"v_cvt_pk_bf16_f32 v115, v106, v107\n"
"v_mfma_f32_16x16x32_bf16 a[32:35], v[52:55], v[224:227], a[32:35]\n"
"ds_read_b64_tr_b16 v[48:49], v22 offset:256\n"
"v_fma_mix_f32 v100, v116, v68, v76 op_sel:[0,0,0] op_sel_hi:[1,0,0]\n"
"v_mfma_f32_16x16x32_bf16 a[36:39], v[52:55], v[228:231], a[36:39]\n"
"ds_read_b64_tr_b16 v[50:51], v22 offset:4352\n"
"v_fma_mix_f32 v101, v116, v69, v77 op_sel:[1,0,0] op_sel_hi:[1,0,0]\n"
"v_mfma_f32_16x16x32_bf16 a[40:43], v[52:55], v[232:235], a[40:43]\n"
"v_fma_mix_f32 v102, v117, v70, v78 op_sel:[0,0,0] op_sel_hi:[1,0,0]\n"
"v_fma_mix_f32 v103, v117, v71, v79 op_sel:[1,0,0] op_sel_hi:[1,0,0]\n"
"v_mfma_f32_16x16x32_bf16 a[44:47], v[52:55], v[236:239], a[44:47]\n"
"v_cvt_pk_bf16_f32 v116, v100, v101\n"
"v_cvt_pk_bf16_f32 v117, v102, v103\n"
"v_mfma_f32_16x16x32_bf16 a[48:51], v[56:59], v[224:227], a[48:51]\n"
"ds_read_b64_tr_b16 v[52:53], v22 offset:264\n"
"v_fma_mix_f32 v104, v118, v72, v80 op_sel:[0,0,0] op_sel_hi:[1,0,0]\n"
"v_mfma_f32_16x16x32_bf16 a[52:55], v[56:59], v[228:231], a[52:55]\n"
"ds_read_b64_tr_b16 v[54:55], v22 offset:4360\n"
"v_fma_mix_f32 v105, v118, v73, v81 op_sel:[1,0,0] op_sel_hi:[1,0,0]\n"
"v_mfma_f32_16x16x32_bf16 a[56:59], v[56:59], v[232:235], a[56:59]\n"
"ds_read_b128 v[88:91], v108 offset:2240\n"
"v_fma_mix_f32 v106, v119, v74, v82 op_sel:[0,0,0] op_sel_hi:[1,0,0]\n"
"v_fma_mix_f32 v107, v119, v75, v83 op_sel:[1,0,0] op_sel_hi:[1,0,0]\n"
"v_mfma_f32_16x16x32_bf16 a[60:63], v[56:59], v[236:239], a[60:63]\n"
"v_cvt_pk_bf16_f32 v118, v104, v105\n"
"v_cvt_pk_bf16_f32 v119, v106, v107\n"
"v_mfma_f32_16x16x32_bf16 a[64:67], v[60:63], v[224:227], a[64:67]\n"
"ds_read_b64_tr_b16 v[56:57], v22 offset:512\n"
"v_fma_mix_f32 v100, v120, v68, v76 op_sel:[0,0,0] op_sel_hi:[1,0,0]\n"
"v_mfma_f32_16x16x32_bf16 a[68:71], v[60:63], v[228:231], a[68:71]\n"
"ds_read_b64_tr_b16 v[58:59], v22 offset:4608\n"
"v_fma_mix_f32 v101, v120, v69, v77 op_sel:[1,0,0] op_sel_hi:[1,0,0]\n"
"v_mfma_f32_16x16x32_bf16 a[72:75], v[60:63], v[232:235], a[72:75]\n"
"v_fma_mix_f32 v102, v121, v70, v78 op_sel:[0,0,0] op_sel_hi:[1,0,0]\n"
"v_fma_mix_f32 v103, v121, v71, v79 op_sel:[1,0,0] op_sel_hi:[1,0,0]\n"
"v_mfma_f32_16x16x32_bf16 a[76:79], v[60:63], v[236:239], a[76:79]\n"
"buffer_load_dwordx4 v12, s[12:15], s38 offen offset:3072 lds\n"
"v_cvt_pk_bf16_f32 v120, v100, v101\n"
"v_cvt_pk_bf16_f32 v121, v102, v103\n"
"v_mfma_f32_16x16x32_bf16 a[80:83], v[64:67], v[224:227], a[80:83]\n"
"ds_read_b64_tr_b16 v[60:61], v22 offset:520\n"
"v_fma_mix_f32 v104, v122, v72, v80 op_sel:[0,0,0] op_sel_hi:[1,0,0]\n"
"v_mfma_f32_16x16x32_bf16 a[84:87], v[64:67], v[228:231], a[84:87]\n"
"ds_read_b64_tr_b16 v[62:63], v22 offset:4616\n"
"v_fma_mix_f32 v105, v122, v73, v81 op_sel:[1,0,0] op_sel_hi:[1,0,0]\n"
"v_mfma_f32_16x16x32_bf16 a[88:91], v[64:67], v[232:235], a[88:91]\n"
"ds_read_b128 v[92:95], v108 offset:4480\n"
"v_fma_mix_f32 v106, v123, v74, v82 op_sel:[0,0,0] op_sel_hi:[1,0,0]\n"
"v_fma_mix_f32 v107, v123, v75, v83 op_sel:[1,0,0] op_sel_hi:[1,0,0]\n"
"v_mfma_f32_16x16x32_bf16 a[92:95], v[64:67], v[236:239], a[92:95]\n"
"v_cvt_pk_bf16_f32 v122, v104, v105\n"
"v_cvt_pk_bf16_f32 v123, v106, v107\n"
"v_mfma_f32_16x16x32_bf16 a[96:99], v[32:35], v[224:227], a[96:99]\n"
"ds_read_b64_tr_b16 v[64:65], v22 offset:768\n"
"v_fma_mix_f32 v100, v124, v68, v76 op_sel:[0,0,0] op_sel_hi:[1,0,0]\n"
"v_mfma_f32_16x16x32_bf16 a[100:103], v[32:35], v[228:231], a[100:103]\n"
"ds_read_b64_tr_b16 v[66:67], v22 offset:4864\n"
"v_fma_mix_f32 v101, v124, v69, v77 op_sel:[1,0,0] op_sel_hi:[1,0,0]\n"
"v_mfma_f32_16x16x32_bf16 a[104:107], v[32:35], v[232:235], a[104:107]\n"
"v_fma_mix_f32 v102, v125, v70, v78 op_sel:[0,0,0] op_sel_hi:[1,0,0]\n"
"v_fma_mix_f32 v103, v125, v71, v79 op_sel:[1,0,0] op_sel_hi:[1,0,0]\n"
"v_mfma_f32_16x16x32_bf16 a[108:111], v[32:35], v[236:239], a[108:111]\n"
"v_cvt_pk_bf16_f32 v124, v100, v101\n"
"v_cvt_pk_bf16_f32 v125, v102, v103\n"
"v_mfma_f32_16x16x32_bf16 a[112:115], v[36:39], v[224:227], a[112:115]\n"
"ds_read_b64_tr_b16 v[32:33], v22 offset:776\n"
"v_fma_mix_f32 v104, v126, v72, v80 op_sel:[0,0,0] op_sel_hi:[1,0,0]\n"
"v_mfma_f32_16x16x32_bf16 a[116:119], v[36:39], v[228:231], a[116:119]\n"
"ds_read_b64_tr_b16 v[34:35], v22 offset:4872\n"
"v_fma_mix_f32 v105, v126, v73, v81 op_sel:[1,0,0] op_sel_hi:[1,0,0]\n"
"v_mfma_f32_16x16x32_bf16 a[120:123], v[36:39], v[232:235], a[120:123]\n"
"ds_read_b128 v[96:99], v108 offset:4544\n"
"v_fma_mix_f32 v106, v127, v74, v82 op_sel:[0,0,0] op_sel_hi:[1,0,0]\n"
"v_fma_mix_f32 v107, v127, v75, v83 op_sel:[1,0,0] op_sel_hi:[1,0,0]\n"
"v_mfma_f32_16x16x32_bf16 a[124:127], v[36:39], v[236:239], a[124:127]\n"
"v_cvt_pk_bf16_f32 v126, v104, v105\n"
"v_cvt_pk_bf16_f32 v127, v106, v107\n"
"s_waitcnt lgkmcnt(0)\n"
"v_mfma_f32_16x16x32_bf16 a[0:3], v[40:43], v[112:115], a[0:3]\n"
"ds_read_b64_tr_b16 v[36:37], v22 offset:8192\n"
"v_fma_mix_f32 v100, v128, v84, v92 op_sel:[0,0,0] op_sel_hi:[1,0,0]\n"
"v_mfma_f32_16x16x32_bf16 a[4:7], v[40:43], v[116:119], a[4:7]\n"
"ds_read_b64_tr_b16 v[38:39], v22 offset:12288\n"
"v_fma_mix_f32 v101, v128, v85, v93 op_sel:[1,0,0] op_sel_hi:[1,0,0]\n"
"v_mfma_f32_16x16x32_bf16 a[8:11], v[40:43], v[120:123], a[8:11]\n"
"v_fma_mix_f32 v102, v129, v86, v94 op_sel:[0,0,0] op_sel_hi:[1,0,0]\n"
"v_fma_mix_f32 v103, v129, v87, v95 op_sel:[1,0,0] op_sel_hi:[1,0,0]\n"
"v_mfma_f32_16x16x32_bf16 a[12:15], v[40:43], v[124:127], a[12:15]\n"
"v_cvt_pk_bf16_f32 v128, v100, v101\n"
"v_cvt_pk_bf16_f32 v129, v102, v103\n"
"v_mfma_f32_16x16x32_bf16 a[16:19], v[44:47], v[112:115], a[16:19]\n"
"ds_read_b64_tr_b16 v[40:41], v22 offset:8200\n"
"v_fma_mix_f32 v104, v130, v88, v96 op_sel:[0,0,0] op_sel_hi:[1,0,0]\n"
"v_mfma_f32_16x16x32_bf16 a[20:23], v[44:47], v[116:119], a[20:23]\n"
"ds_read_b64_tr_b16 v[42:43], v22 offset:12296\n"
"v_fma_mix_f32 v105, v130, v89, v97 op_sel:[1,0,0] op_sel_hi:[1,0,0]\n"
"v_mfma_f32_16x16x32_bf16 a[24:27], v[44:47], v[120:123], a[24:27]\n"
"v_fma_mix_f32 v106, v131, v90, v98 op_sel:[0,0,0] op_sel_hi:[1,0,0]\n"
"v_fma_mix_f32 v107, v131, v91, v99 op_sel:[1,0,0] op_sel_hi:[1,0,0]\n"
"v_mfma_f32_16x16x32_bf16 a[28:31], v[44:47], v[124:127], a[28:31]\n"
"v_cvt_pk_bf16_f32 v130, v104, v105\n"
"v_cvt_pk_bf16_f32 v131, v106, v107\n"
"v_mfma_f32_16x16x32_bf16 a[32:35], v[48:51], v[112:115], a[32:35]\n"
"ds_read_b64_tr_b16 v[44:45], v22 offset:8448\n"
"v_fma_mix_f32 v100, v132, v84, v92 op_sel:[0,0,0] op_sel_hi:[1,0,0]\n"
"v_mfma_f32_16x16x32_bf16 a[36:39], v[48:51], v[116:119], a[36:39]\n"
"ds_read_b64_tr_b16 v[46:47], v22 offset:12544\n"
"v_fma_mix_f32 v101, v132, v85, v93 op_sel:[1,0,0] op_sel_hi:[1,0,0]\n"
"v_mfma_f32_16x16x32_bf16 a[40:43], v[48:51], v[120:123], a[40:43]\n"
"v_fma_mix_f32 v102, v133, v86, v94 op_sel:[0,0,0] op_sel_hi:[1,0,0]\n"
"v_fma_mix_f32 v103, v133, v87, v95 op_sel:[1,0,0] op_sel_hi:[1,0,0]\n"
"v_mfma_f32_16x16x32_bf16 a[44:47], v[48:51], v[124:127], a[44:47]\n"
"v_cvt_pk_bf16_f32 v132, v100, v101\n"
"v_cvt_pk_bf16_f32 v133, v102, v103\n"
"v_mfma_f32_16x16x32_bf16 a[48:51], v[52:55], v[112:115], a[48:51]\n"
"ds_read_b64_tr_b16 v[48:49], v22 offset:8456\n"
"v_fma_mix_f32 v104, v134, v88, v96 op_sel:[0,0,0] op_sel_hi:[1,0,0]\n"
"v_mfma_f32_16x16x32_bf16 a[52:55], v[52:55], v[116:119], a[52:55]\n"
"ds_read_b64_tr_b16 v[50:51], v22 offset:12552\n"
"v_fma_mix_f32 v105, v134, v89, v97 op_sel:[1,0,0] op_sel_hi:[1,0,0]\n"
"v_mfma_f32_16x16x32_bf16 a[56:59], v[52:55], v[120:123], a[56:59]\n"
"v_fma_mix_f32 v106, v135, v90, v98 op_sel:[0,0,0] op_sel_hi:[1,0,0]\n"
"v_fma_mix_f32 v107, v135, v91, v99 op_sel:[1,0,0] op_sel_hi:[1,0,0]\n"
"v_mfma_f32_16x16x32_bf16 a[60:63], v[52:55], v[124:127], a[60:63]\n"
"v_cvt_pk_bf16_f32 v134, v104, v105\n"
"v_cvt_pk_bf16_f32 v135, v106, v107\n"
"v_mfma_f32_16x16x32_bf16 a[64:67], v[56:59], v[112:115], a[64:67]\n"
"ds_read_b64_tr_b16 v[52:53], v22 offset:8704\n"
"v_fma_mix_f32 v100, v136, v84, v92 op_sel:[0,0,0] op_sel_hi:[1,0,0]\n"
"v_mfma_f32_16x16x32_bf16 a[68:71], v[56:59], v[116:119], a[68:71]\n"
"ds_read_b64_tr_b16 v[54:55], v22 offset:12800\n"
"v_fma_mix_f32 v101, v136, v85, v93 op_sel:[1,0,0] op_sel_hi:[1,0,0]\n"
"v_mfma_f32_16x16x32_bf16 a[72:75], v[56:59], v[120:123], a[72:75]\n"
"v_fma_mix_f32 v102, v137, v86, v94 op_sel:[0,0,0] op_sel_hi:[1,0,0]\n"
"v_fma_mix_f32 v103, v137, v87, v95 op_sel:[1,0,0] op_sel_hi:[1,0,0]\n"
"v_mfma_f32_16x16x32_bf16 a[76:79], v[56:59], v[124:127], a[76:79]\n"
"v_cvt_pk_bf16_f32 v136, v100, v101\n"
"v_cvt_pk_bf16_f32 v137, v102, v103\n"
"v_mfma_f32_16x16x32_bf16 a[80:83], v[60:63], v[112:115], a[80:83]\n"
"ds_read_b64_tr_b16 v[56:57], v22 offset:8712\n"
"v_fma_mix_f32 v104, v138, v88, v96 op_sel:[0,0,0] op_sel_hi:[1,0,0]\n"
"v_mfma_f32_16x16x32_bf16 a[84:87], v[60:63], v[116:119], a[84:87]\n"
"ds_read_b64_tr_b16 v[58:59], v22 offset:12808\n"
"v_fma_mix_f32 v105, v138, v89, v97 op_sel:[1,0,0] op_sel_hi:[1,0,0]\n"
"v_mfma_f32_16x16x32_bf16 a[88:91], v[60:63], v[120:123], a[88:91]\n"
"v_fma_mix_f32 v106, v139, v90, v98 op_sel:[0,0,0] op_sel_hi:[1,0,0]\n"
"v_fma_mix_f32 v107, v139, v91, v99 op_sel:[1,0,0] op_sel_hi:[1,0,0]\n"
"v_mfma_f32_16x16x32_bf16 a[92:95], v[60:63], v[124:127], a[92:95]\n"
"v_cvt_pk_bf16_f32 v138, v104, v105\n"
"v_cvt_pk_bf16_f32 v139, v106, v107\n"
"v_mfma_f32_16x16x32_bf16 a[96:99], v[64:67], v[112:115], a[96:99]\n"
"ds_read_b64_tr_b16 v[60:61], v22 offset:8960\n"
"v_fma_mix_f32 v100, v140, v84, v92 op_sel:[0,0,0] op_sel_hi:[1,0,0]\n"
"v_mfma_f32_16x16x32_bf16 a[100:103], v[64:67], v[116:119], a[100:103]\n"
"ds_read_b64_tr_b16 v[62:63], v22 offset:13056\n"
"v_fma_mix_f32 v101, v140, v85, v93 op_sel:[1,0,0] op_sel_hi:[1,0,0]\n"
"v_mfma_f32_16x16x32_bf16 a[104:107], v[64:67], v[120:123], a[104:107]\n"
"v_fma_mix_f32 v102, v141, v86, v94 op_sel:[0,0,0] op_sel_hi:[1,0,0]\n"
"v_fma_mix_f32 v103, v141, v87, v95 op_sel:[1,0,0] op_sel_hi:[1,0,0]\n"
"v_mfma_f32_16x16x32_bf16 a[108:111], v[64:67], v[124:127], a[108:111]\n"
"v_cvt_pk_bf16_f32 v140, v100, v101\n"
"v_cvt_pk_bf16_f32 v141, v102, v103\n"
"v_mfma_f32_16x16x32_bf16 a[112:115], v[32:35], v[112:115], a[112:115]\n"
"ds_read_b64_tr_b16 v[64:65], v22 offset:8968\n"
"v_fma_mix_f32 v104, v142, v88, v96 op_sel:[0,0,0] op_sel_hi:[1,0,0]\n"
"v_mfma_f32_16x16x32_bf16 a[116:119], v[32:35], v[116:119], a[116:119]\n"
"ds_read_b64_tr_b16 v[66:67], v22 offset:13064\n"
"v_fma_mix_f32 v105, v142, v89, v97 op_sel:[1,0,0] op_sel_hi:[1,0,0]\n"
"v_mfma_f32_16x16x32_bf16 a[120:123], v[32:35], v[120:123], a[120:123]\n"
"v_fma_mix_f32 v106, v143, v90, v98 op_sel:[0,0,0] op_sel_hi:[1,0,0]\n"
"v_fma_mix_f32 v107, v143, v91, v99 op_sel:[1,0,0] op_sel_hi:[1,0,0]\n"
"v_mfma_f32_16x16x32_bf16 a[124:127], v[32:35], v[124:127], a[124:127]\n"
"v_cvt_pk_bf16_f32 v142, v104, v105\n"
"v_cvt_pk_bf16_f32 v143, v106, v107\n"
"s_waitcnt lgkmcnt(8)\n"
"v_mfma_f32_16x16x32_bf16 a[0:3], v[36:39], v[128:131], a[0:3]\n"
"v_mfma_f32_16x16x32_bf16 a[4:7], v[36:39], v[132:135], a[4:7]\n"
"v_mfma_f32_16x16x32_bf16 a[8:11], v[36:39], v[136:139], a[8:11]\n"
"v_mfma_f32_16x16x32_bf16 a[12:15], v[36:39], v[140:143], a[12:15]\n"
"v_mfma_f32_16x16x32_bf16 a[16:19], v[40:43], v[128:131], a[16:19]\n"
"v_mfma_f32_16x16x32_bf16 a[20:23], v[40:43], v[132:135], a[20:23]\n"
"v_mfma_f32_16x16x32_bf16 a[24:27], v[40:43], v[136:139], a[24:27]\n"
"v_mfma_f32_16x16x32_bf16 a[28:31], v[40:43], v[140:143], a[28:31]\n"
"v_mfma_f32_16x16x32_bf16 a[32:35], v[44:47], v[128:131], a[32:35]\n"
"v_mfma_f32_16x16x32_bf16 a[36:39], v[44:47], v[132:135], a[36:39]\n"
"v_mfma_f32_16x16x32_bf16 a[40:43], v[44:47], v[136:139], a[40:43]\n"
"v_mfma_f32_16x16x32_bf16 a[44:47], v[44:47], v[140:143], a[44:47]\n"
"v_accvgpr_read_b32 v100, a0\n"
"v_accvgpr_read_b32 v101, a1\n"
"v_accvgpr_read_b32 v102, a2\n"
"v_accvgpr_read_b32 v103, a3\n"
"v_accvgpr_read_b32 v104, a16\n"
"v_mfma_f32_16x16x32_bf16 a[48:51], v[48:51], v[128:131], a[48:51]\n"
"v_accvgpr_read_b32 v105, a17\n"
"v_accvgpr_read_b32 v106, a18\n"
"v_accvgpr_read_b32 v107, a19\n"
"v_cvt_pk_bf16_f32 v180, v100, v101\n"
"v_cvt_pk_bf16_f32 v181, v102, v103\n"
"v_mfma_f32_16x16x32_bf16 a[52:55], v[48:51], v[132:135], a[52:55]\n"
"v_cvt_pk_bf16_f32 v182, v104, v105\n"
"v_cvt_pk_bf16_f32 v183, v106, v107\n"
"v_accvgpr_read_b32 v100, a4\n"
"v_accvgpr_read_b32 v101, a5\n"
"v_accvgpr_read_b32 v102, a6\n"
"v_mfma_f32_16x16x32_bf16 a[56:59], v[48:51], v[136:139], a[56:59]\n"
"v_accvgpr_read_b32 v103, a7\n"
"v_accvgpr_read_b32 v104, a20\n"
"v_accvgpr_read_b32 v105, a21\n"
"v_accvgpr_read_b32 v106, a22\n"
"v_accvgpr_read_b32 v107, a23\n"
"v_mfma_f32_16x16x32_bf16 a[60:63], v[48:51], v[140:143], a[60:63]\n"
"v_cvt_pk_bf16_f32 v196, v100, v101\n"
"v_cvt_pk_bf16_f32 v197, v102, v103\n"
"v_cvt_pk_bf16_f32 v198, v104, v105\n"
"v_cvt_pk_bf16_f32 v199, v106, v107\n"
"v_accvgpr_read_b32 v100, a8\n"
"s_waitcnt lgkmcnt(0)\n"
"v_mfma_f32_16x16x32_bf16 a[64:67], v[52:55], v[128:131], a[64:67]\n"
"v_accvgpr_read_b32 v101, a9\n"
"v_accvgpr_read_b32 v102, a10\n"
"v_accvgpr_read_b32 v103, a11\n"
"v_accvgpr_read_b32 v104, a24\n"
"v_accvgpr_read_b32 v105, a25\n"
"v_mfma_f32_16x16x32_bf16 a[68:71], v[52:55], v[132:135], a[68:71]\n"
"v_accvgpr_read_b32 v106, a26\n"
"v_accvgpr_read_b32 v107, a27\n"
"v_cvt_pk_bf16_f32 v212, v100, v101\n"
"v_cvt_pk_bf16_f32 v213, v102, v103\n"
"v_cvt_pk_bf16_f32 v214, v104, v105\n"
"v_mfma_f32_16x16x32_bf16 a[72:75], v[52:55], v[136:139], a[72:75]\n"
"v_cvt_pk_bf16_f32 v215, v106, v107\n"
"v_accvgpr_read_b32 v100, a12\n"
"v_accvgpr_read_b32 v101, a13\n"
"v_accvgpr_read_b32 v102, a14\n"
"v_accvgpr_read_b32 v103, a15\n"
"v_mfma_f32_16x16x32_bf16 a[76:79], v[52:55], v[140:143], a[76:79]\n"
"v_accvgpr_read_b32 v104, a28\n"
"v_accvgpr_read_b32 v105, a29\n"
"v_accvgpr_read_b32 v106, a30\n"
"v_accvgpr_read_b32 v107, a31\n"
"v_cvt_pk_bf16_f32 v228, v100, v101\n"
"v_mfma_f32_16x16x32_bf16 a[80:83], v[56:59], v[128:131], a[80:83]\n"
"v_cvt_pk_bf16_f32 v229, v102, v103\n"
"v_cvt_pk_bf16_f32 v230, v104, v105\n"
"v_cvt_pk_bf16_f32 v231, v106, v107\n"
"v_accvgpr_read_b32 v100, a32\n"
"v_accvgpr_read_b32 v101, a33\n"
"v_mfma_f32_16x16x32_bf16 a[84:87], v[56:59], v[132:135], a[84:87]\n"
"v_accvgpr_read_b32 v102, a34\n"
"v_accvgpr_read_b32 v103, a35\n"
"v_accvgpr_read_b32 v104, a48\n"
"v_accvgpr_read_b32 v105, a49\n"
"v_accvgpr_read_b32 v106, a50\n"
"v_mfma_f32_16x16x32_bf16 a[88:91], v[56:59], v[136:139], a[88:91]\n"
"v_accvgpr_read_b32 v107, a51\n"
"v_cvt_pk_bf16_f32 v184, v100, v101\n"
"v_cvt_pk_bf16_f32 v185, v102, v103\n"
"v_cvt_pk_bf16_f32 v186, v104, v105\n"
"v_cvt_pk_bf16_f32 v187, v106, v107\n"
"v_mfma_f32_16x16x32_bf16 a[92:95], v[56:59], v[140:143], a[92:95]\n"
"v_accvgpr_read_b32 v100, a36\n"
"v_accvgpr_read_b32 v101, a37\n"
"v_accvgpr_read_b32 v102, a38\n"
"v_accvgpr_read_b32 v103, a39\n"
"v_accvgpr_read_b32 v104, a52\n"
"v_mfma_f32_16x16x32_bf16 a[96:99], v[60:63], v[128:131], a[96:99]\n"
"v_accvgpr_read_b32 v105, a53\n"
"v_accvgpr_read_b32 v106, a54\n"
"v_accvgpr_read_b32 v107, a55\n"
"v_cvt_pk_bf16_f32 v200, v100, v101\n"
"v_cvt_pk_bf16_f32 v201, v102, v103\n"
"v_mfma_f32_16x16x32_bf16 a[100:103], v[60:63], v[132:135], a[100:103]\n"
"v_cvt_pk_bf16_f32 v202, v104, v105\n"
"v_cvt_pk_bf16_f32 v203, v106, v107\n"
"v_accvgpr_read_b32 v100, a40\n"
"v_accvgpr_read_b32 v101, a41\n"
"v_accvgpr_read_b32 v102, a42\n"
"v_mfma_f32_16x16x32_bf16 a[104:107], v[60:63], v[136:139], a[104:107]\n"
"v_accvgpr_read_b32 v103, a43\n"
"v_accvgpr_read_b32 v104, a56\n"
"v_accvgpr_read_b32 v105, a57\n"
"v_accvgpr_read_b32 v106, a58\n"
"v_accvgpr_read_b32 v107, a59\n"
"v_mfma_f32_16x16x32_bf16 a[108:111], v[60:63], v[140:143], a[108:111]\n"
"v_cvt_pk_bf16_f32 v216, v100, v101\n"
"v_cvt_pk_bf16_f32 v217, v102, v103\n"
"v_cvt_pk_bf16_f32 v218, v104, v105\n"
"v_cvt_pk_bf16_f32 v219, v106, v107\n"
"v_accvgpr_read_b32 v100, a44\n"
"v_mfma_f32_16x16x32_bf16 a[112:115], v[64:67], v[128:131], a[112:115]\n"
"v_accvgpr_read_b32 v101, a45\n"
"v_accvgpr_read_b32 v102, a46\n"
"v_accvgpr_read_b32 v103, a47\n"
"v_accvgpr_read_b32 v104, a60\n"
"v_accvgpr_read_b32 v105, a61\n"
"v_mfma_f32_16x16x32_bf16 a[116:119], v[64:67], v[132:135], a[116:119]\n"
"v_accvgpr_read_b32 v106, a62\n"
"v_accvgpr_read_b32 v107, a63\n"
"v_cvt_pk_bf16_f32 v232, v100, v101\n"
"v_cvt_pk_bf16_f32 v233, v102, v103\n"
"v_cvt_pk_bf16_f32 v234, v104, v105\n"
"v_mfma_f32_16x16x32_bf16 a[120:123], v[64:67], v[136:139], a[120:123]\n"
"v_cvt_pk_bf16_f32 v235, v106, v107\n"
"v_accvgpr_read_b32 v100, a64\n"
"v_accvgpr_read_b32 v101, a65\n"
"v_accvgpr_read_b32 v102, a66\n"
"v_accvgpr_read_b32 v103, a67\n"
"v_mfma_f32_16x16x32_bf16 a[124:127], v[64:67], v[140:143], a[124:127]\n"
"v_accvgpr_read_b32 v104, a80\n"
"v_accvgpr_read_b32 v105, a81\n"
"v_accvgpr_read_b32 v106, a82\n"
"v_accvgpr_read_b32 v107, a83\n"
"v_cvt_pk_bf16_f32 v188, v100, v101\n"
"s_barrier\n"
"ds_read_b128 a[128:131], v8 offset:0\n"
"ds_read_b128 a[132:135], v9 offset:0\n"
"ds_read_b128 a[136:139], v10 offset:0\n"
"ds_read_b128 a[140:143], v11 offset:0\n"
"ds_read_b128 a[144:147], v8 offset:4096\n"
"ds_read_b128 a[148:151], v9 offset:4096\n"
"ds_read_b128 a[152:155], v10 offset:4096\n"
"ds_read_b128 a[156:159], v11 offset:4096\n"
"v_cvt_pk_bf16_f32 v189, v102, v103\n"
"v_cvt_pk_bf16_f32 v190, v104, v105\n"
"v_cvt_pk_bf16_f32 v191, v106, v107\n"
"v_accvgpr_read_b32 v100, a68\n"
"v_accvgpr_read_b32 v101, a69\n"
"v_accvgpr_read_b32 v102, a70\n"
"v_accvgpr_read_b32 v103, a71\n"
"v_accvgpr_read_b32 v104, a84\n"
"v_accvgpr_read_b32 v105, a85\n"
"v_accvgpr_read_b32 v106, a86\n"
"v_accvgpr_read_b32 v107, a87\n"
"v_cvt_pk_bf16_f32 v204, v100, v101\n"
"v_cvt_pk_bf16_f32 v205, v102, v103\n"
"v_cvt_pk_bf16_f32 v206, v104, v105\n"
"v_cvt_pk_bf16_f32 v207, v106, v107\n"
"v_accvgpr_read_b32 v100, a72\n"
"v_accvgpr_read_b32 v101, a73\n"
"v_accvgpr_read_b32 v102, a74\n"
"v_accvgpr_read_b32 v103, a75\n"
"v_accvgpr_read_b32 v104, a88\n"
"v_accvgpr_read_b32 v105, a89\n"
"v_accvgpr_read_b32 v106, a90\n"
"v_accvgpr_read_b32 v107, a91\n"
"v_cvt_pk_bf16_f32 v220, v100, v101\n"
"v_cvt_pk_bf16_f32 v221, v102, v103\n"
"v_cvt_pk_bf16_f32 v222, v104, v105\n"
"v_cvt_pk_bf16_f32 v223, v106, v107\n"
"v_accvgpr_read_b32 v100, a76\n"
"v_accvgpr_read_b32 v101, a77\n"
"v_accvgpr_read_b32 v102, a78\n"
"v_accvgpr_read_b32 v103, a79\n"
"v_accvgpr_read_b32 v104, a92\n"
"v_accvgpr_read_b32 v105, a93\n"
"v_accvgpr_read_b32 v106, a94\n"
"v_accvgpr_read_b32 v107, a95\n"
"v_cvt_pk_bf16_f32 v236, v100, v101\n"
"v_cvt_pk_bf16_f32 v237, v102, v103\n"
"v_cvt_pk_bf16_f32 v238, v104, v105\n"
"v_cvt_pk_bf16_f32 v239, v106, v107\n"
"v_accvgpr_read_b32 v100, a96\n"
"v_accvgpr_read_b32 v101, a97\n"
"v_accvgpr_read_b32 v102, a98\n"
"v_accvgpr_read_b32 v103, a99\n"
"v_accvgpr_read_b32 v104, a112\n"
"v_accvgpr_read_b32 v105, a113\n"
"v_accvgpr_read_b32 v106, a114\n"
"v_accvgpr_read_b32 v107, a115\n"
"v_cvt_pk_bf16_f32 v192, v100, v101\n"
"v_cvt_pk_bf16_f32 v193, v102, v103\n"
"v_cvt_pk_bf16_f32 v194, v104, v105\n"
"v_cvt_pk_bf16_f32 v195, v106, v107\n"
"v_accvgpr_read_b32 v100, a100\n"
"v_accvgpr_read_b32 v101, a101\n"
"v_accvgpr_read_b32 v102, a102\n"
"v_accvgpr_read_b32 v103, a103\n"
"v_accvgpr_read_b32 v104, a116\n"
"v_accvgpr_read_b32 v105, a117\n"
"v_accvgpr_read_b32 v106, a118\n"
"v_accvgpr_read_b32 v107, a119\n"
"v_cvt_pk_bf16_f32 v208, v100, v101\n"
"v_cvt_pk_bf16_f32 v209, v102, v103\n"
"v_cvt_pk_bf16_f32 v210, v104, v105\n"
"v_cvt_pk_bf16_f32 v211, v106, v107\n"
"v_accvgpr_read_b32 v100, a104\n"
"v_accvgpr_read_b32 v101, a105\n"
"v_accvgpr_read_b32 v102, a106\n"
"v_accvgpr_read_b32 v103, a107\n"
"v_accvgpr_read_b32 v104, a120\n"
"v_accvgpr_read_b32 v105, a121\n"
"v_accvgpr_read_b32 v106, a122\n"
"v_accvgpr_read_b32 v107, a123\n"
"v_cvt_pk_bf16_f32 v224, v100, v101\n"
"v_cvt_pk_bf16_f32 v225, v102, v103\n"
"v_cvt_pk_bf16_f32 v226, v104, v105\n"
"v_cvt_pk_bf16_f32 v227, v106, v107\n"
"v_accvgpr_read_b32 v100, a108\n"
"v_accvgpr_read_b32 v101, a109\n"
"v_accvgpr_read_b32 v102, a110\n"
"v_accvgpr_read_b32 v103, a111\n"
"v_accvgpr_read_b32 v104, a124\n"
"v_accvgpr_read_b32 v105, a125\n"
"v_accvgpr_read_b32 v106, a126\n"
"v_accvgpr_read_b32 v107, a127\n"
"v_cvt_pk_bf16_f32 v240, v100, v101\n"
"v_cvt_pk_bf16_f32 v241, v102, v103\n"
"v_cvt_pk_bf16_f32 v242, v104, v105\n"
"v_cvt_pk_bf16_f32 v243, v106, v107\n"
"s_waitcnt lgkmcnt(0)\n"
"v_mfma_f32_16x16x32_bf16 v[48:51], a[128:131], v[180:183], 0\n"
"ds_read_b64_tr_b16 v[116:117], v16 offset:0\n"
"v_mov_b32_e32 v112, 0x3f803f80\n"
"v_mov_b32_e32 v113, 0x3f803f80\n"
"v_mov_b32_e32 v114, 0x3f803f80\n"
"v_mfma_f32_16x16x32_bf16 v[48:51], a[132:135], v[184:187], v[48:51]\n"
"v_mov_b32_e32 v115, 0x3f803f80\n"
"v_mfma_f32_16x16x32_bf16 v[48:51], a[136:139], v[188:191], v[48:51]\n"
"ds_read_b64_tr_b16 v[118:119], v16 offset:4096\n"
"v_mfma_f32_16x16x32_bf16 v[48:51], a[140:143], v[192:195], v[48:51]\n"
"v_mfma_f32_16x16x32_bf16 v[52:55], a[144:147], v[180:183], 0\n"
"ds_read_b64_tr_b16 v[120:121], v13 offset:0\n"
"v_mfma_f32_16x16x32_bf16 v[52:55], a[148:151], v[184:187], v[52:55]\n"
"v_mfma_f32_16x16x32_bf16 v[52:55], a[152:155], v[188:191], v[52:55]\n"
"ds_read_b64_tr_b16 v[122:123], v13 offset:4096\n"
"v_mfma_f32_16x16x32_bf16 v[52:55], a[156:159], v[192:195], v[52:55]\n"
"v_mfma_f32_16x16x32_bf16 v[56:59], a[128:131], v[196:199], 0\n"
"ds_read_b64_tr_b16 v[124:125], v16 offset:256\n"
"v_mfma_f32_16x16x32_bf16 v[56:59], a[132:135], v[200:203], v[56:59]\n"
"v_mfma_f32_16x16x32_bf16 v[56:59], a[136:139], v[204:207], v[56:59]\n"
"ds_read_b64_tr_b16 v[126:127], v16 offset:4352\n"
"v_max3_f32 v24, v48, v49, v50\n"
"s_nop 1\n"
"v_max3_f32 v24, v24, v51, v52\n"
"v_max3_f32 v24, v24, v53, v54\n"
"v_mfma_f32_16x16x32_bf16 v[56:59], a[140:143], v[208:211], v[56:59]\n"
"v_max_f32_e32 v24, v24, v55\n"
"v_mov_b32_e32 v26, v24\n"
"s_nop 1\n"
"v_permlane16_swap_b32_e32 v24, v26\n"
"v_mfma_f32_16x16x32_bf16 v[60:63], a[144:147], v[196:199], 0\n"
"ds_read_b64_tr_b16 v[128:129], v13 offset:256\n"
"v_max_f32_e32 v24, v24, v26\n"
"v_mov_b32_e32 v26, v24\n"
"s_nop 1\n"
"v_permlane32_swap_b32_e32 v24, v26\n"
"v_mfma_f32_16x16x32_bf16 v[60:63], a[148:151], v[200:203], v[60:63]\n"
"v_max_f32_e32 v24, v24, v26\n"
"v_add_f32_e32 v24, 0x42700000, v24\n"
"v_sub_f32_e32 v32, 0, v24\n"
"v_mfma_f32_16x16x32_bf16 v[60:63], a[152:155], v[204:207], v[60:63]\n"
"ds_read_b64_tr_b16 v[130:131], v13 offset:4352\n"
"v_sub_f32_e32 v33, 0, v24\n"
"v_sub_f32_e32 v34, 0, v24\n"
"v_sub_f32_e32 v35, 0, v24\n"
"v_mfma_f32_16x16x32_bf16 v[60:63], a[156:159], v[208:211], v[60:63]\n"
"v_sub_f32_e32 v48, v48, v24\n"
"v_sub_f32_e32 v49, v49, v24\n"
"v_sub_f32_e32 v50, v50, v24\n"
"v_mfma_f32_16x16x32_bf16 v[64:67], a[128:131], v[212:215], 0\n"
"ds_read_b64_tr_b16 v[132:133], v16 offset:512\n"
"v_sub_f32_e32 v51, v51, v24\n"
"v_sub_f32_e32 v52, v52, v24\n"
"v_sub_f32_e32 v53, v53, v24\n"
"v_mfma_f32_16x16x32_bf16 v[64:67], a[132:135], v[216:219], v[64:67]\n"
"v_sub_f32_e32 v54, v54, v24\n"
"v_sub_f32_e32 v55, v55, v24\n"
"v_mfma_f32_16x16x32_bf16 v[64:67], a[136:139], v[220:223], v[64:67]\n"
"ds_read_b64_tr_b16 v[134:135], v16 offset:4608\n"
"v_max3_f32 v25, v56, v57, v58\n"
"v_max3_f32 v25, v25, v59, v60\n"
"v_max3_f32 v25, v25, v61, v62\n"
"v_mfma_f32_16x16x32_bf16 v[64:67], a[140:143], v[224:227], v[64:67]\n"
"v_max_f32_e32 v25, v25, v63\n"
"v_mov_b32_e32 v27, v25\n"
"s_nop 1\n"
"v_permlane16_swap_b32_e32 v25, v27\n"
"v_mfma_f32_16x16x32_bf16 v[68:71], a[144:147], v[212:215], 0\n"
"ds_read_b64_tr_b16 v[136:137], v13 offset:512\n"
"v_max_f32_e32 v25, v25, v27\n"
"v_mov_b32_e32 v27, v25\n"
"s_nop 1\n"
"v_permlane32_swap_b32_e32 v25, v27\n"
"v_mfma_f32_16x16x32_bf16 v[68:71], a[148:151], v[216:219], v[68:71]\n"
"v_max_f32_e32 v25, v25, v27\n"
"v_add_f32_e32 v25, 0x42700000, v25\n"
"v_sub_f32_e32 v36, 0, v25\n"
"v_mfma_f32_16x16x32_bf16 v[68:71], a[152:155], v[220:223], v[68:71]\n"
"ds_read_b64_tr_b16 v[138:139], v13 offset:4608\n"
"v_sub_f32_e32 v37, 0, v25\n"
"v_sub_f32_e32 v38, 0, v25\n"
"v_sub_f32_e32 v39, 0, v25\n"
"v_mfma_f32_16x16x32_bf16 v[68:71], a[156:159], v[224:227], v[68:71]\n"
"v_sub_f32_e32 v56, v56, v25\n"
"v_sub_f32_e32 v57, v57, v25\n"
"v_sub_f32_e32 v58, v58, v25\n"
"v_mfma_f32_16x16x32_bf16 v[72:75], a[128:131], v[228:231], 0\n"
"ds_read_b64_tr_b16 v[140:141], v16 offset:768\n"
"v_sub_f32_e32 v59, v59, v25\n"
"v_sub_f32_e32 v60, v60, v25\n"
"v_sub_f32_e32 v61, v61, v25\n"
"v_mfma_f32_16x16x32_bf16 v[72:75], a[132:135], v[232:235], v[72:75]\n"
"v_sub_f32_e32 v62, v62, v25\n"
"v_sub_f32_e32 v63, v63, v25\n"
"v_mfma_f32_16x16x32_bf16 v[72:75], a[136:139], v[236:239], v[72:75]\n"
"ds_read_b64_tr_b16 v[142:143], v16 offset:4864\n"
"v_max3_f32 v24, v64, v65, v66\n"
"v_max3_f32 v24, v24, v67, v68\n"
"v_max3_f32 v24, v24, v69, v70\n"
"v_mfma_f32_16x16x32_bf16 v[72:75], a[140:143], v[240:243], v[72:75]\n"
"v_max_f32_e32 v24, v24, v71\n"
"v_mov_b32_e32 v26, v24\n"
"s_nop 1\n"
"v_permlane16_swap_b32_e32 v24, v26\n"
"v_mfma_f32_16x16x32_bf16 v[76:79], a[144:147], v[228:231], 0\n"
"ds_read_b64_tr_b16 v[144:145], v13 offset:768\n"
"v_max_f32_e32 v24, v24, v26\n"
"v_mov_b32_e32 v26, v24\n"
"s_nop 1\n"
"v_permlane32_swap_b32_e32 v24, v26\n"
"v_mfma_f32_16x16x32_bf16 v[76:79], a[148:151], v[232:235], v[76:79]\n"
"v_max_f32_e32 v24, v24, v26\n"
"v_add_f32_e32 v24, 0x42700000, v24\n"
"v_sub_f32_e32 v40, 0, v24\n"
"v_mfma_f32_16x16x32_bf16 v[76:79], a[152:155], v[236:239], v[76:79]\n"
"ds_read_b64_tr_b16 v[146:147], v13 offset:4864\n"
"v_sub_f32_e32 v41, 0, v24\n"
"v_sub_f32_e32 v42, 0, v24\n"
"v_sub_f32_e32 v43, 0, v24\n"
"v_mfma_f32_16x16x32_bf16 v[76:79], a[156:159], v[240:243], v[76:79]\n"
"v_sub_f32_e32 v64, v64, v24\n"
"v_sub_f32_e32 v65, v65, v24\n"
"v_sub_f32_e32 v66, v66, v24\n"
"v_sub_f32_e32 v67, v67, v24\n"
"v_sub_f32_e32 v68, v68, v24\n"
"v_sub_f32_e32 v69, v69, v24\n"
"v_sub_f32_e32 v70, v70, v24\n"
"v_sub_f32_e32 v71, v71, v24\n"
"v_max3_f32 v25, v72, v73, v74\n"
"v_max3_f32 v25, v25, v75, v76\n"
"v_max3_f32 v25, v25, v77, v78\n"
"v_max_f32_e32 v25, v25, v79\n"
"v_mov_b32_e32 v27, v25\n"
"s_nop 1\n"
"v_permlane16_swap_b32_e32 v25, v27\n"
"v_max_f32_e32 v25, v25, v27\n"
"v_mov_b32_e32 v27, v25\n"
"s_nop 1\n"
"v_permlane32_swap_b32_e32 v25, v27\n"
"v_max_f32_e32 v25, v25, v27\n"
"v_add_f32_e32 v25, 0x42700000, v25\n"
"v_sub_f32_e32 v44, 0, v25\n"
"v_sub_f32_e32 v45, 0, v25\n"
"v_sub_f32_e32 v46, 0, v25\n"
"v_sub_f32_e32 v47, 0, v25\n"
"v_sub_f32_e32 v72, v72, v25\n"
"v_sub_f32_e32 v73, v73, v25\n"
"v_sub_f32_e32 v74, v74, v25\n"
"v_sub_f32_e32 v75, v75, v25\n"
"v_sub_f32_e32 v76, v76, v25\n"
"v_sub_f32_e32 v77, v77, v25\n"
"v_sub_f32_e32 v78, v78, v25\n"
"v_sub_f32_e32 v79, v79, v25\n"
"v_exp_f32_e32 v48, v48\n"
"v_exp_f32_e32 v49, v49\n"
"ds_read_b128 a[160:163], v8 offset:8192\n"
"v_cvt_pk_bf16_f32 v80, v48, v49\n"
"v_exp_f32_e32 v50, v50\n"
"ds_read_b128 a[164:167], v9 offset:8192\n"
"v_exp_f32_e32 v51, v51\n"
"s_nop 0\n"
"v_cvt_pk_bf16_f32 v81, v50, v51\n"
"v_exp_f32_e32 v52, v52\n"
"ds_read_b128 a[168:171], v10 offset:8192\n"
"v_exp_f32_e32 v53, v53\n"
"ds_read_b64_tr_b16 v[148:149], v16 offset:8192\n"
"v_cvt_pk_bf16_f32 v82, v52, v53\n"
"v_exp_f32_e32 v54, v54\n"
"buffer_load_dwordx4 a[192:195], v18, s[24:27], s41 offen\n"
"s_add_u32 s41, s41, s47\n"
"v_exp_f32_e32 v55, v55\n"
"ds_read_b128 a[172:175], v11 offset:8192\n"
"v_cvt_pk_bf16_f32 v83, v54, v55\n"
"ds_read_b64_tr_b16 v[150:151], v16 offset:12288\n"
"v_exp_f32_e32 v56, v56\n"
"s_waitcnt lgkmcnt(6)\n"
"v_mfma_f32_16x16x32_bf16 a[0:3], v[116:119], v[80:83], 0\n"
"v_mfma_f32_16x16x32_bf16 a[16:19], v[120:123], v[80:83], 0\n"
"v_exp_f32_e32 v57, v57\n"
"v_mfma_f32_16x16x32_bf16 a[32:35], v[124:127], v[80:83], 0\n"
"ds_read_b128 a[176:179], v8 offset:12288\n"
"v_cvt_pk_bf16_f32 v84, v56, v57\n"
"v_mfma_f32_16x16x32_bf16 a[48:51], v[128:131], v[80:83], 0\n"
"v_exp_f32_e32 v58, v58\n"
"v_mfma_f32_16x16x32_bf16 a[64:67], v[132:135], v[80:83], 0\n"
"ds_read_b64_tr_b16 v[152:153], v13 offset:8192\n"
"v_mfma_f32_16x16x32_bf16 a[80:83], v[136:139], v[80:83], 0\n"
"v_exp_f32_e32 v59, v59\n"
"v_mfma_f32_16x16x32_bf16 a[96:99], v[140:143], v[80:83], 0\n"
"v_cvt_pk_bf16_f32 v85, v58, v59\n"
"v_mfma_f32_16x16x32_bf16 a[112:115], v[144:147], v[80:83], 0\n"
"v_exp_f32_e32 v60, v60\n"
"v_mfma_f32_16x16x32_bf16 v[96:99], v[112:115], v[80:83], 0\n"
"ds_read_b128 a[180:183], v9 offset:12288\n"
"v_exp_f32_e32 v61, v61\n"
"ds_read_b64_tr_b16 v[154:155], v13 offset:12288\n"
"v_cvt_pk_bf16_f32 v86, v60, v61\n"
"v_exp_f32_e32 v62, v62\n"
"v_exp_f32_e32 v63, v63\n"
"ds_read_b128 a[184:187], v10 offset:12288\n"
"v_cvt_pk_bf16_f32 v87, v62, v63\n"
"ds_read_b64_tr_b16 v[156:157], v16 offset:8448\n"
"v_exp_f32_e32 v64, v64\n"
"v_mfma_f32_16x16x32_bf16 a[4:7], v[116:119], v[84:87], 0\n"
"v_mfma_f32_16x16x32_bf16 a[20:23], v[120:123], v[84:87], 0\n"
"v_exp_f32_e32 v65, v65\n"
"v_mfma_f32_16x16x32_bf16 a[36:39], v[124:127], v[84:87], 0\n"
"ds_read_b128 a[188:191], v11 offset:12288\n"
"v_cvt_pk_bf16_f32 v88, v64, v65\n"
"v_mfma_f32_16x16x32_bf16 a[52:55], v[128:131], v[84:87], 0\n"
"v_exp_f32_e32 v66, v66\n"
"v_mfma_f32_16x16x32_bf16 a[68:71], v[132:135], v[84:87], 0\n"
"ds_read_b64_tr_b16 v[158:159], v16 offset:12544\n"
"v_mfma_f32_16x16x32_bf16 a[84:87], v[136:139], v[84:87], 0\n"
"v_exp_f32_e32 v67, v67\n"
"v_mfma_f32_16x16x32_bf16 a[100:103], v[140:143], v[84:87], 0\n"
"v_cvt_pk_bf16_f32 v89, v66, v67\n"
"v_mfma_f32_16x16x32_bf16 a[116:119], v[144:147], v[84:87], 0\n"
"v_exp_f32_e32 v68, v68\n"
"v_mfma_f32_16x16x32_bf16 v[100:103], v[112:115], v[84:87], 0\n"
"ds_read_b64_tr_b16 v[160:161], v13 offset:8448\n"
"v_exp_f32_e32 v69, v69\n"
"ds_read_b64_tr_b16 v[162:163], v13 offset:12544\n"
"v_cvt_pk_bf16_f32 v90, v68, v69\n"
"v_exp_f32_e32 v70, v70\n"
"v_exp_f32_e32 v71, v71\n"
"ds_read_b64_tr_b16 v[164:165], v16 offset:8704\n"
"v_cvt_pk_bf16_f32 v91, v70, v71\n"
"s_waitcnt lgkmcnt(4)\n"
"ds_read_b64_tr_b16 v[166:167], v16 offset:12800\n"
"v_exp_f32_e32 v72, v72\n"
"v_mfma_f32_16x16x32_bf16 a[8:11], v[116:119], v[88:91], 0\n"
"v_mfma_f32_16x16x32_bf16 a[24:27], v[120:123], v[88:91], 0\n"
"v_exp_f32_e32 v73, v73\n"
"v_mfma_f32_16x16x32_bf16 a[40:43], v[124:127], v[88:91], 0\n"
"ds_read_b64_tr_b16 v[168:169], v13 offset:8704\n"
"v_cvt_pk_bf16_f32 v92, v72, v73\n"
"v_mfma_f32_16x16x32_bf16 a[56:59], v[128:131], v[88:91], 0\n"
"v_exp_f32_e32 v74, v74\n"
"v_mfma_f32_16x16x32_bf16 a[72:75], v[132:135], v[88:91], 0\n"
"ds_read_b64_tr_b16 v[170:171], v13 offset:12800\n"
"v_mfma_f32_16x16x32_bf16 a[88:91], v[136:139], v[88:91], 0\n"
"v_exp_f32_e32 v75, v75\n"
"v_mfma_f32_16x16x32_bf16 a[104:107], v[140:143], v[88:91], 0\n"
"v_cvt_pk_bf16_f32 v93, v74, v75\n"
"v_mfma_f32_16x16x32_bf16 a[120:123], v[144:147], v[88:91], 0\n"
"v_exp_f32_e32 v76, v76\n"
"v_mfma_f32_16x16x32_bf16 v[104:107], v[112:115], v[88:91], 0\n"
"ds_read_b64_tr_b16 v[172:173], v16 offset:8960\n"
"v_mfma_f32_16x16x32_bf16 v[48:51], a[160:163], v[180:183], v[32:35]\n"
"v_exp_f32_e32 v77, v77\n"
"v_mfma_f32_16x16x32_bf16 v[48:51], a[164:167], v[184:187], v[48:51]\n"
"ds_read_b64_tr_b16 v[174:175], v16 offset:13056\n"
"v_cvt_pk_bf16_f32 v94, v76, v77\n"
"v_mfma_f32_16x16x32_bf16 v[48:51], a[168:171], v[188:191], v[48:51]\n"
"v_exp_f32_e32 v78, v78\n"
"v_mfma_f32_16x16x32_bf16 v[48:51], a[172:175], v[192:195], v[48:51]\n"
"s_waitcnt vmcnt(9)\n"
"s_barrier\n"
"v_mfma_f32_16x16x32_bf16 v[52:55], a[176:179], v[180:183], v[32:35]\n"
"v_exp_f32_e32 v79, v79\n"
"v_mfma_f32_16x16x32_bf16 v[52:55], a[180:183], v[184:187], v[52:55]\n"
"ds_read_b64_tr_b16 v[176:177], v13 offset:8960\n"
"v_cvt_pk_bf16_f32 v95, v78, v79\n"
"v_mfma_f32_16x16x32_bf16 v[52:55], a[184:187], v[188:191], v[52:55]\n"
"ds_read_b64_tr_b16 v[178:179], v13 offset:13056\n"
"s_add_u32 s38, s38, 16384\n"
"v_mfma_f32_16x16x32_bf16 v[52:55], a[188:191], v[192:195], v[52:55]\n"
"v_exp_f32_e32 v48, v48\n"
"v_mfma_f32_16x16x32_bf16 a[12:15], v[116:119], v[92:95], 0\n"
"s_add_u32 m0, s44, 49152\n"
"v_mfma_f32_16x16x32_bf16 a[28:31], v[120:123], v[92:95], 0\n"
"v_exp_f32_e32 v49, v49\n"
"v_mfma_f32_16x16x32_bf16 a[44:47], v[124:127], v[92:95], 0\n"
"ds_read_b128 a[128:131], v8 offset:16384\n"
"v_cvt_pk_bf16_f32 v80, v48, v49\n"
"v_mfma_f32_16x16x32_bf16 a[60:63], v[128:131], v[92:95], 0\n"
"v_exp_f32_e32 v50, v50\n"
"v_mfma_f32_16x16x32_bf16 a[76:79], v[132:135], v[92:95], 0\n"
"ds_read_b128 a[132:135], v9 offset:16384\n"
"v_mfma_f32_16x16x32_bf16 a[92:95], v[136:139], v[92:95], 0\n"
"v_exp_f32_e32 v51, v51\n"
"v_mfma_f32_16x16x32_bf16 a[108:111], v[140:143], v[92:95], 0\n"
"buffer_load_dwordx4 v1, s[8:11], s38 offen lds\n"
"v_mfma_f32_16x16x32_bf16 a[124:127], v[144:147], v[92:95], 0\n"
"v_exp_f32_e32 v52, v52\n"
"v_mfma_f32_16x16x32_bf16 v[108:111], v[112:115], v[92:95], 0\n"
"ds_read_b128 a[136:139], v10 offset:16384\n"
"v_cvt_pk_bf16_f32 v81, v50, v51\n"
"v_mfma_f32_16x16x32_bf16 v[56:59], a[160:163], v[196:199], v[36:39]\n"
"v_exp_f32_e32 v53, v53\n"
"v_mfma_f32_16x16x32_bf16 v[56:59], a[164:167], v[200:203], v[56:59]\n"
"ds_read_b64_tr_b16 v[116:117], v16 offset:16384\n"
"v_cvt_pk_bf16_f32 v82, v52, v53\n"
"v_mfma_f32_16x16x32_bf16 v[56:59], a[168:171], v[204:207], v[56:59]\n"
"v_exp_f32_e32 v54, v54\n"
"v_mfma_f32_16x16x32_bf16 v[56:59], a[172:175], v[208:211], v[56:59]\n"
"buffer_load_dwordx4 a[196:199], v18, s[24:27], s41 offen\n"
"v_mfma_f32_16x16x32_bf16 v[60:63], a[176:179], v[196:199], v[36:39]\n"
"v_exp_f32_e32 v55, v55\n"
"v_mfma_f32_16x16x32_bf16 v[60:63], a[180:183], v[200:203], v[60:63]\n"
"ds_read_b128 a[140:143], v11 offset:16384\n"
"v_cvt_pk_bf16_f32 v83, v54, v55\n"
"v_mfma_f32_16x16x32_bf16 v[60:63], a[184:187], v[204:207], v[60:63]\n"
"ds_read_b64_tr_b16 v[118:119], v16 offset:20480\n"
"s_waitcnt lgkmcnt(6)\n"
"v_mfma_f32_16x16x32_bf16 v[60:63], a[188:191], v[208:211], v[60:63]\n"
"v_exp_f32_e32 v56, v56\n"
"v_mfma_f32_16x16x32_bf16 a[0:3], v[148:151], v[80:83], a[0:3]\n"
"buffer_load_dwordx4 v2, s[8:11], s38 offen offset:1024 lds\n"
"v_mfma_f32_16x16x32_bf16 a[16:19], v[152:155], v[80:83], a[16:19]\n"
"v_exp_f32_e32 v57, v57\n"
"v_mfma_f32_16x16x32_bf16 a[32:35], v[156:159], v[80:83], a[32:35]\n"
"ds_read_b128 a[144:147], v8 offset:20480\n"
"v_cvt_pk_bf16_f32 v84, v56, v57\n"
"v_mfma_f32_16x16x32_bf16 a[48:51], v[160:163], v[80:83], a[48:51]\n"
"v_exp_f32_e32 v58, v58\n"
"v_mfma_f32_16x16x32_bf16 a[64:67], v[164:167], v[80:83], a[64:67]\n"
"ds_read_b64_tr_b16 v[120:121], v13 offset:16384\n"
"s_add_u32 s41, s41, s47\n"
"v_mfma_f32_16x16x32_bf16 a[80:83], v[168:171], v[80:83], a[80:83]\n"
"v_exp_f32_e32 v59, v59\n"
"v_mfma_f32_16x16x32_bf16 a[96:99], v[172:175], v[80:83], a[96:99]\n"
"v_cvt_pk_bf16_f32 v85, v58, v59\n"
"v_mfma_f32_16x16x32_bf16 a[112:115], v[176:179], v[80:83], a[112:115]\n"
"v_exp_f32_e32 v60, v60\n"
"v_mfma_f32_16x16x32_bf16 v[96:99], v[112:115], v[80:83], v[96:99]\n"
"ds_read_b128 a[148:151], v9 offset:20480\n"
"v_mfma_f32_16x16x32_bf16 v[64:67], a[160:163], v[212:215], v[40:43]\n"
"v_exp_f32_e32 v61, v61\n"
"v_mfma_f32_16x16x32_bf16 v[64:67], a[164:167], v[216:219], v[64:67]\n"
"ds_read_b64_tr_b16 v[122:123], v13 offset:20480\n"
"v_cvt_pk_bf16_f32 v86, v60, v61\n"
"v_mfma_f32_16x16x32_bf16 v[64:67], a[168:171], v[220:223], v[64:67]\n"
"v_exp_f32_e32 v62, v62\n"
"v_mfma_f32_16x16x32_bf16 v[64:67], a[172:175], v[224:227], v[64:67]\n"
"v_mfma_f32_16x16x32_bf16 v[68:71], a[176:179], v[212:215], v[40:43]\n"
"v_exp_f32_e32 v63, v63\n"
"v_mfma_f32_16x16x32_bf16 v[68:71], a[180:183], v[216:219], v[68:71]\n"
"ds_read_b128 a[152:155], v10 offset:20480\n"
"v_cvt_pk_bf16_f32 v87, v62, v63\n"
"v_mfma_f32_16x16x32_bf16 v[68:71], a[184:187], v[220:223], v[68:71]\n"
"ds_read_b64_tr_b16 v[124:125], v16 offset:16640\n"
"v_mfma_f32_16x16x32_bf16 v[68:71], a[188:191], v[224:227], v[68:71]\n"
"v_exp_f32_e32 v64, v64\n"
"v_mfma_f32_16x16x32_bf16 a[4:7], v[148:151], v[84:87], a[4:7]\n"
"buffer_load_dwordx4 v3, s[8:11], s38 offen offset:2048 lds\n"
"v_mfma_f32_16x16x32_bf16 a[20:23], v[152:155], v[84:87], a[20:23]\n"
"v_exp_f32_e32 v65, v65\n"
"v_mfma_f32_16x16x32_bf16 a[36:39], v[156:159], v[84:87], a[36:39]\n"
"ds_read_b128 a[156:159], v11 offset:20480\n"
"v_cvt_pk_bf16_f32 v88, v64, v65\n"
"v_mfma_f32_16x16x32_bf16 a[52:55], v[160:163], v[84:87], a[52:55]\n"
"v_exp_f32_e32 v66, v66\n"
"v_mfma_f32_16x16x32_bf16 a[68:71], v[164:167], v[84:87], a[68:71]\n"
"ds_read_b64_tr_b16 v[126:127], v16 offset:20736\n"
"v_mfma_f32_16x16x32_bf16 a[84:87], v[168:171], v[84:87], a[84:87]\n"
"v_exp_f32_e32 v67, v67\n"
"v_mfma_f32_16x16x32_bf16 a[100:103], v[172:175], v[84:87], a[100:103]\n"
"v_cvt_pk_bf16_f32 v89, v66, v67\n"
"v_mfma_f32_16x16x32_bf16 a[116:119], v[176:179], v[84:87], a[116:119]\n"
"v_exp_f32_e32 v68, v68\n"
"v_mfma_f32_16x16x32_bf16 v[100:103], v[112:115], v[84:87], v[100:103]\n"
"ds_read_b64_tr_b16 v[128:129], v13 offset:16640\n"
"v_mfma_f32_16x16x32_bf16 v[72:75], a[160:163], v[228:231], v[44:47]\n"
"v_exp_f32_e32 v69, v69\n"
"v_mfma_f32_16x16x32_bf16 v[72:75], a[164:167], v[232:235], v[72:75]\n"
"ds_read_b64_tr_b16 v[130:131], v13 offset:20736\n"
"v_cvt_pk_bf16_f32 v90, v68, v69\n"
"v_mfma_f32_16x16x32_bf16 v[72:75], a[168:171], v[236:239], v[72:75]\n"
"v_exp_f32_e32 v70, v70\n"
"v_mfma_f32_16x16x32_bf16 v[72:75], a[172:175], v[240:243], v[72:75]\n"
"v_mfma_f32_16x16x32_bf16 v[76:79], a[176:179], v[228:231], v[44:47]\n"
"v_exp_f32_e32 v71, v71\n"
"v_mfma_f32_16x16x32_bf16 v[76:79], a[180:183], v[232:235], v[76:79]\n"
"ds_read_b64_tr_b16 v[132:133], v16 offset:16896\n"
"v_cvt_pk_bf16_f32 v91, v70, v71\n"
"v_mfma_f32_16x16x32_bf16 v[76:79], a[184:187], v[236:239], v[76:79]\n"
"ds_read_b64_tr_b16 v[134:135], v16 offset:20992\n"
"s_waitcnt lgkmcnt(5)\n"
"v_mfma_f32_16x16x32_bf16 v[76:79], a[188:191], v[240:243], v[76:79]\n"
"v_exp_f32_e32 v72, v72\n"
"v_mfma_f32_16x16x32_bf16 a[8:11], v[148:151], v[88:91], a[8:11]\n"
"buffer_load_dwordx4 v4, s[8:11], s38 offen offset:3072 lds\n"
"v_mfma_f32_16x16x32_bf16 a[24:27], v[152:155], v[88:91], a[24:27]\n"
"v_exp_f32_e32 v73, v73\n"
"v_mfma_f32_16x16x32_bf16 a[40:43], v[156:159], v[88:91], a[40:43]\n"
"ds_read_b64_tr_b16 v[136:137], v13 offset:16896\n"
"v_cvt_pk_bf16_f32 v92, v72, v73\n"
"v_mfma_f32_16x16x32_bf16 a[56:59], v[160:163], v[88:91], a[56:59]\n"
"v_exp_f32_e32 v74, v74\n"
"v_mfma_f32_16x16x32_bf16 a[72:75], v[164:167], v[88:91], a[72:75]\n"
"ds_read_b64_tr_b16 v[138:139], v13 offset:20992\n"
"v_mfma_f32_16x16x32_bf16 a[88:91], v[168:171], v[88:91], a[88:91]\n"
"v_exp_f32_e32 v75, v75\n"
"v_mfma_f32_16x16x32_bf16 a[104:107], v[172:175], v[88:91], a[104:107]\n"
"v_cvt_pk_bf16_f32 v93, v74, v75\n"
"v_mfma_f32_16x16x32_bf16 a[120:123], v[176:179], v[88:91], a[120:123]\n"
"v_exp_f32_e32 v76, v76\n"
"v_mfma_f32_16x16x32_bf16 v[104:107], v[112:115], v[88:91], v[104:107]\n"
"ds_read_b64_tr_b16 v[140:141], v16 offset:17152\n"
"v_mfma_f32_16x16x32_bf16 v[48:51], a[128:131], v[180:183], v[32:35]\n"
"v_exp_f32_e32 v77, v77\n"
"v_mfma_f32_16x16x32_bf16 v[48:51], a[132:135], v[184:187], v[48:51]\n"
"ds_read_b64_tr_b16 v[142:143], v16 offset:21248\n"
"v_cvt_pk_bf16_f32 v94, v76, v77\n"
"v_mfma_f32_16x16x32_bf16 v[48:51], a[136:139], v[188:191], v[48:51]\n"
"v_exp_f32_e32 v78, v78\n"
"v_mfma_f32_16x16x32_bf16 v[48:51], a[140:143], v[192:195], v[48:51]\n"
"s_add_u32 m0, s44, 114688\n"
"v_mfma_f32_16x16x32_bf16 v[52:55], a[144:147], v[180:183], v[32:35]\n"
"v_exp_f32_e32 v79, v79\n"
"v_mfma_f32_16x16x32_bf16 v[52:55], a[148:151], v[184:187], v[52:55]\n"
"ds_read_b64_tr_b16 v[144:145], v13 offset:17152\n"
"v_cvt_pk_bf16_f32 v95, v78, v79\n"
"v_mfma_f32_16x16x32_bf16 v[52:55], a[152:155], v[188:191], v[52:55]\n"
"ds_read_b64_tr_b16 v[146:147], v13 offset:21248\n"
"v_mfma_f32_16x16x32_bf16 v[52:55], a[156:159], v[192:195], v[52:55]\n"
"v_exp_f32_e32 v48, v48\n"
"v_mfma_f32_16x16x32_bf16 a[12:15], v[148:151], v[92:95], a[12:15]\n"
"v_mfma_f32_16x16x32_bf16 a[28:31], v[152:155], v[92:95], a[28:31]\n"
"v_exp_f32_e32 v49, v49\n"
"v_mfma_f32_16x16x32_bf16 a[44:47], v[156:159], v[92:95], a[44:47]\n"
"ds_read_b128 a[160:163], v8 offset:24576\n"
"v_cvt_pk_bf16_f32 v80, v48, v49\n"
"v_mfma_f32_16x16x32_bf16 a[60:63], v[160:163], v[92:95], a[60:63]\n"
"v_exp_f32_e32 v50, v50\n"
"v_mfma_f32_16x16x32_bf16 a[76:79], v[164:167], v[92:95], a[76:79]\n"
"ds_read_b128 a[164:167], v9 offset:24576\n"
"v_mfma_f32_16x16x32_bf16 a[92:95], v[168:171], v[92:95], a[92:95]\n"
"v_exp_f32_e32 v51, v51\n"
"v_mfma_f32_16x16x32_bf16 a[108:111], v[172:175], v[92:95], a[108:111]\n"
"buffer_load_dwordx4 v5, s[12:15], s38 offen lds\n"
"v_mfma_f32_16x16x32_bf16 a[124:127], v[176:179], v[92:95], a[124:127]\n"
"v_exp_f32_e32 v52, v52\n"
"v_mfma_f32_16x16x32_bf16 v[108:111], v[112:115], v[92:95], v[108:111]\n"
"ds_read_b128 a[168:171], v10 offset:24576\n"
"v_cvt_pk_bf16_f32 v81, v50, v51\n"
"v_mfma_f32_16x16x32_bf16 v[56:59], a[128:131], v[196:199], v[36:39]\n"
"v_exp_f32_e32 v53, v53\n"
"v_mfma_f32_16x16x32_bf16 v[56:59], a[132:135], v[200:203], v[56:59]\n"
"ds_read_b64_tr_b16 v[148:149], v16 offset:24576\n"
"v_cvt_pk_bf16_f32 v82, v52, v53\n"
"v_mfma_f32_16x16x32_bf16 v[56:59], a[136:139], v[204:207], v[56:59]\n"
"v_exp_f32_e32 v54, v54\n"
"v_mfma_f32_16x16x32_bf16 v[56:59], a[140:143], v[208:211], v[56:59]\n"
"buffer_load_dwordx4 a[200:203], v18, s[24:27], s41 offen\n"
"v_mfma_f32_16x16x32_bf16 v[60:63], a[144:147], v[196:199], v[36:39]\n"
"v_exp_f32_e32 v55, v55\n"
"v_mfma_f32_16x16x32_bf16 v[60:63], a[148:151], v[200:203], v[60:63]\n"
"ds_read_b128 a[172:175], v11 offset:24576\n"
"v_cvt_pk_bf16_f32 v83, v54, v55\n"
"v_mfma_f32_16x16x32_bf16 v[60:63], a[152:155], v[204:207], v[60:63]\n"
"ds_read_b64_tr_b16 v[150:151], v16 offset:28672\n"
"s_waitcnt lgkmcnt(6)\n"
"v_mfma_f32_16x16x32_bf16 v[60:63], a[156:159], v[208:211], v[60:63]\n"
"v_exp_f32_e32 v56, v56\n"
"v_mfma_f32_16x16x32_bf16 a[0:3], v[116:119], v[80:83], a[0:3]\n"
"buffer_load_dwordx4 v12, s[12:15], s38 offen offset:1024 lds\n"
"v_mfma_f32_16x16x32_bf16 a[16:19], v[120:123], v[80:83], a[16:19]\n"
"v_exp_f32_e32 v57, v57\n"
"v_mfma_f32_16x16x32_bf16 a[32:35], v[124:127], v[80:83], a[32:35]\n"
"ds_read_b128 a[176:179], v8 offset:28672\n"
"v_cvt_pk_bf16_f32 v84, v56, v57\n"
"v_mfma_f32_16x16x32_bf16 a[48:51], v[128:131], v[80:83], a[48:51]\n"
"v_exp_f32_e32 v58, v58\n"
"v_mfma_f32_16x16x32_bf16 a[64:67], v[132:135], v[80:83], a[64:67]\n"
"ds_read_b64_tr_b16 v[152:153], v13 offset:24576\n"
"s_add_u32 s41, s41, s47\n"
"v_mfma_f32_16x16x32_bf16 a[80:83], v[136:139], v[80:83], a[80:83]\n"
"v_exp_f32_e32 v59, v59\n"
"v_mfma_f32_16x16x32_bf16 a[96:99], v[140:143], v[80:83], a[96:99]\n"
"v_cvt_pk_bf16_f32 v85, v58, v59\n"
"v_mfma_f32_16x16x32_bf16 a[112:115], v[144:147], v[80:83], a[112:115]\n"
"v_exp_f32_e32 v60, v60\n"
"v_mfma_f32_16x16x32_bf16 v[96:99], v[112:115], v[80:83], v[96:99]\n"
"ds_read_b128 a[180:183], v9 offset:28672\n"
"v_mfma_f32_16x16x32_bf16 v[64:67], a[128:131], v[212:215], v[40:43]\n"
"v_exp_f32_e32 v61, v61\n"
"v_mfma_f32_16x16x32_bf16 v[64:67], a[132:135], v[216:219], v[64:67]\n"
"ds_read_b64_tr_b16 v[154:155], v13 offset:28672\n"
"v_cvt_pk_bf16_f32 v86, v60, v61\n"
"v_mfma_f32_16x16x32_bf16 v[64:67], a[136:139], v[220:223], v[64:67]\n"
"v_exp_f32_e32 v62, v62\n"
"v_mfma_f32_16x16x32_bf16 v[64:67], a[140:143], v[224:227], v[64:67]\n"
"v_mfma_f32_16x16x32_bf16 v[68:71], a[144:147], v[212:215], v[40:43]\n"
"v_exp_f32_e32 v63, v63\n"
"v_mfma_f32_16x16x32_bf16 v[68:71], a[148:151], v[216:219], v[68:71]\n"
"ds_read_b128 a[184:187], v10 offset:28672\n"
"v_cvt_pk_bf16_f32 v87, v62, v63\n"
"v_mfma_f32_16x16x32_bf16 v[68:71], a[152:155], v[220:223], v[68:71]\n"
"ds_read_b64_tr_b16 v[156:157], v16 offset:24832\n"
"v_mfma_f32_16x16x32_bf16 v[68:71], a[156:159], v[224:227], v[68:71]\n"
"v_exp_f32_e32 v64, v64\n"
"v_mfma_f32_16x16x32_bf16 a[4:7], v[116:119], v[84:87], a[4:7]\n"
"buffer_load_dwordx4 v5, s[12:15], s38 offen offset:2048 lds\n"
"v_mfma_f32_16x16x32_bf16 a[20:23], v[120:123], v[84:87], a[20:23]\n"
"v_exp_f32_e32 v65, v65\n"
"v_mfma_f32_16x16x32_bf16 a[36:39], v[124:127], v[84:87], a[36:39]\n"
"ds_read_b128 a[188:191], v11 offset:28672\n"
"v_cvt_pk_bf16_f32 v88, v64, v65\n"
"v_mfma_f32_16x16x32_bf16 a[52:55], v[128:131], v[84:87], a[52:55]\n"
"v_exp_f32_e32 v66, v66\n"
"v_mfma_f32_16x16x32_bf16 a[68:71], v[132:135], v[84:87], a[68:71]\n"
"ds_read_b64_tr_b16 v[158:159], v16 offset:28928\n"
"v_mfma_f32_16x16x32_bf16 a[84:87], v[136:139], v[84:87], a[84:87]\n"
"v_exp_f32_e32 v67, v67\n"
"v_mfma_f32_16x16x32_bf16 a[100:103], v[140:143], v[84:87], a[100:103]\n"
"v_cvt_pk_bf16_f32 v89, v66, v67\n"
"v_mfma_f32_16x16x32_bf16 a[116:119], v[144:147], v[84:87], a[116:119]\n"
"v_exp_f32_e32 v68, v68\n"
"v_mfma_f32_16x16x32_bf16 v[100:103], v[112:115], v[84:87], v[100:103]\n"
"ds_read_b64_tr_b16 v[160:161], v13 offset:24832\n"
"v_mfma_f32_16x16x32_bf16 v[72:75], a[128:131], v[228:231], v[44:47]\n"
"v_exp_f32_e32 v69, v69\n"
"v_mfma_f32_16x16x32_bf16 v[72:75], a[132:135], v[232:235], v[72:75]\n"
"ds_read_b64_tr_b16 v[162:163], v13 offset:28928\n"
"v_cvt_pk_bf16_f32 v90, v68, v69\n"
"v_mfma_f32_16x16x32_bf16 v[72:75], a[136:139], v[236:239], v[72:75]\n"
"v_exp_f32_e32 v70, v70\n"
"v_mfma_f32_16x16x32_bf16 v[72:75], a[140:143], v[240:243], v[72:75]\n"
"v_mfma_f32_16x16x32_bf16 v[76:79], a[144:147], v[228:231], v[44:47]\n"
"v_exp_f32_e32 v71, v71\n"
"v_mfma_f32_16x16x32_bf16 v[76:79], a[148:151], v[232:235], v[76:79]\n"
"ds_read_b64_tr_b16 v[164:165], v16 offset:25088\n"
"v_cvt_pk_bf16_f32 v91, v70, v71\n"
"v_mfma_f32_16x16x32_bf16 v[76:79], a[152:155], v[236:239], v[76:79]\n"
"ds_read_b64_tr_b16 v[166:167], v16 offset:29184\n"
"s_waitcnt lgkmcnt(5)\n"
"v_mfma_f32_16x16x32_bf16 v[76:79], a[156:159], v[240:243], v[76:79]\n"
"v_exp_f32_e32 v72, v72\n"
"v_mfma_f32_16x16x32_bf16 a[8:11], v[116:119], v[88:91], a[8:11]\n"
"buffer_load_dwordx4 v12, s[12:15], s38 offen offset:3072 lds\n"
"v_mfma_f32_16x16x32_bf16 a[24:27], v[120:123], v[88:91], a[24:27]\n"
"v_exp_f32_e32 v73, v73\n"
"v_mfma_f32_16x16x32_bf16 a[40:43], v[124:127], v[88:91], a[40:43]\n"
"ds_read_b64_tr_b16 v[168:169], v13 offset:25088\n"
"s_mov_b32 m0, 148480\n"
"s_mov_b32 s40, 0\n"
"buffer_load_dword v14, s[56:59], s40 offen lds\n"
"v_mfma_f32_16x16x32_bf16 a[56:59], v[128:131], v[88:91], a[56:59]\n"
"v_exp_f32_e32 v74, v74\n"
"v_mfma_f32_16x16x32_bf16 a[72:75], v[132:135], v[88:91], a[72:75]\n"
"ds_read_b64_tr_b16 v[170:171], v13 offset:29184\n"
"v_cvt_pk_bf16_f32 v92, v72, v73\n"
"v_mfma_f32_16x16x32_bf16 a[88:91], v[136:139], v[88:91], a[88:91]\n"
"v_exp_f32_e32 v75, v75\n"
"v_mfma_f32_16x16x32_bf16 a[104:107], v[140:143], v[88:91], a[104:107]\n"
"v_cvt_pk_bf16_f32 v93, v74, v75\n"
"v_mfma_f32_16x16x32_bf16 a[120:123], v[144:147], v[88:91], a[120:123]\n"
"v_exp_f32_e32 v76, v76\n"
"v_mfma_f32_16x16x32_bf16 v[104:107], v[112:115], v[88:91], v[104:107]\n"
"ds_read_b64_tr_b16 v[172:173], v16 offset:25344\n"
"v_mfma_f32_16x16x32_bf16 v[48:51], a[160:163], v[180:183], v[32:35]\n"
"v_exp_f32_e32 v77, v77\n"
"v_mfma_f32_16x16x32_bf16 v[48:51], a[164:167], v[184:187], v[48:51]\n"
"ds_read_b64_tr_b16 v[174:175], v16 offset:29440\n"
"v_cvt_pk_bf16_f32 v94, v76, v77\n"
"v_mfma_f32_16x16x32_bf16 v[48:51], a[168:171], v[188:191], v[48:51]\n"
"v_exp_f32_e32 v78, v78\n"
"v_mfma_f32_16x16x32_bf16 v[48:51], a[172:175], v[192:195], v[48:51]\n"
"s_waitcnt vmcnt(12)\n"
"s_barrier\n"
"v_mfma_f32_16x16x32_bf16 v[52:55], a[176:179], v[180:183], v[32:35]\n"
"v_exp_f32_e32 v79, v79\n"
"v_mfma_f32_16x16x32_bf16 v[52:55], a[180:183], v[184:187], v[52:55]\n"
"ds_read_b64_tr_b16 v[176:177], v13 offset:25344\n"
"v_cvt_pk_bf16_f32 v95, v78, v79\n"
"v_mfma_f32_16x16x32_bf16 v[52:55], a[184:187], v[188:191], v[52:55]\n"
"ds_read_b64_tr_b16 v[178:179], v13 offset:29440\n"
"s_add_u32 s38, s38, 16384\n"
"v_mfma_f32_16x16x32_bf16 v[52:55], a[188:191], v[192:195], v[52:55]\n"
"v_exp_f32_e32 v48, v48\n"
"v_mfma_f32_16x16x32_bf16 a[12:15], v[116:119], v[92:95], a[12:15]\n"
"s_add_u32 m0, s44, 0\n"
"v_mfma_f32_16x16x32_bf16 a[28:31], v[120:123], v[92:95], a[28:31]\n"
"v_exp_f32_e32 v49, v49\n"
"v_mfma_f32_16x16x32_bf16 a[44:47], v[124:127], v[92:95], a[44:47]\n"
"ds_read_b128 a[128:131], v8 offset:32768\n"
"v_cvt_pk_bf16_f32 v80, v48, v49\n"
"v_mfma_f32_16x16x32_bf16 a[60:63], v[128:131], v[92:95], a[60:63]\n"
"v_exp_f32_e32 v50, v50\n"
"v_mfma_f32_16x16x32_bf16 a[76:79], v[132:135], v[92:95], a[76:79]\n"
"ds_read_b128 a[132:135], v9 offset:32768\n"
"v_mfma_f32_16x16x32_bf16 a[92:95], v[136:139], v[92:95], a[92:95]\n"
"v_exp_f32_e32 v51, v51\n"
"v_mfma_f32_16x16x32_bf16 a[108:111], v[140:143], v[92:95], a[108:111]\n"
"buffer_load_dwordx4 v1, s[8:11], s38 offen lds\n"
"v_mfma_f32_16x16x32_bf16 a[124:127], v[144:147], v[92:95], a[124:127]\n"
"v_exp_f32_e32 v52, v52\n"
"v_mfma_f32_16x16x32_bf16 v[108:111], v[112:115], v[92:95], v[108:111]\n"
"ds_read_b128 a[136:139], v10 offset:32768\n"
"v_cvt_pk_bf16_f32 v81, v50, v51\n"
"v_mfma_f32_16x16x32_bf16 v[56:59], a[160:163], v[196:199], v[36:39]\n"
"v_exp_f32_e32 v53, v53\n"
"v_mfma_f32_16x16x32_bf16 v[56:59], a[164:167], v[200:203], v[56:59]\n"
"ds_read_b64_tr_b16 v[116:117], v16 offset:32768\n"
"v_cvt_pk_bf16_f32 v82, v52, v53\n"
"v_mfma_f32_16x16x32_bf16 v[56:59], a[168:171], v[204:207], v[56:59]\n"
"v_exp_f32_e32 v54, v54\n"
"v_mfma_f32_16x16x32_bf16 v[56:59], a[172:175], v[208:211], v[56:59]\n"
"s_waitcnt lgkmcnt(4)\n"
"v_mfma_f32_16x16x32_bf16 v[60:63], a[176:179], v[196:199], v[36:39]\n"
"v_exp_f32_e32 v55, v55\n"
"v_mfma_f32_16x16x32_bf16 v[60:63], a[180:183], v[200:203], v[60:63]\n"
"ds_read_b128 a[140:143], v11 offset:32768\n"
"v_cvt_pk_bf16_f32 v83, v54, v55\n"
"v_mfma_f32_16x16x32_bf16 v[60:63], a[184:187], v[204:207], v[60:63]\n"
"ds_read_b64_tr_b16 v[118:119], v16 offset:36864\n"
"v_mfma_f32_16x16x32_bf16 v[60:63], a[188:191], v[208:211], v[60:63]\n"
"v_exp_f32_e32 v56, v56\n"
"v_mfma_f32_16x16x32_bf16 a[0:3], v[148:151], v[80:83], a[0:3]\n"
"buffer_load_dwordx4 v2, s[8:11], s38 offen offset:1024 lds\n"
"v_mfma_f32_16x16x32_bf16 a[16:19], v[152:155], v[80:83], a[16:19]\n"
"v_exp_f32_e32 v57, v57\n"
"v_mfma_f32_16x16x32_bf16 a[32:35], v[156:159], v[80:83], a[32:35]\n"
"ds_read_b128 a[144:147], v8 offset:36864\n"
"v_cvt_pk_bf16_f32 v84, v56, v57\n"
"v_mfma_f32_16x16x32_bf16 a[48:51], v[160:163], v[80:83], a[48:51]\n"
"v_exp_f32_e32 v58, v58\n"
"v_mfma_f32_16x16x32_bf16 a[64:67], v[164:167], v[80:83], a[64:67]\n"
"ds_read_b64_tr_b16 v[120:121], v13 offset:32768\n"
"v_mfma_f32_16x16x32_bf16 a[80:83], v[168:171], v[80:83], a[80:83]\n"
"v_exp_f32_e32 v59, v59\n"
"v_mfma_f32_16x16x32_bf16 a[96:99], v[172:175], v[80:83], a[96:99]\n"
"v_cvt_pk_bf16_f32 v85, v58, v59\n"
"v_mfma_f32_16x16x32_bf16 a[112:115], v[176:179], v[80:83], a[112:115]\n"
"v_exp_f32_e32 v60, v60\n"
"v_mfma_f32_16x16x32_bf16 v[96:99], v[112:115], v[80:83], v[96:99]\n"
"ds_read_b128 a[148:151], v9 offset:36864\n"
"v_mfma_f32_16x16x32_bf16 v[64:67], a[160:163], v[212:215], v[40:43]\n"
"v_exp_f32_e32 v61, v61\n"
"v_mfma_f32_16x16x32_bf16 v[64:67], a[164:167], v[216:219], v[64:67]\n"
"ds_read_b64_tr_b16 v[122:123], v13 offset:36864\n"
"v_cvt_pk_bf16_f32 v86, v60, v61\n"
"v_mfma_f32_16x16x32_bf16 v[64:67], a[168:171], v[220:223], v[64:67]\n"
"v_exp_f32_e32 v62, v62\n"
"v_mfma_f32_16x16x32_bf16 v[64:67], a[172:175], v[224:227], v[64:67]\n"
"v_mfma_f32_16x16x32_bf16 v[68:71], a[176:179], v[212:215], v[40:43]\n"
"v_exp_f32_e32 v63, v63\n"
"v_mfma_f32_16x16x32_bf16 v[68:71], a[180:183], v[216:219], v[68:71]\n"
"ds_read_b128 a[152:155], v10 offset:36864\n"
"v_cvt_pk_bf16_f32 v87, v62, v63\n"
"v_mfma_f32_16x16x32_bf16 v[68:71], a[184:187], v[220:223], v[68:71]\n"
"ds_read_b64_tr_b16 v[124:125], v16 offset:33024\n"
"v_mfma_f32_16x16x32_bf16 v[68:71], a[188:191], v[224:227], v[68:71]\n"
"v_exp_f32_e32 v64, v64\n"
"v_mfma_f32_16x16x32_bf16 a[4:7], v[148:151], v[84:87], a[4:7]\n"
"buffer_load_dwordx4 v3, s[8:11], s38 offen offset:2048 lds\n"
"v_mfma_f32_16x16x32_bf16 a[20:23], v[152:155], v[84:87], a[20:23]\n"
"v_exp_f32_e32 v65, v65\n"
"v_mfma_f32_16x16x32_bf16 a[36:39], v[156:159], v[84:87], a[36:39]\n"
"ds_read_b128 a[156:159], v11 offset:36864\n"
"v_cvt_pk_bf16_f32 v88, v64, v65\n"
"v_mfma_f32_16x16x32_bf16 a[52:55], v[160:163], v[84:87], a[52:55]\n"
"v_exp_f32_e32 v66, v66\n"
"v_mfma_f32_16x16x32_bf16 a[68:71], v[164:167], v[84:87], a[68:71]\n"
"ds_read_b64_tr_b16 v[126:127], v16 offset:37120\n"
"v_mfma_f32_16x16x32_bf16 a[84:87], v[168:171], v[84:87], a[84:87]\n"
"v_exp_f32_e32 v67, v67\n"
"v_mfma_f32_16x16x32_bf16 a[100:103], v[172:175], v[84:87], a[100:103]\n"
"v_cvt_pk_bf16_f32 v89, v66, v67\n"
"v_mfma_f32_16x16x32_bf16 a[116:119], v[176:179], v[84:87], a[116:119]\n"
"v_exp_f32_e32 v68, v68\n"
"v_mfma_f32_16x16x32_bf16 v[100:103], v[112:115], v[84:87], v[100:103]\n"
"ds_read_b64_tr_b16 v[128:129], v13 offset:33024\n"
"v_mfma_f32_16x16x32_bf16 v[72:75], a[160:163], v[228:231], v[44:47]\n"
"v_exp_f32_e32 v69, v69\n"
"v_mfma_f32_16x16x32_bf16 v[72:75], a[164:167], v[232:235], v[72:75]\n"
"ds_read_b64_tr_b16 v[130:131], v13 offset:37120\n"
"v_cvt_pk_bf16_f32 v90, v68, v69\n"
"v_mfma_f32_16x16x32_bf16 v[72:75], a[168:171], v[236:239], v[72:75]\n"
"v_exp_f32_e32 v70, v70\n"
"v_mfma_f32_16x16x32_bf16 v[72:75], a[172:175], v[240:243], v[72:75]\n"
"v_mfma_f32_16x16x32_bf16 v[76:79], a[176:179], v[228:231], v[44:47]\n"
"v_exp_f32_e32 v71, v71\n"
"v_mfma_f32_16x16x32_bf16 v[76:79], a[180:183], v[232:235], v[76:79]\n"
"ds_read_b64_tr_b16 v[132:133], v16 offset:33280\n"
"v_cvt_pk_bf16_f32 v91, v70, v71\n"
"v_mfma_f32_16x16x32_bf16 v[76:79], a[184:187], v[236:239], v[76:79]\n"
"ds_read_b64_tr_b16 v[134:135], v16 offset:37376\n"
"s_waitcnt lgkmcnt(5)\n"
"v_mfma_f32_16x16x32_bf16 v[76:79], a[188:191], v[240:243], v[76:79]\n"
"v_exp_f32_e32 v72, v72\n"
"v_mfma_f32_16x16x32_bf16 a[8:11], v[148:151], v[88:91], a[8:11]\n"
"buffer_load_dwordx4 v4, s[8:11], s38 offen offset:3072 lds\n"
"v_mfma_f32_16x16x32_bf16 a[24:27], v[152:155], v[88:91], a[24:27]\n"
"v_exp_f32_e32 v73, v73\n"
"v_mfma_f32_16x16x32_bf16 a[40:43], v[156:159], v[88:91], a[40:43]\n"
"ds_read_b64_tr_b16 v[136:137], v13 offset:33280\n"
"s_mov_b32 m0, 148480\n"
"s_mov_b32 s40, 32768\n"
"buffer_load_dword v14, s[56:59], s40 offen lds\n"
"v_mfma_f32_16x16x32_bf16 a[56:59], v[160:163], v[88:91], a[56:59]\n"
"v_exp_f32_e32 v74, v74\n"
"v_mfma_f32_16x16x32_bf16 a[72:75], v[164:167], v[88:91], a[72:75]\n"
"ds_read_b64_tr_b16 v[138:139], v13 offset:37376\n"
"v_cvt_pk_bf16_f32 v92, v72, v73\n"
"v_mfma_f32_16x16x32_bf16 a[88:91], v[168:171], v[88:91], a[88:91]\n"
"v_exp_f32_e32 v75, v75\n"
"v_mfma_f32_16x16x32_bf16 a[104:107], v[172:175], v[88:91], a[104:107]\n"
"v_cvt_pk_bf16_f32 v93, v74, v75\n"
"v_mfma_f32_16x16x32_bf16 a[120:123], v[176:179], v[88:91], a[120:123]\n"
"v_exp_f32_e32 v76, v76\n"
"v_mfma_f32_16x16x32_bf16 v[104:107], v[112:115], v[88:91], v[104:107]\n"
"ds_read_b64_tr_b16 v[140:141], v16 offset:33536\n"
"v_mfma_f32_16x16x32_bf16 v[48:51], a[128:131], v[180:183], v[32:35]\n"
"v_exp_f32_e32 v77, v77\n"
"v_mfma_f32_16x16x32_bf16 v[48:51], a[132:135], v[184:187], v[48:51]\n"
"ds_read_b64_tr_b16 v[142:143], v16 offset:37632\n"
"v_cvt_pk_bf16_f32 v94, v76, v77\n"
"v_mfma_f32_16x16x32_bf16 v[48:51], a[136:139], v[188:191], v[48:51]\n"
"v_exp_f32_e32 v78, v78\n"
"v_mfma_f32_16x16x32_bf16 v[48:51], a[140:143], v[192:195], v[48:51]\n"
"s_add_u32 m0, s44, 65536\n"
"v_mfma_f32_16x16x32_bf16 v[52:55], a[144:147], v[180:183], v[32:35]\n"
"v_exp_f32_e32 v79, v79\n"
"v_mfma_f32_16x16x32_bf16 v[52:55], a[148:151], v[184:187], v[52:55]\n"
"ds_read_b64_tr_b16 v[144:145], v13 offset:33536\n"
"v_cvt_pk_bf16_f32 v95, v78, v79\n"
"v_mfma_f32_16x16x32_bf16 v[52:55], a[152:155], v[188:191], v[52:55]\n"
"ds_read_b64_tr_b16 v[146:147], v13 offset:37632\n"
"v_mfma_f32_16x16x32_bf16 v[52:55], a[156:159], v[192:195], v[52:55]\n"
"v_exp_f32_e32 v48, v48\n"
"v_mfma_f32_16x16x32_bf16 a[12:15], v[148:151], v[92:95], a[12:15]\n"
"v_mfma_f32_16x16x32_bf16 a[28:31], v[152:155], v[92:95], a[28:31]\n"
"v_exp_f32_e32 v49, v49\n"
"v_mfma_f32_16x16x32_bf16 a[44:47], v[156:159], v[92:95], a[44:47]\n"
"ds_read_b128 a[160:163], v8 offset:40960\n"
"v_cvt_pk_bf16_f32 v80, v48, v49\n"
"v_mfma_f32_16x16x32_bf16 a[60:63], v[160:163], v[92:95], a[60:63]\n"
"v_exp_f32_e32 v50, v50\n"
"v_mfma_f32_16x16x32_bf16 a[76:79], v[164:167], v[92:95], a[76:79]\n"
"ds_read_b128 a[164:167], v9 offset:40960\n"
"v_mfma_f32_16x16x32_bf16 a[92:95], v[168:171], v[92:95], a[92:95]\n"
"v_exp_f32_e32 v51, v51\n"
"v_mfma_f32_16x16x32_bf16 a[108:111], v[172:175], v[92:95], a[108:111]\n"
"buffer_load_dwordx4 v5, s[12:15], s38 offen lds\n"
"v_mfma_f32_16x16x32_bf16 a[124:127], v[176:179], v[92:95], a[124:127]\n"
"v_exp_f32_e32 v52, v52\n"
"v_mfma_f32_16x16x32_bf16 v[108:111], v[112:115], v[92:95], v[108:111]\n"
"ds_read_b128 a[168:171], v10 offset:40960\n"
"v_cvt_pk_bf16_f32 v81, v50, v51\n"
"v_mfma_f32_16x16x32_bf16 v[56:59], a[128:131], v[196:199], v[36:39]\n"
"v_exp_f32_e32 v53, v53\n"
"v_mfma_f32_16x16x32_bf16 v[56:59], a[132:135], v[200:203], v[56:59]\n"
"ds_read_b64_tr_b16 v[148:149], v16 offset:40960\n"
"v_cvt_pk_bf16_f32 v82, v52, v53\n"
"v_mfma_f32_16x16x32_bf16 v[56:59], a[136:139], v[204:207], v[56:59]\n"
"v_exp_f32_e32 v54, v54\n"
"v_mfma_f32_16x16x32_bf16 v[56:59], a[140:143], v[208:211], v[56:59]\n"
"buffer_load_dwordx4 a[204:207], v18, s[24:27], s41 offen\n"
"v_mfma_f32_16x16x32_bf16 v[60:63], a[144:147], v[196:199], v[36:39]\n"
"v_exp_f32_e32 v55, v55\n"
"v_mfma_f32_16x16x32_bf16 v[60:63], a[148:151], v[200:203], v[60:63]\n"
"ds_read_b128 a[172:175], v11 offset:40960\n"
"v_cvt_pk_bf16_f32 v83, v54, v55\n"
"v_mfma_f32_16x16x32_bf16 v[60:63], a[152:155], v[204:207], v[60:63]\n"
"ds_read_b64_tr_b16 v[150:151], v16 offset:45056\n"
"s_waitcnt lgkmcnt(6)\n"
"v_mfma_f32_16x16x32_bf16 v[60:63], a[156:159], v[208:211], v[60:63]\n"
"v_exp_f32_e32 v56, v56\n"
"v_mfma_f32_16x16x32_bf16 a[0:3], v[116:119], v[80:83], a[0:3]\n"
"buffer_load_dwordx4 v12, s[12:15], s38 offen offset:1024 lds\n"
"v_mfma_f32_16x16x32_bf16 a[16:19], v[120:123], v[80:83], a[16:19]\n"
"v_exp_f32_e32 v57, v57\n"
"v_mfma_f32_16x16x32_bf16 a[32:35], v[124:127], v[80:83], a[32:35]\n"
"ds_read_b128 a[176:179], v8 offset:45056\n"
"v_cvt_pk_bf16_f32 v84, v56, v57\n"
"v_mfma_f32_16x16x32_bf16 a[48:51], v[128:131], v[80:83], a[48:51]\n"
"v_exp_f32_e32 v58, v58\n"
"v_mfma_f32_16x16x32_bf16 a[64:67], v[132:135], v[80:83], a[64:67]\n"
"ds_read_b64_tr_b16 v[152:153], v13 offset:40960\n"
"s_add_u32 s41, s41, s47\n"
"v_mfma_f32_16x16x32_bf16 a[80:83], v[136:139], v[80:83], a[80:83]\n"
"v_exp_f32_e32 v59, v59\n"
"v_mfma_f32_16x16x32_bf16 a[96:99], v[140:143], v[80:83], a[96:99]\n"
"v_cvt_pk_bf16_f32 v85, v58, v59\n"
"v_mfma_f32_16x16x32_bf16 a[112:115], v[144:147], v[80:83], a[112:115]\n"
"v_exp_f32_e32 v60, v60\n"
"v_mfma_f32_16x16x32_bf16 v[96:99], v[112:115], v[80:83], v[96:99]\n"
"ds_read_b128 a[180:183], v9 offset:45056\n"
"v_mfma_f32_16x16x32_bf16 v[64:67], a[128:131], v[212:215], v[40:43]\n"
"v_exp_f32_e32 v61, v61\n"
"v_mfma_f32_16x16x32_bf16 v[64:67], a[132:135], v[216:219], v[64:67]\n"
"ds_read_b64_tr_b16 v[154:155], v13 offset:45056\n"
"v_cvt_pk_bf16_f32 v86, v60, v61\n"
"v_mfma_f32_16x16x32_bf16 v[64:67], a[136:139], v[220:223], v[64:67]\n"
"v_exp_f32_e32 v62, v62\n"
"v_mfma_f32_16x16x32_bf16 v[64:67], a[140:143], v[224:227], v[64:67]\n"
"v_mfma_f32_16x16x32_bf16 v[68:71], a[144:147], v[212:215], v[40:43]\n"
"v_exp_f32_e32 v63, v63\n"
"v_mfma_f32_16x16x32_bf16 v[68:71], a[148:151], v[216:219], v[68:71]\n"
"ds_read_b128 a[184:187], v10 offset:45056\n"
"v_cvt_pk_bf16_f32 v87, v62, v63\n"
"v_mfma_f32_16x16x32_bf16 v[68:71], a[152:155], v[220:223], v[68:71]\n"
"ds_read_b64_tr_b16 v[156:157], v16 offset:41216\n"
"v_mfma_f32_16x16x32_bf16 v[68:71], a[156:159], v[224:227], v[68:71]\n"
"v_exp_f32_e32 v64, v64\n"
"v_mfma_f32_16x16x32_bf16 a[4:7], v[116:119], v[84:87], a[4:7]\n"
"buffer_load_dwordx4 v5, s[12:15], s38 offen offset:2048 lds\n"
"v_mfma_f32_16x16x32_bf16 a[20:23], v[120:123], v[84:87], a[20:23]\n"
"v_exp_f32_e32 v65, v65\n"
"v_mfma_f32_16x16x32_bf16 a[36:39], v[124:127], v[84:87], a[36:39]\n"
"ds_read_b128 a[188:191], v11 offset:45056\n"
"v_cvt_pk_bf16_f32 v88, v64, v65\n"
"v_mfma_f32_16x16x32_bf16 a[52:55], v[128:131], v[84:87], a[52:55]\n"
"v_exp_f32_e32 v66, v66\n"
"v_mfma_f32_16x16x32_bf16 a[68:71], v[132:135], v[84:87], a[68:71]\n"
"ds_read_b64_tr_b16 v[158:159], v16 offset:45312\n"
"v_mfma_f32_16x16x32_bf16 a[84:87], v[136:139], v[84:87], a[84:87]\n"
"v_exp_f32_e32 v67, v67\n"
"v_mfma_f32_16x16x32_bf16 a[100:103], v[140:143], v[84:87], a[100:103]\n"
"v_cvt_pk_bf16_f32 v89, v66, v67\n"
"v_mfma_f32_16x16x32_bf16 a[116:119], v[144:147], v[84:87], a[116:119]\n"
"v_exp_f32_e32 v68, v68\n"
"v_mfma_f32_16x16x32_bf16 v[100:103], v[112:115], v[84:87], v[100:103]\n"
"ds_read_b64_tr_b16 v[160:161], v13 offset:41216\n"
"v_mfma_f32_16x16x32_bf16 v[72:75], a[128:131], v[228:231], v[44:47]\n"
"v_exp_f32_e32 v69, v69\n"
"v_mfma_f32_16x16x32_bf16 v[72:75], a[132:135], v[232:235], v[72:75]\n"
"ds_read_b64_tr_b16 v[162:163], v13 offset:45312\n"
"v_cvt_pk_bf16_f32 v90, v68, v69\n"
"v_mfma_f32_16x16x32_bf16 v[72:75], a[136:139], v[236:239], v[72:75]\n"
"v_exp_f32_e32 v70, v70\n"
"v_mfma_f32_16x16x32_bf16 v[72:75], a[140:143], v[240:243], v[72:75]\n"
"v_mfma_f32_16x16x32_bf16 v[76:79], a[144:147], v[228:231], v[44:47]\n"
"v_exp_f32_e32 v71, v71\n"
"v_mfma_f32_16x16x32_bf16 v[76:79], a[148:151], v[232:235], v[76:79]\n"
"ds_read_b64_tr_b16 v[164:165], v16 offset:41472\n"
"v_cvt_pk_bf16_f32 v91, v70, v71\n"
"v_mfma_f32_16x16x32_bf16 v[76:79], a[152:155], v[236:239], v[76:79]\n"
"ds_read_b64_tr_b16 v[166:167], v16 offset:45568\n"
"s_waitcnt lgkmcnt(5)\n"
"v_mfma_f32_16x16x32_bf16 v[76:79], a[156:159], v[240:243], v[76:79]\n"
"v_exp_f32_e32 v72, v72\n"
"v_mfma_f32_16x16x32_bf16 a[8:11], v[116:119], v[88:91], a[8:11]\n"
"buffer_load_dwordx4 v12, s[12:15], s38 offen offset:3072 lds\n"
"v_mfma_f32_16x16x32_bf16 a[24:27], v[120:123], v[88:91], a[24:27]\n"
"v_exp_f32_e32 v73, v73\n"
"v_mfma_f32_16x16x32_bf16 a[40:43], v[124:127], v[88:91], a[40:43]\n"
"ds_read_b64_tr_b16 v[168:169], v13 offset:41472\n"
"v_cvt_pk_bf16_f32 v92, v72, v73\n"
"v_mfma_f32_16x16x32_bf16 a[56:59], v[128:131], v[88:91], a[56:59]\n"
"v_exp_f32_e32 v74, v74\n"
"v_mfma_f32_16x16x32_bf16 a[72:75], v[132:135], v[88:91], a[72:75]\n"
"ds_read_b64_tr_b16 v[170:171], v13 offset:45568\n"
"v_mfma_f32_16x16x32_bf16 a[88:91], v[136:139], v[88:91], a[88:91]\n"
"v_exp_f32_e32 v75, v75\n"
"v_mfma_f32_16x16x32_bf16 a[104:107], v[140:143], v[88:91], a[104:107]\n"
"v_cvt_pk_bf16_f32 v93, v74, v75\n"
"v_mfma_f32_16x16x32_bf16 a[120:123], v[144:147], v[88:91], a[120:123]\n"
"v_exp_f32_e32 v76, v76\n"
"v_mfma_f32_16x16x32_bf16 v[104:107], v[112:115], v[88:91], v[104:107]\n"
"ds_read_b64_tr_b16 v[172:173], v16 offset:41728\n"
"v_mfma_f32_16x16x32_bf16 v[48:51], a[160:163], v[180:183], v[32:35]\n"
"v_exp_f32_e32 v77, v77\n"
"v_mfma_f32_16x16x32_bf16 v[48:51], a[164:167], v[184:187], v[48:51]\n"
"ds_read_b64_tr_b16 v[174:175], v16 offset:45824\n"
"v_cvt_pk_bf16_f32 v94, v76, v77\n"
"v_mfma_f32_16x16x32_bf16 v[48:51], a[168:171], v[188:191], v[48:51]\n"
"v_exp_f32_e32 v78, v78\n"
"v_mfma_f32_16x16x32_bf16 v[48:51], a[172:175], v[192:195], v[48:51]\n"
"s_waitcnt vmcnt(11)\n"
"s_barrier\n"
"v_mfma_f32_16x16x32_bf16 v[52:55], a[176:179], v[180:183], v[32:35]\n"
"v_exp_f32_e32 v79, v79\n"
"v_mfma_f32_16x16x32_bf16 v[52:55], a[180:183], v[184:187], v[52:55]\n"
"ds_read_b64_tr_b16 v[176:177], v13 offset:41728\n"
"v_cvt_pk_bf16_f32 v95, v78, v79\n"
"v_mfma_f32_16x16x32_bf16 v[52:55], a[184:187], v[188:191], v[52:55]\n"
"ds_read_b64_tr_b16 v[178:179], v13 offset:45824\n"
"s_add_u32 s38, s38, 16384\n"
"v_mfma_f32_16x16x32_bf16 v[52:55], a[188:191], v[192:195], v[52:55]\n"
"v_exp_f32_e32 v48, v48\n"
"v_mfma_f32_16x16x32_bf16 a[12:15], v[116:119], v[92:95], a[12:15]\n"
"s_add_u32 m0, s44, 16384\n"
"v_readlane_b32 s43, v29, s42\n"
"v_mfma_f32_16x16x32_bf16 a[28:31], v[120:123], v[92:95], a[28:31]\n"
"v_exp_f32_e32 v49, v49\n"
"v_mfma_f32_16x16x32_bf16 a[44:47], v[124:127], v[92:95], a[44:47]\n"
"ds_read_b128 a[128:131], v8 offset:49152\n"
"v_cvt_pk_bf16_f32 v80, v48, v49\n"
"v_mfma_f32_16x16x32_bf16 a[60:63], v[128:131], v[92:95], a[60:63]\n"
"v_exp_f32_e32 v50, v50\n"
"v_mfma_f32_16x16x32_bf16 a[76:79], v[132:135], v[92:95], a[76:79]\n"
"ds_read_b128 a[132:135], v9 offset:49152\n"
"v_readlane_b32 s39, v30, s42\n"
"v_mfma_f32_16x16x32_bf16 a[92:95], v[136:139], v[92:95], a[92:95]\n"
"v_exp_f32_e32 v51, v51\n"
"v_mfma_f32_16x16x32_bf16 a[108:111], v[140:143], v[92:95], a[108:111]\n"
"buffer_load_dwordx4 v1, s[8:11], s38 offen lds\n"
"v_mfma_f32_16x16x32_bf16 a[124:127], v[144:147], v[92:95], a[124:127]\n"
"v_exp_f32_e32 v52, v52\n"
"v_mfma_f32_16x16x32_bf16 v[108:111], v[112:115], v[92:95], v[108:111]\n"
"ds_read_b128 a[136:139], v10 offset:49152\n"
"v_cvt_pk_bf16_f32 v81, v50, v51\n"
"v_mfma_f32_16x16x32_bf16 v[56:59], a[160:163], v[196:199], v[36:39]\n"
"v_exp_f32_e32 v53, v53\n"
"v_mfma_f32_16x16x32_bf16 v[56:59], a[164:167], v[200:203], v[56:59]\n"
"ds_read_b64_tr_b16 v[116:117], v16 offset:49152\n"
"v_cvt_pk_bf16_f32 v82, v52, v53\n"
"v_mfma_f32_16x16x32_bf16 v[56:59], a[168:171], v[204:207], v[56:59]\n"
"v_exp_f32_e32 v54, v54\n"
"v_mfma_f32_16x16x32_bf16 v[56:59], a[172:175], v[208:211], v[56:59]\n"
"buffer_load_dwordx4 a[208:211], v18, s[24:27], s41 offen\n"
"v_mfma_f32_16x16x32_bf16 v[60:63], a[176:179], v[196:199], v[36:39]\n"
"v_exp_f32_e32 v55, v55\n"
"v_mfma_f32_16x16x32_bf16 v[60:63], a[180:183], v[200:203], v[60:63]\n"
"ds_read_b128 a[140:143], v11 offset:49152\n"
"v_cvt_pk_bf16_f32 v83, v54, v55\n"
"v_mfma_f32_16x16x32_bf16 v[60:63], a[184:187], v[204:207], v[60:63]\n"
"ds_read_b64_tr_b16 v[118:119], v16 offset:53248\n"
"s_waitcnt lgkmcnt(6)\n"
"v_mfma_f32_16x16x32_bf16 v[60:63], a[188:191], v[208:211], v[60:63]\n"
"v_exp_f32_e32 v56, v56\n"
"v_mfma_f32_16x16x32_bf16 a[0:3], v[148:151], v[80:83], a[0:3]\n"
"buffer_load_dwordx4 v2, s[8:11], s38 offen offset:1024 lds\n"
"v_mfma_f32_16x16x32_bf16 a[16:19], v[152:155], v[80:83], a[16:19]\n"
"v_exp_f32_e32 v57, v57\n"
"v_mfma_f32_16x16x32_bf16 a[32:35], v[156:159], v[80:83], a[32:35]\n"
"ds_read_b128 a[144:147], v8 offset:53248\n"
"v_cvt_pk_bf16_f32 v84, v56, v57\n"
"v_mfma_f32_16x16x32_bf16 a[48:51], v[160:163], v[80:83], a[48:51]\n"
"v_exp_f32_e32 v58, v58\n"
"v_mfma_f32_16x16x32_bf16 a[64:67], v[164:167], v[80:83], a[64:67]\n"
"ds_read_b64_tr_b16 v[120:121], v13 offset:49152\n"
"s_add_u32 s41, s41, s47\n"
"v_mfma_f32_16x16x32_bf16 a[80:83], v[168:171], v[80:83], a[80:83]\n"
"v_exp_f32_e32 v59, v59\n"
"v_mfma_f32_16x16x32_bf16 a[96:99], v[172:175], v[80:83], a[96:99]\n"
"v_cvt_pk_bf16_f32 v85, v58, v59\n"
"v_mfma_f32_16x16x32_bf16 a[112:115], v[176:179], v[80:83], a[112:115]\n"
"v_exp_f32_e32 v60, v60\n"
"v_mfma_f32_16x16x32_bf16 v[96:99], v[112:115], v[80:83], v[96:99]\n"
"ds_read_b128 a[148:151], v9 offset:53248\n"
"v_mfma_f32_16x16x32_bf16 v[64:67], a[160:163], v[212:215], v[40:43]\n"
"v_exp_f32_e32 v61, v61\n"
"v_mfma_f32_16x16x32_bf16 v[64:67], a[164:167], v[216:219], v[64:67]\n"
"ds_read_b64_tr_b16 v[122:123], v13 offset:53248\n"
"v_cvt_pk_bf16_f32 v86, v60, v61\n"
"v_mfma_f32_16x16x32_bf16 v[64:67], a[168:171], v[220:223], v[64:67]\n"
"v_exp_f32_e32 v62, v62\n"
"v_mfma_f32_16x16x32_bf16 v[64:67], a[172:175], v[224:227], v[64:67]\n"
"v_mfma_f32_16x16x32_bf16 v[68:71], a[176:179], v[212:215], v[40:43]\n"
"v_exp_f32_e32 v63, v63\n"
"v_mfma_f32_16x16x32_bf16 v[68:71], a[180:183], v[216:219], v[68:71]\n"
"ds_read_b128 a[152:155], v10 offset:53248\n"
"v_cvt_pk_bf16_f32 v87, v62, v63\n"
"v_mfma_f32_16x16x32_bf16 v[68:71], a[184:187], v[220:223], v[68:71]\n"
"ds_read_b64_tr_b16 v[124:125], v16 offset:49408\n"
"v_mfma_f32_16x16x32_bf16 v[68:71], a[188:191], v[224:227], v[68:71]\n"
"v_exp_f32_e32 v64, v64\n"
"v_mfma_f32_16x16x32_bf16 a[4:7], v[148:151], v[84:87], a[4:7]\n"
"buffer_load_dwordx4 v3, s[8:11], s38 offen offset:2048 lds\n"
"v_mfma_f32_16x16x32_bf16 a[20:23], v[152:155], v[84:87], a[20:23]\n"
"v_exp_f32_e32 v65, v65\n"
"v_mfma_f32_16x16x32_bf16 a[36:39], v[156:159], v[84:87], a[36:39]\n"
"ds_read_b128 a[156:159], v11 offset:53248\n"
"v_cvt_pk_bf16_f32 v88, v64, v65\n"
"v_mfma_f32_16x16x32_bf16 a[52:55], v[160:163], v[84:87], a[52:55]\n"
"v_exp_f32_e32 v66, v66\n"
"v_mfma_f32_16x16x32_bf16 a[68:71], v[164:167], v[84:87], a[68:71]\n"
"ds_read_b64_tr_b16 v[126:127], v16 offset:53504\n"
"v_mfma_f32_16x16x32_bf16 a[84:87], v[168:171], v[84:87], a[84:87]\n"
"v_exp_f32_e32 v67, v67\n"
"v_mfma_f32_16x16x32_bf16 a[100:103], v[172:175], v[84:87], a[100:103]\n"
"v_cvt_pk_bf16_f32 v89, v66, v67\n"
"v_mfma_f32_16x16x32_bf16 a[116:119], v[176:179], v[84:87], a[116:119]\n"
"v_exp_f32_e32 v68, v68\n"
"v_mfma_f32_16x16x32_bf16 v[100:103], v[112:115], v[84:87], v[100:103]\n"
"ds_read_b64_tr_b16 v[128:129], v13 offset:49408\n"
"v_mfma_f32_16x16x32_bf16 v[72:75], a[160:163], v[228:231], v[44:47]\n"
"v_exp_f32_e32 v69, v69\n"
"v_mfma_f32_16x16x32_bf16 v[72:75], a[164:167], v[232:235], v[72:75]\n"
"ds_read_b64_tr_b16 v[130:131], v13 offset:53504\n"
"v_cvt_pk_bf16_f32 v90, v68, v69\n"
"v_mfma_f32_16x16x32_bf16 v[72:75], a[168:171], v[236:239], v[72:75]\n"
"v_exp_f32_e32 v70, v70\n"
"v_mfma_f32_16x16x32_bf16 v[72:75], a[172:175], v[240:243], v[72:75]\n"
"v_mad_u32_u24 v31, v28, s43, v19\n"
"v_mfma_f32_16x16x32_bf16 v[76:79], a[176:179], v[228:231], v[44:47]\n"
"v_exp_f32_e32 v71, v71\n"
"v_mfma_f32_16x16x32_bf16 v[76:79], a[180:183], v[232:235], v[76:79]\n"
"ds_read_b64_tr_b16 v[132:133], v16 offset:49664\n"
"v_cvt_pk_bf16_f32 v91, v70, v71\n"
"v_mfma_f32_16x16x32_bf16 v[76:79], a[184:187], v[236:239], v[76:79]\n"
"ds_read_b64_tr_b16 v[134:135], v16 offset:53760\n"
"s_waitcnt lgkmcnt(5)\n"
"v_mfma_f32_16x16x32_bf16 v[76:79], a[188:191], v[240:243], v[76:79]\n"
"v_exp_f32_e32 v72, v72\n"
"v_mfma_f32_16x16x32_bf16 a[8:11], v[148:151], v[88:91], a[8:11]\n"
"buffer_load_dwordx4 v4, s[8:11], s38 offen offset:3072 lds\n"
"v_mfma_f32_16x16x32_bf16 a[24:27], v[152:155], v[88:91], a[24:27]\n"
"v_exp_f32_e32 v73, v73\n"
"v_mfma_f32_16x16x32_bf16 a[40:43], v[156:159], v[88:91], a[40:43]\n"
"ds_read_b64_tr_b16 v[136:137], v13 offset:49664\n"
"s_add_u32 s42, s42, 1\n"
"v_mfma_f32_16x16x32_bf16 a[56:59], v[160:163], v[88:91], a[56:59]\n"
"v_exp_f32_e32 v74, v74\n"
"v_mfma_f32_16x16x32_bf16 a[72:75], v[164:167], v[88:91], a[72:75]\n"
"ds_read_b64_tr_b16 v[138:139], v13 offset:53760\n"
"v_cvt_pk_bf16_f32 v92, v72, v73\n"
"v_mfma_f32_16x16x32_bf16 a[88:91], v[168:171], v[88:91], a[88:91]\n"
"v_exp_f32_e32 v75, v75\n"
"v_mfma_f32_16x16x32_bf16 a[104:107], v[172:175], v[88:91], a[104:107]\n"
"buffer_store_dwordx4 a[192:195], v31, s[28:31], s39 offen sc1\n"
"v_mfma_f32_16x16x32_bf16 a[120:123], v[176:179], v[88:91], a[120:123]\n"
"v_exp_f32_e32 v76, v76\n"
"v_mfma_f32_16x16x32_bf16 v[104:107], v[112:115], v[88:91], v[104:107]\n"
"ds_read_b64_tr_b16 v[140:141], v16 offset:49920\n"
"v_cvt_pk_bf16_f32 v93, v74, v75\n"
"v_mfma_f32_16x16x32_bf16 v[48:51], a[128:131], v[180:183], v[32:35]\n"
"v_exp_f32_e32 v77, v77\n"
"v_mfma_f32_16x16x32_bf16 v[48:51], a[132:135], v[184:187], v[48:51]\n"
"ds_read_b64_tr_b16 v[142:143], v16 offset:54016\n"
"v_cvt_pk_bf16_f32 v94, v76, v77\n"
"v_mfma_f32_16x16x32_bf16 v[48:51], a[136:139], v[188:191], v[48:51]\n"
"v_exp_f32_e32 v78, v78\n"
"v_mfma_f32_16x16x32_bf16 v[48:51], a[140:143], v[192:195], v[48:51]\n"
"s_add_u32 m0, s44, 81920\n"
"v_mfma_f32_16x16x32_bf16 v[52:55], a[144:147], v[180:183], v[32:35]\n"
"v_exp_f32_e32 v79, v79\n"
"v_mfma_f32_16x16x32_bf16 v[52:55], a[148:151], v[184:187], v[52:55]\n"
"ds_read_b64_tr_b16 v[144:145], v13 offset:49920\n"
"v_cvt_pk_bf16_f32 v95, v78, v79\n"
"v_mfma_f32_16x16x32_bf16 v[52:55], a[152:155], v[188:191], v[52:55]\n"
"ds_read_b64_tr_b16 v[146:147], v13 offset:54016\n"
"v_mfma_f32_16x16x32_bf16 v[52:55], a[156:159], v[192:195], v[52:55]\n"
"v_exp_f32_e32 v48, v48\n"
"v_mfma_f32_16x16x32_bf16 a[12:15], v[148:151], v[92:95], a[12:15]\n"
"v_readlane_b32 s43, v29, s42\n"
"v_readlane_b32 s39, v30, s42\n"
"v_mfma_f32_16x16x32_bf16 a[28:31], v[152:155], v[92:95], a[28:31]\n"
"v_exp_f32_e32 v49, v49\n"
"v_mfma_f32_16x16x32_bf16 a[44:47], v[156:159], v[92:95], a[44:47]\n"
"ds_read_b128 a[160:163], v8 offset:57344\n"
"v_cvt_pk_bf16_f32 v80, v48, v49\n"
"v_mfma_f32_16x16x32_bf16 a[60:63], v[160:163], v[92:95], a[60:63]\n"
"v_exp_f32_e32 v50, v50\n"
"v_mfma_f32_16x16x32_bf16 a[76:79], v[164:167], v[92:95], a[76:79]\n"
"ds_read_b128 a[164:167], v9 offset:57344\n"
"v_mfma_f32_16x16x32_bf16 a[92:95], v[168:171], v[92:95], a[92:95]\n"
"v_exp_f32_e32 v51, v51\n"
"v_mfma_f32_16x16x32_bf16 a[108:111], v[172:175], v[92:95], a[108:111]\n"
"buffer_load_dwordx4 v5, s[12:15], s38 offen lds\n"
"v_mfma_f32_16x16x32_bf16 a[124:127], v[176:179], v[92:95], a[124:127]\n"
"v_exp_f32_e32 v52, v52\n"
"v_mfma_f32_16x16x32_bf16 v[108:111], v[112:115], v[92:95], v[108:111]\n"
"ds_read_b128 a[168:171], v10 offset:57344\n"
"v_cvt_pk_bf16_f32 v81, v50, v51\n"
"v_mfma_f32_16x16x32_bf16 v[56:59], a[128:131], v[196:199], v[36:39]\n"
"v_exp_f32_e32 v53, v53\n"
"v_mfma_f32_16x16x32_bf16 v[56:59], a[132:135], v[200:203], v[56:59]\n"
"ds_read_b64_tr_b16 v[148:149], v16 offset:57344\n"
"v_cvt_pk_bf16_f32 v82, v52, v53\n"
"v_mfma_f32_16x16x32_bf16 v[56:59], a[136:139], v[204:207], v[56:59]\n"
"v_exp_f32_e32 v54, v54\n"
"v_mfma_f32_16x16x32_bf16 v[56:59], a[140:143], v[208:211], v[56:59]\n"
"buffer_load_dwordx4 a[212:215], v18, s[24:27], s41 offen\n"
"v_mfma_f32_16x16x32_bf16 v[60:63], a[144:147], v[196:199], v[36:39]\n"
"v_exp_f32_e32 v55, v55\n"
"v_mfma_f32_16x16x32_bf16 v[60:63], a[148:151], v[200:203], v[60:63]\n"
"ds_read_b128 a[172:175], v11 offset:57344\n"
"v_cvt_pk_bf16_f32 v83, v54, v55\n"
"v_mfma_f32_16x16x32_bf16 v[60:63], a[152:155], v[204:207], v[60:63]\n"
"ds_read_b64_tr_b16 v[150:151], v16 offset:61440\n"
"s_waitcnt lgkmcnt(6)\n"
"v_mfma_f32_16x16x32_bf16 v[60:63], a[156:159], v[208:211], v[60:63]\n"
"v_exp_f32_e32 v56, v56\n"
"v_mfma_f32_16x16x32_bf16 a[0:3], v[116:119], v[80:83], a[0:3]\n"
"buffer_load_dwordx4 v12, s[12:15], s38 offen offset:1024 lds\n"
"v_mfma_f32_16x16x32_bf16 a[16:19], v[120:123], v[80:83], a[16:19]\n"
"v_exp_f32_e32 v57, v57\n"
"v_mfma_f32_16x16x32_bf16 a[32:35], v[124:127], v[80:83], a[32:35]\n"
"ds_read_b128 a[176:179], v8 offset:61440\n"
"v_cvt_pk_bf16_f32 v84, v56, v57\n"
"v_mfma_f32_16x16x32_bf16 a[48:51], v[128:131], v[80:83], a[48:51]\n"
"v_exp_f32_e32 v58, v58\n"
"v_mfma_f32_16x16x32_bf16 a[64:67], v[132:135], v[80:83], a[64:67]\n"
"ds_read_b64_tr_b16 v[152:153], v13 offset:57344\n"
"s_add_u32 s41, s41, s47\n"
"v_mfma_f32_16x16x32_bf16 a[80:83], v[136:139], v[80:83], a[80:83]\n"
"v_exp_f32_e32 v59, v59\n"
"v_mfma_f32_16x16x32_bf16 a[96:99], v[140:143], v[80:83], a[96:99]\n"
"v_cvt_pk_bf16_f32 v85, v58, v59\n"
"v_mfma_f32_16x16x32_bf16 a[112:115], v[144:147], v[80:83], a[112:115]\n"
"v_exp_f32_e32 v60, v60\n"
"v_mfma_f32_16x16x32_bf16 v[96:99], v[112:115], v[80:83], v[96:99]\n"
"ds_read_b128 a[180:183], v9 offset:61440\n"
"v_mfma_f32_16x16x32_bf16 v[64:67], a[128:131], v[212:215], v[40:43]\n"
"v_exp_f32_e32 v61, v61\n"
"v_mfma_f32_16x16x32_bf16 v[64:67], a[132:135], v[216:219], v[64:67]\n"
"ds_read_b64_tr_b16 v[154:155], v13 offset:61440\n"
"v_cvt_pk_bf16_f32 v86, v60, v61\n"
"v_mfma_f32_16x16x32_bf16 v[64:67], a[136:139], v[220:223], v[64:67]\n"
"v_exp_f32_e32 v62, v62\n"
"v_mfma_f32_16x16x32_bf16 v[64:67], a[140:143], v[224:227], v[64:67]\n"
"v_mfma_f32_16x16x32_bf16 v[68:71], a[144:147], v[212:215], v[40:43]\n"
"v_exp_f32_e32 v63, v63\n"
"v_mfma_f32_16x16x32_bf16 v[68:71], a[148:151], v[216:219], v[68:71]\n"
"ds_read_b128 a[184:187], v10 offset:61440\n"
"v_cvt_pk_bf16_f32 v87, v62, v63\n"
"v_mfma_f32_16x16x32_bf16 v[68:71], a[152:155], v[220:223], v[68:71]\n"
"ds_read_b64_tr_b16 v[156:157], v16 offset:57600\n"
"v_mfma_f32_16x16x32_bf16 v[68:71], a[156:159], v[224:227], v[68:71]\n"
"v_exp_f32_e32 v64, v64\n"
"v_mfma_f32_16x16x32_bf16 a[4:7], v[116:119], v[84:87], a[4:7]\n"
"buffer_load_dwordx4 v5, s[12:15], s38 offen offset:2048 lds\n"
"v_mfma_f32_16x16x32_bf16 a[20:23], v[120:123], v[84:87], a[20:23]\n"
"v_exp_f32_e32 v65, v65\n"
"v_mfma_f32_16x16x32_bf16 a[36:39], v[124:127], v[84:87], a[36:39]\n"
"ds_read_b128 a[188:191], v11 offset:61440\n"
"v_cvt_pk_bf16_f32 v88, v64, v65\n"
"v_mfma_f32_16x16x32_bf16 a[52:55], v[128:131], v[84:87], a[52:55]\n"
"v_exp_f32_e32 v66, v66\n"
"v_mfma_f32_16x16x32_bf16 a[68:71], v[132:135], v[84:87], a[68:71]\n"
"ds_read_b64_tr_b16 v[158:159], v16 offset:61696\n"
"v_mfma_f32_16x16x32_bf16 a[84:87], v[136:139], v[84:87], a[84:87]\n"
"v_exp_f32_e32 v67, v67\n"
"v_mfma_f32_16x16x32_bf16 a[100:103], v[140:143], v[84:87], a[100:103]\n"
"v_cvt_pk_bf16_f32 v89, v66, v67\n"
"v_mfma_f32_16x16x32_bf16 a[116:119], v[144:147], v[84:87], a[116:119]\n"
"v_exp_f32_e32 v68, v68\n"
"v_mfma_f32_16x16x32_bf16 v[100:103], v[112:115], v[84:87], v[100:103]\n"
"ds_read_b64_tr_b16 v[160:161], v13 offset:57600\n"
"v_mfma_f32_16x16x32_bf16 v[72:75], a[128:131], v[228:231], v[44:47]\n"
"v_exp_f32_e32 v69, v69\n"
"v_mfma_f32_16x16x32_bf16 v[72:75], a[132:135], v[232:235], v[72:75]\n"
"ds_read_b64_tr_b16 v[162:163], v13 offset:61696\n"
"v_cvt_pk_bf16_f32 v90, v68, v69\n"
"v_mfma_f32_16x16x32_bf16 v[72:75], a[136:139], v[236:239], v[72:75]\n"
"v_exp_f32_e32 v70, v70\n"
"v_mfma_f32_16x16x32_bf16 v[72:75], a[140:143], v[240:243], v[72:75]\n"
"v_mad_u32_u24 v31, v28, s43, v19\n"
"v_mfma_f32_16x16x32_bf16 v[76:79], a[144:147], v[228:231], v[44:47]\n"
"v_exp_f32_e32 v71, v71\n"
"v_mfma_f32_16x16x32_bf16 v[76:79], a[148:151], v[232:235], v[76:79]\n"
"ds_read_b64_tr_b16 v[164:165], v16 offset:57856\n"
"v_cvt_pk_bf16_f32 v91, v70, v71\n"
"v_mfma_f32_16x16x32_bf16 v[76:79], a[152:155], v[236:239], v[76:79]\n"
"ds_read_b64_tr_b16 v[166:167], v16 offset:61952\n"
"s_waitcnt lgkmcnt(5)\n"
"v_mfma_f32_16x16x32_bf16 v[76:79], a[156:159], v[240:243], v[76:79]\n"
"v_exp_f32_e32 v72, v72\n"
"v_mfma_f32_16x16x32_bf16 a[8:11], v[116:119], v[88:91], a[8:11]\n"
"buffer_load_dwordx4 v12, s[12:15], s38 offen offset:3072 lds\n"
"v_mfma_f32_16x16x32_bf16 a[24:27], v[120:123], v[88:91], a[24:27]\n"
"v_exp_f32_e32 v73, v73\n"
"v_mfma_f32_16x16x32_bf16 a[40:43], v[124:127], v[88:91], a[40:43]\n"
"ds_read_b64_tr_b16 v[168:169], v13 offset:57856\n"
"s_add_u32 s42, s42, 1\n"
"v_mfma_f32_16x16x32_bf16 a[56:59], v[128:131], v[88:91], a[56:59]\n"
"v_exp_f32_e32 v74, v74\n"
"v_mfma_f32_16x16x32_bf16 a[72:75], v[132:135], v[88:91], a[72:75]\n"
"ds_read_b64_tr_b16 v[170:171], v13 offset:61952\n"
"v_cvt_pk_bf16_f32 v92, v72, v73\n"
"v_mfma_f32_16x16x32_bf16 a[88:91], v[136:139], v[88:91], a[88:91]\n"
"v_exp_f32_e32 v75, v75\n"
"v_mfma_f32_16x16x32_bf16 a[104:107], v[140:143], v[88:91], a[104:107]\n"
"buffer_store_dwordx4 a[196:199], v31, s[28:31], s39 offen sc1\n"
"v_mfma_f32_16x16x32_bf16 a[120:123], v[144:147], v[88:91], a[120:123]\n"
"v_exp_f32_e32 v76, v76\n"
"v_mfma_f32_16x16x32_bf16 v[104:107], v[112:115], v[88:91], v[104:107]\n"
"ds_read_b64_tr_b16 v[172:173], v16 offset:58112\n"
"v_cvt_pk_bf16_f32 v93, v74, v75\n"
"v_mfma_f32_16x16x32_bf16 v[48:51], a[160:163], v[180:183], v[32:35]\n"
"v_exp_f32_e32 v77, v77\n"
"v_mfma_f32_16x16x32_bf16 v[48:51], a[164:167], v[184:187], v[48:51]\n"
"ds_read_b64_tr_b16 v[174:175], v16 offset:62208\n"
"v_cvt_pk_bf16_f32 v94, v76, v77\n"
"v_mfma_f32_16x16x32_bf16 v[48:51], a[168:171], v[188:191], v[48:51]\n"
"v_exp_f32_e32 v78, v78\n"
"v_mfma_f32_16x16x32_bf16 v[48:51], a[172:175], v[192:195], v[48:51]\n"
"s_waitcnt vmcnt(12)\n"
"s_barrier\n"
"v_mfma_f32_16x16x32_bf16 v[52:55], a[176:179], v[180:183], v[32:35]\n"
"v_exp_f32_e32 v79, v79\n"
"v_mfma_f32_16x16x32_bf16 v[52:55], a[180:183], v[184:187], v[52:55]\n"
"ds_read_b64_tr_b16 v[176:177], v13 offset:58112\n"
"v_cvt_pk_bf16_f32 v95, v78, v79\n"
"v_mfma_f32_16x16x32_bf16 v[52:55], a[184:187], v[188:191], v[52:55]\n"
"ds_read_b64_tr_b16 v[178:179], v13 offset:62208\n"
"s_add_u32 s38, s38, 16384\n"
"v_mfma_f32_16x16x32_bf16 v[52:55], a[188:191], v[192:195], v[52:55]\n"
"v_exp_f32_e32 v48, v48\n"
"v_mfma_f32_16x16x32_bf16 a[12:15], v[116:119], v[92:95], a[12:15]\n"
"s_add_u32 m0, s44, 32768\n"
"v_readlane_b32 s43, v29, s42\n"
"v_mfma_f32_16x16x32_bf16 a[28:31], v[120:123], v[92:95], a[28:31]\n"
"v_exp_f32_e32 v49, v49\n"
"v_mfma_f32_16x16x32_bf16 a[44:47], v[124:127], v[92:95], a[44:47]\n"
"ds_read_b128 a[128:131], v8 offset:0\n"
"v_cvt_pk_bf16_f32 v80, v48, v49\n"
"v_mfma_f32_16x16x32_bf16 a[60:63], v[128:131], v[92:95], a[60:63]\n"
"v_exp_f32_e32 v50, v50\n"
"v_mfma_f32_16x16x32_bf16 a[76:79], v[132:135], v[92:95], a[76:79]\n"
"ds_read_b128 a[132:135], v9 offset:0\n"
"v_readlane_b32 s39, v30, s42\n"
"v_mfma_f32_16x16x32_bf16 a[92:95], v[136:139], v[92:95], a[92:95]\n"
"v_exp_f32_e32 v51, v51\n"
"v_mfma_f32_16x16x32_bf16 a[108:111], v[140:143], v[92:95], a[108:111]\n"
"buffer_load_dwordx4 v1, s[8:11], s38 offen lds\n"
"v_mfma_f32_16x16x32_bf16 a[124:127], v[144:147], v[92:95], a[124:127]\n"
"v_exp_f32_e32 v52, v52\n"
"v_mfma_f32_16x16x32_bf16 v[108:111], v[112:115], v[92:95], v[108:111]\n"
"ds_read_b128 a[136:139], v10 offset:0\n"
"v_cvt_pk_bf16_f32 v81, v50, v51\n"
"v_mfma_f32_16x16x32_bf16 v[56:59], a[160:163], v[196:199], v[36:39]\n"
"v_exp_f32_e32 v53, v53\n"
"v_mfma_f32_16x16x32_bf16 v[56:59], a[164:167], v[200:203], v[56:59]\n"
"ds_read_b64_tr_b16 v[116:117], v16 offset:0\n"
"v_cvt_pk_bf16_f32 v82, v52, v53\n"
"v_mfma_f32_16x16x32_bf16 v[56:59], a[168:171], v[204:207], v[56:59]\n"
"v_exp_f32_e32 v54, v54\n"
"v_mfma_f32_16x16x32_bf16 v[56:59], a[172:175], v[208:211], v[56:59]\n"
"s_waitcnt lgkmcnt(4)\n"
"v_mfma_f32_16x16x32_bf16 v[60:63], a[176:179], v[196:199], v[36:39]\n"
"v_exp_f32_e32 v55, v55\n"
"v_mfma_f32_16x16x32_bf16 v[60:63], a[180:183], v[200:203], v[60:63]\n"
"ds_read_b128 a[140:143], v11 offset:0\n"
"v_cvt_pk_bf16_f32 v83, v54, v55\n"
"v_mfma_f32_16x16x32_bf16 v[60:63], a[184:187], v[204:207], v[60:63]\n"
"ds_read_b64_tr_b16 v[118:119], v16 offset:4096\n"
"v_mfma_f32_16x16x32_bf16 v[60:63], a[188:191], v[208:211], v[60:63]\n"
"v_exp_f32_e32 v56, v56\n"
"v_mfma_f32_16x16x32_bf16 a[0:3], v[148:151], v[80:83], a[0:3]\n"
"buffer_load_dwordx4 v2, s[8:11], s38 offen offset:1024 lds\n"
"v_mfma_f32_16x16x32_bf16 a[16:19], v[152:155], v[80:83], a[16:19]\n"
"v_exp_f32_e32 v57, v57\n"
"v_mfma_f32_16x16x32_bf16 a[32:35], v[156:159], v[80:83], a[32:35]\n"
"ds_read_b128 a[144:147], v8 offset:4096\n"
"v_cvt_pk_bf16_f32 v84, v56, v57\n"
"v_mfma_f32_16x16x32_bf16 a[48:51], v[160:163], v[80:83], a[48:51]\n"
"v_exp_f32_e32 v58, v58\n"
"v_mfma_f32_16x16x32_bf16 a[64:67], v[164:167], v[80:83], a[64:67]\n"
"ds_read_b64_tr_b16 v[120:121], v13 offset:0\n"
"v_mfma_f32_16x16x32_bf16 a[80:83], v[168:171], v[80:83], a[80:83]\n"
"v_exp_f32_e32 v59, v59\n"
"v_mfma_f32_16x16x32_bf16 a[96:99], v[172:175], v[80:83], a[96:99]\n"
"v_cvt_pk_bf16_f32 v85, v58, v59\n"
"v_mfma_f32_16x16x32_bf16 a[112:115], v[176:179], v[80:83], a[112:115]\n"
"v_exp_f32_e32 v60, v60\n"
"v_mfma_f32_16x16x32_bf16 v[96:99], v[112:115], v[80:83], v[96:99]\n"
"ds_read_b128 a[148:151], v9 offset:4096\n"
"v_mfma_f32_16x16x32_bf16 v[64:67], a[160:163], v[212:215], v[40:43]\n"
"v_exp_f32_e32 v61, v61\n"
"v_mfma_f32_16x16x32_bf16 v[64:67], a[164:167], v[216:219], v[64:67]\n"
"ds_read_b64_tr_b16 v[122:123], v13 offset:4096\n"
"v_cvt_pk_bf16_f32 v86, v60, v61\n"
"v_mfma_f32_16x16x32_bf16 v[64:67], a[168:171], v[220:223], v[64:67]\n"
"v_exp_f32_e32 v62, v62\n"
"v_mfma_f32_16x16x32_bf16 v[64:67], a[172:175], v[224:227], v[64:67]\n"
"v_mfma_f32_16x16x32_bf16 v[68:71], a[176:179], v[212:215], v[40:43]\n"
"v_exp_f32_e32 v63, v63\n"
"v_mfma_f32_16x16x32_bf16 v[68:71], a[180:183], v[216:219], v[68:71]\n"
"ds_read_b128 a[152:155], v10 offset:4096\n"
"v_cvt_pk_bf16_f32 v87, v62, v63\n"
"v_mfma_f32_16x16x32_bf16 v[68:71], a[184:187], v[220:223], v[68:71]\n"
"ds_read_b64_tr_b16 v[124:125], v16 offset:256\n"
"v_mfma_f32_16x16x32_bf16 v[68:71], a[188:191], v[224:227], v[68:71]\n"
"v_exp_f32_e32 v64, v64\n"
"v_mfma_f32_16x16x32_bf16 a[4:7], v[148:151], v[84:87], a[4:7]\n"
"buffer_load_dwordx4 v3, s[8:11], s38 offen offset:2048 lds\n"
"v_mfma_f32_16x16x32_bf16 a[20:23], v[152:155], v[84:87], a[20:23]\n"
"v_exp_f32_e32 v65, v65\n"
"v_mfma_f32_16x16x32_bf16 a[36:39], v[156:159], v[84:87], a[36:39]\n"
"ds_read_b128 a[156:159], v11 offset:4096\n"
"v_cvt_pk_bf16_f32 v88, v64, v65\n"
"v_mfma_f32_16x16x32_bf16 a[52:55], v[160:163], v[84:87], a[52:55]\n"
"v_exp_f32_e32 v66, v66\n"
"v_mfma_f32_16x16x32_bf16 a[68:71], v[164:167], v[84:87], a[68:71]\n"
"ds_read_b64_tr_b16 v[126:127], v16 offset:4352\n"
"v_mfma_f32_16x16x32_bf16 a[84:87], v[168:171], v[84:87], a[84:87]\n"
"v_exp_f32_e32 v67, v67\n"
"v_mfma_f32_16x16x32_bf16 a[100:103], v[172:175], v[84:87], a[100:103]\n"
"v_cvt_pk_bf16_f32 v89, v66, v67\n"
"v_mfma_f32_16x16x32_bf16 a[116:119], v[176:179], v[84:87], a[116:119]\n"
"v_exp_f32_e32 v68, v68\n"
"v_mfma_f32_16x16x32_bf16 v[100:103], v[112:115], v[84:87], v[100:103]\n"
"ds_read_b64_tr_b16 v[128:129], v13 offset:256\n"
"v_mfma_f32_16x16x32_bf16 v[72:75], a[160:163], v[228:231], v[44:47]\n"
"v_exp_f32_e32 v69, v69\n"
"v_mfma_f32_16x16x32_bf16 v[72:75], a[164:167], v[232:235], v[72:75]\n"
"ds_read_b64_tr_b16 v[130:131], v13 offset:4352\n"
"v_cvt_pk_bf16_f32 v90, v68, v69\n"
"v_mfma_f32_16x16x32_bf16 v[72:75], a[168:171], v[236:239], v[72:75]\n"
"v_exp_f32_e32 v70, v70\n"
"v_mfma_f32_16x16x32_bf16 v[72:75], a[172:175], v[240:243], v[72:75]\n"
"v_mad_u32_u24 v31, v28, s43, v19\n"
"v_mfma_f32_16x16x32_bf16 v[76:79], a[176:179], v[228:231], v[44:47]\n"
"v_exp_f32_e32 v71, v71\n"
"v_mfma_f32_16x16x32_bf16 v[76:79], a[180:183], v[232:235], v[76:79]\n"
"ds_read_b64_tr_b16 v[132:133], v16 offset:512\n"
"v_cvt_pk_bf16_f32 v91, v70, v71\n"
"v_mfma_f32_16x16x32_bf16 v[76:79], a[184:187], v[236:239], v[76:79]\n"
"ds_read_b64_tr_b16 v[134:135], v16 offset:4608\n"
"s_waitcnt lgkmcnt(5)\n"
"v_mfma_f32_16x16x32_bf16 v[76:79], a[188:191], v[240:243], v[76:79]\n"
"v_exp_f32_e32 v72, v72\n"
"v_mfma_f32_16x16x32_bf16 a[8:11], v[148:151], v[88:91], a[8:11]\n"
"buffer_load_dwordx4 v4, s[8:11], s38 offen offset:3072 lds\n"
"v_mfma_f32_16x16x32_bf16 a[24:27], v[152:155], v[88:91], a[24:27]\n"
"v_exp_f32_e32 v73, v73\n"
"v_mfma_f32_16x16x32_bf16 a[40:43], v[156:159], v[88:91], a[40:43]\n"
"ds_read_b64_tr_b16 v[136:137], v13 offset:512\n"
"s_add_u32 s42, s42, 1\n"
"v_mfma_f32_16x16x32_bf16 a[56:59], v[160:163], v[88:91], a[56:59]\n"
"v_exp_f32_e32 v74, v74\n"
"v_mfma_f32_16x16x32_bf16 a[72:75], v[164:167], v[88:91], a[72:75]\n"
"ds_read_b64_tr_b16 v[138:139], v13 offset:4608\n"
"v_cvt_pk_bf16_f32 v92, v72, v73\n"
"v_mfma_f32_16x16x32_bf16 a[88:91], v[168:171], v[88:91], a[88:91]\n"
"v_exp_f32_e32 v75, v75\n"
"v_mfma_f32_16x16x32_bf16 a[104:107], v[172:175], v[88:91], a[104:107]\n"
"buffer_store_dwordx4 a[200:203], v31, s[28:31], s39 offen sc1\n"
"v_mfma_f32_16x16x32_bf16 a[120:123], v[176:179], v[88:91], a[120:123]\n"
"v_exp_f32_e32 v76, v76\n"
"v_mfma_f32_16x16x32_bf16 v[104:107], v[112:115], v[88:91], v[104:107]\n"
"ds_read_b64_tr_b16 v[140:141], v16 offset:768\n"
"v_cvt_pk_bf16_f32 v93, v74, v75\n"
"s_mov_b32 s40, 6\n"
"Lattn_loop_%=:\n"
"v_mfma_f32_16x16x32_bf16 v[48:51], a[128:131], v[180:183], v[32:35]\n"
"v_exp_f32_e32 v77, v77\n"
"v_mfma_f32_16x16x32_bf16 v[48:51], a[132:135], v[184:187], v[48:51]\n"
"ds_read_b64_tr_b16 v[142:143], v16 offset:4864\n"
"v_cvt_pk_bf16_f32 v94, v76, v77\n"
"v_mfma_f32_16x16x32_bf16 v[48:51], a[136:139], v[188:191], v[48:51]\n"
"v_exp_f32_e32 v78, v78\n"
"v_mfma_f32_16x16x32_bf16 v[48:51], a[140:143], v[192:195], v[48:51]\n"
"s_add_u32 m0, s44, 98304\n"
"v_mfma_f32_16x16x32_bf16 v[52:55], a[144:147], v[180:183], v[32:35]\n"
"v_exp_f32_e32 v79, v79\n"
"v_mfma_f32_16x16x32_bf16 v[52:55], a[148:151], v[184:187], v[52:55]\n"
"ds_read_b64_tr_b16 v[144:145], v13 offset:768\n"
"v_cvt_pk_bf16_f32 v95, v78, v79\n"
"v_mfma_f32_16x16x32_bf16 v[52:55], a[152:155], v[188:191], v[52:55]\n"
"ds_read_b64_tr_b16 v[146:147], v13 offset:4864\n"
"v_mfma_f32_16x16x32_bf16 v[52:55], a[156:159], v[192:195], v[52:55]\n"
"v_exp_f32_e32 v48, v48\n"
"v_mfma_f32_16x16x32_bf16 a[12:15], v[148:151], v[92:95], a[12:15]\n"
"v_mfma_f32_16x16x32_bf16 a[28:31], v[152:155], v[92:95], a[28:31]\n"
"v_exp_f32_e32 v49, v49\n"
"v_mfma_f32_16x16x32_bf16 a[44:47], v[156:159], v[92:95], a[44:47]\n"
"ds_read_b128 a[160:163], v8 offset:8192\n"
"v_cvt_pk_bf16_f32 v80, v48, v49\n"
"v_mfma_f32_16x16x32_bf16 a[60:63], v[160:163], v[92:95], a[60:63]\n"
"v_exp_f32_e32 v50, v50\n"
"v_mfma_f32_16x16x32_bf16 a[76:79], v[164:167], v[92:95], a[76:79]\n"
"ds_read_b128 a[164:167], v9 offset:8192\n"
"v_mfma_f32_16x16x32_bf16 a[92:95], v[168:171], v[92:95], a[92:95]\n"
"v_exp_f32_e32 v51, v51\n"
"v_mfma_f32_16x16x32_bf16 a[108:111], v[172:175], v[92:95], a[108:111]\n"
"buffer_load_dwordx4 v5, s[12:15], s38 offen lds\n"
"v_mfma_f32_16x16x32_bf16 a[124:127], v[176:179], v[92:95], a[124:127]\n"
"v_exp_f32_e32 v52, v52\n"
"v_mfma_f32_16x16x32_bf16 v[108:111], v[112:115], v[92:95], v[108:111]\n"
"ds_read_b128 a[168:171], v10 offset:8192\n"
"v_cvt_pk_bf16_f32 v81, v50, v51\n"
"v_mfma_f32_16x16x32_bf16 v[56:59], a[128:131], v[196:199], v[36:39]\n"
"v_exp_f32_e32 v53, v53\n"
"v_mfma_f32_16x16x32_bf16 v[56:59], a[132:135], v[200:203], v[56:59]\n"
"ds_read_b64_tr_b16 v[148:149], v16 offset:8192\n"
"v_cvt_pk_bf16_f32 v82, v52, v53\n"
"v_mfma_f32_16x16x32_bf16 v[56:59], a[136:139], v[204:207], v[56:59]\n"
"v_exp_f32_e32 v54, v54\n"
"v_mfma_f32_16x16x32_bf16 v[56:59], a[140:143], v[208:211], v[56:59]\n"
"buffer_load_dwordx4 a[192:195], v18, s[24:27], s41 offen\n"
"v_mfma_f32_16x16x32_bf16 v[60:63], a[144:147], v[196:199], v[36:39]\n"
"v_exp_f32_e32 v55, v55\n"
"v_mfma_f32_16x16x32_bf16 v[60:63], a[148:151], v[200:203], v[60:63]\n"
"ds_read_b128 a[172:175], v11 offset:8192\n"
"v_cvt_pk_bf16_f32 v83, v54, v55\n"
"v_mfma_f32_16x16x32_bf16 v[60:63], a[152:155], v[204:207], v[60:63]\n"
"ds_read_b64_tr_b16 v[150:151], v16 offset:12288\n"
"s_waitcnt lgkmcnt(6)\n"
"v_mfma_f32_16x16x32_bf16 v[60:63], a[156:159], v[208:211], v[60:63]\n"
"v_exp_f32_e32 v56, v56\n"
"v_mfma_f32_16x16x32_bf16 a[0:3], v[116:119], v[80:83], a[0:3]\n"
"buffer_load_dwordx4 v12, s[12:15], s38 offen offset:1024 lds\n"
"v_mfma_f32_16x16x32_bf16 a[16:19], v[120:123], v[80:83], a[16:19]\n"
"v_exp_f32_e32 v57, v57\n"
"v_mfma_f32_16x16x32_bf16 a[32:35], v[124:127], v[80:83], a[32:35]\n"
"ds_read_b128 a[176:179], v8 offset:12288\n"
"v_cvt_pk_bf16_f32 v84, v56, v57\n"
"v_mfma_f32_16x16x32_bf16 a[48:51], v[128:131], v[80:83], a[48:51]\n"
"v_exp_f32_e32 v58, v58\n"
"v_mfma_f32_16x16x32_bf16 a[64:67], v[132:135], v[80:83], a[64:67]\n"
"ds_read_b64_tr_b16 v[152:153], v13 offset:8192\n"
"s_add_u32 s41, s41, s47\n"
"v_mfma_f32_16x16x32_bf16 a[80:83], v[136:139], v[80:83], a[80:83]\n"
"v_exp_f32_e32 v59, v59\n"
"v_mfma_f32_16x16x32_bf16 a[96:99], v[140:143], v[80:83], a[96:99]\n"
"v_cvt_pk_bf16_f32 v85, v58, v59\n"
"v_mfma_f32_16x16x32_bf16 a[112:115], v[144:147], v[80:83], a[112:115]\n"
"v_exp_f32_e32 v60, v60\n"
"v_mfma_f32_16x16x32_bf16 v[96:99], v[112:115], v[80:83], v[96:99]\n"
"ds_read_b128 a[180:183], v9 offset:12288\n"
"v_mfma_f32_16x16x32_bf16 v[64:67], a[128:131], v[212:215], v[40:43]\n"
"v_exp_f32_e32 v61, v61\n"
"v_mfma_f32_16x16x32_bf16 v[64:67], a[132:135], v[216:219], v[64:67]\n"
"ds_read_b64_tr_b16 v[154:155], v13 offset:12288\n"
"v_cvt_pk_bf16_f32 v86, v60, v61\n"
"v_mfma_f32_16x16x32_bf16 v[64:67], a[136:139], v[220:223], v[64:67]\n"
"v_exp_f32_e32 v62, v62\n"
"v_mfma_f32_16x16x32_bf16 v[64:67], a[140:143], v[224:227], v[64:67]\n"
"v_mfma_f32_16x16x32_bf16 v[68:71], a[144:147], v[212:215], v[40:43]\n"
"v_exp_f32_e32 v63, v63\n"
"v_mfma_f32_16x16x32_bf16 v[68:71], a[148:151], v[216:219], v[68:71]\n"
"ds_read_b128 a[184:187], v10 offset:12288\n"
"v_cvt_pk_bf16_f32 v87, v62, v63\n"
"v_mfma_f32_16x16x32_bf16 v[68:71], a[152:155], v[220:223], v[68:71]\n"
"ds_read_b64_tr_b16 v[156:157], v16 offset:8448\n"
"v_mfma_f32_16x16x32_bf16 v[68:71], a[156:159], v[224:227], v[68:71]\n"
"v_exp_f32_e32 v64, v64\n"
"v_mfma_f32_16x16x32_bf16 a[4:7], v[116:119], v[84:87], a[4:7]\n"
"buffer_load_dwordx4 v5, s[12:15], s38 offen offset:2048 lds\n"
"v_mfma_f32_16x16x32_bf16 a[20:23], v[120:123], v[84:87], a[20:23]\n"
"v_exp_f32_e32 v65, v65\n"
"v_mfma_f32_16x16x32_bf16 a[36:39], v[124:127], v[84:87], a[36:39]\n"
"ds_read_b128 a[188:191], v11 offset:12288\n"
"v_cvt_pk_bf16_f32 v88, v64, v65\n"
"v_mfma_f32_16x16x32_bf16 a[52:55], v[128:131], v[84:87], a[52:55]\n"
"v_exp_f32_e32 v66, v66\n"
"v_mfma_f32_16x16x32_bf16 a[68:71], v[132:135], v[84:87], a[68:71]\n"
"ds_read_b64_tr_b16 v[158:159], v16 offset:12544\n"
"v_mfma_f32_16x16x32_bf16 a[84:87], v[136:139], v[84:87], a[84:87]\n"
"v_exp_f32_e32 v67, v67\n"
"v_mfma_f32_16x16x32_bf16 a[100:103], v[140:143], v[84:87], a[100:103]\n"
"v_cvt_pk_bf16_f32 v89, v66, v67\n"
"v_mfma_f32_16x16x32_bf16 a[116:119], v[144:147], v[84:87], a[116:119]\n"
"v_exp_f32_e32 v68, v68\n"
"v_mfma_f32_16x16x32_bf16 v[100:103], v[112:115], v[84:87], v[100:103]\n"
"ds_read_b64_tr_b16 v[160:161], v13 offset:8448\n"
"v_mfma_f32_16x16x32_bf16 v[72:75], a[128:131], v[228:231], v[44:47]\n"
"v_exp_f32_e32 v69, v69\n"
"v_mfma_f32_16x16x32_bf16 v[72:75], a[132:135], v[232:235], v[72:75]\n"
"ds_read_b64_tr_b16 v[162:163], v13 offset:12544\n"
"v_cvt_pk_bf16_f32 v90, v68, v69\n"
"v_mfma_f32_16x16x32_bf16 v[72:75], a[136:139], v[236:239], v[72:75]\n"
"v_exp_f32_e32 v70, v70\n"
"v_mfma_f32_16x16x32_bf16 v[72:75], a[140:143], v[240:243], v[72:75]\n"
"v_mfma_f32_16x16x32_bf16 v[76:79], a[144:147], v[228:231], v[44:47]\n"
"v_exp_f32_e32 v71, v71\n"
"v_mfma_f32_16x16x32_bf16 v[76:79], a[148:151], v[232:235], v[76:79]\n"
"ds_read_b64_tr_b16 v[164:165], v16 offset:8704\n"
"v_cvt_pk_bf16_f32 v91, v70, v71\n"
"v_mfma_f32_16x16x32_bf16 v[76:79], a[152:155], v[236:239], v[76:79]\n"
"ds_read_b64_tr_b16 v[166:167], v16 offset:12800\n"
"s_waitcnt lgkmcnt(5)\n"
"v_mfma_f32_16x16x32_bf16 v[76:79], a[156:159], v[240:243], v[76:79]\n"
"v_exp_f32_e32 v72, v72\n"
"v_mfma_f32_16x16x32_bf16 a[8:11], v[116:119], v[88:91], a[8:11]\n"
"buffer_load_dwordx4 v12, s[12:15], s38 offen offset:3072 lds\n"
"v_mfma_f32_16x16x32_bf16 a[24:27], v[120:123], v[88:91], a[24:27]\n"
"v_exp_f32_e32 v73, v73\n"
"v_mfma_f32_16x16x32_bf16 a[40:43], v[124:127], v[88:91], a[40:43]\n"
"ds_read_b64_tr_b16 v[168:169], v13 offset:8704\n"
"v_cvt_pk_bf16_f32 v92, v72, v73\n"
"v_mfma_f32_16x16x32_bf16 a[56:59], v[128:131], v[88:91], a[56:59]\n"
"v_exp_f32_e32 v74, v74\n"
"v_mfma_f32_16x16x32_bf16 a[72:75], v[132:135], v[88:91], a[72:75]\n"
"ds_read_b64_tr_b16 v[170:171], v13 offset:12800\n"
"v_mfma_f32_16x16x32_bf16 a[88:91], v[136:139], v[88:91], a[88:91]\n"
"v_exp_f32_e32 v75, v75\n"
"v_mfma_f32_16x16x32_bf16 a[104:107], v[140:143], v[88:91], a[104:107]\n"
"v_cvt_pk_bf16_f32 v93, v74, v75\n"
"v_mfma_f32_16x16x32_bf16 a[120:123], v[144:147], v[88:91], a[120:123]\n"
"v_exp_f32_e32 v76, v76\n"
"v_mfma_f32_16x16x32_bf16 v[104:107], v[112:115], v[88:91], v[104:107]\n"
"ds_read_b64_tr_b16 v[172:173], v16 offset:8960\n"
"v_mfma_f32_16x16x32_bf16 v[48:51], a[160:163], v[180:183], v[32:35]\n"
"v_exp_f32_e32 v77, v77\n"
"v_mfma_f32_16x16x32_bf16 v[48:51], a[164:167], v[184:187], v[48:51]\n"
"ds_read_b64_tr_b16 v[174:175], v16 offset:13056\n"
"v_cvt_pk_bf16_f32 v94, v76, v77\n"
"v_mfma_f32_16x16x32_bf16 v[48:51], a[168:171], v[188:191], v[48:51]\n"
"v_exp_f32_e32 v78, v78\n"
"v_mfma_f32_16x16x32_bf16 v[48:51], a[172:175], v[192:195], v[48:51]\n"
"s_waitcnt vmcnt(11)\n"
"s_barrier\n"
"v_mfma_f32_16x16x32_bf16 v[52:55], a[176:179], v[180:183], v[32:35]\n"
"v_exp_f32_e32 v79, v79\n"
"v_mfma_f32_16x16x32_bf16 v[52:55], a[180:183], v[184:187], v[52:55]\n"
"ds_read_b64_tr_b16 v[176:177], v13 offset:8960\n"
"v_cvt_pk_bf16_f32 v95, v78, v79\n"
"v_mfma_f32_16x16x32_bf16 v[52:55], a[184:187], v[188:191], v[52:55]\n"
"ds_read_b64_tr_b16 v[178:179], v13 offset:13056\n"
"s_add_u32 s38, s38, 16384\n"
"v_mfma_f32_16x16x32_bf16 v[52:55], a[188:191], v[192:195], v[52:55]\n"
"v_exp_f32_e32 v48, v48\n"
"v_mfma_f32_16x16x32_bf16 a[12:15], v[116:119], v[92:95], a[12:15]\n"
"s_add_u32 m0, s44, 49152\n"
"v_readlane_b32 s43, v29, s42\n"
"v_mfma_f32_16x16x32_bf16 a[28:31], v[120:123], v[92:95], a[28:31]\n"
"v_exp_f32_e32 v49, v49\n"
"v_mfma_f32_16x16x32_bf16 a[44:47], v[124:127], v[92:95], a[44:47]\n"
"ds_read_b128 a[128:131], v8 offset:16384\n"
"v_cvt_pk_bf16_f32 v80, v48, v49\n"
"v_mfma_f32_16x16x32_bf16 a[60:63], v[128:131], v[92:95], a[60:63]\n"
"v_exp_f32_e32 v50, v50\n"
"v_mfma_f32_16x16x32_bf16 a[76:79], v[132:135], v[92:95], a[76:79]\n"
"ds_read_b128 a[132:135], v9 offset:16384\n"
"v_readlane_b32 s39, v30, s42\n"
"v_mfma_f32_16x16x32_bf16 a[92:95], v[136:139], v[92:95], a[92:95]\n"
"v_exp_f32_e32 v51, v51\n"
"v_mfma_f32_16x16x32_bf16 a[108:111], v[140:143], v[92:95], a[108:111]\n"
"buffer_load_dwordx4 v1, s[8:11], s38 offen lds\n"
"v_mfma_f32_16x16x32_bf16 a[124:127], v[144:147], v[92:95], a[124:127]\n"
"v_exp_f32_e32 v52, v52\n"
"v_mfma_f32_16x16x32_bf16 v[108:111], v[112:115], v[92:95], v[108:111]\n"
"ds_read_b128 a[136:139], v10 offset:16384\n"
"v_cvt_pk_bf16_f32 v81, v50, v51\n"
"v_mfma_f32_16x16x32_bf16 v[56:59], a[160:163], v[196:199], v[36:39]\n"
"v_exp_f32_e32 v53, v53\n"
"v_mfma_f32_16x16x32_bf16 v[56:59], a[164:167], v[200:203], v[56:59]\n"
"ds_read_b64_tr_b16 v[116:117], v16 offset:16384\n"
"v_cvt_pk_bf16_f32 v82, v52, v53\n"
"v_mfma_f32_16x16x32_bf16 v[56:59], a[168:171], v[204:207], v[56:59]\n"
"v_exp_f32_e32 v54, v54\n"
"v_mfma_f32_16x16x32_bf16 v[56:59], a[172:175], v[208:211], v[56:59]\n"
"buffer_load_dwordx4 a[196:199], v18, s[24:27], s41 offen\n"
"v_mfma_f32_16x16x32_bf16 v[60:63], a[176:179], v[196:199], v[36:39]\n"
"v_exp_f32_e32 v55, v55\n"
"v_mfma_f32_16x16x32_bf16 v[60:63], a[180:183], v[200:203], v[60:63]\n"
"ds_read_b128 a[140:143], v11 offset:16384\n"
"v_cvt_pk_bf16_f32 v83, v54, v55\n"
"v_mfma_f32_16x16x32_bf16 v[60:63], a[184:187], v[204:207], v[60:63]\n"
"ds_read_b64_tr_b16 v[118:119], v16 offset:20480\n"
"s_waitcnt lgkmcnt(6)\n"
"v_mfma_f32_16x16x32_bf16 v[60:63], a[188:191], v[208:211], v[60:63]\n"
"v_exp_f32_e32 v56, v56\n"
"v_mfma_f32_16x16x32_bf16 a[0:3], v[148:151], v[80:83], a[0:3]\n"
"buffer_load_dwordx4 v2, s[8:11], s38 offen offset:1024 lds\n"
"v_mfma_f32_16x16x32_bf16 a[16:19], v[152:155], v[80:83], a[16:19]\n"
"v_exp_f32_e32 v57, v57\n"
"v_mfma_f32_16x16x32_bf16 a[32:35], v[156:159], v[80:83], a[32:35]\n"
"ds_read_b128 a[144:147], v8 offset:20480\n"
"v_cvt_pk_bf16_f32 v84, v56, v57\n"
"v_mfma_f32_16x16x32_bf16 a[48:51], v[160:163], v[80:83], a[48:51]\n"
"v_exp_f32_e32 v58, v58\n"
"v_mfma_f32_16x16x32_bf16 a[64:67], v[164:167], v[80:83], a[64:67]\n"
"ds_read_b64_tr_b16 v[120:121], v13 offset:16384\n"
"s_add_u32 s41, s41, s47\n"
"v_mfma_f32_16x16x32_bf16 a[80:83], v[168:171], v[80:83], a[80:83]\n"
"v_exp_f32_e32 v59, v59\n"
"v_mfma_f32_16x16x32_bf16 a[96:99], v[172:175], v[80:83], a[96:99]\n"
"v_cvt_pk_bf16_f32 v85, v58, v59\n"
"v_mfma_f32_16x16x32_bf16 a[112:115], v[176:179], v[80:83], a[112:115]\n"
"v_exp_f32_e32 v60, v60\n"
"v_mfma_f32_16x16x32_bf16 v[96:99], v[112:115], v[80:83], v[96:99]\n"
"ds_read_b128 a[148:151], v9 offset:20480\n"
"v_mfma_f32_16x16x32_bf16 v[64:67], a[160:163], v[212:215], v[40:43]\n"
"v_exp_f32_e32 v61, v61\n"
"v_mfma_f32_16x16x32_bf16 v[64:67], a[164:167], v[216:219], v[64:67]\n"
"ds_read_b64_tr_b16 v[122:123], v13 offset:20480\n"
"v_cvt_pk_bf16_f32 v86, v60, v61\n"
"v_mfma_f32_16x16x32_bf16 v[64:67], a[168:171], v[220:223], v[64:67]\n"
"v_exp_f32_e32 v62, v62\n"
"v_mfma_f32_16x16x32_bf16 v[64:67], a[172:175], v[224:227], v[64:67]\n"
"v_mfma_f32_16x16x32_bf16 v[68:71], a[176:179], v[212:215], v[40:43]\n"
"v_exp_f32_e32 v63, v63\n"
"v_mfma_f32_16x16x32_bf16 v[68:71], a[180:183], v[216:219], v[68:71]\n"
"ds_read_b128 a[152:155], v10 offset:20480\n"
"v_cvt_pk_bf16_f32 v87, v62, v63\n"
"v_mfma_f32_16x16x32_bf16 v[68:71], a[184:187], v[220:223], v[68:71]\n"
"ds_read_b64_tr_b16 v[124:125], v16 offset:16640\n"
"v_mfma_f32_16x16x32_bf16 v[68:71], a[188:191], v[224:227], v[68:71]\n"
"v_exp_f32_e32 v64, v64\n"
"v_mfma_f32_16x16x32_bf16 a[4:7], v[148:151], v[84:87], a[4:7]\n"
"buffer_load_dwordx4 v3, s[8:11], s38 offen offset:2048 lds\n"
"v_mfma_f32_16x16x32_bf16 a[20:23], v[152:155], v[84:87], a[20:23]\n"
"v_exp_f32_e32 v65, v65\n"
"v_mfma_f32_16x16x32_bf16 a[36:39], v[156:159], v[84:87], a[36:39]\n"
"ds_read_b128 a[156:159], v11 offset:20480\n"
"v_cvt_pk_bf16_f32 v88, v64, v65\n"
"v_mfma_f32_16x16x32_bf16 a[52:55], v[160:163], v[84:87], a[52:55]\n"
"v_exp_f32_e32 v66, v66\n"
"v_mfma_f32_16x16x32_bf16 a[68:71], v[164:167], v[84:87], a[68:71]\n"
"ds_read_b64_tr_b16 v[126:127], v16 offset:20736\n"
"v_mfma_f32_16x16x32_bf16 a[84:87], v[168:171], v[84:87], a[84:87]\n"
"v_exp_f32_e32 v67, v67\n"
"v_mfma_f32_16x16x32_bf16 a[100:103], v[172:175], v[84:87], a[100:103]\n"
"v_cvt_pk_bf16_f32 v89, v66, v67\n"
"v_mfma_f32_16x16x32_bf16 a[116:119], v[176:179], v[84:87], a[116:119]\n"
"v_exp_f32_e32 v68, v68\n"
"v_mfma_f32_16x16x32_bf16 v[100:103], v[112:115], v[84:87], v[100:103]\n"
"ds_read_b64_tr_b16 v[128:129], v13 offset:16640\n"
"v_mfma_f32_16x16x32_bf16 v[72:75], a[160:163], v[228:231], v[44:47]\n"
"v_exp_f32_e32 v69, v69\n"
"v_mfma_f32_16x16x32_bf16 v[72:75], a[164:167], v[232:235], v[72:75]\n"
"ds_read_b64_tr_b16 v[130:131], v13 offset:20736\n"
"v_cvt_pk_bf16_f32 v90, v68, v69\n"
"v_mfma_f32_16x16x32_bf16 v[72:75], a[168:171], v[236:239], v[72:75]\n"
"v_exp_f32_e32 v70, v70\n"
"v_mfma_f32_16x16x32_bf16 v[72:75], a[172:175], v[240:243], v[72:75]\n"
"v_mad_u32_u24 v31, v28, s43, v19\n"
"v_mfma_f32_16x16x32_bf16 v[76:79], a[176:179], v[228:231], v[44:47]\n"
"v_exp_f32_e32 v71, v71\n"
"v_mfma_f32_16x16x32_bf16 v[76:79], a[180:183], v[232:235], v[76:79]\n"
"ds_read_b64_tr_b16 v[132:133], v16 offset:16896\n"
"v_cvt_pk_bf16_f32 v91, v70, v71\n"
"v_mfma_f32_16x16x32_bf16 v[76:79], a[184:187], v[236:239], v[76:79]\n"
"ds_read_b64_tr_b16 v[134:135], v16 offset:20992\n"
"s_waitcnt lgkmcnt(5)\n"
"v_mfma_f32_16x16x32_bf16 v[76:79], a[188:191], v[240:243], v[76:79]\n"
"v_exp_f32_e32 v72, v72\n"
"v_mfma_f32_16x16x32_bf16 a[8:11], v[148:151], v[88:91], a[8:11]\n"
"buffer_load_dwordx4 v4, s[8:11], s38 offen offset:3072 lds\n"
"v_mfma_f32_16x16x32_bf16 a[24:27], v[152:155], v[88:91], a[24:27]\n"
"v_exp_f32_e32 v73, v73\n"
"v_mfma_f32_16x16x32_bf16 a[40:43], v[156:159], v[88:91], a[40:43]\n"
"ds_read_b64_tr_b16 v[136:137], v13 offset:16896\n"
"s_add_u32 s42, s42, 1\n"
"v_mfma_f32_16x16x32_bf16 a[56:59], v[160:163], v[88:91], a[56:59]\n"
"v_exp_f32_e32 v74, v74\n"
"v_mfma_f32_16x16x32_bf16 a[72:75], v[164:167], v[88:91], a[72:75]\n"
"ds_read_b64_tr_b16 v[138:139], v13 offset:20992\n"
"v_cvt_pk_bf16_f32 v92, v72, v73\n"
"v_mfma_f32_16x16x32_bf16 a[88:91], v[168:171], v[88:91], a[88:91]\n"
"v_exp_f32_e32 v75, v75\n"
"v_mfma_f32_16x16x32_bf16 a[104:107], v[172:175], v[88:91], a[104:107]\n"
"buffer_store_dwordx4 a[204:207], v31, s[28:31], s39 offen sc1\n"
"v_mfma_f32_16x16x32_bf16 a[120:123], v[176:179], v[88:91], a[120:123]\n"
"v_exp_f32_e32 v76, v76\n"
"v_mfma_f32_16x16x32_bf16 v[104:107], v[112:115], v[88:91], v[104:107]\n"
"ds_read_b64_tr_b16 v[140:141], v16 offset:17152\n"
"v_cvt_pk_bf16_f32 v93, v74, v75\n"
"v_mfma_f32_16x16x32_bf16 v[48:51], a[128:131], v[180:183], v[32:35]\n"
"v_exp_f32_e32 v77, v77\n"
"v_mfma_f32_16x16x32_bf16 v[48:51], a[132:135], v[184:187], v[48:51]\n"
"ds_read_b64_tr_b16 v[142:143], v16 offset:21248\n"
"v_cvt_pk_bf16_f32 v94, v76, v77\n"
"v_mfma_f32_16x16x32_bf16 v[48:51], a[136:139], v[188:191], v[48:51]\n"
"v_exp_f32_e32 v78, v78\n"
"v_mfma_f32_16x16x32_bf16 v[48:51], a[140:143], v[192:195], v[48:51]\n"
"s_add_u32 m0, s44, 114688\n"
"v_mfma_f32_16x16x32_bf16 v[52:55], a[144:147], v[180:183], v[32:35]\n"
"v_exp_f32_e32 v79, v79\n"
"v_mfma_f32_16x16x32_bf16 v[52:55], a[148:151], v[184:187], v[52:55]\n"
"ds_read_b64_tr_b16 v[144:145], v13 offset:17152\n"
"v_cvt_pk_bf16_f32 v95, v78, v79\n"
"v_mfma_f32_16x16x32_bf16 v[52:55], a[152:155], v[188:191], v[52:55]\n"
"ds_read_b64_tr_b16 v[146:147], v13 offset:21248\n"
"v_mfma_f32_16x16x32_bf16 v[52:55], a[156:159], v[192:195], v[52:55]\n"
"v_exp_f32_e32 v48, v48\n"
"v_mfma_f32_16x16x32_bf16 a[12:15], v[148:151], v[92:95], a[12:15]\n"
"v_readlane_b32 s43, v29, s42\n"
"v_readlane_b32 s39, v30, s42\n"
"v_mfma_f32_16x16x32_bf16 a[28:31], v[152:155], v[92:95], a[28:31]\n"
"v_exp_f32_e32 v49, v49\n"
"v_mfma_f32_16x16x32_bf16 a[44:47], v[156:159], v[92:95], a[44:47]\n"
"ds_read_b128 a[160:163], v8 offset:24576\n"
"v_cvt_pk_bf16_f32 v80, v48, v49\n"
"v_mfma_f32_16x16x32_bf16 a[60:63], v[160:163], v[92:95], a[60:63]\n"
"v_exp_f32_e32 v50, v50\n"
"v_mfma_f32_16x16x32_bf16 a[76:79], v[164:167], v[92:95], a[76:79]\n"
"ds_read_b128 a[164:167], v9 offset:24576\n"
"v_mfma_f32_16x16x32_bf16 a[92:95], v[168:171], v[92:95], a[92:95]\n"
"v_exp_f32_e32 v51, v51\n"
"v_mfma_f32_16x16x32_bf16 a[108:111], v[172:175], v[92:95], a[108:111]\n"
"buffer_load_dwordx4 v5, s[12:15], s38 offen lds\n"
"v_mfma_f32_16x16x32_bf16 a[124:127], v[176:179], v[92:95], a[124:127]\n"
"v_exp_f32_e32 v52, v52\n"
"v_mfma_f32_16x16x32_bf16 v[108:111], v[112:115], v[92:95], v[108:111]\n"
"ds_read_b128 a[168:171], v10 offset:24576\n"
"v_cvt_pk_bf16_f32 v81, v50, v51\n"
"v_mfma_f32_16x16x32_bf16 v[56:59], a[128:131], v[196:199], v[36:39]\n"
"v_exp_f32_e32 v53, v53\n"
"v_mfma_f32_16x16x32_bf16 v[56:59], a[132:135], v[200:203], v[56:59]\n"
"ds_read_b64_tr_b16 v[148:149], v16 offset:24576\n"
"v_cvt_pk_bf16_f32 v82, v52, v53\n"
"v_mfma_f32_16x16x32_bf16 v[56:59], a[136:139], v[204:207], v[56:59]\n"
"v_exp_f32_e32 v54, v54\n"
"v_mfma_f32_16x16x32_bf16 v[56:59], a[140:143], v[208:211], v[56:59]\n"
"buffer_load_dwordx4 a[200:203], v18, s[24:27], s41 offen\n"
"v_mfma_f32_16x16x32_bf16 v[60:63], a[144:147], v[196:199], v[36:39]\n"
"v_exp_f32_e32 v55, v55\n"
"v_mfma_f32_16x16x32_bf16 v[60:63], a[148:151], v[200:203], v[60:63]\n"
"ds_read_b128 a[172:175], v11 offset:24576\n"
"v_cvt_pk_bf16_f32 v83, v54, v55\n"
"v_mfma_f32_16x16x32_bf16 v[60:63], a[152:155], v[204:207], v[60:63]\n"
"ds_read_b64_tr_b16 v[150:151], v16 offset:28672\n"
"s_waitcnt lgkmcnt(6)\n"
"v_mfma_f32_16x16x32_bf16 v[60:63], a[156:159], v[208:211], v[60:63]\n"
"v_exp_f32_e32 v56, v56\n"
"v_mfma_f32_16x16x32_bf16 a[0:3], v[116:119], v[80:83], a[0:3]\n"
"buffer_load_dwordx4 v12, s[12:15], s38 offen offset:1024 lds\n"
"v_mfma_f32_16x16x32_bf16 a[16:19], v[120:123], v[80:83], a[16:19]\n"
"v_exp_f32_e32 v57, v57\n"
"v_mfma_f32_16x16x32_bf16 a[32:35], v[124:127], v[80:83], a[32:35]\n"
"ds_read_b128 a[176:179], v8 offset:28672\n"
"v_cvt_pk_bf16_f32 v84, v56, v57\n"
"v_mfma_f32_16x16x32_bf16 a[48:51], v[128:131], v[80:83], a[48:51]\n"
"v_exp_f32_e32 v58, v58\n"
"v_mfma_f32_16x16x32_bf16 a[64:67], v[132:135], v[80:83], a[64:67]\n"
"ds_read_b64_tr_b16 v[152:153], v13 offset:24576\n"
"s_add_u32 s41, s41, s47\n"
"v_mfma_f32_16x16x32_bf16 a[80:83], v[136:139], v[80:83], a[80:83]\n"
"v_exp_f32_e32 v59, v59\n"
"v_mfma_f32_16x16x32_bf16 a[96:99], v[140:143], v[80:83], a[96:99]\n"
"v_cvt_pk_bf16_f32 v85, v58, v59\n"
"v_mfma_f32_16x16x32_bf16 a[112:115], v[144:147], v[80:83], a[112:115]\n"
"v_exp_f32_e32 v60, v60\n"
"v_mfma_f32_16x16x32_bf16 v[96:99], v[112:115], v[80:83], v[96:99]\n"
"ds_read_b128 a[180:183], v9 offset:28672\n"
"v_mfma_f32_16x16x32_bf16 v[64:67], a[128:131], v[212:215], v[40:43]\n"
"v_exp_f32_e32 v61, v61\n"
"v_mfma_f32_16x16x32_bf16 v[64:67], a[132:135], v[216:219], v[64:67]\n"
"ds_read_b64_tr_b16 v[154:155], v13 offset:28672\n"
"v_cvt_pk_bf16_f32 v86, v60, v61\n"
"v_mfma_f32_16x16x32_bf16 v[64:67], a[136:139], v[220:223], v[64:67]\n"
"v_exp_f32_e32 v62, v62\n"
"v_mfma_f32_16x16x32_bf16 v[64:67], a[140:143], v[224:227], v[64:67]\n"
"v_mfma_f32_16x16x32_bf16 v[68:71], a[144:147], v[212:215], v[40:43]\n"
"v_exp_f32_e32 v63, v63\n"
"v_mfma_f32_16x16x32_bf16 v[68:71], a[148:151], v[216:219], v[68:71]\n"
"ds_read_b128 a[184:187], v10 offset:28672\n"
"v_cvt_pk_bf16_f32 v87, v62, v63\n"
"v_mfma_f32_16x16x32_bf16 v[68:71], a[152:155], v[220:223], v[68:71]\n"
"ds_read_b64_tr_b16 v[156:157], v16 offset:24832\n"
"v_mfma_f32_16x16x32_bf16 v[68:71], a[156:159], v[224:227], v[68:71]\n"
"v_exp_f32_e32 v64, v64\n"
"v_mfma_f32_16x16x32_bf16 a[4:7], v[116:119], v[84:87], a[4:7]\n"
"buffer_load_dwordx4 v5, s[12:15], s38 offen offset:2048 lds\n"
"v_mfma_f32_16x16x32_bf16 a[20:23], v[120:123], v[84:87], a[20:23]\n"
"v_exp_f32_e32 v65, v65\n"
"v_mfma_f32_16x16x32_bf16 a[36:39], v[124:127], v[84:87], a[36:39]\n"
"ds_read_b128 a[188:191], v11 offset:28672\n"
"v_cvt_pk_bf16_f32 v88, v64, v65\n"
"v_mfma_f32_16x16x32_bf16 a[52:55], v[128:131], v[84:87], a[52:55]\n"
"v_exp_f32_e32 v66, v66\n"
"v_mfma_f32_16x16x32_bf16 a[68:71], v[132:135], v[84:87], a[68:71]\n"
"ds_read_b64_tr_b16 v[158:159], v16 offset:28928\n"
"v_mfma_f32_16x16x32_bf16 a[84:87], v[136:139], v[84:87], a[84:87]\n"
"v_exp_f32_e32 v67, v67\n"
"v_mfma_f32_16x16x32_bf16 a[100:103], v[140:143], v[84:87], a[100:103]\n"
"v_cvt_pk_bf16_f32 v89, v66, v67\n"
"v_mfma_f32_16x16x32_bf16 a[116:119], v[144:147], v[84:87], a[116:119]\n"
"v_exp_f32_e32 v68, v68\n"
"v_mfma_f32_16x16x32_bf16 v[100:103], v[112:115], v[84:87], v[100:103]\n"
"ds_read_b64_tr_b16 v[160:161], v13 offset:24832\n"
"v_mfma_f32_16x16x32_bf16 v[72:75], a[128:131], v[228:231], v[44:47]\n"
"v_exp_f32_e32 v69, v69\n"
"v_mfma_f32_16x16x32_bf16 v[72:75], a[132:135], v[232:235], v[72:75]\n"
"ds_read_b64_tr_b16 v[162:163], v13 offset:28928\n"
"v_cvt_pk_bf16_f32 v90, v68, v69\n"
"v_mfma_f32_16x16x32_bf16 v[72:75], a[136:139], v[236:239], v[72:75]\n"
"v_exp_f32_e32 v70, v70\n"
"v_mfma_f32_16x16x32_bf16 v[72:75], a[140:143], v[240:243], v[72:75]\n"
"v_mad_u32_u24 v31, v28, s43, v19\n"
"v_mfma_f32_16x16x32_bf16 v[76:79], a[144:147], v[228:231], v[44:47]\n"
"v_exp_f32_e32 v71, v71\n"
"v_mfma_f32_16x16x32_bf16 v[76:79], a[148:151], v[232:235], v[76:79]\n"
"ds_read_b64_tr_b16 v[164:165], v16 offset:25088\n"
"v_cvt_pk_bf16_f32 v91, v70, v71\n"
"v_mfma_f32_16x16x32_bf16 v[76:79], a[152:155], v[236:239], v[76:79]\n"
"ds_read_b64_tr_b16 v[166:167], v16 offset:29184\n"
"s_waitcnt lgkmcnt(5)\n"
"v_mfma_f32_16x16x32_bf16 v[76:79], a[156:159], v[240:243], v[76:79]\n"
"v_exp_f32_e32 v72, v72\n"
"v_mfma_f32_16x16x32_bf16 a[8:11], v[116:119], v[88:91], a[8:11]\n"
"buffer_load_dwordx4 v12, s[12:15], s38 offen offset:3072 lds\n"
"v_mfma_f32_16x16x32_bf16 a[24:27], v[120:123], v[88:91], a[24:27]\n"
"v_exp_f32_e32 v73, v73\n"
"v_mfma_f32_16x16x32_bf16 a[40:43], v[124:127], v[88:91], a[40:43]\n"
"ds_read_b64_tr_b16 v[168:169], v13 offset:25088\n"
"s_add_u32 s42, s42, 1\n"
"v_mfma_f32_16x16x32_bf16 a[56:59], v[128:131], v[88:91], a[56:59]\n"
"v_exp_f32_e32 v74, v74\n"
"v_mfma_f32_16x16x32_bf16 a[72:75], v[132:135], v[88:91], a[72:75]\n"
"ds_read_b64_tr_b16 v[170:171], v13 offset:29184\n"
"v_cvt_pk_bf16_f32 v92, v72, v73\n"
"v_mfma_f32_16x16x32_bf16 a[88:91], v[136:139], v[88:91], a[88:91]\n"
"v_exp_f32_e32 v75, v75\n"
"v_mfma_f32_16x16x32_bf16 a[104:107], v[140:143], v[88:91], a[104:107]\n"
"buffer_store_dwordx4 a[208:211], v31, s[28:31], s39 offen sc1\n"
"v_mfma_f32_16x16x32_bf16 a[120:123], v[144:147], v[88:91], a[120:123]\n"
"v_exp_f32_e32 v76, v76\n"
"v_mfma_f32_16x16x32_bf16 v[104:107], v[112:115], v[88:91], v[104:107]\n"
"ds_read_b64_tr_b16 v[172:173], v16 offset:25344\n"
"v_cvt_pk_bf16_f32 v93, v74, v75\n"
"v_mfma_f32_16x16x32_bf16 v[48:51], a[160:163], v[180:183], v[32:35]\n"
"v_exp_f32_e32 v77, v77\n"
"v_mfma_f32_16x16x32_bf16 v[48:51], a[164:167], v[184:187], v[48:51]\n"
"ds_read_b64_tr_b16 v[174:175], v16 offset:29440\n"
"v_cvt_pk_bf16_f32 v94, v76, v77\n"
"v_mfma_f32_16x16x32_bf16 v[48:51], a[168:171], v[188:191], v[48:51]\n"
"v_exp_f32_e32 v78, v78\n"
"v_mfma_f32_16x16x32_bf16 v[48:51], a[172:175], v[192:195], v[48:51]\n"
"s_waitcnt vmcnt(12)\n"
"s_barrier\n"
"v_mfma_f32_16x16x32_bf16 v[52:55], a[176:179], v[180:183], v[32:35]\n"
"v_exp_f32_e32 v79, v79\n"
"v_mfma_f32_16x16x32_bf16 v[52:55], a[180:183], v[184:187], v[52:55]\n"
"ds_read_b64_tr_b16 v[176:177], v13 offset:25344\n"
"v_cvt_pk_bf16_f32 v95, v78, v79\n"
"v_mfma_f32_16x16x32_bf16 v[52:55], a[184:187], v[188:191], v[52:55]\n"
"ds_read_b64_tr_b16 v[178:179], v13 offset:29440\n"
"s_add_u32 s38, s38, 16384\n"
"v_mfma_f32_16x16x32_bf16 v[52:55], a[188:191], v[192:195], v[52:55]\n"
"v_exp_f32_e32 v48, v48\n"
"v_mfma_f32_16x16x32_bf16 a[12:15], v[116:119], v[92:95], a[12:15]\n"
"s_add_u32 m0, s44, 0\n"
"v_readlane_b32 s43, v29, s42\n"
"v_mfma_f32_16x16x32_bf16 a[28:31], v[120:123], v[92:95], a[28:31]\n"
"v_exp_f32_e32 v49, v49\n"
"v_mfma_f32_16x16x32_bf16 a[44:47], v[124:127], v[92:95], a[44:47]\n"
"ds_read_b128 a[128:131], v8 offset:32768\n"
"v_cvt_pk_bf16_f32 v80, v48, v49\n"
"v_mfma_f32_16x16x32_bf16 a[60:63], v[128:131], v[92:95], a[60:63]\n"
"v_exp_f32_e32 v50, v50\n"
"v_mfma_f32_16x16x32_bf16 a[76:79], v[132:135], v[92:95], a[76:79]\n"
"ds_read_b128 a[132:135], v9 offset:32768\n"
"v_readlane_b32 s39, v30, s42\n"
"v_mfma_f32_16x16x32_bf16 a[92:95], v[136:139], v[92:95], a[92:95]\n"
"v_exp_f32_e32 v51, v51\n"
"v_mfma_f32_16x16x32_bf16 a[108:111], v[140:143], v[92:95], a[108:111]\n"
"buffer_load_dwordx4 v1, s[8:11], s38 offen lds\n"
"v_mfma_f32_16x16x32_bf16 a[124:127], v[144:147], v[92:95], a[124:127]\n"
"v_exp_f32_e32 v52, v52\n"
"v_mfma_f32_16x16x32_bf16 v[108:111], v[112:115], v[92:95], v[108:111]\n"
"ds_read_b128 a[136:139], v10 offset:32768\n"
"v_cvt_pk_bf16_f32 v81, v50, v51\n"
"v_mfma_f32_16x16x32_bf16 v[56:59], a[160:163], v[196:199], v[36:39]\n"
"v_exp_f32_e32 v53, v53\n"
"v_mfma_f32_16x16x32_bf16 v[56:59], a[164:167], v[200:203], v[56:59]\n"
"ds_read_b64_tr_b16 v[116:117], v16 offset:32768\n"
"v_cvt_pk_bf16_f32 v82, v52, v53\n"
"v_mfma_f32_16x16x32_bf16 v[56:59], a[168:171], v[204:207], v[56:59]\n"
"v_exp_f32_e32 v54, v54\n"
"v_mfma_f32_16x16x32_bf16 v[56:59], a[172:175], v[208:211], v[56:59]\n"
"s_waitcnt lgkmcnt(4)\n"
"v_mfma_f32_16x16x32_bf16 v[60:63], a[176:179], v[196:199], v[36:39]\n"
"v_exp_f32_e32 v55, v55\n"
"v_mfma_f32_16x16x32_bf16 v[60:63], a[180:183], v[200:203], v[60:63]\n"
"ds_read_b128 a[140:143], v11 offset:32768\n"
"v_cvt_pk_bf16_f32 v83, v54, v55\n"
"v_mfma_f32_16x16x32_bf16 v[60:63], a[184:187], v[204:207], v[60:63]\n"
"ds_read_b64_tr_b16 v[118:119], v16 offset:36864\n"
"v_mfma_f32_16x16x32_bf16 v[60:63], a[188:191], v[208:211], v[60:63]\n"
"v_exp_f32_e32 v56, v56\n"
"v_mfma_f32_16x16x32_bf16 a[0:3], v[148:151], v[80:83], a[0:3]\n"
"buffer_load_dwordx4 v2, s[8:11], s38 offen offset:1024 lds\n"
"v_mfma_f32_16x16x32_bf16 a[16:19], v[152:155], v[80:83], a[16:19]\n"
"v_exp_f32_e32 v57, v57\n"
"v_mfma_f32_16x16x32_bf16 a[32:35], v[156:159], v[80:83], a[32:35]\n"
"ds_read_b128 a[144:147], v8 offset:36864\n"
"v_cvt_pk_bf16_f32 v84, v56, v57\n"
"v_mfma_f32_16x16x32_bf16 a[48:51], v[160:163], v[80:83], a[48:51]\n"
"v_exp_f32_e32 v58, v58\n"
"v_mfma_f32_16x16x32_bf16 a[64:67], v[164:167], v[80:83], a[64:67]\n"
"ds_read_b64_tr_b16 v[120:121], v13 offset:32768\n"
"v_mfma_f32_16x16x32_bf16 a[80:83], v[168:171], v[80:83], a[80:83]\n"
"v_exp_f32_e32 v59, v59\n"
"v_mfma_f32_16x16x32_bf16 a[96:99], v[172:175], v[80:83], a[96:99]\n"
"v_cvt_pk_bf16_f32 v85, v58, v59\n"
"v_mfma_f32_16x16x32_bf16 a[112:115], v[176:179], v[80:83], a[112:115]\n"
"v_exp_f32_e32 v60, v60\n"
"v_mfma_f32_16x16x32_bf16 v[96:99], v[112:115], v[80:83], v[96:99]\n"
"ds_read_b128 a[148:151], v9 offset:36864\n"
"v_mfma_f32_16x16x32_bf16 v[64:67], a[160:163], v[212:215], v[40:43]\n"
"v_exp_f32_e32 v61, v61\n"
"v_mfma_f32_16x16x32_bf16 v[64:67], a[164:167], v[216:219], v[64:67]\n"
"ds_read_b64_tr_b16 v[122:123], v13 offset:36864\n"
"v_cvt_pk_bf16_f32 v86, v60, v61\n"
"v_mfma_f32_16x16x32_bf16 v[64:67], a[168:171], v[220:223], v[64:67]\n"
"v_exp_f32_e32 v62, v62\n"
"v_mfma_f32_16x16x32_bf16 v[64:67], a[172:175], v[224:227], v[64:67]\n"
"v_mfma_f32_16x16x32_bf16 v[68:71], a[176:179], v[212:215], v[40:43]\n"
"v_exp_f32_e32 v63, v63\n"
"v_mfma_f32_16x16x32_bf16 v[68:71], a[180:183], v[216:219], v[68:71]\n"
"ds_read_b128 a[152:155], v10 offset:36864\n"
"v_cvt_pk_bf16_f32 v87, v62, v63\n"
"v_mfma_f32_16x16x32_bf16 v[68:71], a[184:187], v[220:223], v[68:71]\n"
"ds_read_b64_tr_b16 v[124:125], v16 offset:33024\n"
"v_mfma_f32_16x16x32_bf16 v[68:71], a[188:191], v[224:227], v[68:71]\n"
"v_exp_f32_e32 v64, v64\n"
"v_mfma_f32_16x16x32_bf16 a[4:7], v[148:151], v[84:87], a[4:7]\n"
"buffer_load_dwordx4 v3, s[8:11], s38 offen offset:2048 lds\n"
"v_mfma_f32_16x16x32_bf16 a[20:23], v[152:155], v[84:87], a[20:23]\n"
"v_exp_f32_e32 v65, v65\n"
"v_mfma_f32_16x16x32_bf16 a[36:39], v[156:159], v[84:87], a[36:39]\n"
"ds_read_b128 a[156:159], v11 offset:36864\n"
"v_cvt_pk_bf16_f32 v88, v64, v65\n"
"v_mfma_f32_16x16x32_bf16 a[52:55], v[160:163], v[84:87], a[52:55]\n"
"v_exp_f32_e32 v66, v66\n"
"v_mfma_f32_16x16x32_bf16 a[68:71], v[164:167], v[84:87], a[68:71]\n"
"ds_read_b64_tr_b16 v[126:127], v16 offset:37120\n"
"v_mfma_f32_16x16x32_bf16 a[84:87], v[168:171], v[84:87], a[84:87]\n"
"v_exp_f32_e32 v67, v67\n"
"v_mfma_f32_16x16x32_bf16 a[100:103], v[172:175], v[84:87], a[100:103]\n"
"v_cvt_pk_bf16_f32 v89, v66, v67\n"
"v_mfma_f32_16x16x32_bf16 a[116:119], v[176:179], v[84:87], a[116:119]\n"
"v_exp_f32_e32 v68, v68\n"
"v_mfma_f32_16x16x32_bf16 v[100:103], v[112:115], v[84:87], v[100:103]\n"
"ds_read_b64_tr_b16 v[128:129], v13 offset:33024\n"
"v_mfma_f32_16x16x32_bf16 v[72:75], a[160:163], v[228:231], v[44:47]\n"
"v_exp_f32_e32 v69, v69\n"
"v_mfma_f32_16x16x32_bf16 v[72:75], a[164:167], v[232:235], v[72:75]\n"
"ds_read_b64_tr_b16 v[130:131], v13 offset:37120\n"
"v_cvt_pk_bf16_f32 v90, v68, v69\n"
"v_mfma_f32_16x16x32_bf16 v[72:75], a[168:171], v[236:239], v[72:75]\n"
"v_exp_f32_e32 v70, v70\n"
"v_mfma_f32_16x16x32_bf16 v[72:75], a[172:175], v[240:243], v[72:75]\n"
"v_mad_u32_u24 v31, v28, s43, v19\n"
"v_mfma_f32_16x16x32_bf16 v[76:79], a[176:179], v[228:231], v[44:47]\n"
"v_exp_f32_e32 v71, v71\n"
"v_mfma_f32_16x16x32_bf16 v[76:79], a[180:183], v[232:235], v[76:79]\n"
"ds_read_b64_tr_b16 v[132:133], v16 offset:33280\n"
"v_cvt_pk_bf16_f32 v91, v70, v71\n"
"v_mfma_f32_16x16x32_bf16 v[76:79], a[184:187], v[236:239], v[76:79]\n"
"ds_read_b64_tr_b16 v[134:135], v16 offset:37376\n"
"s_waitcnt lgkmcnt(5)\n"
"v_mfma_f32_16x16x32_bf16 v[76:79], a[188:191], v[240:243], v[76:79]\n"
"v_exp_f32_e32 v72, v72\n"
"v_mfma_f32_16x16x32_bf16 a[8:11], v[148:151], v[88:91], a[8:11]\n"
"buffer_load_dwordx4 v4, s[8:11], s38 offen offset:3072 lds\n"
"v_mfma_f32_16x16x32_bf16 a[24:27], v[152:155], v[88:91], a[24:27]\n"
"v_exp_f32_e32 v73, v73\n"
"v_mfma_f32_16x16x32_bf16 a[40:43], v[156:159], v[88:91], a[40:43]\n"
"ds_read_b64_tr_b16 v[136:137], v13 offset:33280\n"
"s_add_u32 s42, s42, 1\n"
"v_mfma_f32_16x16x32_bf16 a[56:59], v[160:163], v[88:91], a[56:59]\n"
"v_exp_f32_e32 v74, v74\n"
"v_mfma_f32_16x16x32_bf16 a[72:75], v[164:167], v[88:91], a[72:75]\n"
"ds_read_b64_tr_b16 v[138:139], v13 offset:37376\n"
"v_cvt_pk_bf16_f32 v92, v72, v73\n"
"v_mfma_f32_16x16x32_bf16 a[88:91], v[168:171], v[88:91], a[88:91]\n"
"v_exp_f32_e32 v75, v75\n"
"v_mfma_f32_16x16x32_bf16 a[104:107], v[172:175], v[88:91], a[104:107]\n"
"buffer_store_dwordx4 a[212:215], v31, s[28:31], s39 offen sc1\n"
"v_mfma_f32_16x16x32_bf16 a[120:123], v[176:179], v[88:91], a[120:123]\n"
"v_exp_f32_e32 v76, v76\n"
"v_mfma_f32_16x16x32_bf16 v[104:107], v[112:115], v[88:91], v[104:107]\n"
"ds_read_b64_tr_b16 v[140:141], v16 offset:33536\n"
"v_cvt_pk_bf16_f32 v93, v74, v75\n"
"v_mfma_f32_16x16x32_bf16 v[48:51], a[128:131], v[180:183], v[32:35]\n"
"v_exp_f32_e32 v77, v77\n"
"v_mfma_f32_16x16x32_bf16 v[48:51], a[132:135], v[184:187], v[48:51]\n"
"ds_read_b64_tr_b16 v[142:143], v16 offset:37632\n"
"v_cvt_pk_bf16_f32 v94, v76, v77\n"
"v_mfma_f32_16x16x32_bf16 v[48:51], a[136:139], v[188:191], v[48:51]\n"
"v_exp_f32_e32 v78, v78\n"
"v_mfma_f32_16x16x32_bf16 v[48:51], a[140:143], v[192:195], v[48:51]\n"
"s_add_u32 m0, s44, 65536\n"
"v_mfma_f32_16x16x32_bf16 v[52:55], a[144:147], v[180:183], v[32:35]\n"
"v_exp_f32_e32 v79, v79\n"
"v_mfma_f32_16x16x32_bf16 v[52:55], a[148:151], v[184:187], v[52:55]\n"
"ds_read_b64_tr_b16 v[144:145], v13 offset:33536\n"
"v_cvt_pk_bf16_f32 v95, v78, v79\n"
"v_mfma_f32_16x16x32_bf16 v[52:55], a[152:155], v[188:191], v[52:55]\n"
"ds_read_b64_tr_b16 v[146:147], v13 offset:37632\n"
"v_mfma_f32_16x16x32_bf16 v[52:55], a[156:159], v[192:195], v[52:55]\n"
"v_exp_f32_e32 v48, v48\n"
"v_mfma_f32_16x16x32_bf16 a[12:15], v[148:151], v[92:95], a[12:15]\n"
"v_mfma_f32_16x16x32_bf16 a[28:31], v[152:155], v[92:95], a[28:31]\n"
"v_exp_f32_e32 v49, v49\n"
"v_mfma_f32_16x16x32_bf16 a[44:47], v[156:159], v[92:95], a[44:47]\n"
"ds_read_b128 a[160:163], v8 offset:40960\n"
"v_cvt_pk_bf16_f32 v80, v48, v49\n"
"v_mfma_f32_16x16x32_bf16 a[60:63], v[160:163], v[92:95], a[60:63]\n"
"v_exp_f32_e32 v50, v50\n"
"v_mfma_f32_16x16x32_bf16 a[76:79], v[164:167], v[92:95], a[76:79]\n"
"ds_read_b128 a[164:167], v9 offset:40960\n"
"v_mfma_f32_16x16x32_bf16 a[92:95], v[168:171], v[92:95], a[92:95]\n"
"v_exp_f32_e32 v51, v51\n"
"v_mfma_f32_16x16x32_bf16 a[108:111], v[172:175], v[92:95], a[108:111]\n"
"buffer_load_dwordx4 v5, s[12:15], s38 offen lds\n"
"v_mfma_f32_16x16x32_bf16 a[124:127], v[176:179], v[92:95], a[124:127]\n"
"v_exp_f32_e32 v52, v52\n"
"v_mfma_f32_16x16x32_bf16 v[108:111], v[112:115], v[92:95], v[108:111]\n"
"ds_read_b128 a[168:171], v10 offset:40960\n"
"v_cvt_pk_bf16_f32 v81, v50, v51\n"
"v_mfma_f32_16x16x32_bf16 v[56:59], a[128:131], v[196:199], v[36:39]\n"
"v_exp_f32_e32 v53, v53\n"
"v_mfma_f32_16x16x32_bf16 v[56:59], a[132:135], v[200:203], v[56:59]\n"
"ds_read_b64_tr_b16 v[148:149], v16 offset:40960\n"
"v_cvt_pk_bf16_f32 v82, v52, v53\n"
"v_mfma_f32_16x16x32_bf16 v[56:59], a[136:139], v[204:207], v[56:59]\n"
"v_exp_f32_e32 v54, v54\n"
"v_mfma_f32_16x16x32_bf16 v[56:59], a[140:143], v[208:211], v[56:59]\n"
"buffer_load_dwordx4 a[204:207], v18, s[24:27], s41 offen\n"
"v_mfma_f32_16x16x32_bf16 v[60:63], a[144:147], v[196:199], v[36:39]\n"
"v_exp_f32_e32 v55, v55\n"
"v_mfma_f32_16x16x32_bf16 v[60:63], a[148:151], v[200:203], v[60:63]\n"
"ds_read_b128 a[172:175], v11 offset:40960\n"
"v_cvt_pk_bf16_f32 v83, v54, v55\n"
"v_mfma_f32_16x16x32_bf16 v[60:63], a[152:155], v[204:207], v[60:63]\n"
"ds_read_b64_tr_b16 v[150:151], v16 offset:45056\n"
"s_waitcnt lgkmcnt(6)\n"
"v_mfma_f32_16x16x32_bf16 v[60:63], a[156:159], v[208:211], v[60:63]\n"
"v_exp_f32_e32 v56, v56\n"
"v_mfma_f32_16x16x32_bf16 a[0:3], v[116:119], v[80:83], a[0:3]\n"
"buffer_load_dwordx4 v12, s[12:15], s38 offen offset:1024 lds\n"
"v_mfma_f32_16x16x32_bf16 a[16:19], v[120:123], v[80:83], a[16:19]\n"
"v_exp_f32_e32 v57, v57\n"
"v_mfma_f32_16x16x32_bf16 a[32:35], v[124:127], v[80:83], a[32:35]\n"
"ds_read_b128 a[176:179], v8 offset:45056\n"
"v_cvt_pk_bf16_f32 v84, v56, v57\n"
"v_mfma_f32_16x16x32_bf16 a[48:51], v[128:131], v[80:83], a[48:51]\n"
"v_exp_f32_e32 v58, v58\n"
"v_mfma_f32_16x16x32_bf16 a[64:67], v[132:135], v[80:83], a[64:67]\n"
"ds_read_b64_tr_b16 v[152:153], v13 offset:40960\n"
"s_add_u32 s41, s41, s47\n"
"v_mfma_f32_16x16x32_bf16 a[80:83], v[136:139], v[80:83], a[80:83]\n"
"v_exp_f32_e32 v59, v59\n"
"v_mfma_f32_16x16x32_bf16 a[96:99], v[140:143], v[80:83], a[96:99]\n"
"v_cvt_pk_bf16_f32 v85, v58, v59\n"
"v_mfma_f32_16x16x32_bf16 a[112:115], v[144:147], v[80:83], a[112:115]\n"
"v_exp_f32_e32 v60, v60\n"
"v_mfma_f32_16x16x32_bf16 v[96:99], v[112:115], v[80:83], v[96:99]\n"
"ds_read_b128 a[180:183], v9 offset:45056\n"
"v_mfma_f32_16x16x32_bf16 v[64:67], a[128:131], v[212:215], v[40:43]\n"
"v_exp_f32_e32 v61, v61\n"
"v_mfma_f32_16x16x32_bf16 v[64:67], a[132:135], v[216:219], v[64:67]\n"
"ds_read_b64_tr_b16 v[154:155], v13 offset:45056\n"
"v_cvt_pk_bf16_f32 v86, v60, v61\n"
"v_mfma_f32_16x16x32_bf16 v[64:67], a[136:139], v[220:223], v[64:67]\n"
"v_exp_f32_e32 v62, v62\n"
"v_mfma_f32_16x16x32_bf16 v[64:67], a[140:143], v[224:227], v[64:67]\n"
"v_mfma_f32_16x16x32_bf16 v[68:71], a[144:147], v[212:215], v[40:43]\n"
"v_exp_f32_e32 v63, v63\n"
"v_mfma_f32_16x16x32_bf16 v[68:71], a[148:151], v[216:219], v[68:71]\n"
"ds_read_b128 a[184:187], v10 offset:45056\n"
"v_cvt_pk_bf16_f32 v87, v62, v63\n"
"v_mfma_f32_16x16x32_bf16 v[68:71], a[152:155], v[220:223], v[68:71]\n"
"ds_read_b64_tr_b16 v[156:157], v16 offset:41216\n"
"v_mfma_f32_16x16x32_bf16 v[68:71], a[156:159], v[224:227], v[68:71]\n"
"v_exp_f32_e32 v64, v64\n"
"v_mfma_f32_16x16x32_bf16 a[4:7], v[116:119], v[84:87], a[4:7]\n"
"buffer_load_dwordx4 v5, s[12:15], s38 offen offset:2048 lds\n"
"v_mfma_f32_16x16x32_bf16 a[20:23], v[120:123], v[84:87], a[20:23]\n"
"v_exp_f32_e32 v65, v65\n"
"v_mfma_f32_16x16x32_bf16 a[36:39], v[124:127], v[84:87], a[36:39]\n"
"ds_read_b128 a[188:191], v11 offset:45056\n"
"v_cvt_pk_bf16_f32 v88, v64, v65\n"
"v_mfma_f32_16x16x32_bf16 a[52:55], v[128:131], v[84:87], a[52:55]\n"
"v_exp_f32_e32 v66, v66\n"
"v_mfma_f32_16x16x32_bf16 a[68:71], v[132:135], v[84:87], a[68:71]\n"
"ds_read_b64_tr_b16 v[158:159], v16 offset:45312\n"
"v_mfma_f32_16x16x32_bf16 a[84:87], v[136:139], v[84:87], a[84:87]\n"
"v_exp_f32_e32 v67, v67\n"
"v_mfma_f32_16x16x32_bf16 a[100:103], v[140:143], v[84:87], a[100:103]\n"
"v_cvt_pk_bf16_f32 v89, v66, v67\n"
"v_mfma_f32_16x16x32_bf16 a[116:119], v[144:147], v[84:87], a[116:119]\n"
"v_exp_f32_e32 v68, v68\n"
"v_mfma_f32_16x16x32_bf16 v[100:103], v[112:115], v[84:87], v[100:103]\n"
"ds_read_b64_tr_b16 v[160:161], v13 offset:41216\n"
"v_mfma_f32_16x16x32_bf16 v[72:75], a[128:131], v[228:231], v[44:47]\n"
"v_exp_f32_e32 v69, v69\n"
"v_mfma_f32_16x16x32_bf16 v[72:75], a[132:135], v[232:235], v[72:75]\n"
"ds_read_b64_tr_b16 v[162:163], v13 offset:45312\n"
"v_cvt_pk_bf16_f32 v90, v68, v69\n"
"v_mfma_f32_16x16x32_bf16 v[72:75], a[136:139], v[236:239], v[72:75]\n"
"v_exp_f32_e32 v70, v70\n"
"v_mfma_f32_16x16x32_bf16 v[72:75], a[140:143], v[240:243], v[72:75]\n"
"v_mfma_f32_16x16x32_bf16 v[76:79], a[144:147], v[228:231], v[44:47]\n"
"v_exp_f32_e32 v71, v71\n"
"v_mfma_f32_16x16x32_bf16 v[76:79], a[148:151], v[232:235], v[76:79]\n"
"ds_read_b64_tr_b16 v[164:165], v16 offset:41472\n"
"v_cvt_pk_bf16_f32 v91, v70, v71\n"
"v_mfma_f32_16x16x32_bf16 v[76:79], a[152:155], v[236:239], v[76:79]\n"
"ds_read_b64_tr_b16 v[166:167], v16 offset:45568\n"
"s_waitcnt lgkmcnt(5)\n"
"v_mfma_f32_16x16x32_bf16 v[76:79], a[156:159], v[240:243], v[76:79]\n"
"v_exp_f32_e32 v72, v72\n"
"v_mfma_f32_16x16x32_bf16 a[8:11], v[116:119], v[88:91], a[8:11]\n"
"buffer_load_dwordx4 v12, s[12:15], s38 offen offset:3072 lds\n"
"v_mfma_f32_16x16x32_bf16 a[24:27], v[120:123], v[88:91], a[24:27]\n"
"v_exp_f32_e32 v73, v73\n"
"v_mfma_f32_16x16x32_bf16 a[40:43], v[124:127], v[88:91], a[40:43]\n"
"ds_read_b64_tr_b16 v[168:169], v13 offset:41472\n"
"v_cvt_pk_bf16_f32 v92, v72, v73\n"
"v_mfma_f32_16x16x32_bf16 a[56:59], v[128:131], v[88:91], a[56:59]\n"
"v_exp_f32_e32 v74, v74\n"
"v_mfma_f32_16x16x32_bf16 a[72:75], v[132:135], v[88:91], a[72:75]\n"
"ds_read_b64_tr_b16 v[170:171], v13 offset:45568\n"
"v_mfma_f32_16x16x32_bf16 a[88:91], v[136:139], v[88:91], a[88:91]\n"
"v_exp_f32_e32 v75, v75\n"
"v_mfma_f32_16x16x32_bf16 a[104:107], v[140:143], v[88:91], a[104:107]\n"
"v_cvt_pk_bf16_f32 v93, v74, v75\n"
"v_mfma_f32_16x16x32_bf16 a[120:123], v[144:147], v[88:91], a[120:123]\n"
"v_exp_f32_e32 v76, v76\n"
"v_mfma_f32_16x16x32_bf16 v[104:107], v[112:115], v[88:91], v[104:107]\n"
"ds_read_b64_tr_b16 v[172:173], v16 offset:41728\n"
"v_mfma_f32_16x16x32_bf16 v[48:51], a[160:163], v[180:183], v[32:35]\n"
"v_exp_f32_e32 v77, v77\n"
"v_mfma_f32_16x16x32_bf16 v[48:51], a[164:167], v[184:187], v[48:51]\n"
"ds_read_b64_tr_b16 v[174:175], v16 offset:45824\n"
"v_cvt_pk_bf16_f32 v94, v76, v77\n"
"v_mfma_f32_16x16x32_bf16 v[48:51], a[168:171], v[188:191], v[48:51]\n"
"v_exp_f32_e32 v78, v78\n"
"v_mfma_f32_16x16x32_bf16 v[48:51], a[172:175], v[192:195], v[48:51]\n"
"s_waitcnt vmcnt(11)\n"
"s_barrier\n"
"v_mfma_f32_16x16x32_bf16 v[52:55], a[176:179], v[180:183], v[32:35]\n"
"v_exp_f32_e32 v79, v79\n"
"v_mfma_f32_16x16x32_bf16 v[52:55], a[180:183], v[184:187], v[52:55]\n"
"ds_read_b64_tr_b16 v[176:177], v13 offset:41728\n"
"v_cvt_pk_bf16_f32 v95, v78, v79\n"
"v_mfma_f32_16x16x32_bf16 v[52:55], a[184:187], v[188:191], v[52:55]\n"
"ds_read_b64_tr_b16 v[178:179], v13 offset:45824\n"
"s_add_u32 s38, s38, 16384\n"
"v_mfma_f32_16x16x32_bf16 v[52:55], a[188:191], v[192:195], v[52:55]\n"
"v_exp_f32_e32 v48, v48\n"
"v_mfma_f32_16x16x32_bf16 a[12:15], v[116:119], v[92:95], a[12:15]\n"
"s_add_u32 m0, s44, 16384\n"
"v_readlane_b32 s43, v29, s42\n"
"v_mfma_f32_16x16x32_bf16 a[28:31], v[120:123], v[92:95], a[28:31]\n"
"v_exp_f32_e32 v49, v49\n"
"v_mfma_f32_16x16x32_bf16 a[44:47], v[124:127], v[92:95], a[44:47]\n"
"ds_read_b128 a[128:131], v8 offset:49152\n"
"v_cvt_pk_bf16_f32 v80, v48, v49\n"
"v_mfma_f32_16x16x32_bf16 a[60:63], v[128:131], v[92:95], a[60:63]\n"
"v_exp_f32_e32 v50, v50\n"
"v_mfma_f32_16x16x32_bf16 a[76:79], v[132:135], v[92:95], a[76:79]\n"
"ds_read_b128 a[132:135], v9 offset:49152\n"
"v_readlane_b32 s39, v30, s42\n"
"v_mfma_f32_16x16x32_bf16 a[92:95], v[136:139], v[92:95], a[92:95]\n"
"v_exp_f32_e32 v51, v51\n"
"v_mfma_f32_16x16x32_bf16 a[108:111], v[140:143], v[92:95], a[108:111]\n"
"buffer_load_dwordx4 v1, s[8:11], s38 offen lds\n"
"v_mfma_f32_16x16x32_bf16 a[124:127], v[144:147], v[92:95], a[124:127]\n"
"v_exp_f32_e32 v52, v52\n"
"v_mfma_f32_16x16x32_bf16 v[108:111], v[112:115], v[92:95], v[108:111]\n"
"ds_read_b128 a[136:139], v10 offset:49152\n"
"v_cvt_pk_bf16_f32 v81, v50, v51\n"
"v_mfma_f32_16x16x32_bf16 v[56:59], a[160:163], v[196:199], v[36:39]\n"
"v_exp_f32_e32 v53, v53\n"
"v_mfma_f32_16x16x32_bf16 v[56:59], a[164:167], v[200:203], v[56:59]\n"
"ds_read_b64_tr_b16 v[116:117], v16 offset:49152\n"
"v_cvt_pk_bf16_f32 v82, v52, v53\n"
"v_mfma_f32_16x16x32_bf16 v[56:59], a[168:171], v[204:207], v[56:59]\n"
"v_exp_f32_e32 v54, v54\n"
"v_mfma_f32_16x16x32_bf16 v[56:59], a[172:175], v[208:211], v[56:59]\n"
"buffer_load_dwordx4 a[208:211], v18, s[24:27], s41 offen\n"
"v_mfma_f32_16x16x32_bf16 v[60:63], a[176:179], v[196:199], v[36:39]\n"
"v_exp_f32_e32 v55, v55\n"
"v_mfma_f32_16x16x32_bf16 v[60:63], a[180:183], v[200:203], v[60:63]\n"
"ds_read_b128 a[140:143], v11 offset:49152\n"
"v_cvt_pk_bf16_f32 v83, v54, v55\n"
"v_mfma_f32_16x16x32_bf16 v[60:63], a[184:187], v[204:207], v[60:63]\n"
"ds_read_b64_tr_b16 v[118:119], v16 offset:53248\n"
"s_waitcnt lgkmcnt(6)\n"
"v_mfma_f32_16x16x32_bf16 v[60:63], a[188:191], v[208:211], v[60:63]\n"
"v_exp_f32_e32 v56, v56\n"
"v_mfma_f32_16x16x32_bf16 a[0:3], v[148:151], v[80:83], a[0:3]\n"
"buffer_load_dwordx4 v2, s[8:11], s38 offen offset:1024 lds\n"
"v_mfma_f32_16x16x32_bf16 a[16:19], v[152:155], v[80:83], a[16:19]\n"
"v_exp_f32_e32 v57, v57\n"
"v_mfma_f32_16x16x32_bf16 a[32:35], v[156:159], v[80:83], a[32:35]\n"
"ds_read_b128 a[144:147], v8 offset:53248\n"
"v_cvt_pk_bf16_f32 v84, v56, v57\n"
"v_mfma_f32_16x16x32_bf16 a[48:51], v[160:163], v[80:83], a[48:51]\n"
"v_exp_f32_e32 v58, v58\n"
"v_mfma_f32_16x16x32_bf16 a[64:67], v[164:167], v[80:83], a[64:67]\n"
"ds_read_b64_tr_b16 v[120:121], v13 offset:49152\n"
"s_add_u32 s41, s41, s47\n"
"v_mfma_f32_16x16x32_bf16 a[80:83], v[168:171], v[80:83], a[80:83]\n"
"v_exp_f32_e32 v59, v59\n"
"v_mfma_f32_16x16x32_bf16 a[96:99], v[172:175], v[80:83], a[96:99]\n"
"v_cvt_pk_bf16_f32 v85, v58, v59\n"
"v_mfma_f32_16x16x32_bf16 a[112:115], v[176:179], v[80:83], a[112:115]\n"
"v_exp_f32_e32 v60, v60\n"
"v_mfma_f32_16x16x32_bf16 v[96:99], v[112:115], v[80:83], v[96:99]\n"
"ds_read_b128 a[148:151], v9 offset:53248\n"
"v_mfma_f32_16x16x32_bf16 v[64:67], a[160:163], v[212:215], v[40:43]\n"
"v_exp_f32_e32 v61, v61\n"
"v_mfma_f32_16x16x32_bf16 v[64:67], a[164:167], v[216:219], v[64:67]\n"
"ds_read_b64_tr_b16 v[122:123], v13 offset:53248\n"
"v_cvt_pk_bf16_f32 v86, v60, v61\n"
"v_mfma_f32_16x16x32_bf16 v[64:67], a[168:171], v[220:223], v[64:67]\n"
"v_exp_f32_e32 v62, v62\n"
"v_mfma_f32_16x16x32_bf16 v[64:67], a[172:175], v[224:227], v[64:67]\n"
"v_mfma_f32_16x16x32_bf16 v[68:71], a[176:179], v[212:215], v[40:43]\n"
"v_exp_f32_e32 v63, v63\n"
"v_mfma_f32_16x16x32_bf16 v[68:71], a[180:183], v[216:219], v[68:71]\n"
"ds_read_b128 a[152:155], v10 offset:53248\n"
"v_cvt_pk_bf16_f32 v87, v62, v63\n"
"v_mfma_f32_16x16x32_bf16 v[68:71], a[184:187], v[220:223], v[68:71]\n"
"ds_read_b64_tr_b16 v[124:125], v16 offset:49408\n"
"v_mfma_f32_16x16x32_bf16 v[68:71], a[188:191], v[224:227], v[68:71]\n"
"v_exp_f32_e32 v64, v64\n"
"v_mfma_f32_16x16x32_bf16 a[4:7], v[148:151], v[84:87], a[4:7]\n"
"buffer_load_dwordx4 v3, s[8:11], s38 offen offset:2048 lds\n"
"v_mfma_f32_16x16x32_bf16 a[20:23], v[152:155], v[84:87], a[20:23]\n"
"v_exp_f32_e32 v65, v65\n"
"v_mfma_f32_16x16x32_bf16 a[36:39], v[156:159], v[84:87], a[36:39]\n"
"ds_read_b128 a[156:159], v11 offset:53248\n"
"v_cvt_pk_bf16_f32 v88, v64, v65\n"
"v_mfma_f32_16x16x32_bf16 a[52:55], v[160:163], v[84:87], a[52:55]\n"
"v_exp_f32_e32 v66, v66\n"
"v_mfma_f32_16x16x32_bf16 a[68:71], v[164:167], v[84:87], a[68:71]\n"
"ds_read_b64_tr_b16 v[126:127], v16 offset:53504\n"
"v_mfma_f32_16x16x32_bf16 a[84:87], v[168:171], v[84:87], a[84:87]\n"
"v_exp_f32_e32 v67, v67\n"
"v_mfma_f32_16x16x32_bf16 a[100:103], v[172:175], v[84:87], a[100:103]\n"
"v_cvt_pk_bf16_f32 v89, v66, v67\n"
"v_mfma_f32_16x16x32_bf16 a[116:119], v[176:179], v[84:87], a[116:119]\n"
"v_exp_f32_e32 v68, v68\n"
"v_mfma_f32_16x16x32_bf16 v[100:103], v[112:115], v[84:87], v[100:103]\n"
"ds_read_b64_tr_b16 v[128:129], v13 offset:49408\n"
"v_mfma_f32_16x16x32_bf16 v[72:75], a[160:163], v[228:231], v[44:47]\n"
"v_exp_f32_e32 v69, v69\n"
"v_mfma_f32_16x16x32_bf16 v[72:75], a[164:167], v[232:235], v[72:75]\n"
"ds_read_b64_tr_b16 v[130:131], v13 offset:53504\n"
"v_cvt_pk_bf16_f32 v90, v68, v69\n"
"v_mfma_f32_16x16x32_bf16 v[72:75], a[168:171], v[236:239], v[72:75]\n"
"v_exp_f32_e32 v70, v70\n"
"v_mfma_f32_16x16x32_bf16 v[72:75], a[172:175], v[240:243], v[72:75]\n"
"v_mad_u32_u24 v31, v28, s43, v19\n"
"v_mfma_f32_16x16x32_bf16 v[76:79], a[176:179], v[228:231], v[44:47]\n"
"v_exp_f32_e32 v71, v71\n"
"v_mfma_f32_16x16x32_bf16 v[76:79], a[180:183], v[232:235], v[76:79]\n"
"ds_read_b64_tr_b16 v[132:133], v16 offset:49664\n"
"v_cvt_pk_bf16_f32 v91, v70, v71\n"
"v_mfma_f32_16x16x32_bf16 v[76:79], a[184:187], v[236:239], v[76:79]\n"
"ds_read_b64_tr_b16 v[134:135], v16 offset:53760\n"
"s_waitcnt lgkmcnt(5)\n"
"v_mfma_f32_16x16x32_bf16 v[76:79], a[188:191], v[240:243], v[76:79]\n"
"v_exp_f32_e32 v72, v72\n"
"v_mfma_f32_16x16x32_bf16 a[8:11], v[148:151], v[88:91], a[8:11]\n"
"buffer_load_dwordx4 v4, s[8:11], s38 offen offset:3072 lds\n"
"v_mfma_f32_16x16x32_bf16 a[24:27], v[152:155], v[88:91], a[24:27]\n"
"v_exp_f32_e32 v73, v73\n"
"v_mfma_f32_16x16x32_bf16 a[40:43], v[156:159], v[88:91], a[40:43]\n"
"ds_read_b64_tr_b16 v[136:137], v13 offset:49664\n"
"s_add_u32 s42, s42, 1\n"
"v_mfma_f32_16x16x32_bf16 a[56:59], v[160:163], v[88:91], a[56:59]\n"
"v_exp_f32_e32 v74, v74\n"
"v_mfma_f32_16x16x32_bf16 a[72:75], v[164:167], v[88:91], a[72:75]\n"
"ds_read_b64_tr_b16 v[138:139], v13 offset:53760\n"
"v_cvt_pk_bf16_f32 v92, v72, v73\n"
"v_mfma_f32_16x16x32_bf16 a[88:91], v[168:171], v[88:91], a[88:91]\n"
"v_exp_f32_e32 v75, v75\n"
"v_mfma_f32_16x16x32_bf16 a[104:107], v[172:175], v[88:91], a[104:107]\n"
"buffer_store_dwordx4 a[192:195], v31, s[28:31], s39 offen sc1\n"
"v_mfma_f32_16x16x32_bf16 a[120:123], v[176:179], v[88:91], a[120:123]\n"
"v_exp_f32_e32 v76, v76\n"
"v_mfma_f32_16x16x32_bf16 v[104:107], v[112:115], v[88:91], v[104:107]\n"
"ds_read_b64_tr_b16 v[140:141], v16 offset:49920\n"
"v_cvt_pk_bf16_f32 v93, v74, v75\n"
"v_mfma_f32_16x16x32_bf16 v[48:51], a[128:131], v[180:183], v[32:35]\n"
"v_exp_f32_e32 v77, v77\n"
"v_mfma_f32_16x16x32_bf16 v[48:51], a[132:135], v[184:187], v[48:51]\n"
"ds_read_b64_tr_b16 v[142:143], v16 offset:54016\n"
"v_cvt_pk_bf16_f32 v94, v76, v77\n"
"v_mfma_f32_16x16x32_bf16 v[48:51], a[136:139], v[188:191], v[48:51]\n"
"v_exp_f32_e32 v78, v78\n"
"v_mfma_f32_16x16x32_bf16 v[48:51], a[140:143], v[192:195], v[48:51]\n"
"s_add_u32 m0, s44, 81920\n"
"v_mfma_f32_16x16x32_bf16 v[52:55], a[144:147], v[180:183], v[32:35]\n"
"v_exp_f32_e32 v79, v79\n"
"v_mfma_f32_16x16x32_bf16 v[52:55], a[148:151], v[184:187], v[52:55]\n"
"ds_read_b64_tr_b16 v[144:145], v13 offset:49920\n"
"v_cvt_pk_bf16_f32 v95, v78, v79\n"
"v_mfma_f32_16x16x32_bf16 v[52:55], a[152:155], v[188:191], v[52:55]\n"
"ds_read_b64_tr_b16 v[146:147], v13 offset:54016\n"
"v_mfma_f32_16x16x32_bf16 v[52:55], a[156:159], v[192:195], v[52:55]\n"
"v_exp_f32_e32 v48, v48\n"
"v_mfma_f32_16x16x32_bf16 a[12:15], v[148:151], v[92:95], a[12:15]\n"
"v_readlane_b32 s43, v29, s42\n"
"v_readlane_b32 s39, v30, s42\n"
"v_mfma_f32_16x16x32_bf16 a[28:31], v[152:155], v[92:95], a[28:31]\n"
"v_exp_f32_e32 v49, v49\n"
"v_mfma_f32_16x16x32_bf16 a[44:47], v[156:159], v[92:95], a[44:47]\n"
"ds_read_b128 a[160:163], v8 offset:57344\n"
"v_cvt_pk_bf16_f32 v80, v48, v49\n"
"v_mfma_f32_16x16x32_bf16 a[60:63], v[160:163], v[92:95], a[60:63]\n"
"v_exp_f32_e32 v50, v50\n"
"v_mfma_f32_16x16x32_bf16 a[76:79], v[164:167], v[92:95], a[76:79]\n"
"ds_read_b128 a[164:167], v9 offset:57344\n"
"v_mfma_f32_16x16x32_bf16 a[92:95], v[168:171], v[92:95], a[92:95]\n"
"v_exp_f32_e32 v51, v51\n"
"v_mfma_f32_16x16x32_bf16 a[108:111], v[172:175], v[92:95], a[108:111]\n"
"buffer_load_dwordx4 v5, s[12:15], s38 offen lds\n"
"v_mfma_f32_16x16x32_bf16 a[124:127], v[176:179], v[92:95], a[124:127]\n"
"v_exp_f32_e32 v52, v52\n"
"v_mfma_f32_16x16x32_bf16 v[108:111], v[112:115], v[92:95], v[108:111]\n"
"ds_read_b128 a[168:171], v10 offset:57344\n"
"v_cvt_pk_bf16_f32 v81, v50, v51\n"
"v_mfma_f32_16x16x32_bf16 v[56:59], a[128:131], v[196:199], v[36:39]\n"
"v_exp_f32_e32 v53, v53\n"
"v_mfma_f32_16x16x32_bf16 v[56:59], a[132:135], v[200:203], v[56:59]\n"
"ds_read_b64_tr_b16 v[148:149], v16 offset:57344\n"
"v_cvt_pk_bf16_f32 v82, v52, v53\n"
"v_mfma_f32_16x16x32_bf16 v[56:59], a[136:139], v[204:207], v[56:59]\n"
"v_exp_f32_e32 v54, v54\n"
"v_mfma_f32_16x16x32_bf16 v[56:59], a[140:143], v[208:211], v[56:59]\n"
"buffer_load_dwordx4 a[212:215], v18, s[24:27], s41 offen\n"
"v_mfma_f32_16x16x32_bf16 v[60:63], a[144:147], v[196:199], v[36:39]\n"
"v_exp_f32_e32 v55, v55\n"
"v_mfma_f32_16x16x32_bf16 v[60:63], a[148:151], v[200:203], v[60:63]\n"
"ds_read_b128 a[172:175], v11 offset:57344\n"
"v_cvt_pk_bf16_f32 v83, v54, v55\n"
"v_mfma_f32_16x16x32_bf16 v[60:63], a[152:155], v[204:207], v[60:63]\n"
"ds_read_b64_tr_b16 v[150:151], v16 offset:61440\n"
"s_waitcnt lgkmcnt(6)\n"
"v_mfma_f32_16x16x32_bf16 v[60:63], a[156:159], v[208:211], v[60:63]\n"
"v_exp_f32_e32 v56, v56\n"
"v_mfma_f32_16x16x32_bf16 a[0:3], v[116:119], v[80:83], a[0:3]\n"
"buffer_load_dwordx4 v12, s[12:15], s38 offen offset:1024 lds\n"
"v_mfma_f32_16x16x32_bf16 a[16:19], v[120:123], v[80:83], a[16:19]\n"
"v_exp_f32_e32 v57, v57\n"
"v_mfma_f32_16x16x32_bf16 a[32:35], v[124:127], v[80:83], a[32:35]\n"
"ds_read_b128 a[176:179], v8 offset:61440\n"
"v_cvt_pk_bf16_f32 v84, v56, v57\n"
"v_mfma_f32_16x16x32_bf16 a[48:51], v[128:131], v[80:83], a[48:51]\n"
"v_exp_f32_e32 v58, v58\n"
"v_mfma_f32_16x16x32_bf16 a[64:67], v[132:135], v[80:83], a[64:67]\n"
"ds_read_b64_tr_b16 v[152:153], v13 offset:57344\n"
"s_add_u32 s41, s41, s47\n"
"v_mfma_f32_16x16x32_bf16 a[80:83], v[136:139], v[80:83], a[80:83]\n"
"v_exp_f32_e32 v59, v59\n"
"v_mfma_f32_16x16x32_bf16 a[96:99], v[140:143], v[80:83], a[96:99]\n"
"v_cvt_pk_bf16_f32 v85, v58, v59\n"
"v_mfma_f32_16x16x32_bf16 a[112:115], v[144:147], v[80:83], a[112:115]\n"
"v_exp_f32_e32 v60, v60\n"
"v_mfma_f32_16x16x32_bf16 v[96:99], v[112:115], v[80:83], v[96:99]\n"
"ds_read_b128 a[180:183], v9 offset:61440\n"
"v_mfma_f32_16x16x32_bf16 v[64:67], a[128:131], v[212:215], v[40:43]\n"
"v_exp_f32_e32 v61, v61\n"
"v_mfma_f32_16x16x32_bf16 v[64:67], a[132:135], v[216:219], v[64:67]\n"
"ds_read_b64_tr_b16 v[154:155], v13 offset:61440\n"
"v_cvt_pk_bf16_f32 v86, v60, v61\n"
"v_mfma_f32_16x16x32_bf16 v[64:67], a[136:139], v[220:223], v[64:67]\n"
"v_exp_f32_e32 v62, v62\n"
"v_mfma_f32_16x16x32_bf16 v[64:67], a[140:143], v[224:227], v[64:67]\n"
"v_mfma_f32_16x16x32_bf16 v[68:71], a[144:147], v[212:215], v[40:43]\n"
"v_exp_f32_e32 v63, v63\n"
"v_mfma_f32_16x16x32_bf16 v[68:71], a[148:151], v[216:219], v[68:71]\n"
"ds_read_b128 a[184:187], v10 offset:61440\n"
"v_cvt_pk_bf16_f32 v87, v62, v63\n"
"v_mfma_f32_16x16x32_bf16 v[68:71], a[152:155], v[220:223], v[68:71]\n"
"ds_read_b64_tr_b16 v[156:157], v16 offset:57600\n"
"v_mfma_f32_16x16x32_bf16 v[68:71], a[156:159], v[224:227], v[68:71]\n"
"v_exp_f32_e32 v64, v64\n"
"v_mfma_f32_16x16x32_bf16 a[4:7], v[116:119], v[84:87], a[4:7]\n"
"buffer_load_dwordx4 v5, s[12:15], s38 offen offset:2048 lds\n"
"v_mfma_f32_16x16x32_bf16 a[20:23], v[120:123], v[84:87], a[20:23]\n"
"v_exp_f32_e32 v65, v65\n"
"v_mfma_f32_16x16x32_bf16 a[36:39], v[124:127], v[84:87], a[36:39]\n"
"ds_read_b128 a[188:191], v11 offset:61440\n"
"v_cvt_pk_bf16_f32 v88, v64, v65\n"
"v_mfma_f32_16x16x32_bf16 a[52:55], v[128:131], v[84:87], a[52:55]\n"
"v_exp_f32_e32 v66, v66\n"
"v_mfma_f32_16x16x32_bf16 a[68:71], v[132:135], v[84:87], a[68:71]\n"
"ds_read_b64_tr_b16 v[158:159], v16 offset:61696\n"
"v_mfma_f32_16x16x32_bf16 a[84:87], v[136:139], v[84:87], a[84:87]\n"
"v_exp_f32_e32 v67, v67\n"
"v_mfma_f32_16x16x32_bf16 a[100:103], v[140:143], v[84:87], a[100:103]\n"
"v_cvt_pk_bf16_f32 v89, v66, v67\n"
"v_mfma_f32_16x16x32_bf16 a[116:119], v[144:147], v[84:87], a[116:119]\n"
"v_exp_f32_e32 v68, v68\n"
"v_mfma_f32_16x16x32_bf16 v[100:103], v[112:115], v[84:87], v[100:103]\n"
"ds_read_b64_tr_b16 v[160:161], v13 offset:57600\n"
"v_mfma_f32_16x16x32_bf16 v[72:75], a[128:131], v[228:231], v[44:47]\n"
"v_exp_f32_e32 v69, v69\n"
"v_mfma_f32_16x16x32_bf16 v[72:75], a[132:135], v[232:235], v[72:75]\n"
"ds_read_b64_tr_b16 v[162:163], v13 offset:61696\n"
"v_cvt_pk_bf16_f32 v90, v68, v69\n"
"v_mfma_f32_16x16x32_bf16 v[72:75], a[136:139], v[236:239], v[72:75]\n"
"v_exp_f32_e32 v70, v70\n"
"v_mfma_f32_16x16x32_bf16 v[72:75], a[140:143], v[240:243], v[72:75]\n"
"v_mad_u32_u24 v31, v28, s43, v19\n"
"v_mfma_f32_16x16x32_bf16 v[76:79], a[144:147], v[228:231], v[44:47]\n"
"v_exp_f32_e32 v71, v71\n"
"v_mfma_f32_16x16x32_bf16 v[76:79], a[148:151], v[232:235], v[76:79]\n"
"ds_read_b64_tr_b16 v[164:165], v16 offset:57856\n"
"v_cvt_pk_bf16_f32 v91, v70, v71\n"
"v_mfma_f32_16x16x32_bf16 v[76:79], a[152:155], v[236:239], v[76:79]\n"
"ds_read_b64_tr_b16 v[166:167], v16 offset:61952\n"
"s_waitcnt lgkmcnt(5)\n"
"v_mfma_f32_16x16x32_bf16 v[76:79], a[156:159], v[240:243], v[76:79]\n"
"v_exp_f32_e32 v72, v72\n"
"v_mfma_f32_16x16x32_bf16 a[8:11], v[116:119], v[88:91], a[8:11]\n"
"buffer_load_dwordx4 v12, s[12:15], s38 offen offset:3072 lds\n"
"v_mfma_f32_16x16x32_bf16 a[24:27], v[120:123], v[88:91], a[24:27]\n"
"v_exp_f32_e32 v73, v73\n"
"v_mfma_f32_16x16x32_bf16 a[40:43], v[124:127], v[88:91], a[40:43]\n"
"ds_read_b64_tr_b16 v[168:169], v13 offset:57856\n"
"s_add_u32 s42, s42, 1\n"
"v_mfma_f32_16x16x32_bf16 a[56:59], v[128:131], v[88:91], a[56:59]\n"
"v_exp_f32_e32 v74, v74\n"
"v_mfma_f32_16x16x32_bf16 a[72:75], v[132:135], v[88:91], a[72:75]\n"
"ds_read_b64_tr_b16 v[170:171], v13 offset:61952\n"
"v_cvt_pk_bf16_f32 v92, v72, v73\n"
"v_mfma_f32_16x16x32_bf16 a[88:91], v[136:139], v[88:91], a[88:91]\n"
"v_exp_f32_e32 v75, v75\n"
"v_mfma_f32_16x16x32_bf16 a[104:107], v[140:143], v[88:91], a[104:107]\n"
"buffer_store_dwordx4 a[196:199], v31, s[28:31], s39 offen sc1\n"
"v_mfma_f32_16x16x32_bf16 a[120:123], v[144:147], v[88:91], a[120:123]\n"
"v_exp_f32_e32 v76, v76\n"
"v_mfma_f32_16x16x32_bf16 v[104:107], v[112:115], v[88:91], v[104:107]\n"
"ds_read_b64_tr_b16 v[172:173], v16 offset:58112\n"
"v_cvt_pk_bf16_f32 v93, v74, v75\n"
"v_mfma_f32_16x16x32_bf16 v[48:51], a[160:163], v[180:183], v[32:35]\n"
"v_exp_f32_e32 v77, v77\n"
"v_mfma_f32_16x16x32_bf16 v[48:51], a[164:167], v[184:187], v[48:51]\n"
"ds_read_b64_tr_b16 v[174:175], v16 offset:62208\n"
"v_cvt_pk_bf16_f32 v94, v76, v77\n"
"v_mfma_f32_16x16x32_bf16 v[48:51], a[168:171], v[188:191], v[48:51]\n"
"v_exp_f32_e32 v78, v78\n"
"v_mfma_f32_16x16x32_bf16 v[48:51], a[172:175], v[192:195], v[48:51]\n"
"s_waitcnt vmcnt(12)\n"
"s_barrier\n"
"s_cmp_eq_u32 s6, s40\n"
"s_cbranch_scc0 Lattn_sig_%=\n"
"v_mov_b32_e32 v22, 0\n"
"v_mov_b32_e32 v23, 1\n"
"s_mov_b64 exec, 1\n"
"global_atomic_add v22, v23, s[4:5]\n"
"s_mov_b64 exec, -1\n"
"Lattn_sig_%=:\n"
"v_mfma_f32_16x16x32_bf16 v[52:55], a[176:179], v[180:183], v[32:35]\n"
"v_exp_f32_e32 v79, v79\n"
"v_mfma_f32_16x16x32_bf16 v[52:55], a[180:183], v[184:187], v[52:55]\n"
"ds_read_b64_tr_b16 v[176:177], v13 offset:58112\n"
"v_cvt_pk_bf16_f32 v95, v78, v79\n"
"v_mfma_f32_16x16x32_bf16 v[52:55], a[184:187], v[188:191], v[52:55]\n"
"ds_read_b64_tr_b16 v[178:179], v13 offset:62208\n"
"s_add_u32 s38, s38, 16384\n"
"v_mfma_f32_16x16x32_bf16 v[52:55], a[188:191], v[192:195], v[52:55]\n"
"v_exp_f32_e32 v48, v48\n"
"v_mfma_f32_16x16x32_bf16 a[12:15], v[116:119], v[92:95], a[12:15]\n"
"s_add_u32 m0, s44, 32768\n"
"v_readlane_b32 s43, v29, s42\n"
"v_mfma_f32_16x16x32_bf16 a[28:31], v[120:123], v[92:95], a[28:31]\n"
"v_exp_f32_e32 v49, v49\n"
"v_mfma_f32_16x16x32_bf16 a[44:47], v[124:127], v[92:95], a[44:47]\n"
"ds_read_b128 a[128:131], v8 offset:0\n"
"v_cvt_pk_bf16_f32 v80, v48, v49\n"
"v_mfma_f32_16x16x32_bf16 a[60:63], v[128:131], v[92:95], a[60:63]\n"
"v_exp_f32_e32 v50, v50\n"
"v_mfma_f32_16x16x32_bf16 a[76:79], v[132:135], v[92:95], a[76:79]\n"
"ds_read_b128 a[132:135], v9 offset:0\n"
"v_readlane_b32 s39, v30, s42\n"
"v_mfma_f32_16x16x32_bf16 a[92:95], v[136:139], v[92:95], a[92:95]\n"
"v_exp_f32_e32 v51, v51\n"
"v_mfma_f32_16x16x32_bf16 a[108:111], v[140:143], v[92:95], a[108:111]\n"
"buffer_load_dwordx4 v1, s[8:11], s38 offen lds\n"
"v_mfma_f32_16x16x32_bf16 a[124:127], v[144:147], v[92:95], a[124:127]\n"
"v_exp_f32_e32 v52, v52\n"
"v_mfma_f32_16x16x32_bf16 v[108:111], v[112:115], v[92:95], v[108:111]\n"
"ds_read_b128 a[136:139], v10 offset:0\n"
"v_cvt_pk_bf16_f32 v81, v50, v51\n"
"v_mfma_f32_16x16x32_bf16 v[56:59], a[160:163], v[196:199], v[36:39]\n"
"v_exp_f32_e32 v53, v53\n"
"v_mfma_f32_16x16x32_bf16 v[56:59], a[164:167], v[200:203], v[56:59]\n"
"ds_read_b64_tr_b16 v[116:117], v16 offset:0\n"
"v_cvt_pk_bf16_f32 v82, v52, v53\n"
"v_mfma_f32_16x16x32_bf16 v[56:59], a[168:171], v[204:207], v[56:59]\n"
"v_exp_f32_e32 v54, v54\n"
"v_mfma_f32_16x16x32_bf16 v[56:59], a[172:175], v[208:211], v[56:59]\n"
"s_waitcnt lgkmcnt(4)\n"
"v_mfma_f32_16x16x32_bf16 v[60:63], a[176:179], v[196:199], v[36:39]\n"
"v_exp_f32_e32 v55, v55\n"
"v_mfma_f32_16x16x32_bf16 v[60:63], a[180:183], v[200:203], v[60:63]\n"
"ds_read_b128 a[140:143], v11 offset:0\n"
"v_cvt_pk_bf16_f32 v83, v54, v55\n"
"v_mfma_f32_16x16x32_bf16 v[60:63], a[184:187], v[204:207], v[60:63]\n"
"ds_read_b64_tr_b16 v[118:119], v16 offset:4096\n"
"v_mfma_f32_16x16x32_bf16 v[60:63], a[188:191], v[208:211], v[60:63]\n"
"v_exp_f32_e32 v56, v56\n"
"v_mfma_f32_16x16x32_bf16 a[0:3], v[148:151], v[80:83], a[0:3]\n"
"buffer_load_dwordx4 v2, s[8:11], s38 offen offset:1024 lds\n"
"v_mfma_f32_16x16x32_bf16 a[16:19], v[152:155], v[80:83], a[16:19]\n"
"v_exp_f32_e32 v57, v57\n"
"v_mfma_f32_16x16x32_bf16 a[32:35], v[156:159], v[80:83], a[32:35]\n"
"ds_read_b128 a[144:147], v8 offset:4096\n"
"v_cvt_pk_bf16_f32 v84, v56, v57\n"
"v_mfma_f32_16x16x32_bf16 a[48:51], v[160:163], v[80:83], a[48:51]\n"
"v_exp_f32_e32 v58, v58\n"
"v_mfma_f32_16x16x32_bf16 a[64:67], v[164:167], v[80:83], a[64:67]\n"
"ds_read_b64_tr_b16 v[120:121], v13 offset:0\n"
"v_mfma_f32_16x16x32_bf16 a[80:83], v[168:171], v[80:83], a[80:83]\n"
"v_exp_f32_e32 v59, v59\n"
"v_mfma_f32_16x16x32_bf16 a[96:99], v[172:175], v[80:83], a[96:99]\n"
"v_cvt_pk_bf16_f32 v85, v58, v59\n"
"v_mfma_f32_16x16x32_bf16 a[112:115], v[176:179], v[80:83], a[112:115]\n"
"v_exp_f32_e32 v60, v60\n"
"v_mfma_f32_16x16x32_bf16 v[96:99], v[112:115], v[80:83], v[96:99]\n"
"ds_read_b128 a[148:151], v9 offset:4096\n"
"v_mfma_f32_16x16x32_bf16 v[64:67], a[160:163], v[212:215], v[40:43]\n"
"v_exp_f32_e32 v61, v61\n"
"v_mfma_f32_16x16x32_bf16 v[64:67], a[164:167], v[216:219], v[64:67]\n"
"ds_read_b64_tr_b16 v[122:123], v13 offset:4096\n"
"v_cvt_pk_bf16_f32 v86, v60, v61\n"
"v_mfma_f32_16x16x32_bf16 v[64:67], a[168:171], v[220:223], v[64:67]\n"
"v_exp_f32_e32 v62, v62\n"
"v_mfma_f32_16x16x32_bf16 v[64:67], a[172:175], v[224:227], v[64:67]\n"
"v_mfma_f32_16x16x32_bf16 v[68:71], a[176:179], v[212:215], v[40:43]\n"
"v_exp_f32_e32 v63, v63\n"
"v_mfma_f32_16x16x32_bf16 v[68:71], a[180:183], v[216:219], v[68:71]\n"
"ds_read_b128 a[152:155], v10 offset:4096\n"
"v_cvt_pk_bf16_f32 v87, v62, v63\n"
"v_mfma_f32_16x16x32_bf16 v[68:71], a[184:187], v[220:223], v[68:71]\n"
"ds_read_b64_tr_b16 v[124:125], v16 offset:256\n"
"v_mfma_f32_16x16x32_bf16 v[68:71], a[188:191], v[224:227], v[68:71]\n"
"v_exp_f32_e32 v64, v64\n"
"v_mfma_f32_16x16x32_bf16 a[4:7], v[148:151], v[84:87], a[4:7]\n"
"buffer_load_dwordx4 v3, s[8:11], s38 offen offset:2048 lds\n"
"v_mfma_f32_16x16x32_bf16 a[20:23], v[152:155], v[84:87], a[20:23]\n"
"v_exp_f32_e32 v65, v65\n"
"v_mfma_f32_16x16x32_bf16 a[36:39], v[156:159], v[84:87], a[36:39]\n"
"ds_read_b128 a[156:159], v11 offset:4096\n"
"v_cvt_pk_bf16_f32 v88, v64, v65\n"
"v_mfma_f32_16x16x32_bf16 a[52:55], v[160:163], v[84:87], a[52:55]\n"
"v_exp_f32_e32 v66, v66\n"
"v_mfma_f32_16x16x32_bf16 a[68:71], v[164:167], v[84:87], a[68:71]\n"
"ds_read_b64_tr_b16 v[126:127], v16 offset:4352\n"
"v_mfma_f32_16x16x32_bf16 a[84:87], v[168:171], v[84:87], a[84:87]\n"
"v_exp_f32_e32 v67, v67\n"
"v_mfma_f32_16x16x32_bf16 a[100:103], v[172:175], v[84:87], a[100:103]\n"
"v_cvt_pk_bf16_f32 v89, v66, v67\n"
"v_mfma_f32_16x16x32_bf16 a[116:119], v[176:179], v[84:87], a[116:119]\n"
"v_exp_f32_e32 v68, v68\n"
"v_mfma_f32_16x16x32_bf16 v[100:103], v[112:115], v[84:87], v[100:103]\n"
"ds_read_b64_tr_b16 v[128:129], v13 offset:256\n"
"v_mfma_f32_16x16x32_bf16 v[72:75], a[160:163], v[228:231], v[44:47]\n"
"v_exp_f32_e32 v69, v69\n"
"v_mfma_f32_16x16x32_bf16 v[72:75], a[164:167], v[232:235], v[72:75]\n"
"ds_read_b64_tr_b16 v[130:131], v13 offset:4352\n"
"v_cvt_pk_bf16_f32 v90, v68, v69\n"
"v_mfma_f32_16x16x32_bf16 v[72:75], a[168:171], v[236:239], v[72:75]\n"
"v_exp_f32_e32 v70, v70\n"
"v_mfma_f32_16x16x32_bf16 v[72:75], a[172:175], v[240:243], v[72:75]\n"
"v_mad_u32_u24 v31, v28, s43, v19\n"
"v_mfma_f32_16x16x32_bf16 v[76:79], a[176:179], v[228:231], v[44:47]\n"
"v_exp_f32_e32 v71, v71\n"
"v_mfma_f32_16x16x32_bf16 v[76:79], a[180:183], v[232:235], v[76:79]\n"
"ds_read_b64_tr_b16 v[132:133], v16 offset:512\n"
"v_cvt_pk_bf16_f32 v91, v70, v71\n"
"v_mfma_f32_16x16x32_bf16 v[76:79], a[184:187], v[236:239], v[76:79]\n"
"ds_read_b64_tr_b16 v[134:135], v16 offset:4608\n"
"s_waitcnt lgkmcnt(5)\n"
"v_mfma_f32_16x16x32_bf16 v[76:79], a[188:191], v[240:243], v[76:79]\n"
"v_exp_f32_e32 v72, v72\n"
"v_mfma_f32_16x16x32_bf16 a[8:11], v[148:151], v[88:91], a[8:11]\n"
"buffer_load_dwordx4 v4, s[8:11], s38 offen offset:3072 lds\n"
"v_mfma_f32_16x16x32_bf16 a[24:27], v[152:155], v[88:91], a[24:27]\n"
"v_exp_f32_e32 v73, v73\n"
"v_mfma_f32_16x16x32_bf16 a[40:43], v[156:159], v[88:91], a[40:43]\n"
"ds_read_b64_tr_b16 v[136:137], v13 offset:512\n"
"s_add_u32 s42, s42, 1\n"
"v_mfma_f32_16x16x32_bf16 a[56:59], v[160:163], v[88:91], a[56:59]\n"
"v_exp_f32_e32 v74, v74\n"
"v_mfma_f32_16x16x32_bf16 a[72:75], v[164:167], v[88:91], a[72:75]\n"
"ds_read_b64_tr_b16 v[138:139], v13 offset:4608\n"
"v_cvt_pk_bf16_f32 v92, v72, v73\n"
"v_mfma_f32_16x16x32_bf16 a[88:91], v[168:171], v[88:91], a[88:91]\n"
"v_exp_f32_e32 v75, v75\n"
"v_mfma_f32_16x16x32_bf16 a[104:107], v[172:175], v[88:91], a[104:107]\n"
"buffer_store_dwordx4 a[200:203], v31, s[28:31], s39 offen sc1\n"
"v_mfma_f32_16x16x32_bf16 a[120:123], v[176:179], v[88:91], a[120:123]\n"
"v_exp_f32_e32 v76, v76\n"
"v_mfma_f32_16x16x32_bf16 v[104:107], v[112:115], v[88:91], v[104:107]\n"
"ds_read_b64_tr_b16 v[140:141], v16 offset:768\n"
"v_cvt_pk_bf16_f32 v93, v74, v75\n"
"s_sub_u32 s40, s40, 1\n"
"s_cmp_lg_u32 s40, 0\n"
"s_cbranch_scc1 Lattn_loop_%=\n"
"v_mfma_f32_16x16x32_bf16 v[48:51], a[128:131], v[180:183], v[32:35]\n"
"v_exp_f32_e32 v77, v77\n"
"v_mfma_f32_16x16x32_bf16 v[48:51], a[132:135], v[184:187], v[48:51]\n"
"ds_read_b64_tr_b16 v[142:143], v16 offset:4864\n"
"v_cvt_pk_bf16_f32 v94, v76, v77\n"
"v_mfma_f32_16x16x32_bf16 v[48:51], a[136:139], v[188:191], v[48:51]\n"
"v_exp_f32_e32 v78, v78\n"
"v_mfma_f32_16x16x32_bf16 v[48:51], a[140:143], v[192:195], v[48:51]\n"
"s_add_u32 m0, s44, 98304\n"
"v_mfma_f32_16x16x32_bf16 v[52:55], a[144:147], v[180:183], v[32:35]\n"
"v_exp_f32_e32 v79, v79\n"
"v_mfma_f32_16x16x32_bf16 v[52:55], a[148:151], v[184:187], v[52:55]\n"
"ds_read_b64_tr_b16 v[144:145], v13 offset:768\n"
"v_cvt_pk_bf16_f32 v95, v78, v79\n"
"v_mfma_f32_16x16x32_bf16 v[52:55], a[152:155], v[188:191], v[52:55]\n"
"ds_read_b64_tr_b16 v[146:147], v13 offset:4864\n"
"v_mfma_f32_16x16x32_bf16 v[52:55], a[156:159], v[192:195], v[52:55]\n"
"v_exp_f32_e32 v48, v48\n"
"v_mfma_f32_16x16x32_bf16 a[12:15], v[148:151], v[92:95], a[12:15]\n"
"v_mfma_f32_16x16x32_bf16 a[28:31], v[152:155], v[92:95], a[28:31]\n"
"v_exp_f32_e32 v49, v49\n"
"v_mfma_f32_16x16x32_bf16 a[44:47], v[156:159], v[92:95], a[44:47]\n"
"ds_read_b128 a[160:163], v8 offset:8192\n"
"v_cvt_pk_bf16_f32 v80, v48, v49\n"
"v_mfma_f32_16x16x32_bf16 a[60:63], v[160:163], v[92:95], a[60:63]\n"
"v_exp_f32_e32 v50, v50\n"
"v_mfma_f32_16x16x32_bf16 a[76:79], v[164:167], v[92:95], a[76:79]\n"
"ds_read_b128 a[164:167], v9 offset:8192\n"
"v_mfma_f32_16x16x32_bf16 a[92:95], v[168:171], v[92:95], a[92:95]\n"
"v_exp_f32_e32 v51, v51\n"
"v_mfma_f32_16x16x32_bf16 a[108:111], v[172:175], v[92:95], a[108:111]\n"
"buffer_load_dwordx4 v5, s[12:15], s38 offen lds\n"
"v_mfma_f32_16x16x32_bf16 a[124:127], v[176:179], v[92:95], a[124:127]\n"
"v_exp_f32_e32 v52, v52\n"
"v_mfma_f32_16x16x32_bf16 v[108:111], v[112:115], v[92:95], v[108:111]\n"
"ds_read_b128 a[168:171], v10 offset:8192\n"
"v_cvt_pk_bf16_f32 v81, v50, v51\n"
"v_mfma_f32_16x16x32_bf16 v[56:59], a[128:131], v[196:199], v[36:39]\n"
"v_exp_f32_e32 v53, v53\n"
"v_mfma_f32_16x16x32_bf16 v[56:59], a[132:135], v[200:203], v[56:59]\n"
"ds_read_b64_tr_b16 v[148:149], v16 offset:8192\n"
"v_cvt_pk_bf16_f32 v82, v52, v53\n"
"v_mfma_f32_16x16x32_bf16 v[56:59], a[136:139], v[204:207], v[56:59]\n"
"v_exp_f32_e32 v54, v54\n"
"v_mfma_f32_16x16x32_bf16 v[56:59], a[140:143], v[208:211], v[56:59]\n"
"v_and_b32_e32 v24, 112, v7\n"
"v_lshlrev_b32_e32 v24, 4, v24\n"
"global_load_dword v25, v24, s[2:3] sc1\n"
"v_mfma_f32_16x16x32_bf16 v[60:63], a[144:147], v[196:199], v[36:39]\n"
"v_exp_f32_e32 v55, v55\n"
"v_mfma_f32_16x16x32_bf16 v[60:63], a[148:151], v[200:203], v[60:63]\n"
"ds_read_b128 a[172:175], v11 offset:8192\n"
"v_cvt_pk_bf16_f32 v83, v54, v55\n"
"v_mfma_f32_16x16x32_bf16 v[60:63], a[152:155], v[204:207], v[60:63]\n"
"ds_read_b64_tr_b16 v[150:151], v16 offset:12288\n"
"s_waitcnt lgkmcnt(6)\n"
"v_mfma_f32_16x16x32_bf16 v[60:63], a[156:159], v[208:211], v[60:63]\n"
"v_exp_f32_e32 v56, v56\n"
"v_mfma_f32_16x16x32_bf16 a[0:3], v[116:119], v[80:83], a[0:3]\n"
"buffer_load_dwordx4 v12, s[12:15], s38 offen offset:1024 lds\n"
"v_mfma_f32_16x16x32_bf16 a[16:19], v[120:123], v[80:83], a[16:19]\n"
"v_exp_f32_e32 v57, v57\n"
"v_mfma_f32_16x16x32_bf16 a[32:35], v[124:127], v[80:83], a[32:35]\n"
"ds_read_b128 a[176:179], v8 offset:12288\n"
"v_cvt_pk_bf16_f32 v84, v56, v57\n"
"v_mfma_f32_16x16x32_bf16 a[48:51], v[128:131], v[80:83], a[48:51]\n"
"v_exp_f32_e32 v58, v58\n"
"v_mfma_f32_16x16x32_bf16 a[64:67], v[132:135], v[80:83], a[64:67]\n"
"ds_read_b64_tr_b16 v[152:153], v13 offset:8192\n"
"v_mfma_f32_16x16x32_bf16 a[80:83], v[136:139], v[80:83], a[80:83]\n"
"v_exp_f32_e32 v59, v59\n"
"v_mfma_f32_16x16x32_bf16 a[96:99], v[140:143], v[80:83], a[96:99]\n"
"v_cvt_pk_bf16_f32 v85, v58, v59\n"
"v_mfma_f32_16x16x32_bf16 a[112:115], v[144:147], v[80:83], a[112:115]\n"
"v_exp_f32_e32 v60, v60\n"
"v_mfma_f32_16x16x32_bf16 v[96:99], v[112:115], v[80:83], v[96:99]\n"
"ds_read_b128 a[180:183], v9 offset:12288\n"
"v_mfma_f32_16x16x32_bf16 v[64:67], a[128:131], v[212:215], v[40:43]\n"
"v_exp_f32_e32 v61, v61\n"
"v_mfma_f32_16x16x32_bf16 v[64:67], a[132:135], v[216:219], v[64:67]\n"
"ds_read_b64_tr_b16 v[154:155], v13 offset:12288\n"
"v_cvt_pk_bf16_f32 v86, v60, v61\n"
"v_mfma_f32_16x16x32_bf16 v[64:67], a[136:139], v[220:223], v[64:67]\n"
"v_exp_f32_e32 v62, v62\n"
"v_mfma_f32_16x16x32_bf16 v[64:67], a[140:143], v[224:227], v[64:67]\n"
"v_mfma_f32_16x16x32_bf16 v[68:71], a[144:147], v[212:215], v[40:43]\n"
"v_exp_f32_e32 v63, v63\n"
"v_mfma_f32_16x16x32_bf16 v[68:71], a[148:151], v[216:219], v[68:71]\n"
"ds_read_b128 a[184:187], v10 offset:12288\n"
"v_cvt_pk_bf16_f32 v87, v62, v63\n"
"v_mfma_f32_16x16x32_bf16 v[68:71], a[152:155], v[220:223], v[68:71]\n"
"ds_read_b64_tr_b16 v[156:157], v16 offset:8448\n"
"v_mfma_f32_16x16x32_bf16 v[68:71], a[156:159], v[224:227], v[68:71]\n"
"v_exp_f32_e32 v64, v64\n"
"v_mfma_f32_16x16x32_bf16 a[4:7], v[116:119], v[84:87], a[4:7]\n"
"buffer_load_dwordx4 v5, s[12:15], s38 offen offset:2048 lds\n"
"v_mfma_f32_16x16x32_bf16 a[20:23], v[120:123], v[84:87], a[20:23]\n"
"v_exp_f32_e32 v65, v65\n"
"v_mfma_f32_16x16x32_bf16 a[36:39], v[124:127], v[84:87], a[36:39]\n"
"ds_read_b128 a[188:191], v11 offset:12288\n"
"v_cvt_pk_bf16_f32 v88, v64, v65\n"
"v_mfma_f32_16x16x32_bf16 a[52:55], v[128:131], v[84:87], a[52:55]\n"
"v_exp_f32_e32 v66, v66\n"
"v_mfma_f32_16x16x32_bf16 a[68:71], v[132:135], v[84:87], a[68:71]\n"
"ds_read_b64_tr_b16 v[158:159], v16 offset:12544\n"
"v_mfma_f32_16x16x32_bf16 a[84:87], v[136:139], v[84:87], a[84:87]\n"
"v_exp_f32_e32 v67, v67\n"
"v_mfma_f32_16x16x32_bf16 a[100:103], v[140:143], v[84:87], a[100:103]\n"
"v_cvt_pk_bf16_f32 v89, v66, v67\n"
"v_mfma_f32_16x16x32_bf16 a[116:119], v[144:147], v[84:87], a[116:119]\n"
"v_exp_f32_e32 v68, v68\n"
"v_mfma_f32_16x16x32_bf16 v[100:103], v[112:115], v[84:87], v[100:103]\n"
"ds_read_b64_tr_b16 v[160:161], v13 offset:8448\n"
"v_mfma_f32_16x16x32_bf16 v[72:75], a[128:131], v[228:231], v[44:47]\n"
"v_exp_f32_e32 v69, v69\n"
"v_mfma_f32_16x16x32_bf16 v[72:75], a[132:135], v[232:235], v[72:75]\n"
"ds_read_b64_tr_b16 v[162:163], v13 offset:12544\n"
"v_cvt_pk_bf16_f32 v90, v68, v69\n"
"v_mfma_f32_16x16x32_bf16 v[72:75], a[136:139], v[236:239], v[72:75]\n"
"v_exp_f32_e32 v70, v70\n"
"v_mfma_f32_16x16x32_bf16 v[72:75], a[140:143], v[240:243], v[72:75]\n"
"v_mfma_f32_16x16x32_bf16 v[76:79], a[144:147], v[228:231], v[44:47]\n"
"v_exp_f32_e32 v71, v71\n"
"v_mfma_f32_16x16x32_bf16 v[76:79], a[148:151], v[232:235], v[76:79]\n"
"ds_read_b64_tr_b16 v[164:165], v16 offset:8704\n"
"v_cvt_pk_bf16_f32 v91, v70, v71\n"
"v_mfma_f32_16x16x32_bf16 v[76:79], a[152:155], v[236:239], v[76:79]\n"
"ds_read_b64_tr_b16 v[166:167], v16 offset:12800\n"
"s_waitcnt lgkmcnt(5)\n"
"v_mfma_f32_16x16x32_bf16 v[76:79], a[156:159], v[240:243], v[76:79]\n"
"v_exp_f32_e32 v72, v72\n"
"v_mfma_f32_16x16x32_bf16 a[8:11], v[116:119], v[88:91], a[8:11]\n"
"buffer_load_dwordx4 v12, s[12:15], s38 offen offset:3072 lds\n"
"v_mfma_f32_16x16x32_bf16 a[24:27], v[120:123], v[88:91], a[24:27]\n"
"v_exp_f32_e32 v73, v73\n"
"v_mfma_f32_16x16x32_bf16 a[40:43], v[124:127], v[88:91], a[40:43]\n"
"ds_read_b64_tr_b16 v[168:169], v13 offset:8704\n"
"v_cvt_pk_bf16_f32 v92, v72, v73\n"
"v_mfma_f32_16x16x32_bf16 a[56:59], v[128:131], v[88:91], a[56:59]\n"
"v_exp_f32_e32 v74, v74\n"
"v_mfma_f32_16x16x32_bf16 a[72:75], v[132:135], v[88:91], a[72:75]\n"
"ds_read_b64_tr_b16 v[170:171], v13 offset:12800\n"
"v_mfma_f32_16x16x32_bf16 a[88:91], v[136:139], v[88:91], a[88:91]\n"
"v_exp_f32_e32 v75, v75\n"
"v_mfma_f32_16x16x32_bf16 a[104:107], v[140:143], v[88:91], a[104:107]\n"
"v_cvt_pk_bf16_f32 v93, v74, v75\n"
"v_mfma_f32_16x16x32_bf16 a[120:123], v[144:147], v[88:91], a[120:123]\n"
"v_exp_f32_e32 v76, v76\n"
"v_mfma_f32_16x16x32_bf16 v[104:107], v[112:115], v[88:91], v[104:107]\n"
"ds_read_b64_tr_b16 v[172:173], v16 offset:8960\n"
"v_mfma_f32_16x16x32_bf16 v[48:51], a[160:163], v[180:183], v[32:35]\n"
"v_exp_f32_e32 v77, v77\n"
"v_mfma_f32_16x16x32_bf16 v[48:51], a[164:167], v[184:187], v[48:51]\n"
"ds_read_b64_tr_b16 v[174:175], v16 offset:13056\n"
"v_cvt_pk_bf16_f32 v94, v76, v77\n"
"v_mfma_f32_16x16x32_bf16 v[48:51], a[168:171], v[188:191], v[48:51]\n"
"v_exp_f32_e32 v78, v78\n"
"v_mfma_f32_16x16x32_bf16 v[48:51], a[172:175], v[192:195], v[48:51]\n"
"s_waitcnt vmcnt(11)\n"
"s_barrier\n"
"v_mfma_f32_16x16x32_bf16 v[52:55], a[176:179], v[180:183], v[32:35]\n"
"v_exp_f32_e32 v79, v79\n"
"v_mfma_f32_16x16x32_bf16 v[52:55], a[180:183], v[184:187], v[52:55]\n"
"ds_read_b64_tr_b16 v[176:177], v13 offset:8960\n"
"v_cvt_pk_bf16_f32 v95, v78, v79\n"
"v_mfma_f32_16x16x32_bf16 v[52:55], a[184:187], v[188:191], v[52:55]\n"
"ds_read_b64_tr_b16 v[178:179], v13 offset:13056\n"
"s_add_u32 s38, s38, 16384\n"
"v_mfma_f32_16x16x32_bf16 v[52:55], a[188:191], v[192:195], v[52:55]\n"
"v_exp_f32_e32 v48, v48\n"
"v_mfma_f32_16x16x32_bf16 a[12:15], v[116:119], v[92:95], a[12:15]\n"
"s_add_u32 m0, s44, 49152\n"
"v_mfma_f32_16x16x32_bf16 a[28:31], v[120:123], v[92:95], a[28:31]\n"
"v_exp_f32_e32 v49, v49\n"
"v_mfma_f32_16x16x32_bf16 a[44:47], v[124:127], v[92:95], a[44:47]\n"
"ds_read_b128 a[128:131], v8 offset:16384\n"
"v_cvt_pk_bf16_f32 v80, v48, v49\n"
"v_mfma_f32_16x16x32_bf16 a[60:63], v[128:131], v[92:95], a[60:63]\n"
"v_exp_f32_e32 v50, v50\n"
"v_mfma_f32_16x16x32_bf16 a[76:79], v[132:135], v[92:95], a[76:79]\n"
"ds_read_b128 a[132:135], v9 offset:16384\n"
"v_mfma_f32_16x16x32_bf16 a[92:95], v[136:139], v[92:95], a[92:95]\n"
"v_exp_f32_e32 v51, v51\n"
"v_mfma_f32_16x16x32_bf16 a[108:111], v[140:143], v[92:95], a[108:111]\n"
"buffer_load_dwordx4 v1, s[8:11], s38 offen lds\n"
"v_mfma_f32_16x16x32_bf16 a[124:127], v[144:147], v[92:95], a[124:127]\n"
"v_exp_f32_e32 v52, v52\n"
"v_mfma_f32_16x16x32_bf16 v[108:111], v[112:115], v[92:95], v[108:111]\n"
"ds_read_b128 a[136:139], v10 offset:16384\n"
"v_cvt_pk_bf16_f32 v81, v50, v51\n"
"v_mfma_f32_16x16x32_bf16 v[56:59], a[160:163], v[196:199], v[36:39]\n"
"v_exp_f32_e32 v53, v53\n"
"v_mfma_f32_16x16x32_bf16 v[56:59], a[164:167], v[200:203], v[56:59]\n"
"ds_read_b64_tr_b16 v[116:117], v16 offset:16384\n"
"v_cvt_pk_bf16_f32 v82, v52, v53\n"
"v_mfma_f32_16x16x32_bf16 v[56:59], a[168:171], v[204:207], v[56:59]\n"
"v_exp_f32_e32 v54, v54\n"
"v_mfma_f32_16x16x32_bf16 v[56:59], a[172:175], v[208:211], v[56:59]\n"
"s_waitcnt lgkmcnt(4)\n"
"v_mfma_f32_16x16x32_bf16 v[60:63], a[176:179], v[196:199], v[36:39]\n"
"v_exp_f32_e32 v55, v55\n"
"v_mfma_f32_16x16x32_bf16 v[60:63], a[180:183], v[200:203], v[60:63]\n"
"ds_read_b128 a[140:143], v11 offset:16384\n"
"v_cvt_pk_bf16_f32 v83, v54, v55\n"
"v_mfma_f32_16x16x32_bf16 v[60:63], a[184:187], v[204:207], v[60:63]\n"
"ds_read_b64_tr_b16 v[118:119], v16 offset:20480\n"
"v_mfma_f32_16x16x32_bf16 v[60:63], a[188:191], v[208:211], v[60:63]\n"
"v_exp_f32_e32 v56, v56\n"
"v_mfma_f32_16x16x32_bf16 a[0:3], v[148:151], v[80:83], a[0:3]\n"
"buffer_load_dwordx4 v2, s[8:11], s38 offen offset:1024 lds\n"
"v_mfma_f32_16x16x32_bf16 a[16:19], v[152:155], v[80:83], a[16:19]\n"
"v_exp_f32_e32 v57, v57\n"
"v_mfma_f32_16x16x32_bf16 a[32:35], v[156:159], v[80:83], a[32:35]\n"
"ds_read_b128 a[144:147], v8 offset:20480\n"
"v_cvt_pk_bf16_f32 v84, v56, v57\n"
"v_mfma_f32_16x16x32_bf16 a[48:51], v[160:163], v[80:83], a[48:51]\n"
"v_exp_f32_e32 v58, v58\n"
"v_mfma_f32_16x16x32_bf16 a[64:67], v[164:167], v[80:83], a[64:67]\n"
"ds_read_b64_tr_b16 v[120:121], v13 offset:16384\n"
"v_mfma_f32_16x16x32_bf16 a[80:83], v[168:171], v[80:83], a[80:83]\n"
"v_exp_f32_e32 v59, v59\n"
"v_mfma_f32_16x16x32_bf16 a[96:99], v[172:175], v[80:83], a[96:99]\n"
"v_cvt_pk_bf16_f32 v85, v58, v59\n"
"v_mfma_f32_16x16x32_bf16 a[112:115], v[176:179], v[80:83], a[112:115]\n"
"v_exp_f32_e32 v60, v60\n"
"v_mfma_f32_16x16x32_bf16 v[96:99], v[112:115], v[80:83], v[96:99]\n"
"ds_read_b128 a[148:151], v9 offset:20480\n"
"v_mfma_f32_16x16x32_bf16 v[64:67], a[160:163], v[212:215], v[40:43]\n"
"v_exp_f32_e32 v61, v61\n"
"v_mfma_f32_16x16x32_bf16 v[64:67], a[164:167], v[216:219], v[64:67]\n"
"ds_read_b64_tr_b16 v[122:123], v13 offset:20480\n"
"v_cvt_pk_bf16_f32 v86, v60, v61\n"
"v_mfma_f32_16x16x32_bf16 v[64:67], a[168:171], v[220:223], v[64:67]\n"
"v_exp_f32_e32 v62, v62\n"
"v_mfma_f32_16x16x32_bf16 v[64:67], a[172:175], v[224:227], v[64:67]\n"
"v_mfma_f32_16x16x32_bf16 v[68:71], a[176:179], v[212:215], v[40:43]\n"
"v_exp_f32_e32 v63, v63\n"
"v_mfma_f32_16x16x32_bf16 v[68:71], a[180:183], v[216:219], v[68:71]\n"
"ds_read_b128 a[152:155], v10 offset:20480\n"
"v_cvt_pk_bf16_f32 v87, v62, v63\n"
"v_mfma_f32_16x16x32_bf16 v[68:71], a[184:187], v[220:223], v[68:71]\n"
"ds_read_b64_tr_b16 v[124:125], v16 offset:16640\n"
"v_mfma_f32_16x16x32_bf16 v[68:71], a[188:191], v[224:227], v[68:71]\n"
"v_exp_f32_e32 v64, v64\n"
"v_mfma_f32_16x16x32_bf16 a[4:7], v[148:151], v[84:87], a[4:7]\n"
"buffer_load_dwordx4 v3, s[8:11], s38 offen offset:2048 lds\n"
"v_mfma_f32_16x16x32_bf16 a[20:23], v[152:155], v[84:87], a[20:23]\n"
"v_exp_f32_e32 v65, v65\n"
"v_mfma_f32_16x16x32_bf16 a[36:39], v[156:159], v[84:87], a[36:39]\n"
"ds_read_b128 a[156:159], v11 offset:20480\n"
"v_cvt_pk_bf16_f32 v88, v64, v65\n"
"v_mfma_f32_16x16x32_bf16 a[52:55], v[160:163], v[84:87], a[52:55]\n"
"v_exp_f32_e32 v66, v66\n"
"v_mfma_f32_16x16x32_bf16 a[68:71], v[164:167], v[84:87], a[68:71]\n"
"ds_read_b64_tr_b16 v[126:127], v16 offset:20736\n"
"v_mfma_f32_16x16x32_bf16 a[84:87], v[168:171], v[84:87], a[84:87]\n"
"v_exp_f32_e32 v67, v67\n"
"v_mfma_f32_16x16x32_bf16 a[100:103], v[172:175], v[84:87], a[100:103]\n"
"v_cvt_pk_bf16_f32 v89, v66, v67\n"
"v_mfma_f32_16x16x32_bf16 a[116:119], v[176:179], v[84:87], a[116:119]\n"
"v_exp_f32_e32 v68, v68\n"
"v_mfma_f32_16x16x32_bf16 v[100:103], v[112:115], v[84:87], v[100:103]\n"
"ds_read_b64_tr_b16 v[128:129], v13 offset:16640\n"
"v_mfma_f32_16x16x32_bf16 v[72:75], a[160:163], v[228:231], v[44:47]\n"
"v_exp_f32_e32 v69, v69\n"
"v_mfma_f32_16x16x32_bf16 v[72:75], a[164:167], v[232:235], v[72:75]\n"
"ds_read_b64_tr_b16 v[130:131], v13 offset:20736\n"
"v_cvt_pk_bf16_f32 v90, v68, v69\n"
"v_mfma_f32_16x16x32_bf16 v[72:75], a[168:171], v[236:239], v[72:75]\n"
"v_exp_f32_e32 v70, v70\n"
"v_mfma_f32_16x16x32_bf16 v[72:75], a[172:175], v[240:243], v[72:75]\n"
"v_mfma_f32_16x16x32_bf16 v[76:79], a[176:179], v[228:231], v[44:47]\n"
"v_exp_f32_e32 v71, v71\n"
"v_mfma_f32_16x16x32_bf16 v[76:79], a[180:183], v[232:235], v[76:79]\n"
"ds_read_b64_tr_b16 v[132:133], v16 offset:16896\n"
"v_cvt_pk_bf16_f32 v91, v70, v71\n"
"v_mfma_f32_16x16x32_bf16 v[76:79], a[184:187], v[236:239], v[76:79]\n"
"ds_read_b64_tr_b16 v[134:135], v16 offset:20992\n"
"s_waitcnt lgkmcnt(5)\n"
"v_mfma_f32_16x16x32_bf16 v[76:79], a[188:191], v[240:243], v[76:79]\n"
"v_exp_f32_e32 v72, v72\n"
"v_mfma_f32_16x16x32_bf16 a[8:11], v[148:151], v[88:91], a[8:11]\n"
"buffer_load_dwordx4 v4, s[8:11], s38 offen offset:3072 lds\n"
"v_mfma_f32_16x16x32_bf16 a[24:27], v[152:155], v[88:91], a[24:27]\n"
"v_exp_f32_e32 v73, v73\n"
"v_mfma_f32_16x16x32_bf16 a[40:43], v[156:159], v[88:91], a[40:43]\n"
"ds_read_b64_tr_b16 v[136:137], v13 offset:16896\n"
"v_cvt_pk_bf16_f32 v92, v72, v73\n"
"v_mfma_f32_16x16x32_bf16 a[56:59], v[160:163], v[88:91], a[56:59]\n"
"v_exp_f32_e32 v74, v74\n"
"v_mfma_f32_16x16x32_bf16 a[72:75], v[164:167], v[88:91], a[72:75]\n"
"ds_read_b64_tr_b16 v[138:139], v13 offset:20992\n"
"v_mfma_f32_16x16x32_bf16 a[88:91], v[168:171], v[88:91], a[88:91]\n"
"v_exp_f32_e32 v75, v75\n"
"v_mfma_f32_16x16x32_bf16 a[104:107], v[172:175], v[88:91], a[104:107]\n"
"v_cvt_pk_bf16_f32 v93, v74, v75\n"
"v_mfma_f32_16x16x32_bf16 a[120:123], v[176:179], v[88:91], a[120:123]\n"
"v_exp_f32_e32 v76, v76\n"
"v_mfma_f32_16x16x32_bf16 v[104:107], v[112:115], v[88:91], v[104:107]\n"
"ds_read_b64_tr_b16 v[140:141], v16 offset:17152\n"
"v_mfma_f32_16x16x32_bf16 v[48:51], a[128:131], v[180:183], v[32:35]\n"
"v_exp_f32_e32 v77, v77\n"
"v_mfma_f32_16x16x32_bf16 v[48:51], a[132:135], v[184:187], v[48:51]\n"
"ds_read_b64_tr_b16 v[142:143], v16 offset:21248\n"
"v_cvt_pk_bf16_f32 v94, v76, v77\n"
"v_mfma_f32_16x16x32_bf16 v[48:51], a[136:139], v[188:191], v[48:51]\n"
"v_exp_f32_e32 v78, v78\n"
"v_mfma_f32_16x16x32_bf16 v[48:51], a[140:143], v[192:195], v[48:51]\n"
"s_waitcnt vmcnt(7)\n"
"s_mov_b32 s40, 1048576\n"
"Lattn_poll_%=:\n"
"v_cmp_gt_u32_e32 vcc, 32, v25\n"
"s_cmp_eq_u64 vcc, 0\n"
"s_cbranch_scc1 Lattn_polld_%=\n"
"s_sleep 2\n"
"global_load_dword v25, v24, s[2:3] sc1\n"
"s_waitcnt vmcnt(0)\n"
"s_sub_u32 s40, s40, 1\n"
"s_cmp_lg_u32 s40, 0\n"
"s_cbranch_scc1 Lattn_poll_%=\n"
"Lattn_polld_%=:\n"
"v_mfma_f32_16x16x32_bf16 v[52:55], a[144:147], v[180:183], v[32:35]\n"
"v_exp_f32_e32 v79, v79\n"
"v_mfma_f32_16x16x32_bf16 v[52:55], a[148:151], v[184:187], v[52:55]\n"
"ds_read_b64_tr_b16 v[144:145], v13 offset:17152\n"
"v_cvt_pk_bf16_f32 v95, v78, v79\n"
"v_mfma_f32_16x16x32_bf16 v[52:55], a[152:155], v[188:191], v[52:55]\n"
"ds_read_b64_tr_b16 v[146:147], v13 offset:21248\n"
"s_add_u32 m0, s44, 114688\n"
"v_mfma_f32_16x16x32_bf16 v[52:55], a[156:159], v[192:195], v[52:55]\n"
"v_exp_f32_e32 v48, v48\n"
"v_mfma_f32_16x16x32_bf16 a[12:15], v[148:151], v[92:95], a[12:15]\n"
"v_mfma_f32_16x16x32_bf16 a[28:31], v[152:155], v[92:95], a[28:31]\n"
"v_exp_f32_e32 v49, v49\n"
"v_mfma_f32_16x16x32_bf16 a[44:47], v[156:159], v[92:95], a[44:47]\n"
"ds_read_b128 a[160:163], v8 offset:24576\n"
"v_cvt_pk_bf16_f32 v80, v48, v49\n"
"v_mfma_f32_16x16x32_bf16 a[60:63], v[160:163], v[92:95], a[60:63]\n"
"v_exp_f32_e32 v50, v50\n"
"v_mfma_f32_16x16x32_bf16 a[76:79], v[164:167], v[92:95], a[76:79]\n"
"ds_read_b128 a[164:167], v9 offset:24576\n"
"v_mfma_f32_16x16x32_bf16 a[92:95], v[168:171], v[92:95], a[92:95]\n"
"v_exp_f32_e32 v51, v51\n"
"v_mfma_f32_16x16x32_bf16 a[108:111], v[172:175], v[92:95], a[108:111]\n"
"buffer_load_dwordx4 v5, s[12:15], s38 offen lds\n"
"v_mfma_f32_16x16x32_bf16 a[124:127], v[176:179], v[92:95], a[124:127]\n"
"v_exp_f32_e32 v52, v52\n"
"v_mfma_f32_16x16x32_bf16 v[108:111], v[112:115], v[92:95], v[108:111]\n"
"ds_read_b128 a[168:171], v10 offset:24576\n"
"v_cvt_pk_bf16_f32 v81, v50, v51\n"
"v_mfma_f32_16x16x32_bf16 v[56:59], a[128:131], v[196:199], v[36:39]\n"
"v_exp_f32_e32 v53, v53\n"
"v_mfma_f32_16x16x32_bf16 v[56:59], a[132:135], v[200:203], v[56:59]\n"
"ds_read_b64_tr_b16 v[148:149], v16 offset:24576\n"
"v_cvt_pk_bf16_f32 v82, v52, v53\n"
"v_mfma_f32_16x16x32_bf16 v[56:59], a[136:139], v[204:207], v[56:59]\n"
"v_exp_f32_e32 v54, v54\n"
"v_mfma_f32_16x16x32_bf16 v[56:59], a[140:143], v[208:211], v[56:59]\n"
"s_waitcnt lgkmcnt(4)\n"
"v_mfma_f32_16x16x32_bf16 v[60:63], a[144:147], v[196:199], v[36:39]\n"
"v_exp_f32_e32 v55, v55\n"
"v_mfma_f32_16x16x32_bf16 v[60:63], a[148:151], v[200:203], v[60:63]\n"
"ds_read_b128 a[172:175], v11 offset:24576\n"
"v_cvt_pk_bf16_f32 v83, v54, v55\n"
"v_mfma_f32_16x16x32_bf16 v[60:63], a[152:155], v[204:207], v[60:63]\n"
"ds_read_b64_tr_b16 v[150:151], v16 offset:28672\n"
"v_mfma_f32_16x16x32_bf16 v[60:63], a[156:159], v[208:211], v[60:63]\n"
"v_exp_f32_e32 v56, v56\n"
"v_mfma_f32_16x16x32_bf16 a[0:3], v[116:119], v[80:83], a[0:3]\n"
"buffer_load_dwordx4 v12, s[12:15], s38 offen offset:1024 lds\n"
"v_mfma_f32_16x16x32_bf16 a[16:19], v[120:123], v[80:83], a[16:19]\n"
"v_exp_f32_e32 v57, v57\n"
"v_mfma_f32_16x16x32_bf16 a[32:35], v[124:127], v[80:83], a[32:35]\n"
"ds_read_b128 a[176:179], v8 offset:28672\n"
"v_cvt_pk_bf16_f32 v84, v56, v57\n"
"v_mfma_f32_16x16x32_bf16 a[48:51], v[128:131], v[80:83], a[48:51]\n"
"v_exp_f32_e32 v58, v58\n"
"v_mfma_f32_16x16x32_bf16 a[64:67], v[132:135], v[80:83], a[64:67]\n"
"ds_read_b64_tr_b16 v[152:153], v13 offset:24576\n"
"v_mfma_f32_16x16x32_bf16 a[80:83], v[136:139], v[80:83], a[80:83]\n"
"v_exp_f32_e32 v59, v59\n"
"v_mfma_f32_16x16x32_bf16 a[96:99], v[140:143], v[80:83], a[96:99]\n"
"v_cvt_pk_bf16_f32 v85, v58, v59\n"
"v_mfma_f32_16x16x32_bf16 a[112:115], v[144:147], v[80:83], a[112:115]\n"
"v_exp_f32_e32 v60, v60\n"
"v_mfma_f32_16x16x32_bf16 v[96:99], v[112:115], v[80:83], v[96:99]\n"
"ds_read_b128 a[180:183], v9 offset:28672\n"
"v_mfma_f32_16x16x32_bf16 v[64:67], a[128:131], v[212:215], v[40:43]\n"
"v_exp_f32_e32 v61, v61\n"
"v_mfma_f32_16x16x32_bf16 v[64:67], a[132:135], v[216:219], v[64:67]\n"
"ds_read_b64_tr_b16 v[154:155], v13 offset:28672\n"
"v_cvt_pk_bf16_f32 v86, v60, v61\n"
"v_mfma_f32_16x16x32_bf16 v[64:67], a[136:139], v[220:223], v[64:67]\n"
"v_exp_f32_e32 v62, v62\n"
"v_mfma_f32_16x16x32_bf16 v[64:67], a[140:143], v[224:227], v[64:67]\n"
"v_mfma_f32_16x16x32_bf16 v[68:71], a[144:147], v[212:215], v[40:43]\n"
"v_exp_f32_e32 v63, v63\n"
"v_mfma_f32_16x16x32_bf16 v[68:71], a[148:151], v[216:219], v[68:71]\n"
"ds_read_b128 a[184:187], v10 offset:28672\n"
"v_cvt_pk_bf16_f32 v87, v62, v63\n"
"v_mfma_f32_16x16x32_bf16 v[68:71], a[152:155], v[220:223], v[68:71]\n"
"ds_read_b64_tr_b16 v[156:157], v16 offset:24832\n"
"v_mfma_f32_16x16x32_bf16 v[68:71], a[156:159], v[224:227], v[68:71]\n"
"v_exp_f32_e32 v64, v64\n"
"v_mfma_f32_16x16x32_bf16 a[4:7], v[116:119], v[84:87], a[4:7]\n"
"buffer_load_dwordx4 v5, s[12:15], s38 offen offset:2048 lds\n"
"v_mfma_f32_16x16x32_bf16 a[20:23], v[120:123], v[84:87], a[20:23]\n"
"v_exp_f32_e32 v65, v65\n"
"v_mfma_f32_16x16x32_bf16 a[36:39], v[124:127], v[84:87], a[36:39]\n"
"ds_read_b128 a[188:191], v11 offset:28672\n"
"v_cvt_pk_bf16_f32 v88, v64, v65\n"
"v_mfma_f32_16x16x32_bf16 a[52:55], v[128:131], v[84:87], a[52:55]\n"
"v_exp_f32_e32 v66, v66\n"
"v_mfma_f32_16x16x32_bf16 a[68:71], v[132:135], v[84:87], a[68:71]\n"
"ds_read_b64_tr_b16 v[158:159], v16 offset:28928\n"
"v_mfma_f32_16x16x32_bf16 a[84:87], v[136:139], v[84:87], a[84:87]\n"
"v_exp_f32_e32 v67, v67\n"
"v_mfma_f32_16x16x32_bf16 a[100:103], v[140:143], v[84:87], a[100:103]\n"
"v_cvt_pk_bf16_f32 v89, v66, v67\n"
"v_mfma_f32_16x16x32_bf16 a[116:119], v[144:147], v[84:87], a[116:119]\n"
"v_exp_f32_e32 v68, v68\n"
"v_mfma_f32_16x16x32_bf16 v[100:103], v[112:115], v[84:87], v[100:103]\n"
"ds_read_b64_tr_b16 v[160:161], v13 offset:24832\n"
"v_mfma_f32_16x16x32_bf16 v[72:75], a[128:131], v[228:231], v[44:47]\n"
"v_exp_f32_e32 v69, v69\n"
"v_mfma_f32_16x16x32_bf16 v[72:75], a[132:135], v[232:235], v[72:75]\n"
"ds_read_b64_tr_b16 v[162:163], v13 offset:28928\n"
"v_cvt_pk_bf16_f32 v90, v68, v69\n"
"v_mfma_f32_16x16x32_bf16 v[72:75], a[136:139], v[236:239], v[72:75]\n"
"v_exp_f32_e32 v70, v70\n"
"v_mfma_f32_16x16x32_bf16 v[72:75], a[140:143], v[240:243], v[72:75]\n"
"v_mfma_f32_16x16x32_bf16 v[76:79], a[144:147], v[228:231], v[44:47]\n"
"v_exp_f32_e32 v71, v71\n"
"v_mfma_f32_16x16x32_bf16 v[76:79], a[148:151], v[232:235], v[76:79]\n"
"ds_read_b64_tr_b16 v[164:165], v16 offset:25088\n"
"v_cvt_pk_bf16_f32 v91, v70, v71\n"
"v_mfma_f32_16x16x32_bf16 v[76:79], a[152:155], v[236:239], v[76:79]\n"
"ds_read_b64_tr_b16 v[166:167], v16 offset:29184\n"
"s_waitcnt lgkmcnt(5)\n"
"v_mfma_f32_16x16x32_bf16 v[76:79], a[156:159], v[240:243], v[76:79]\n"
"v_exp_f32_e32 v72, v72\n"
"v_mfma_f32_16x16x32_bf16 a[8:11], v[116:119], v[88:91], a[8:11]\n"
"buffer_load_dwordx4 v12, s[12:15], s38 offen offset:3072 lds\n"
"v_mfma_f32_16x16x32_bf16 a[24:27], v[120:123], v[88:91], a[24:27]\n"
"v_exp_f32_e32 v73, v73\n"
"v_mfma_f32_16x16x32_bf16 a[40:43], v[124:127], v[88:91], a[40:43]\n"
"ds_read_b64_tr_b16 v[168:169], v13 offset:25088\n"
"v_cvt_pk_bf16_f32 v92, v72, v73\n"
"v_mfma_f32_16x16x32_bf16 a[56:59], v[128:131], v[88:91], a[56:59]\n"
"v_exp_f32_e32 v74, v74\n"
"v_mfma_f32_16x16x32_bf16 a[72:75], v[132:135], v[88:91], a[72:75]\n"
"ds_read_b64_tr_b16 v[170:171], v13 offset:29184\n"
"v_mfma_f32_16x16x32_bf16 a[88:91], v[136:139], v[88:91], a[88:91]\n"
"v_exp_f32_e32 v75, v75\n"
"v_mfma_f32_16x16x32_bf16 a[104:107], v[140:143], v[88:91], a[104:107]\n"
"v_cvt_pk_bf16_f32 v93, v74, v75\n"
"v_mfma_f32_16x16x32_bf16 a[120:123], v[144:147], v[88:91], a[120:123]\n"
"v_exp_f32_e32 v76, v76\n"
"v_mfma_f32_16x16x32_bf16 v[104:107], v[112:115], v[88:91], v[104:107]\n"
"ds_read_b64_tr_b16 v[172:173], v16 offset:25344\n"
"v_mfma_f32_16x16x32_bf16 v[48:51], a[160:163], v[180:183], v[32:35]\n"
"v_exp_f32_e32 v77, v77\n"
"v_mfma_f32_16x16x32_bf16 v[48:51], a[164:167], v[184:187], v[48:51]\n"
"ds_read_b64_tr_b16 v[174:175], v16 offset:29440\n"
"v_cvt_pk_bf16_f32 v94, v76, v77\n"
"v_mfma_f32_16x16x32_bf16 v[48:51], a[168:171], v[188:191], v[48:51]\n"
"v_exp_f32_e32 v78, v78\n"
"v_mfma_f32_16x16x32_bf16 v[48:51], a[172:175], v[192:195], v[48:51]\n"
"s_waitcnt vmcnt(8)\n"
"s_barrier\n"
"v_mfma_f32_16x16x32_bf16 v[52:55], a[176:179], v[180:183], v[32:35]\n"
"v_exp_f32_e32 v79, v79\n"
"v_mfma_f32_16x16x32_bf16 v[52:55], a[180:183], v[184:187], v[52:55]\n"
"ds_read_b64_tr_b16 v[176:177], v13 offset:25344\n"
"v_cvt_pk_bf16_f32 v95, v78, v79\n"
"v_mfma_f32_16x16x32_bf16 v[52:55], a[184:187], v[188:191], v[52:55]\n"
"ds_read_b64_tr_b16 v[178:179], v13 offset:29440\n"
"v_mfma_f32_16x16x32_bf16 v[52:55], a[188:191], v[192:195], v[52:55]\n"
"v_exp_f32_e32 v48, v48\n"
"v_mfma_f32_16x16x32_bf16 a[12:15], v[116:119], v[92:95], a[12:15]\n"
"v_mfma_f32_16x16x32_bf16 a[28:31], v[120:123], v[92:95], a[28:31]\n"
"v_exp_f32_e32 v49, v49\n"
"v_mfma_f32_16x16x32_bf16 a[44:47], v[124:127], v[92:95], a[44:47]\n"
"ds_read_b128 a[128:131], v8 offset:32768\n"
"v_cvt_pk_bf16_f32 v80, v48, v49\n"
"v_mfma_f32_16x16x32_bf16 a[60:63], v[128:131], v[92:95], a[60:63]\n"
"v_exp_f32_e32 v50, v50\n"
"v_mfma_f32_16x16x32_bf16 a[76:79], v[132:135], v[92:95], a[76:79]\n"
"ds_read_b128 a[132:135], v9 offset:32768\n"
"v_mfma_f32_16x16x32_bf16 a[92:95], v[136:139], v[92:95], a[92:95]\n"
"v_exp_f32_e32 v51, v51\n"
"v_mfma_f32_16x16x32_bf16 a[108:111], v[140:143], v[92:95], a[108:111]\n"
"s_add_u32 m0, s44, 0\n"
"s_add_u32 s39, s45, 0\n"
"buffer_load_dwordx4 v7, s[64:67], s39 offen sc1 lds\n"
"v_mfma_f32_16x16x32_bf16 a[124:127], v[144:147], v[92:95], a[124:127]\n"
"v_exp_f32_e32 v52, v52\n"
"v_mfma_f32_16x16x32_bf16 v[108:111], v[112:115], v[92:95], v[108:111]\n"
"ds_read_b128 a[136:139], v10 offset:32768\n"
"v_cvt_pk_bf16_f32 v81, v50, v51\n"
"v_mfma_f32_16x16x32_bf16 v[56:59], a[160:163], v[196:199], v[36:39]\n"
"v_exp_f32_e32 v53, v53\n"
"v_mfma_f32_16x16x32_bf16 v[56:59], a[164:167], v[200:203], v[56:59]\n"
"ds_read_b64_tr_b16 v[116:117], v16 offset:32768\n"
"v_cvt_pk_bf16_f32 v82, v52, v53\n"
"v_mfma_f32_16x16x32_bf16 v[56:59], a[168:171], v[204:207], v[56:59]\n"
"v_exp_f32_e32 v54, v54\n"
"v_mfma_f32_16x16x32_bf16 v[56:59], a[172:175], v[208:211], v[56:59]\n"
"s_waitcnt lgkmcnt(4)\n"
"v_mfma_f32_16x16x32_bf16 v[60:63], a[176:179], v[196:199], v[36:39]\n"
"v_exp_f32_e32 v55, v55\n"
"v_mfma_f32_16x16x32_bf16 v[60:63], a[180:183], v[200:203], v[60:63]\n"
"ds_read_b128 a[140:143], v11 offset:32768\n"
"v_cvt_pk_bf16_f32 v83, v54, v55\n"
"v_mfma_f32_16x16x32_bf16 v[60:63], a[184:187], v[204:207], v[60:63]\n"
"ds_read_b64_tr_b16 v[118:119], v16 offset:36864\n"
"v_mfma_f32_16x16x32_bf16 v[60:63], a[188:191], v[208:211], v[60:63]\n"
"v_exp_f32_e32 v56, v56\n"
"v_mfma_f32_16x16x32_bf16 a[0:3], v[148:151], v[80:83], a[0:3]\n"
"buffer_load_dwordx4 v7, s[64:67], s39 offen offset:1024 sc1 lds\n"
"v_mfma_f32_16x16x32_bf16 a[16:19], v[152:155], v[80:83], a[16:19]\n"
"v_exp_f32_e32 v57, v57\n"
"v_mfma_f32_16x16x32_bf16 a[32:35], v[156:159], v[80:83], a[32:35]\n"
"ds_read_b128 a[144:147], v8 offset:36864\n"
"v_cvt_pk_bf16_f32 v84, v56, v57\n"
"v_mfma_f32_16x16x32_bf16 a[48:51], v[160:163], v[80:83], a[48:51]\n"
"v_exp_f32_e32 v58, v58\n"
"v_mfma_f32_16x16x32_bf16 a[64:67], v[164:167], v[80:83], a[64:67]\n"
"ds_read_b64_tr_b16 v[120:121], v13 offset:32768\n"
"v_mfma_f32_16x16x32_bf16 a[80:83], v[168:171], v[80:83], a[80:83]\n"
"v_exp_f32_e32 v59, v59\n"
"v_mfma_f32_16x16x32_bf16 a[96:99], v[172:175], v[80:83], a[96:99]\n"
"v_cvt_pk_bf16_f32 v85, v58, v59\n"
"v_mfma_f32_16x16x32_bf16 a[112:115], v[176:179], v[80:83], a[112:115]\n"
"v_exp_f32_e32 v60, v60\n"
"v_mfma_f32_16x16x32_bf16 v[96:99], v[112:115], v[80:83], v[96:99]\n"
"ds_read_b128 a[148:151], v9 offset:36864\n"
"v_mfma_f32_16x16x32_bf16 v[64:67], a[160:163], v[212:215], v[40:43]\n"
"v_exp_f32_e32 v61, v61\n"
"v_mfma_f32_16x16x32_bf16 v[64:67], a[164:167], v[216:219], v[64:67]\n"
"ds_read_b64_tr_b16 v[122:123], v13 offset:36864\n"
"v_cvt_pk_bf16_f32 v86, v60, v61\n"
"v_mfma_f32_16x16x32_bf16 v[64:67], a[168:171], v[220:223], v[64:67]\n"
"v_exp_f32_e32 v62, v62\n"
"v_mfma_f32_16x16x32_bf16 v[64:67], a[172:175], v[224:227], v[64:67]\n"
"v_mfma_f32_16x16x32_bf16 v[68:71], a[176:179], v[212:215], v[40:43]\n"
"v_exp_f32_e32 v63, v63\n"
"v_mfma_f32_16x16x32_bf16 v[68:71], a[180:183], v[216:219], v[68:71]\n"
"ds_read_b128 a[152:155], v10 offset:36864\n"
"v_cvt_pk_bf16_f32 v87, v62, v63\n"
"v_mfma_f32_16x16x32_bf16 v[68:71], a[184:187], v[220:223], v[68:71]\n"
"ds_read_b64_tr_b16 v[124:125], v16 offset:33024\n"
"v_mfma_f32_16x16x32_bf16 v[68:71], a[188:191], v[224:227], v[68:71]\n"
"v_exp_f32_e32 v64, v64\n"
"v_mfma_f32_16x16x32_bf16 a[4:7], v[148:151], v[84:87], a[4:7]\n"
"buffer_load_dwordx4 v7, s[64:67], s39 offen offset:2048 sc1 lds\n"
"v_mfma_f32_16x16x32_bf16 a[20:23], v[152:155], v[84:87], a[20:23]\n"
"v_exp_f32_e32 v65, v65\n"
"v_mfma_f32_16x16x32_bf16 a[36:39], v[156:159], v[84:87], a[36:39]\n"
"ds_read_b128 a[156:159], v11 offset:36864\n"
"v_cvt_pk_bf16_f32 v88, v64, v65\n"
"v_mfma_f32_16x16x32_bf16 a[52:55], v[160:163], v[84:87], a[52:55]\n"
"v_exp_f32_e32 v66, v66\n"
"v_mfma_f32_16x16x32_bf16 a[68:71], v[164:167], v[84:87], a[68:71]\n"
"ds_read_b64_tr_b16 v[126:127], v16 offset:37120\n"
"v_mfma_f32_16x16x32_bf16 a[84:87], v[168:171], v[84:87], a[84:87]\n"
"v_exp_f32_e32 v67, v67\n"
"v_mfma_f32_16x16x32_bf16 a[100:103], v[172:175], v[84:87], a[100:103]\n"
"v_cvt_pk_bf16_f32 v89, v66, v67\n"
"v_mfma_f32_16x16x32_bf16 a[116:119], v[176:179], v[84:87], a[116:119]\n"
"v_exp_f32_e32 v68, v68\n"
"v_mfma_f32_16x16x32_bf16 v[100:103], v[112:115], v[84:87], v[100:103]\n"
"ds_read_b64_tr_b16 v[128:129], v13 offset:33024\n"
"v_mfma_f32_16x16x32_bf16 v[72:75], a[160:163], v[228:231], v[44:47]\n"
"v_exp_f32_e32 v69, v69\n"
"v_mfma_f32_16x16x32_bf16 v[72:75], a[164:167], v[232:235], v[72:75]\n"
"ds_read_b64_tr_b16 v[130:131], v13 offset:37120\n"
"v_cvt_pk_bf16_f32 v90, v68, v69\n"
"v_mfma_f32_16x16x32_bf16 v[72:75], a[168:171], v[236:239], v[72:75]\n"
"v_exp_f32_e32 v70, v70\n"
"v_mfma_f32_16x16x32_bf16 v[72:75], a[172:175], v[240:243], v[72:75]\n"
"v_mfma_f32_16x16x32_bf16 v[76:79], a[176:179], v[228:231], v[44:47]\n"
"v_exp_f32_e32 v71, v71\n"
"v_mfma_f32_16x16x32_bf16 v[76:79], a[180:183], v[232:235], v[76:79]\n"
"ds_read_b64_tr_b16 v[132:133], v16 offset:33280\n"
"v_cvt_pk_bf16_f32 v91, v70, v71\n"
"v_mfma_f32_16x16x32_bf16 v[76:79], a[184:187], v[236:239], v[76:79]\n"
"ds_read_b64_tr_b16 v[134:135], v16 offset:37376\n"
"s_waitcnt lgkmcnt(5)\n"
"v_mfma_f32_16x16x32_bf16 v[76:79], a[188:191], v[240:243], v[76:79]\n"
"v_exp_f32_e32 v72, v72\n"
"v_mfma_f32_16x16x32_bf16 a[8:11], v[148:151], v[88:91], a[8:11]\n"
"buffer_load_dwordx4 v7, s[64:67], s39 offen offset:3072 sc1 lds\n"
"v_mfma_f32_16x16x32_bf16 a[24:27], v[152:155], v[88:91], a[24:27]\n"
"v_exp_f32_e32 v73, v73\n"
"v_mfma_f32_16x16x32_bf16 a[40:43], v[156:159], v[88:91], a[40:43]\n"
"ds_read_b64_tr_b16 v[136:137], v13 offset:33280\n"
"v_cvt_pk_bf16_f32 v92, v72, v73\n"
"v_mfma_f32_16x16x32_bf16 a[56:59], v[160:163], v[88:91], a[56:59]\n"
"v_exp_f32_e32 v74, v74\n"
"v_mfma_f32_16x16x32_bf16 a[72:75], v[164:167], v[88:91], a[72:75]\n"
"ds_read_b64_tr_b16 v[138:139], v13 offset:37376\n"
"v_mfma_f32_16x16x32_bf16 a[88:91], v[168:171], v[88:91], a[88:91]\n"
"v_exp_f32_e32 v75, v75\n"
"v_mfma_f32_16x16x32_bf16 a[104:107], v[172:175], v[88:91], a[104:107]\n"
"v_cvt_pk_bf16_f32 v93, v74, v75\n"
"v_mfma_f32_16x16x32_bf16 a[120:123], v[176:179], v[88:91], a[120:123]\n"
"v_exp_f32_e32 v76, v76\n"
"v_mfma_f32_16x16x32_bf16 v[104:107], v[112:115], v[88:91], v[104:107]\n"
"ds_read_b64_tr_b16 v[140:141], v16 offset:33536\n"
"v_mfma_f32_16x16x32_bf16 v[48:51], a[128:131], v[180:183], v[32:35]\n"
"v_exp_f32_e32 v77, v77\n"
"v_mfma_f32_16x16x32_bf16 v[48:51], a[132:135], v[184:187], v[48:51]\n"
"ds_read_b64_tr_b16 v[142:143], v16 offset:37632\n"
"v_cvt_pk_bf16_f32 v94, v76, v77\n"
"v_mfma_f32_16x16x32_bf16 v[48:51], a[136:139], v[188:191], v[48:51]\n"
"v_exp_f32_e32 v78, v78\n"
"v_mfma_f32_16x16x32_bf16 v[48:51], a[140:143], v[192:195], v[48:51]\n"
"v_mfma_f32_16x16x32_bf16 v[52:55], a[144:147], v[180:183], v[32:35]\n"
"v_exp_f32_e32 v79, v79\n"
"v_mfma_f32_16x16x32_bf16 v[52:55], a[148:151], v[184:187], v[52:55]\n"
"ds_read_b64_tr_b16 v[144:145], v13 offset:33536\n"
"v_cvt_pk_bf16_f32 v95, v78, v79\n"
"v_mfma_f32_16x16x32_bf16 v[52:55], a[152:155], v[188:191], v[52:55]\n"
"ds_read_b64_tr_b16 v[146:147], v13 offset:37632\n"
"v_mfma_f32_16x16x32_bf16 v[52:55], a[156:159], v[192:195], v[52:55]\n"
"v_exp_f32_e32 v48, v48\n"
"v_mfma_f32_16x16x32_bf16 a[12:15], v[148:151], v[92:95], a[12:15]\n"
"v_mfma_f32_16x16x32_bf16 a[28:31], v[152:155], v[92:95], a[28:31]\n"
"v_exp_f32_e32 v49, v49\n"
"v_mfma_f32_16x16x32_bf16 a[44:47], v[156:159], v[92:95], a[44:47]\n"
"ds_read_b128 a[160:163], v8 offset:40960\n"
"v_cvt_pk_bf16_f32 v80, v48, v49\n"
"v_mfma_f32_16x16x32_bf16 a[60:63], v[160:163], v[92:95], a[60:63]\n"
"v_exp_f32_e32 v50, v50\n"
"v_mfma_f32_16x16x32_bf16 a[76:79], v[164:167], v[92:95], a[76:79]\n"
"ds_read_b128 a[164:167], v9 offset:40960\n"
"v_mfma_f32_16x16x32_bf16 a[92:95], v[168:171], v[92:95], a[92:95]\n"
"v_exp_f32_e32 v51, v51\n"
"v_mfma_f32_16x16x32_bf16 a[108:111], v[172:175], v[92:95], a[108:111]\n"
"s_add_u32 m0, s44, 65536\n"
"s_add_u32 s39, s45, 65536\n"
"buffer_load_dwordx4 v7, s[64:67], s39 offen sc1 lds\n"
"v_mfma_f32_16x16x32_bf16 a[124:127], v[176:179], v[92:95], a[124:127]\n"
"v_exp_f32_e32 v52, v52\n"
"v_mfma_f32_16x16x32_bf16 v[108:111], v[112:115], v[92:95], v[108:111]\n"
"ds_read_b128 a[168:171], v10 offset:40960\n"
"v_cvt_pk_bf16_f32 v81, v50, v51\n"
"v_mfma_f32_16x16x32_bf16 v[56:59], a[128:131], v[196:199], v[36:39]\n"
"v_exp_f32_e32 v53, v53\n"
"v_mfma_f32_16x16x32_bf16 v[56:59], a[132:135], v[200:203], v[56:59]\n"
"ds_read_b64_tr_b16 v[148:149], v16 offset:40960\n"
"v_cvt_pk_bf16_f32 v82, v52, v53\n"
"v_mfma_f32_16x16x32_bf16 v[56:59], a[136:139], v[204:207], v[56:59]\n"
"v_exp_f32_e32 v54, v54\n"
"v_mfma_f32_16x16x32_bf16 v[56:59], a[140:143], v[208:211], v[56:59]\n"
"s_waitcnt lgkmcnt(4)\n"
"v_mfma_f32_16x16x32_bf16 v[60:63], a[144:147], v[196:199], v[36:39]\n"
"v_exp_f32_e32 v55, v55\n"
"v_mfma_f32_16x16x32_bf16 v[60:63], a[148:151], v[200:203], v[60:63]\n"
"ds_read_b128 a[172:175], v11 offset:40960\n"
"v_cvt_pk_bf16_f32 v83, v54, v55\n"
"v_mfma_f32_16x16x32_bf16 v[60:63], a[152:155], v[204:207], v[60:63]\n"
"ds_read_b64_tr_b16 v[150:151], v16 offset:45056\n"
"v_mfma_f32_16x16x32_bf16 v[60:63], a[156:159], v[208:211], v[60:63]\n"
"v_exp_f32_e32 v56, v56\n"
"v_mfma_f32_16x16x32_bf16 a[0:3], v[116:119], v[80:83], a[0:3]\n"
"buffer_load_dwordx4 v7, s[64:67], s39 offen offset:1024 sc1 lds\n"
"v_mfma_f32_16x16x32_bf16 a[16:19], v[120:123], v[80:83], a[16:19]\n"
"v_exp_f32_e32 v57, v57\n"
"v_mfma_f32_16x16x32_bf16 a[32:35], v[124:127], v[80:83], a[32:35]\n"
"ds_read_b128 a[176:179], v8 offset:45056\n"
"v_cvt_pk_bf16_f32 v84, v56, v57\n"
"v_mfma_f32_16x16x32_bf16 a[48:51], v[128:131], v[80:83], a[48:51]\n"
"v_exp_f32_e32 v58, v58\n"
"v_mfma_f32_16x16x32_bf16 a[64:67], v[132:135], v[80:83], a[64:67]\n"
"ds_read_b64_tr_b16 v[152:153], v13 offset:40960\n"
"v_mfma_f32_16x16x32_bf16 a[80:83], v[136:139], v[80:83], a[80:83]\n"
"v_exp_f32_e32 v59, v59\n"
"v_mfma_f32_16x16x32_bf16 a[96:99], v[140:143], v[80:83], a[96:99]\n"
"v_cvt_pk_bf16_f32 v85, v58, v59\n"
"v_mfma_f32_16x16x32_bf16 a[112:115], v[144:147], v[80:83], a[112:115]\n"
"v_exp_f32_e32 v60, v60\n"
"v_mfma_f32_16x16x32_bf16 v[96:99], v[112:115], v[80:83], v[96:99]\n"
"ds_read_b128 a[180:183], v9 offset:45056\n"
"v_mfma_f32_16x16x32_bf16 v[64:67], a[128:131], v[212:215], v[40:43]\n"
"v_exp_f32_e32 v61, v61\n"
"v_mfma_f32_16x16x32_bf16 v[64:67], a[132:135], v[216:219], v[64:67]\n"
"ds_read_b64_tr_b16 v[154:155], v13 offset:45056\n"
"v_cvt_pk_bf16_f32 v86, v60, v61\n"
"v_mfma_f32_16x16x32_bf16 v[64:67], a[136:139], v[220:223], v[64:67]\n"
"v_exp_f32_e32 v62, v62\n"
"v_mfma_f32_16x16x32_bf16 v[64:67], a[140:143], v[224:227], v[64:67]\n"
"v_mfma_f32_16x16x32_bf16 v[68:71], a[144:147], v[212:215], v[40:43]\n"
"v_exp_f32_e32 v63, v63\n"
"v_mfma_f32_16x16x32_bf16 v[68:71], a[148:151], v[216:219], v[68:71]\n"
"ds_read_b128 a[184:187], v10 offset:45056\n"
"v_cvt_pk_bf16_f32 v87, v62, v63\n"
"v_mfma_f32_16x16x32_bf16 v[68:71], a[152:155], v[220:223], v[68:71]\n"
"ds_read_b64_tr_b16 v[156:157], v16 offset:41216\n"
"v_mfma_f32_16x16x32_bf16 v[68:71], a[156:159], v[224:227], v[68:71]\n"
"v_exp_f32_e32 v64, v64\n"
"v_mfma_f32_16x16x32_bf16 a[4:7], v[116:119], v[84:87], a[4:7]\n"
"buffer_load_dwordx4 v7, s[64:67], s39 offen offset:2048 sc1 lds\n"
"v_mfma_f32_16x16x32_bf16 a[20:23], v[120:123], v[84:87], a[20:23]\n"
"v_exp_f32_e32 v65, v65\n"
"v_mfma_f32_16x16x32_bf16 a[36:39], v[124:127], v[84:87], a[36:39]\n"
"ds_read_b128 a[188:191], v11 offset:45056\n"
"v_cvt_pk_bf16_f32 v88, v64, v65\n"
"v_mfma_f32_16x16x32_bf16 a[52:55], v[128:131], v[84:87], a[52:55]\n"
"v_exp_f32_e32 v66, v66\n"
"v_mfma_f32_16x16x32_bf16 a[68:71], v[132:135], v[84:87], a[68:71]\n"
"ds_read_b64_tr_b16 v[158:159], v16 offset:45312\n"
"v_mfma_f32_16x16x32_bf16 a[84:87], v[136:139], v[84:87], a[84:87]\n"
"v_exp_f32_e32 v67, v67\n"
"v_mfma_f32_16x16x32_bf16 a[100:103], v[140:143], v[84:87], a[100:103]\n"
"v_cvt_pk_bf16_f32 v89, v66, v67\n"
"v_mfma_f32_16x16x32_bf16 a[116:119], v[144:147], v[84:87], a[116:119]\n"
"v_exp_f32_e32 v68, v68\n"
"v_mfma_f32_16x16x32_bf16 v[100:103], v[112:115], v[84:87], v[100:103]\n"
"ds_read_b64_tr_b16 v[160:161], v13 offset:41216\n"
"v_mfma_f32_16x16x32_bf16 v[72:75], a[128:131], v[228:231], v[44:47]\n"
"v_exp_f32_e32 v69, v69\n"
"v_mfma_f32_16x16x32_bf16 v[72:75], a[132:135], v[232:235], v[72:75]\n"
"ds_read_b64_tr_b16 v[162:163], v13 offset:45312\n"
"v_cvt_pk_bf16_f32 v90, v68, v69\n"
"v_mfma_f32_16x16x32_bf16 v[72:75], a[136:139], v[236:239], v[72:75]\n"
"v_exp_f32_e32 v70, v70\n"
"v_mfma_f32_16x16x32_bf16 v[72:75], a[140:143], v[240:243], v[72:75]\n"
"v_mfma_f32_16x16x32_bf16 v[76:79], a[144:147], v[228:231], v[44:47]\n"
"v_exp_f32_e32 v71, v71\n"
"v_mfma_f32_16x16x32_bf16 v[76:79], a[148:151], v[232:235], v[76:79]\n"
"ds_read_b64_tr_b16 v[164:165], v16 offset:41472\n"
"v_cvt_pk_bf16_f32 v91, v70, v71\n"
"v_mfma_f32_16x16x32_bf16 v[76:79], a[152:155], v[236:239], v[76:79]\n"
"ds_read_b64_tr_b16 v[166:167], v16 offset:45568\n"
"s_waitcnt lgkmcnt(5)\n"
"v_mfma_f32_16x16x32_bf16 v[76:79], a[156:159], v[240:243], v[76:79]\n"
"v_exp_f32_e32 v72, v72\n"
"v_mfma_f32_16x16x32_bf16 a[8:11], v[116:119], v[88:91], a[8:11]\n"
"buffer_load_dwordx4 v7, s[64:67], s39 offen offset:3072 sc1 lds\n"
"v_mfma_f32_16x16x32_bf16 a[24:27], v[120:123], v[88:91], a[24:27]\n"
"v_exp_f32_e32 v73, v73\n"
"v_mfma_f32_16x16x32_bf16 a[40:43], v[124:127], v[88:91], a[40:43]\n"
"ds_read_b64_tr_b16 v[168:169], v13 offset:41472\n"
"v_cvt_pk_bf16_f32 v92, v72, v73\n"
"v_mfma_f32_16x16x32_bf16 a[56:59], v[128:131], v[88:91], a[56:59]\n"
"v_exp_f32_e32 v74, v74\n"
"v_mfma_f32_16x16x32_bf16 a[72:75], v[132:135], v[88:91], a[72:75]\n"
"ds_read_b64_tr_b16 v[170:171], v13 offset:45568\n"
"v_mfma_f32_16x16x32_bf16 a[88:91], v[136:139], v[88:91], a[88:91]\n"
"v_exp_f32_e32 v75, v75\n"
"v_mfma_f32_16x16x32_bf16 a[104:107], v[140:143], v[88:91], a[104:107]\n"
"v_cvt_pk_bf16_f32 v93, v74, v75\n"
"v_mfma_f32_16x16x32_bf16 a[120:123], v[144:147], v[88:91], a[120:123]\n"
"v_exp_f32_e32 v76, v76\n"
"v_mfma_f32_16x16x32_bf16 v[104:107], v[112:115], v[88:91], v[104:107]\n"
"ds_read_b64_tr_b16 v[172:173], v16 offset:41728\n"
"v_mfma_f32_16x16x32_bf16 v[48:51], a[160:163], v[180:183], v[32:35]\n"
"v_exp_f32_e32 v77, v77\n"
"v_mfma_f32_16x16x32_bf16 v[48:51], a[164:167], v[184:187], v[48:51]\n"
"ds_read_b64_tr_b16 v[174:175], v16 offset:45824\n"
"v_cvt_pk_bf16_f32 v94, v76, v77\n"
"v_mfma_f32_16x16x32_bf16 v[48:51], a[168:171], v[188:191], v[48:51]\n"
"v_exp_f32_e32 v78, v78\n"
"v_mfma_f32_16x16x32_bf16 v[48:51], a[172:175], v[192:195], v[48:51]\n"
"s_waitcnt vmcnt(8)\n"
"s_barrier\n"
"v_mfma_f32_16x16x32_bf16 v[52:55], a[176:179], v[180:183], v[32:35]\n"
"v_exp_f32_e32 v79, v79\n"
"v_mfma_f32_16x16x32_bf16 v[52:55], a[180:183], v[184:187], v[52:55]\n"
"ds_read_b64_tr_b16 v[176:177], v13 offset:41728\n"
"v_cvt_pk_bf16_f32 v95, v78, v79\n"
"v_mfma_f32_16x16x32_bf16 v[52:55], a[184:187], v[188:191], v[52:55]\n"
"ds_read_b64_tr_b16 v[178:179], v13 offset:45824\n"
"v_mfma_f32_16x16x32_bf16 v[52:55], a[188:191], v[192:195], v[52:55]\n"
"v_exp_f32_e32 v48, v48\n"
"v_mfma_f32_16x16x32_bf16 a[12:15], v[116:119], v[92:95], a[12:15]\n"
"v_mfma_f32_16x16x32_bf16 a[28:31], v[120:123], v[92:95], a[28:31]\n"
"v_exp_f32_e32 v49, v49\n"
"v_mfma_f32_16x16x32_bf16 a[44:47], v[124:127], v[92:95], a[44:47]\n"
"ds_read_b128 a[128:131], v8 offset:49152\n"
"v_cvt_pk_bf16_f32 v80, v48, v49\n"
"v_mfma_f32_16x16x32_bf16 a[60:63], v[128:131], v[92:95], a[60:63]\n"
"v_exp_f32_e32 v50, v50\n"
"v_mfma_f32_16x16x32_bf16 a[76:79], v[132:135], v[92:95], a[76:79]\n"
"ds_read_b128 a[132:135], v9 offset:49152\n"
"v_mfma_f32_16x16x32_bf16 a[92:95], v[136:139], v[92:95], a[92:95]\n"
"v_exp_f32_e32 v51, v51\n"
"v_mfma_f32_16x16x32_bf16 a[108:111], v[140:143], v[92:95], a[108:111]\n"
"s_add_u32 m0, s44, 16384\n"
"s_add_u32 s39, s45, 16384\n"
"buffer_load_dwordx4 v7, s[64:67], s39 offen sc1 lds\n"
"v_mfma_f32_16x16x32_bf16 a[124:127], v[144:147], v[92:95], a[124:127]\n"
"v_exp_f32_e32 v52, v52\n"
"v_mfma_f32_16x16x32_bf16 v[108:111], v[112:115], v[92:95], v[108:111]\n"
"ds_read_b128 a[136:139], v10 offset:49152\n"
"v_cvt_pk_bf16_f32 v81, v50, v51\n"
"v_mfma_f32_16x16x32_bf16 v[56:59], a[160:163], v[196:199], v[36:39]\n"
"v_exp_f32_e32 v53, v53\n"
"v_mfma_f32_16x16x32_bf16 v[56:59], a[164:167], v[200:203], v[56:59]\n"
"ds_read_b64_tr_b16 v[116:117], v16 offset:49152\n"
"v_cvt_pk_bf16_f32 v82, v52, v53\n"
"v_mfma_f32_16x16x32_bf16 v[56:59], a[168:171], v[204:207], v[56:59]\n"
"v_exp_f32_e32 v54, v54\n"
"v_mfma_f32_16x16x32_bf16 v[56:59], a[172:175], v[208:211], v[56:59]\n"
"s_waitcnt lgkmcnt(4)\n"
"v_mfma_f32_16x16x32_bf16 v[60:63], a[176:179], v[196:199], v[36:39]\n"
"v_exp_f32_e32 v55, v55\n"
"v_mfma_f32_16x16x32_bf16 v[60:63], a[180:183], v[200:203], v[60:63]\n"
"ds_read_b128 a[140:143], v11 offset:49152\n"
"v_cvt_pk_bf16_f32 v83, v54, v55\n"
"v_mfma_f32_16x16x32_bf16 v[60:63], a[184:187], v[204:207], v[60:63]\n"
"ds_read_b64_tr_b16 v[118:119], v16 offset:53248\n"
"v_mfma_f32_16x16x32_bf16 v[60:63], a[188:191], v[208:211], v[60:63]\n"
"v_exp_f32_e32 v56, v56\n"
"v_mfma_f32_16x16x32_bf16 a[0:3], v[148:151], v[80:83], a[0:3]\n"
"buffer_load_dwordx4 v7, s[64:67], s39 offen offset:1024 sc1 lds\n"
"v_mfma_f32_16x16x32_bf16 a[16:19], v[152:155], v[80:83], a[16:19]\n"
"v_exp_f32_e32 v57, v57\n"
"v_mfma_f32_16x16x32_bf16 a[32:35], v[156:159], v[80:83], a[32:35]\n"
"ds_read_b128 a[144:147], v8 offset:53248\n"
"v_cvt_pk_bf16_f32 v84, v56, v57\n"
"v_mfma_f32_16x16x32_bf16 a[48:51], v[160:163], v[80:83], a[48:51]\n"
"v_exp_f32_e32 v58, v58\n"
"v_mfma_f32_16x16x32_bf16 a[64:67], v[164:167], v[80:83], a[64:67]\n"
"ds_read_b64_tr_b16 v[120:121], v13 offset:49152\n"
"v_mfma_f32_16x16x32_bf16 a[80:83], v[168:171], v[80:83], a[80:83]\n"
"v_exp_f32_e32 v59, v59\n"
"v_mfma_f32_16x16x32_bf16 a[96:99], v[172:175], v[80:83], a[96:99]\n"
"v_cvt_pk_bf16_f32 v85, v58, v59\n"
"v_mfma_f32_16x16x32_bf16 a[112:115], v[176:179], v[80:83], a[112:115]\n"
"v_exp_f32_e32 v60, v60\n"
"v_mfma_f32_16x16x32_bf16 v[96:99], v[112:115], v[80:83], v[96:99]\n"
"ds_read_b128 a[148:151], v9 offset:53248\n"
"v_mfma_f32_16x16x32_bf16 v[64:67], a[160:163], v[212:215], v[40:43]\n"
"v_exp_f32_e32 v61, v61\n"
"v_mfma_f32_16x16x32_bf16 v[64:67], a[164:167], v[216:219], v[64:67]\n"
"ds_read_b64_tr_b16 v[122:123], v13 offset:53248\n"
"v_cvt_pk_bf16_f32 v86, v60, v61\n"
"v_mfma_f32_16x16x32_bf16 v[64:67], a[168:171], v[220:223], v[64:67]\n"
"v_exp_f32_e32 v62, v62\n"
"v_mfma_f32_16x16x32_bf16 v[64:67], a[172:175], v[224:227], v[64:67]\n"
"v_mfma_f32_16x16x32_bf16 v[68:71], a[176:179], v[212:215], v[40:43]\n"
"v_exp_f32_e32 v63, v63\n"
"v_mfma_f32_16x16x32_bf16 v[68:71], a[180:183], v[216:219], v[68:71]\n"
"ds_read_b128 a[152:155], v10 offset:53248\n"
"v_cvt_pk_bf16_f32 v87, v62, v63\n"
"v_mfma_f32_16x16x32_bf16 v[68:71], a[184:187], v[220:223], v[68:71]\n"
"ds_read_b64_tr_b16 v[124:125], v16 offset:49408\n"
"v_mfma_f32_16x16x32_bf16 v[68:71], a[188:191], v[224:227], v[68:71]\n"
"v_exp_f32_e32 v64, v64\n"
"v_mfma_f32_16x16x32_bf16 a[4:7], v[148:151], v[84:87], a[4:7]\n"
"buffer_load_dwordx4 v7, s[64:67], s39 offen offset:2048 sc1 lds\n"
"v_mfma_f32_16x16x32_bf16 a[20:23], v[152:155], v[84:87], a[20:23]\n"
"v_exp_f32_e32 v65, v65\n"
"v_mfma_f32_16x16x32_bf16 a[36:39], v[156:159], v[84:87], a[36:39]\n"
"ds_read_b128 a[156:159], v11 offset:53248\n"
"v_cvt_pk_bf16_f32 v88, v64, v65\n"
"v_mfma_f32_16x16x32_bf16 a[52:55], v[160:163], v[84:87], a[52:55]\n"
"v_exp_f32_e32 v66, v66\n"
"v_mfma_f32_16x16x32_bf16 a[68:71], v[164:167], v[84:87], a[68:71]\n"
"ds_read_b64_tr_b16 v[126:127], v16 offset:53504\n"
"v_mfma_f32_16x16x32_bf16 a[84:87], v[168:171], v[84:87], a[84:87]\n"
"v_exp_f32_e32 v67, v67\n"
"v_mfma_f32_16x16x32_bf16 a[100:103], v[172:175], v[84:87], a[100:103]\n"
"v_cvt_pk_bf16_f32 v89, v66, v67\n"
"v_mfma_f32_16x16x32_bf16 a[116:119], v[176:179], v[84:87], a[116:119]\n"
"v_exp_f32_e32 v68, v68\n"
"v_mfma_f32_16x16x32_bf16 v[100:103], v[112:115], v[84:87], v[100:103]\n"
"ds_read_b64_tr_b16 v[128:129], v13 offset:49408\n"
"v_mfma_f32_16x16x32_bf16 v[72:75], a[160:163], v[228:231], v[44:47]\n"
"v_exp_f32_e32 v69, v69\n"
"v_mfma_f32_16x16x32_bf16 v[72:75], a[164:167], v[232:235], v[72:75]\n"
"ds_read_b64_tr_b16 v[130:131], v13 offset:53504\n"
"v_cvt_pk_bf16_f32 v90, v68, v69\n"
"v_mfma_f32_16x16x32_bf16 v[72:75], a[168:171], v[236:239], v[72:75]\n"
"v_exp_f32_e32 v70, v70\n"
"v_mfma_f32_16x16x32_bf16 v[72:75], a[172:175], v[240:243], v[72:75]\n"
"v_mfma_f32_16x16x32_bf16 v[76:79], a[176:179], v[228:231], v[44:47]\n"
"v_exp_f32_e32 v71, v71\n"
"v_mfma_f32_16x16x32_bf16 v[76:79], a[180:183], v[232:235], v[76:79]\n"
"ds_read_b64_tr_b16 v[132:133], v16 offset:49664\n"
"v_cvt_pk_bf16_f32 v91, v70, v71\n"
"v_mfma_f32_16x16x32_bf16 v[76:79], a[184:187], v[236:239], v[76:79]\n"
"ds_read_b64_tr_b16 v[134:135], v16 offset:53760\n"
"s_waitcnt lgkmcnt(5)\n"
"v_mfma_f32_16x16x32_bf16 v[76:79], a[188:191], v[240:243], v[76:79]\n"
"v_exp_f32_e32 v72, v72\n"
"v_mfma_f32_16x16x32_bf16 a[8:11], v[148:151], v[88:91], a[8:11]\n"
"buffer_load_dwordx4 v7, s[64:67], s39 offen offset:3072 sc1 lds\n"
"v_mfma_f32_16x16x32_bf16 a[24:27], v[152:155], v[88:91], a[24:27]\n"
"v_exp_f32_e32 v73, v73\n"
"v_mfma_f32_16x16x32_bf16 a[40:43], v[156:159], v[88:91], a[40:43]\n"
"ds_read_b64_tr_b16 v[136:137], v13 offset:49664\n"
"v_cvt_pk_bf16_f32 v92, v72, v73\n"
"v_mfma_f32_16x16x32_bf16 a[56:59], v[160:163], v[88:91], a[56:59]\n"
"v_exp_f32_e32 v74, v74\n"
"v_mfma_f32_16x16x32_bf16 a[72:75], v[164:167], v[88:91], a[72:75]\n"
"ds_read_b64_tr_b16 v[138:139], v13 offset:53760\n"
"v_mfma_f32_16x16x32_bf16 a[88:91], v[168:171], v[88:91], a[88:91]\n"
"v_exp_f32_e32 v75, v75\n"
"v_mfma_f32_16x16x32_bf16 a[104:107], v[172:175], v[88:91], a[104:107]\n"
"v_cvt_pk_bf16_f32 v93, v74, v75\n"
"v_mfma_f32_16x16x32_bf16 a[120:123], v[176:179], v[88:91], a[120:123]\n"
"v_exp_f32_e32 v76, v76\n"
"v_mfma_f32_16x16x32_bf16 v[104:107], v[112:115], v[88:91], v[104:107]\n"
"ds_read_b64_tr_b16 v[140:141], v16 offset:49920\n"
"v_mfma_f32_16x16x32_bf16 v[48:51], a[128:131], v[180:183], v[32:35]\n"
"v_exp_f32_e32 v77, v77\n"
"v_mfma_f32_16x16x32_bf16 v[48:51], a[132:135], v[184:187], v[48:51]\n"
"ds_read_b64_tr_b16 v[142:143], v16 offset:54016\n"
"v_cvt_pk_bf16_f32 v94, v76, v77\n"
"v_mfma_f32_16x16x32_bf16 v[48:51], a[136:139], v[188:191], v[48:51]\n"
"v_exp_f32_e32 v78, v78\n"
"v_mfma_f32_16x16x32_bf16 v[48:51], a[140:143], v[192:195], v[48:51]\n"
"v_mfma_f32_16x16x32_bf16 v[52:55], a[144:147], v[180:183], v[32:35]\n"
"v_exp_f32_e32 v79, v79\n"
"v_mfma_f32_16x16x32_bf16 v[52:55], a[148:151], v[184:187], v[52:55]\n"
"ds_read_b64_tr_b16 v[144:145], v13 offset:49920\n"
"v_cvt_pk_bf16_f32 v95, v78, v79\n"
"v_mfma_f32_16x16x32_bf16 v[52:55], a[152:155], v[188:191], v[52:55]\n"
"ds_read_b64_tr_b16 v[146:147], v13 offset:54016\n"
"v_mfma_f32_16x16x32_bf16 v[52:55], a[156:159], v[192:195], v[52:55]\n"
"v_exp_f32_e32 v48, v48\n"
"v_mfma_f32_16x16x32_bf16 a[12:15], v[148:151], v[92:95], a[12:15]\n"
"v_mfma_f32_16x16x32_bf16 a[28:31], v[152:155], v[92:95], a[28:31]\n"
"v_exp_f32_e32 v49, v49\n"
"v_mfma_f32_16x16x32_bf16 a[44:47], v[156:159], v[92:95], a[44:47]\n"
"ds_read_b128 a[160:163], v8 offset:57344\n"
"v_cvt_pk_bf16_f32 v80, v48, v49\n"
"v_mfma_f32_16x16x32_bf16 a[60:63], v[160:163], v[92:95], a[60:63]\n"
"v_exp_f32_e32 v50, v50\n"
"v_mfma_f32_16x16x32_bf16 a[76:79], v[164:167], v[92:95], a[76:79]\n"
"ds_read_b128 a[164:167], v9 offset:57344\n"
"v_mfma_f32_16x16x32_bf16 a[92:95], v[168:171], v[92:95], a[92:95]\n"
"v_exp_f32_e32 v51, v51\n"
"v_mfma_f32_16x16x32_bf16 a[108:111], v[172:175], v[92:95], a[108:111]\n"
"s_add_u32 m0, s44, 81920\n"
"s_add_u32 s39, s45, 81920\n"
"buffer_load_dwordx4 v7, s[64:67], s39 offen sc1 lds\n"
"v_mfma_f32_16x16x32_bf16 a[124:127], v[176:179], v[92:95], a[124:127]\n"
"v_exp_f32_e32 v52, v52\n"
"v_mfma_f32_16x16x32_bf16 v[108:111], v[112:115], v[92:95], v[108:111]\n"
"ds_read_b128 a[168:171], v10 offset:57344\n"
"v_cvt_pk_bf16_f32 v81, v50, v51\n"
"v_mfma_f32_16x16x32_bf16 v[56:59], a[128:131], v[196:199], v[36:39]\n"
"v_exp_f32_e32 v53, v53\n"
"v_mfma_f32_16x16x32_bf16 v[56:59], a[132:135], v[200:203], v[56:59]\n"
"ds_read_b64_tr_b16 v[148:149], v16 offset:57344\n"
"v_cvt_pk_bf16_f32 v82, v52, v53\n"
"v_mfma_f32_16x16x32_bf16 v[56:59], a[136:139], v[204:207], v[56:59]\n"
"v_exp_f32_e32 v54, v54\n"
"v_mfma_f32_16x16x32_bf16 v[56:59], a[140:143], v[208:211], v[56:59]\n"
"s_waitcnt lgkmcnt(4)\n"
"v_mfma_f32_16x16x32_bf16 v[60:63], a[144:147], v[196:199], v[36:39]\n"
"v_exp_f32_e32 v55, v55\n"
"v_mfma_f32_16x16x32_bf16 v[60:63], a[148:151], v[200:203], v[60:63]\n"
"ds_read_b128 a[172:175], v11 offset:57344\n"
"v_cvt_pk_bf16_f32 v83, v54, v55\n"
"v_mfma_f32_16x16x32_bf16 v[60:63], a[152:155], v[204:207], v[60:63]\n"
"ds_read_b64_tr_b16 v[150:151], v16 offset:61440\n"
"v_mfma_f32_16x16x32_bf16 v[60:63], a[156:159], v[208:211], v[60:63]\n"
"v_exp_f32_e32 v56, v56\n"
"v_mfma_f32_16x16x32_bf16 a[0:3], v[116:119], v[80:83], a[0:3]\n"
"buffer_load_dwordx4 v7, s[64:67], s39 offen offset:1024 sc1 lds\n"
"v_mfma_f32_16x16x32_bf16 a[16:19], v[120:123], v[80:83], a[16:19]\n"
"v_exp_f32_e32 v57, v57\n"
"v_mfma_f32_16x16x32_bf16 a[32:35], v[124:127], v[80:83], a[32:35]\n"
"ds_read_b128 a[176:179], v8 offset:61440\n"
"v_cvt_pk_bf16_f32 v84, v56, v57\n"
"v_mfma_f32_16x16x32_bf16 a[48:51], v[128:131], v[80:83], a[48:51]\n"
"v_exp_f32_e32 v58, v58\n"
"v_mfma_f32_16x16x32_bf16 a[64:67], v[132:135], v[80:83], a[64:67]\n"
"ds_read_b64_tr_b16 v[152:153], v13 offset:57344\n"
"v_mfma_f32_16x16x32_bf16 a[80:83], v[136:139], v[80:83], a[80:83]\n"
"v_exp_f32_e32 v59, v59\n"
"v_mfma_f32_16x16x32_bf16 a[96:99], v[140:143], v[80:83], a[96:99]\n"
"v_cvt_pk_bf16_f32 v85, v58, v59\n"
"v_mfma_f32_16x16x32_bf16 a[112:115], v[144:147], v[80:83], a[112:115]\n"
"v_exp_f32_e32 v60, v60\n"
"v_mfma_f32_16x16x32_bf16 v[96:99], v[112:115], v[80:83], v[96:99]\n"
"ds_read_b128 a[180:183], v9 offset:61440\n"
"v_mfma_f32_16x16x32_bf16 v[64:67], a[128:131], v[212:215], v[40:43]\n"
"v_exp_f32_e32 v61, v61\n"
"v_mfma_f32_16x16x32_bf16 v[64:67], a[132:135], v[216:219], v[64:67]\n"
"ds_read_b64_tr_b16 v[154:155], v13 offset:61440\n"
"v_cvt_pk_bf16_f32 v86, v60, v61\n"
"v_mfma_f32_16x16x32_bf16 v[64:67], a[136:139], v[220:223], v[64:67]\n"
"v_exp_f32_e32 v62, v62\n"
"v_mfma_f32_16x16x32_bf16 v[64:67], a[140:143], v[224:227], v[64:67]\n"
"v_mfma_f32_16x16x32_bf16 v[68:71], a[144:147], v[212:215], v[40:43]\n"
"v_exp_f32_e32 v63, v63\n"
"v_mfma_f32_16x16x32_bf16 v[68:71], a[148:151], v[216:219], v[68:71]\n"
"ds_read_b128 a[184:187], v10 offset:61440\n"
"v_cvt_pk_bf16_f32 v87, v62, v63\n"
"v_mfma_f32_16x16x32_bf16 v[68:71], a[152:155], v[220:223], v[68:71]\n"
"ds_read_b64_tr_b16 v[156:157], v16 offset:57600\n"
"v_mfma_f32_16x16x32_bf16 v[68:71], a[156:159], v[224:227], v[68:71]\n"
"v_exp_f32_e32 v64, v64\n"
"v_mfma_f32_16x16x32_bf16 a[4:7], v[116:119], v[84:87], a[4:7]\n"
"buffer_load_dwordx4 v7, s[64:67], s39 offen offset:2048 sc1 lds\n"
"v_mfma_f32_16x16x32_bf16 a[20:23], v[120:123], v[84:87], a[20:23]\n"
"v_exp_f32_e32 v65, v65\n"
"v_mfma_f32_16x16x32_bf16 a[36:39], v[124:127], v[84:87], a[36:39]\n"
"ds_read_b128 a[188:191], v11 offset:61440\n"
"v_cvt_pk_bf16_f32 v88, v64, v65\n"
"v_mfma_f32_16x16x32_bf16 a[52:55], v[128:131], v[84:87], a[52:55]\n"
"v_exp_f32_e32 v66, v66\n"
"v_mfma_f32_16x16x32_bf16 a[68:71], v[132:135], v[84:87], a[68:71]\n"
"ds_read_b64_tr_b16 v[158:159], v16 offset:61696\n"
"v_mfma_f32_16x16x32_bf16 a[84:87], v[136:139], v[84:87], a[84:87]\n"
"v_exp_f32_e32 v67, v67\n"
"v_mfma_f32_16x16x32_bf16 a[100:103], v[140:143], v[84:87], a[100:103]\n"
"v_cvt_pk_bf16_f32 v89, v66, v67\n"
"v_mfma_f32_16x16x32_bf16 a[116:119], v[144:147], v[84:87], a[116:119]\n"
"v_exp_f32_e32 v68, v68\n"
"v_mfma_f32_16x16x32_bf16 v[100:103], v[112:115], v[84:87], v[100:103]\n"
"ds_read_b64_tr_b16 v[160:161], v13 offset:57600\n"
"v_mfma_f32_16x16x32_bf16 v[72:75], a[128:131], v[228:231], v[44:47]\n"
"v_exp_f32_e32 v69, v69\n"
"v_mfma_f32_16x16x32_bf16 v[72:75], a[132:135], v[232:235], v[72:75]\n"
"ds_read_b64_tr_b16 v[162:163], v13 offset:61696\n"
"v_cvt_pk_bf16_f32 v90, v68, v69\n"
"v_mfma_f32_16x16x32_bf16 v[72:75], a[136:139], v[236:239], v[72:75]\n"
"v_exp_f32_e32 v70, v70\n"
"v_mfma_f32_16x16x32_bf16 v[72:75], a[140:143], v[240:243], v[72:75]\n"
"v_mfma_f32_16x16x32_bf16 v[76:79], a[144:147], v[228:231], v[44:47]\n"
"v_exp_f32_e32 v71, v71\n"
"v_mfma_f32_16x16x32_bf16 v[76:79], a[148:151], v[232:235], v[76:79]\n"
"ds_read_b64_tr_b16 v[164:165], v16 offset:57856\n"
"v_cvt_pk_bf16_f32 v91, v70, v71\n"
"v_mfma_f32_16x16x32_bf16 v[76:79], a[152:155], v[236:239], v[76:79]\n"
"ds_read_b64_tr_b16 v[166:167], v16 offset:61952\n"
"s_waitcnt lgkmcnt(5)\n"
"v_mfma_f32_16x16x32_bf16 v[76:79], a[156:159], v[240:243], v[76:79]\n"
"v_exp_f32_e32 v72, v72\n"
"v_mfma_f32_16x16x32_bf16 a[8:11], v[116:119], v[88:91], a[8:11]\n"
"buffer_load_dwordx4 v7, s[64:67], s39 offen offset:3072 sc1 lds\n"
"v_mfma_f32_16x16x32_bf16 a[24:27], v[120:123], v[88:91], a[24:27]\n"
"v_exp_f32_e32 v73, v73\n"
"v_mfma_f32_16x16x32_bf16 a[40:43], v[124:127], v[88:91], a[40:43]\n"
"ds_read_b64_tr_b16 v[168:169], v13 offset:57856\n"
"v_cvt_pk_bf16_f32 v92, v72, v73\n"
"v_mfma_f32_16x16x32_bf16 a[56:59], v[128:131], v[88:91], a[56:59]\n"
"v_exp_f32_e32 v74, v74\n"
"v_mfma_f32_16x16x32_bf16 a[72:75], v[132:135], v[88:91], a[72:75]\n"
"ds_read_b64_tr_b16 v[170:171], v13 offset:61952\n"
"v_mfma_f32_16x16x32_bf16 a[88:91], v[136:139], v[88:91], a[88:91]\n"
"v_exp_f32_e32 v75, v75\n"
"v_mfma_f32_16x16x32_bf16 a[104:107], v[140:143], v[88:91], a[104:107]\n"
"v_cvt_pk_bf16_f32 v93, v74, v75\n"
"v_mfma_f32_16x16x32_bf16 a[120:123], v[144:147], v[88:91], a[120:123]\n"
"v_exp_f32_e32 v76, v76\n"
"v_mfma_f32_16x16x32_bf16 v[104:107], v[112:115], v[88:91], v[104:107]\n"
"ds_read_b64_tr_b16 v[172:173], v16 offset:58112\n"
"v_mfma_f32_16x16x32_bf16 v[48:51], a[160:163], v[180:183], v[32:35]\n"
"v_exp_f32_e32 v77, v77\n"
"v_mfma_f32_16x16x32_bf16 v[48:51], a[164:167], v[184:187], v[48:51]\n"
"ds_read_b64_tr_b16 v[174:175], v16 offset:62208\n"
"v_cvt_pk_bf16_f32 v94, v76, v77\n"
"v_mfma_f32_16x16x32_bf16 v[48:51], a[168:171], v[188:191], v[48:51]\n"
"v_exp_f32_e32 v78, v78\n"
"v_mfma_f32_16x16x32_bf16 v[48:51], a[172:175], v[192:195], v[48:51]\n"
"s_barrier\n"
"v_mfma_f32_16x16x32_bf16 v[52:55], a[176:179], v[180:183], v[32:35]\n"
"v_exp_f32_e32 v79, v79\n"
"v_mfma_f32_16x16x32_bf16 v[52:55], a[180:183], v[184:187], v[52:55]\n"
"ds_read_b64_tr_b16 v[176:177], v13 offset:58112\n"
"v_cvt_pk_bf16_f32 v95, v78, v79\n"
"v_mfma_f32_16x16x32_bf16 v[52:55], a[184:187], v[188:191], v[52:55]\n"
"ds_read_b64_tr_b16 v[178:179], v13 offset:62208\n"
"v_mfma_f32_16x16x32_bf16 v[52:55], a[188:191], v[192:195], v[52:55]\n"
"v_exp_f32_e32 v48, v48\n"
"v_mfma_f32_16x16x32_bf16 a[12:15], v[116:119], v[92:95], a[12:15]\n"
"v_mfma_f32_16x16x32_bf16 a[28:31], v[120:123], v[92:95], a[28:31]\n"
"v_exp_f32_e32 v49, v49\n"
"v_mfma_f32_16x16x32_bf16 a[44:47], v[124:127], v[92:95], a[44:47]\n"
"v_cvt_pk_bf16_f32 v80, v48, v49\n"
"v_mfma_f32_16x16x32_bf16 a[60:63], v[128:131], v[92:95], a[60:63]\n"
"v_exp_f32_e32 v50, v50\n"
"v_mfma_f32_16x16x32_bf16 a[76:79], v[132:135], v[92:95], a[76:79]\n"
"v_mfma_f32_16x16x32_bf16 a[92:95], v[136:139], v[92:95], a[92:95]\n"
"v_exp_f32_e32 v51, v51\n"
"v_mfma_f32_16x16x32_bf16 a[108:111], v[140:143], v[92:95], a[108:111]\n"
"s_add_u32 m0, s44, 32768\n"
"s_add_u32 s39, s45, 32768\n"
"buffer_load_dwordx4 v7, s[64:67], s39 offen sc1 lds\n"
"v_mfma_f32_16x16x32_bf16 a[124:127], v[144:147], v[92:95], a[124:127]\n"
"v_exp_f32_e32 v52, v52\n"
"v_mfma_f32_16x16x32_bf16 v[108:111], v[112:115], v[92:95], v[108:111]\n"
"v_cvt_pk_bf16_f32 v81, v50, v51\n"
"v_mfma_f32_16x16x32_bf16 v[56:59], a[160:163], v[196:199], v[36:39]\n"
"v_exp_f32_e32 v53, v53\n"
"v_mfma_f32_16x16x32_bf16 v[56:59], a[164:167], v[200:203], v[56:59]\n"
"v_cvt_pk_bf16_f32 v82, v52, v53\n"
"v_mfma_f32_16x16x32_bf16 v[56:59], a[168:171], v[204:207], v[56:59]\n"
"v_exp_f32_e32 v54, v54\n"
"v_mfma_f32_16x16x32_bf16 v[56:59], a[172:175], v[208:211], v[56:59]\n"
"s_waitcnt lgkmcnt(0)\n"
"v_mfma_f32_16x16x32_bf16 v[60:63], a[176:179], v[196:199], v[36:39]\n"
"v_exp_f32_e32 v55, v55\n"
"v_mfma_f32_16x16x32_bf16 v[60:63], a[180:183], v[200:203], v[60:63]\n"
"v_cvt_pk_bf16_f32 v83, v54, v55\n"
"v_mfma_f32_16x16x32_bf16 v[60:63], a[184:187], v[204:207], v[60:63]\n"
"v_mfma_f32_16x16x32_bf16 v[60:63], a[188:191], v[208:211], v[60:63]\n"
"s_nop 0\n"
"v_exp_f32_e32 v56, v56\n"
"v_mfma_f32_16x16x32_bf16 a[0:3], v[148:151], v[80:83], a[0:3]\n"
"buffer_load_dwordx4 v7, s[64:67], s39 offen offset:1024 sc1 lds\n"
"v_mfma_f32_16x16x32_bf16 a[16:19], v[152:155], v[80:83], a[16:19]\n"
"v_exp_f32_e32 v57, v57\n"
"v_mfma_f32_16x16x32_bf16 a[32:35], v[156:159], v[80:83], a[32:35]\n"
"v_cvt_pk_bf16_f32 v84, v56, v57\n"
"v_mfma_f32_16x16x32_bf16 a[48:51], v[160:163], v[80:83], a[48:51]\n"
"v_exp_f32_e32 v58, v58\n"
"v_mfma_f32_16x16x32_bf16 a[64:67], v[164:167], v[80:83], a[64:67]\n"
"v_mfma_f32_16x16x32_bf16 a[80:83], v[168:171], v[80:83], a[80:83]\n"
"v_exp_f32_e32 v59, v59\n"
"v_mfma_f32_16x16x32_bf16 a[96:99], v[172:175], v[80:83], a[96:99]\n"
"v_cvt_pk_bf16_f32 v85, v58, v59\n"
"v_mfma_f32_16x16x32_bf16 a[112:115], v[176:179], v[80:83], a[112:115]\n"
"v_exp_f32_e32 v60, v60\n"
"v_mfma_f32_16x16x32_bf16 v[96:99], v[112:115], v[80:83], v[96:99]\n"
"v_mfma_f32_16x16x32_bf16 v[64:67], a[160:163], v[212:215], v[40:43]\n"
"v_exp_f32_e32 v61, v61\n"
"v_mfma_f32_16x16x32_bf16 v[64:67], a[164:167], v[216:219], v[64:67]\n"
"v_cvt_pk_bf16_f32 v86, v60, v61\n"
"v_mfma_f32_16x16x32_bf16 v[64:67], a[168:171], v[220:223], v[64:67]\n"
"v_exp_f32_e32 v62, v62\n"
"v_mfma_f32_16x16x32_bf16 v[64:67], a[172:175], v[224:227], v[64:67]\n"
"v_mfma_f32_16x16x32_bf16 v[68:71], a[176:179], v[212:215], v[40:43]\n"
"v_exp_f32_e32 v63, v63\n"
"v_mfma_f32_16x16x32_bf16 v[68:71], a[180:183], v[216:219], v[68:71]\n"
"v_cvt_pk_bf16_f32 v87, v62, v63\n"
"v_mfma_f32_16x16x32_bf16 v[68:71], a[184:187], v[220:223], v[68:71]\n"
"v_mfma_f32_16x16x32_bf16 v[68:71], a[188:191], v[224:227], v[68:71]\n"
"s_nop 1\n"
"v_exp_f32_e32 v64, v64\n"
"v_mfma_f32_16x16x32_bf16 a[4:7], v[148:151], v[84:87], a[4:7]\n"
"buffer_load_dwordx4 v7, s[64:67], s39 offen offset:2048 sc1 lds\n"
"v_mfma_f32_16x16x32_bf16 a[20:23], v[152:155], v[84:87], a[20:23]\n"
"v_exp_f32_e32 v65, v65\n"
"v_mfma_f32_16x16x32_bf16 a[36:39], v[156:159], v[84:87], a[36:39]\n"
"v_cvt_pk_bf16_f32 v88, v64, v65\n"
"v_mfma_f32_16x16x32_bf16 a[52:55], v[160:163], v[84:87], a[52:55]\n"
"v_exp_f32_e32 v66, v66\n"
"v_mfma_f32_16x16x32_bf16 a[68:71], v[164:167], v[84:87], a[68:71]\n"
"v_mfma_f32_16x16x32_bf16 a[84:87], v[168:171], v[84:87], a[84:87]\n"
"v_exp_f32_e32 v67, v67\n"
"v_mfma_f32_16x16x32_bf16 a[100:103], v[172:175], v[84:87], a[100:103]\n"
"v_cvt_pk_bf16_f32 v89, v66, v67\n"
"v_mfma_f32_16x16x32_bf16 a[116:119], v[176:179], v[84:87], a[116:119]\n"
"v_exp_f32_e32 v68, v68\n"
"v_mfma_f32_16x16x32_bf16 v[100:103], v[112:115], v[84:87], v[100:103]\n"
"v_mfma_f32_16x16x32_bf16 v[72:75], a[160:163], v[228:231], v[44:47]\n"
"v_exp_f32_e32 v69, v69\n"
"v_mfma_f32_16x16x32_bf16 v[72:75], a[164:167], v[232:235], v[72:75]\n"
"v_cvt_pk_bf16_f32 v90, v68, v69\n"
"v_mfma_f32_16x16x32_bf16 v[72:75], a[168:171], v[236:239], v[72:75]\n"
"v_exp_f32_e32 v70, v70\n"
"v_mfma_f32_16x16x32_bf16 v[72:75], a[172:175], v[240:243], v[72:75]\n"
"v_mfma_f32_16x16x32_bf16 v[76:79], a[176:179], v[228:231], v[44:47]\n"
"v_exp_f32_e32 v71, v71\n"
"v_mfma_f32_16x16x32_bf16 v[76:79], a[180:183], v[232:235], v[76:79]\n"
"v_cvt_pk_bf16_f32 v91, v70, v71\n"
"v_mfma_f32_16x16x32_bf16 v[76:79], a[184:187], v[236:239], v[76:79]\n"
"v_mfma_f32_16x16x32_bf16 v[76:79], a[188:191], v[240:243], v[76:79]\n"
"s_nop 1\n"
"v_exp_f32_e32 v72, v72\n"
"v_mfma_f32_16x16x32_bf16 a[8:11], v[148:151], v[88:91], a[8:11]\n"
"buffer_load_dwordx4 v7, s[64:67], s39 offen offset:3072 sc1 lds\n"
"v_mfma_f32_16x16x32_bf16 a[24:27], v[152:155], v[88:91], a[24:27]\n"
"v_exp_f32_e32 v73, v73\n"
"v_mfma_f32_16x16x32_bf16 a[40:43], v[156:159], v[88:91], a[40:43]\n"
"v_cvt_pk_bf16_f32 v92, v72, v73\n"
"v_mfma_f32_16x16x32_bf16 a[56:59], v[160:163], v[88:91], a[56:59]\n"
"v_exp_f32_e32 v74, v74\n"
"v_mfma_f32_16x16x32_bf16 a[72:75], v[164:167], v[88:91], a[72:75]\n"
"v_mfma_f32_16x16x32_bf16 a[88:91], v[168:171], v[88:91], a[88:91]\n"
"v_exp_f32_e32 v75, v75\n"
"v_mfma_f32_16x16x32_bf16 a[104:107], v[172:175], v[88:91], a[104:107]\n"
"v_cvt_pk_bf16_f32 v93, v74, v75\n"
"v_mfma_f32_16x16x32_bf16 a[120:123], v[176:179], v[88:91], a[120:123]\n"
"v_exp_f32_e32 v76, v76\n"
"v_mfma_f32_16x16x32_bf16 v[104:107], v[112:115], v[88:91], v[104:107]\n"
"v_exp_f32_e32 v77, v77\n"
"s_nop 0\n"
"v_cvt_pk_bf16_f32 v94, v76, v77\n"
"v_exp_f32_e32 v78, v78\n"
"v_exp_f32_e32 v79, v79\n"
"s_nop 0\n"
"v_cvt_pk_bf16_f32 v95, v78, v79\n"
"s_nop 1\n"
"v_mfma_f32_16x16x32_bf16 a[12:15], v[148:151], v[92:95], a[12:15]\n"
"v_mfma_f32_16x16x32_bf16 a[28:31], v[152:155], v[92:95], a[28:31]\n"
"v_mfma_f32_16x16x32_bf16 a[44:47], v[156:159], v[92:95], a[44:47]\n"
"v_mfma_f32_16x16x32_bf16 a[60:63], v[160:163], v[92:95], a[60:63]\n"
"v_mfma_f32_16x16x32_bf16 a[76:79], v[164:167], v[92:95], a[76:79]\n"
"v_mfma_f32_16x16x32_bf16 a[92:95], v[168:171], v[92:95], a[92:95]\n"
"v_mfma_f32_16x16x32_bf16 a[108:111], v[172:175], v[92:95], a[108:111]\n"
"v_mfma_f32_16x16x32_bf16 a[124:127], v[176:179], v[92:95], a[124:127]\n"
"v_mfma_f32_16x16x32_bf16 v[108:111], v[112:115], v[92:95], v[108:111]\n"
"s_waitcnt lgkmcnt(0)\n"
"s_barrier\n"
"s_nop 13\n"
    : "={a[0:31]}"(o0), "={a[32:63]}"(o1), "={a[64:95]}"(o2), "={a[96:127]}"(o3), "={v[96:111]}"(lv)
    : ATTN_IN : "v22","v23","v24","v25","v26","v27","v31","v32","v33","v34","v35","v36","v37","v38","v39","v40","v41","v42","v43","v44","v45","v46","v47","v48","v49","v50","v51","v52","v53","v54","v55","v56","v57","v58","v59","v60","v61","v62","v63","v64","v65","v66","v67","v68","v69","v70","v71","v72","v73","v74","v75","v76","v77","v78","v79","v80","v81","v82","v83","v84","v85","v86","v87","v88","v89","v90","v91","v92","v93","v94","v95","v112","v113","v114","v115","v116","v117","v118","v119","v120","v121","v122","v123","v124","v125","v126","v127","v128","v129","v130","v131","v132","v133","v134","v135","v136","v137","v138","v139","v140","v141","v142","v143","v144","v145","v146","v147","v148","v149","v150","v151","v152","v153","v154","v155","v156","v157","v158","v159","v160","v161","v162","v163","v164","v165","v166","v167","v168","v169","v170","v171","v172","v173","v174","v175","v176","v177","v178","v179","v180","v181","v182","v183","v184","v185","v186","v187","v188","v189","v190","v191","v192","v193","v194","v195","v196","v197","v198","v199","v200","v201","v202","v203","v204","v205","v206","v207","v208","v209","v210","v211","v212","v213","v214","v215","v216","v217","v218","v219","v220","v221","v222","v223","v224","v225","v226","v227","v228","v229","v230","v231","v232","v233","v234","v235","v236","v237","v238","v239","v240","v241","v242","v243","a128","a129","a130","a131","a132","a133","a134","a135","a136","a137","a138","a139","a140","a141","a142","a143","a144","a145","a146","a147","a148","a149","a150","a151","a152","a153","a154","a155","a156","a157","a158","a159","a160","a161","a162","a163","a164","a165","a166","a167","a168","a169","a170","a171","a172","a173","a174","a175","a176","a177","a178","a179","a180","a181","a182","a183","a184","a185","a186","a187","a188","a189","a190","a191","a192","a193","a194","a195","a196","a197","a198","a199","a200","a201","a202","a203","a204","a205","a206","a207","a208","a209","a210","a211","a212","a213","a214","a215","a216","a217","a218","a219","a220","a221","a222","a223","a224","a225","a226","a227","a228","a229","a230","a231","a232","a233","a234","a235","a236","a237","a238","a239","a240","a241","a242","a243","a244","a245","a246","a247","a248","a249","a250","a251","a252","a253","a254","a255","s36","s37","s38","s39","s40","s41","s42","s43","scc","vcc","memory");
  const unsigned lb = lane_id();
  {
    const unsigned jj = split * 4u + cb, f4 = lb >> 4, g2 = (lb >> 2) & 3u, r4 = lb & 3u;
    const unsigned tok16 = rb * 8u + 2u * (jj >> 2), rr = 4u * (jj & 3u) + r4;
#pragma unroll
    for (int i = 0; i < 3; ++i) {
      const unsigned Q = (i < 2 || three) ? wid + 4u * i : wid, n = 64u * Q + 16u * f4 + 4u * g2;
#pragma unroll
      for (int tb = 0; tb < 2; ++tb) xw[i][tb] = *(const u32x2*)(x + (size_t)((tok16 + tb) * 16u + rr) * DM + n);
      ga[i] = *(const f32x4*)(mod + 2 * DM + n); sh[i] = *(const f32x4*)(mod + 3 * DM + n); sc[i] = *(const f32x4*)(mod + 4 * DM + n);
    }
    GAP();
  }
  float lq[4] = {lv[0], lv[4], lv[8], lv[12]};
  if (LSUM_ADD) {
#pragma unroll
    for (int q = 0; q < 4; ++q) { lq[q] += __shfl_xor(lq[q], 16); lq[q] += __shfl_xor(lq[q], 32); }
  }
  const unsigned op0 = (head >> 3) * 32u + (head & 7u) * 2u;
  const __amdgpu_buffer_rsrc_t ro = mk_rsrc(of + ((size_t)(row0 >> 4) * 128u + op0) * 1024u, (4u * 128u - op0) * 1024u);
  float amax = 0.f;
  {
    const float il[4] = {__builtin_amdgcn_rcpf(lq[0]), __builtin_amdgcn_rcpf(lq[1]), __builtin_amdgcn_rcpf(lq[2]), __builtin_amdgcn_rcpf(lq[3])};
#pragma unroll
    for (int db = 0; db < 4; ++db)
#pragma unroll
      for (int q = 0; q < 4; ++q) {
        float o[8];
#pragma unroll
        for (int e = 0; e < 8; ++e) { const int i = 16 * (e >> 2) + 4 * q + (e & 3); o[e] = (db == 0 ? o0[i] : db == 1 ? o1[i] : db == 2 ? o2[i] : o3[i]); }
        amax += ((o[0] + o[1]) + (o[2] + o[3])) + ((o[4] + o[5]) + (o[6] + o[7]));
#pragma unroll
        for (int e = 0; e < 8; ++e) o[e] *= il[q];
        st16_sc1(ro, lb * 16u + q * 131072u + ((db >> 1) * 16 + (db & 1)) * 1024u, u32x4{cvtpk(o[0], o[1]), cvtpk(o[2], o[3]), cvtpk(o[4], o[5]), cvtpk(o[6], o[7])});
      }
  }
  const float LMAX = 1.2980742e33f;
  const bool bad = !(lq[0] < LMAX) || !(lq[1] < LMAX) || !(lq[2] < LMAX) || !(lq[3] < LMAX) || !(fabsf(amax) < 3.0e38f);
  if (__builtin_expect(__any((int)bad), 0)) attn_slow(h1f, (const float*)(ws + WS_MOD), wq, Kh, Vh, ro, row0 + lb, head, lb);

  CODE_LABEL("Lm_B");
  asm volatile("s_waitcnt vmcnt(8)" ::: "memory");
  signal_cnt<false>(cnt + (CNT_A + y * 4u + (head >> 3)) * 64u, wid);
  sigA = cnt + (CNT_A + y * 4u + (head >> 3)) * 64u + 32u;

  }

  LAS float* red = (LAS float*)(uintptr_t)(ldsb + 147456u);
  {
    const unsigned lane = lane_id(), g = lane >> 4, r = lane & 15u;
    GemmArgs a = gemm_args<0>(ws + WS_OF, ws + WS_WOT + (size_t)(cb * 4u + split) * 294912u, rb * 8u + wid * 2u, split * 32u, ldsb, wid, lane);
    a.srdO = make_srd(ws + WS_SLAB + (size_t)rb * 589824u, 589824u);
    a.srdN = make_srd(ws + WS_WGT + (size_t)ft * 331776u, 331776u);
    a.sig = sigA; a.poll = cnt + (CNT_A + (rb >> 1) * 4u + split) * 64u + 32u;
    a.vo0 = (((4u * wid + (r >> 2)) * 4u + split) * 36u + 9u * cb) * 256u + g * 64u + (r & 3u) * 16u; a.vo1 = 0;
    if (wid < 3u) code_touch(pc, oB, oE, tsc, wid, lane, 24576u);

    wait_cnt(cnt + (CNT_A + (rb >> 1) * 4u + split) * 64u, 8u, wid);

    asm volatile(
"s_nop 4\n"
"s_mov_b32 s22, 0\n"
"buffer_load_dwordx4 a[0:3], v2, s[12:15], s22 offen sc1\n"
"buffer_load_dwordx4 a[4:7], v3, s[12:15], s22 offen sc1\n"
"buffer_load_dwordx4 a[8:11], v2, s[12:15], s22 offen offset:1024 sc1\n"
"buffer_load_dwordx4 a[12:15], v3, s[12:15], s22 offen offset:1024 sc1\n"
"buffer_load_dwordx4 a[16:19], v2, s[12:15], s22 offen offset:2048 sc1\n"
"buffer_load_dwordx4 a[20:23], v3, s[12:15], s22 offen offset:2048 sc1\n"
"s_add_u32 m0, s20, 49152\n"
"s_mov_b32 s21, 49152\n"
"buffer_load_dwordx4 v1, s[8:11], s21 offen sc1 lds\n"
"buffer_load_dwordx4 v1, s[8:11], s21 offen offset:1024 sc1 lds\n"
"buffer_load_dwordx4 v1, s[8:11], s21 offen offset:2048 sc1 lds\n"
"buffer_load_dwordx4 v1, s[8:11], s21 offen offset:3072 sc1 lds\n"
"buffer_load_dwordx4 a[24:27], v2, s[12:15], s22 offen offset:3072 sc1\n"
"buffer_load_dwordx4 a[28:31], v3, s[12:15], s22 offen offset:3072 sc1\n"
"s_mov_b32 s22, 4096\n"
"buffer_load_dwordx4 a[32:35], v2, s[12:15], s22 offen sc1\n"
"buffer_load_dwordx4 a[36:39], v3, s[12:15], s22 offen sc1\n"
"buffer_load_dwordx4 a[40:43], v2, s[12:15], s22 offen offset:1024 sc1\n"
"buffer_load_dwordx4 a[44:47], v3, s[12:15], s22 offen offset:1024 sc1\n"
"buffer_load_dwordx4 a[48:51], v2, s[12:15], s22 offen offset:2048 sc1\n"
"buffer_load_dwordx4 a[52:55], v3, s[12:15], s22 offen offset:2048 sc1\n"
"buffer_load_dwordx4 a[56:59], v2, s[12:15], s22 offen offset:3072 sc1\n"
"buffer_load_dwordx4 a[60:63], v3, s[12:15], s22 offen offset:3072 sc1\n"
"s_mov_b32 s22, 8192\n"
"buffer_load_dwordx4 a[64:67], v2, s[12:15], s22 offen sc1\n"
"buffer_load_dwordx4 a[68:71], v3, s[12:15], s22 offen sc1\n"
"buffer_load_dwordx4 a[72:75], v2, s[12:15], s22 offen offset:1024 sc1\n"
"buffer_load_dwordx4 a[76:79], v3, s[12:15], s22 offen offset:1024 sc1\n"
"ds_read_b64_tr_b16 v[12:13], v6 offset:0\n"
"ds_read_b64_tr_b16 v[14:15], v6 offset:512\n"
"ds_read_b64_tr_b16 v[16:17], v6 offset:1024\n"
"ds_read_b64_tr_b16 v[18:19], v6 offset:1536\n"
"ds_read_b64_tr_b16 v[20:21], v6 offset:2048\n"
"ds_read_b64_tr_b16 v[22:23], v6 offset:2560\n"
"ds_read_b64_tr_b16 v[24:25], v6 offset:3072\n"
"ds_read_b64_tr_b16 v[26:27], v6 offset:3584\n"
"ds_read_b64_tr_b16 v[28:29], v6 offset:4096\n"
"ds_read_b64_tr_b16 v[30:31], v6 offset:4608\n"
"ds_read_b64_tr_b16 v[32:33], v6 offset:5120\n"
"ds_read_b64_tr_b16 v[34:35], v6 offset:5632\n"
"ds_read_b64_tr_b16 v[36:37], v6 offset:6144\n"
"ds_read_b64_tr_b16 v[38:39], v6 offset:6656\n"
"ds_read_b64_tr_b16 v[40:41], v6 offset:7168\n"
"ds_read_b64_tr_b16 v[42:43], v6 offset:7680\n"
"s_waitcnt vmcnt(22)\n"
"s_waitcnt lgkmcnt(12)\n"
"v_mfma_f32_16x16x32_bf16 v[80:83], v[12:15], a[0:3], 0\n"
"ds_read_b64_tr_b16 v[44:45], v6 offset:8192\n"
"v_mfma_f32_16x16x32_bf16 v[84:87], v[12:15], a[4:7], 0\n"
"ds_read_b64_tr_b16 v[46:47], v6 offset:8704\n"
"v_mfma_f32_16x16x32_bf16 v[88:91], v[16:19], a[0:3], 0\n"
"ds_read_b64_tr_b16 v[12:13], v6 offset:9216\n"
"v_mfma_f32_16x16x32_bf16 v[92:95], v[16:19], a[4:7], 0\n"
"ds_read_b64_tr_b16 v[14:15], v6 offset:9728\n"
"s_add_u32 m0, s20, 98304\n"
"s_mov_b32 s21, 98304\n"
"buffer_load_dwordx4 v1, s[8:11], s21 offen sc1 lds\n"
"s_waitcnt lgkmcnt(12)\n"
"v_mfma_f32_16x16x32_bf16 v[96:99], v[20:23], a[0:3], 0\n"
"ds_read_b64_tr_b16 v[16:17], v6 offset:10240\n"
"v_mfma_f32_16x16x32_bf16 v[100:103], v[20:23], a[4:7], 0\n"
"ds_read_b64_tr_b16 v[18:19], v6 offset:10752\n"
"v_mfma_f32_16x16x32_bf16 v[104:107], v[24:27], a[0:3], 0\n"
"ds_read_b64_tr_b16 v[20:21], v6 offset:11264\n"
"v_mfma_f32_16x16x32_bf16 v[108:111], v[24:27], a[4:7], 0\n"
"ds_read_b64_tr_b16 v[22:23], v6 offset:11776\n"
"buffer_load_dwordx4 v1, s[8:11], s21 offen offset:1024 sc1 lds\n"
"s_waitcnt lgkmcnt(12)\n"
"v_mfma_f32_16x16x32_bf16 v[112:115], v[28:31], a[0:3], 0\n"
"ds_read_b64_tr_b16 v[24:25], v6 offset:12288\n"
"v_mfma_f32_16x16x32_bf16 v[116:119], v[28:31], a[4:7], 0\n"
"ds_read_b64_tr_b16 v[26:27], v6 offset:12800\n"
"v_mfma_f32_16x16x32_bf16 v[120:123], v[32:35], a[0:3], 0\n"
"ds_read_b64_tr_b16 v[28:29], v6 offset:13312\n"
"v_mfma_f32_16x16x32_bf16 v[124:127], v[32:35], a[4:7], 0\n"
"ds_read_b64_tr_b16 v[30:31], v6 offset:13824\n"
"buffer_load_dwordx4 v1, s[8:11], s21 offen offset:2048 sc1 lds\n"
"s_waitcnt lgkmcnt(12)\n"
"v_mfma_f32_16x16x32_bf16 v[128:131], v[36:39], a[0:3], 0\n"
"ds_read_b64_tr_b16 v[32:33], v6 offset:14336\n"
"v_mfma_f32_16x16x32_bf16 v[132:135], v[36:39], a[4:7], 0\n"
"ds_read_b64_tr_b16 v[34:35], v6 offset:14848\n"
"s_waitcnt vmcnt(23)\n"
"v_mfma_f32_16x16x32_bf16 v[80:83], v[40:43], a[8:11], v[80:83]\n"
"ds_read_b64_tr_b16 v[36:37], v6 offset:15360\n"
"v_mfma_f32_16x16x32_bf16 v[84:87], v[40:43], a[12:15], v[84:87]\n"
"ds_read_b64_tr_b16 v[38:39], v6 offset:15872\n"
"buffer_load_dwordx4 v1, s[8:11], s21 offen offset:3072 sc1 lds\n"
"s_barrier\n"
"s_cmp_eq_u32 s23, 0\n"
"s_cbranch_scc0 Lg0_sig_%=\n"
"v_mov_b32_e32 v152, 0\n"
"v_mov_b32_e32 v153, 1\n"
"s_mov_b64 exec, 1\n"
"global_atomic_add v152, v153, s[48:49]\n"
"s_mov_b64 exec, -1\n"
"Lg0_sig_%=:\n"
"s_waitcnt lgkmcnt(12)\n"
"v_mfma_f32_16x16x32_bf16 v[88:91], v[44:47], a[8:11], v[88:91]\n"
"ds_read_b64_tr_b16 v[40:41], v6 offset:16384\n"
"v_mfma_f32_16x16x32_bf16 v[92:95], v[44:47], a[12:15], v[92:95]\n"
"ds_read_b64_tr_b16 v[42:43], v6 offset:16896\n"
"v_mfma_f32_16x16x32_bf16 v[96:99], v[12:15], a[8:11], v[96:99]\n"
"ds_read_b64_tr_b16 v[44:45], v6 offset:17408\n"
"v_mfma_f32_16x16x32_bf16 v[100:103], v[12:15], a[12:15], v[100:103]\n"
"ds_read_b64_tr_b16 v[46:47], v6 offset:17920\n"
"buffer_load_dwordx4 a[80:83], v2, s[12:15], s22 offen offset:2048 sc1\n"
"s_waitcnt lgkmcnt(12)\n"
"v_mfma_f32_16x16x32_bf16 v[104:107], v[16:19], a[8:11], v[104:107]\n"
"ds_read_b64_tr_b16 v[12:13], v6 offset:18432\n"
"v_mfma_f32_16x16x32_bf16 v[108:111], v[16:19], a[12:15], v[108:111]\n"
"ds_read_b64_tr_b16 v[14:15], v6 offset:18944\n"
"v_mfma_f32_16x16x32_bf16 v[112:115], v[20:23], a[8:11], v[112:115]\n"
"ds_read_b64_tr_b16 v[16:17], v6 offset:19456\n"
"v_mfma_f32_16x16x32_bf16 v[116:119], v[20:23], a[12:15], v[116:119]\n"
"ds_read_b64_tr_b16 v[18:19], v6 offset:19968\n"
"buffer_load_dwordx4 a[84:87], v3, s[12:15], s22 offen offset:2048 sc1\n"
"s_waitcnt lgkmcnt(12)\n"
"v_mfma_f32_16x16x32_bf16 v[120:123], v[24:27], a[8:11], v[120:123]\n"
"ds_read_b64_tr_b16 v[20:21], v6 offset:20480\n"
"v_mfma_f32_16x16x32_bf16 v[124:127], v[24:27], a[12:15], v[124:127]\n"
"ds_read_b64_tr_b16 v[22:23], v6 offset:20992\n"
"v_mfma_f32_16x16x32_bf16 v[128:131], v[28:31], a[8:11], v[128:131]\n"
"ds_read_b64_tr_b16 v[24:25], v6 offset:21504\n"
"v_mfma_f32_16x16x32_bf16 v[132:135], v[28:31], a[12:15], v[132:135]\n"
"ds_read_b64_tr_b16 v[26:27], v6 offset:22016\n"
"buffer_load_dwordx4 a[88:91], v2, s[12:15], s22 offen offset:3072 sc1\n"
"s_waitcnt vmcnt(25)\n"
"s_waitcnt lgkmcnt(12)\n"
"v_mfma_f32_16x16x32_bf16 v[80:83], v[32:35], a[16:19], v[80:83]\n"
"ds_read_b64_tr_b16 v[28:29], v6 offset:22528\n"
"v_mfma_f32_16x16x32_bf16 v[84:87], v[32:35], a[20:23], v[84:87]\n"
"ds_read_b64_tr_b16 v[30:31], v6 offset:23040\n"
"v_mfma_f32_16x16x32_bf16 v[88:91], v[36:39], a[16:19], v[88:91]\n"
"ds_read_b64_tr_b16 v[32:33], v6 offset:23552\n"
"v_mfma_f32_16x16x32_bf16 v[92:95], v[36:39], a[20:23], v[92:95]\n"
"ds_read_b64_tr_b16 v[34:35], v6 offset:24064\n"
"buffer_load_dwordx4 a[92:95], v3, s[12:15], s22 offen offset:3072 sc1\n"
"s_waitcnt lgkmcnt(12)\n"
"v_mfma_f32_16x16x32_bf16 v[96:99], v[40:43], a[16:19], v[96:99]\n"
"ds_read_b64_tr_b16 v[36:37], v6 offset:24576\n"
"v_mfma_f32_16x16x32_bf16 v[100:103], v[40:43], a[20:23], v[100:103]\n"
"ds_read_b64_tr_b16 v[38:39], v6 offset:25088\n"
"v_mfma_f32_16x16x32_bf16 v[104:107], v[44:47], a[16:19], v[104:107]\n"
"ds_read_b64_tr_b16 v[40:41], v6 offset:25600\n"
"v_mfma_f32_16x16x32_bf16 v[108:111], v[44:47], a[20:23], v[108:111]\n"
"ds_read_b64_tr_b16 v[42:43], v6 offset:26112\n"
"s_mov_b32 s22, 12288\n"
"buffer_load_dwordx4 a[96:99], v2, s[12:15], s22 offen sc1\n"
"s_waitcnt lgkmcnt(12)\n"
"v_mfma_f32_16x16x32_bf16 v[112:115], v[12:15], a[16:19], v[112:115]\n"
"ds_read_b64_tr_b16 v[44:45], v6 offset:26624\n"
"v_mfma_f32_16x16x32_bf16 v[116:119], v[12:15], a[20:23], v[116:119]\n"
"ds_read_b64_tr_b16 v[46:47], v6 offset:27136\n"
"v_mfma_f32_16x16x32_bf16 v[120:123], v[16:19], a[16:19], v[120:123]\n"
"ds_read_b64_tr_b16 v[12:13], v6 offset:27648\n"
"v_mfma_f32_16x16x32_bf16 v[124:127], v[16:19], a[20:23], v[124:127]\n"
"ds_read_b64_tr_b16 v[14:15], v6 offset:28160\n"
"buffer_load_dwordx4 a[100:103], v3, s[12:15], s22 offen sc1\n"
"s_waitcnt lgkmcnt(12)\n"
"v_mfma_f32_16x16x32_bf16 v[128:131], v[20:23], a[16:19], v[128:131]\n"
"ds_read_b64_tr_b16 v[16:17], v6 offset:28672\n"
"v_mfma_f32_16x16x32_bf16 v[132:135], v[20:23], a[20:23], v[132:135]\n"
"ds_read_b64_tr_b16 v[18:19], v6 offset:29184\n"
"s_waitcnt vmcnt(22)\n"
"v_mfma_f32_16x16x32_bf16 v[80:83], v[24:27], a[24:27], v[80:83]\n"
"ds_read_b64_tr_b16 v[20:21], v6 offset:29696\n"
"v_mfma_f32_16x16x32_bf16 v[84:87], v[24:27], a[28:31], v[84:87]\n"
"ds_read_b64_tr_b16 v[22:23], v6 offset:30208\n"
"s_add_u32 m0, s20, 114688\n"
"s_mov_b32 s21, 114688\n"
"buffer_load_dwordx4 v1, s[8:11], s21 offen sc1 lds\n"
"s_waitcnt lgkmcnt(12)\n"
"v_mfma_f32_16x16x32_bf16 v[88:91], v[28:31], a[24:27], v[88:91]\n"
"ds_read_b64_tr_b16 v[24:25], v6 offset:30720\n"
"v_mfma_f32_16x16x32_bf16 v[92:95], v[28:31], a[28:31], v[92:95]\n"
"ds_read_b64_tr_b16 v[26:27], v6 offset:31232\n"
"v_mfma_f32_16x16x32_bf16 v[96:99], v[32:35], a[24:27], v[96:99]\n"
"ds_read_b64_tr_b16 v[28:29], v6 offset:31744\n"
"v_mfma_f32_16x16x32_bf16 v[100:103], v[32:35], a[28:31], v[100:103]\n"
"ds_read_b64_tr_b16 v[30:31], v6 offset:32256\n"
"buffer_load_dwordx4 v1, s[8:11], s21 offen offset:1024 sc1 lds\n"
"s_barrier\n"
"s_waitcnt lgkmcnt(12)\n"
"v_mfma_f32_16x16x32_bf16 v[104:107], v[36:39], a[24:27], v[104:107]\n"
"ds_read_b64_tr_b16 v[32:33], v6 offset:32768\n"
"v_mfma_f32_16x16x32_bf16 v[108:111], v[36:39], a[28:31], v[108:111]\n"
"ds_read_b64_tr_b16 v[34:35], v6 offset:33280\n"
"v_mfma_f32_16x16x32_bf16 v[112:115], v[40:43], a[24:27], v[112:115]\n"
"ds_read_b64_tr_b16 v[36:37], v6 offset:33792\n"
"v_mfma_f32_16x16x32_bf16 v[116:119], v[40:43], a[28:31], v[116:119]\n"
"ds_read_b64_tr_b16 v[38:39], v6 offset:34304\n"
"buffer_load_dwordx4 v1, s[8:11], s21 offen offset:2048 sc1 lds\n"
"s_waitcnt lgkmcnt(12)\n"
"v_mfma_f32_16x16x32_bf16 v[120:123], v[44:47], a[24:27], v[120:123]\n"
"ds_read_b64_tr_b16 v[40:41], v6 offset:34816\n"
"v_mfma_f32_16x16x32_bf16 v[124:127], v[44:47], a[28:31], v[124:127]\n"
"ds_read_b64_tr_b16 v[42:43], v6 offset:35328\n"
"v_mfma_f32_16x16x32_bf16 v[128:131], v[12:15], a[24:27], v[128:131]\n"
"ds_read_b64_tr_b16 v[44:45], v6 offset:35840\n"
"v_mfma_f32_16x16x32_bf16 v[132:135], v[12:15], a[28:31], v[132:135]\n"
"ds_read_b64_tr_b16 v[46:47], v6 offset:36352\n"
"v_mov_b32_e32 v154, 0\n"
"global_load_dword v155, v154, s[50:51] sc1\n"
"s_waitcnt vmcnt(24)\n"
"s_waitcnt lgkmcnt(12)\n"
"v_mfma_f32_16x16x32_bf16 v[80:83], v[16:19], a[32:35], v[80:83]\n"
"ds_read_b64_tr_b16 v[12:13], v6 offset:36864\n"
"v_mfma_f32_16x16x32_bf16 v[84:87], v[16:19], a[36:39], v[84:87]\n"
"ds_read_b64_tr_b16 v[14:15], v6 offset:37376\n"
"v_mfma_f32_16x16x32_bf16 v[88:91], v[20:23], a[32:35], v[88:91]\n"
"ds_read_b64_tr_b16 v[16:17], v6 offset:37888\n"
"v_mfma_f32_16x16x32_bf16 v[92:95], v[20:23], a[36:39], v[92:95]\n"
"ds_read_b64_tr_b16 v[18:19], v6 offset:38400\n"
"buffer_load_dwordx4 v1, s[8:11], s21 offen offset:3072 sc1 lds\n"
"s_waitcnt lgkmcnt(12)\n"
"v_mfma_f32_16x16x32_bf16 v[96:99], v[24:27], a[32:35], v[96:99]\n"
"ds_read_b64_tr_b16 v[20:21], v6 offset:38912\n"
"v_mfma_f32_16x16x32_bf16 v[100:103], v[24:27], a[36:39], v[100:103]\n"
"ds_read_b64_tr_b16 v[22:23], v6 offset:39424\n"
"v_mfma_f32_16x16x32_bf16 v[104:107], v[28:31], a[32:35], v[104:107]\n"
"ds_read_b64_tr_b16 v[24:25], v6 offset:39936\n"
"v_mfma_f32_16x16x32_bf16 v[108:111], v[28:31], a[36:39], v[108:111]\n"
"ds_read_b64_tr_b16 v[26:27], v6 offset:40448\n"
"buffer_load_dwordx4 a[104:107], v2, s[12:15], s22 offen offset:1024 sc1\n"
"s_waitcnt lgkmcnt(12)\n"
"v_mfma_f32_16x16x32_bf16 v[112:115], v[32:35], a[32:35], v[112:115]\n"
"ds_read_b64_tr_b16 v[28:29], v6 offset:40960\n"
"v_mfma_f32_16x16x32_bf16 v[116:119], v[32:35], a[36:39], v[116:119]\n"
"ds_read_b64_tr_b16 v[30:31], v6 offset:41472\n"
"v_mfma_f32_16x16x32_bf16 v[120:123], v[36:39], a[32:35], v[120:123]\n"
"ds_read_b64_tr_b16 v[32:33], v6 offset:41984\n"
"v_mfma_f32_16x16x32_bf16 v[124:127], v[36:39], a[36:39], v[124:127]\n"
"ds_read_b64_tr_b16 v[34:35], v6 offset:42496\n"
"buffer_load_dwordx4 a[108:111], v3, s[12:15], s22 offen offset:1024 sc1\n"
"s_waitcnt lgkmcnt(12)\n"
"v_mfma_f32_16x16x32_bf16 v[128:131], v[40:43], a[32:35], v[128:131]\n"
"ds_read_b64_tr_b16 v[36:37], v6 offset:43008\n"
"v_mfma_f32_16x16x32_bf16 v[132:135], v[40:43], a[36:39], v[132:135]\n"
"ds_read_b64_tr_b16 v[38:39], v6 offset:43520\n"
"s_waitcnt vmcnt(25)\n"
"v_mfma_f32_16x16x32_bf16 v[80:83], v[44:47], a[40:43], v[80:83]\n"
"ds_read_b64_tr_b16 v[40:41], v6 offset:44032\n"
"v_mfma_f32_16x16x32_bf16 v[84:87], v[44:47], a[44:47], v[84:87]\n"
"ds_read_b64_tr_b16 v[42:43], v6 offset:44544\n"
"buffer_load_dwordx4 a[112:115], v2, s[12:15], s22 offen offset:2048 sc1\n"
"s_waitcnt lgkmcnt(12)\n"
"v_mfma_f32_16x16x32_bf16 v[88:91], v[12:15], a[40:43], v[88:91]\n"
"ds_read_b64_tr_b16 v[44:45], v6 offset:45056\n"
"v_mfma_f32_16x16x32_bf16 v[92:95], v[12:15], a[44:47], v[92:95]\n"
"ds_read_b64_tr_b16 v[46:47], v6 offset:45568\n"
"v_mfma_f32_16x16x32_bf16 v[96:99], v[16:19], a[40:43], v[96:99]\n"
"ds_read_b64_tr_b16 v[12:13], v6 offset:46080\n"
"v_mfma_f32_16x16x32_bf16 v[100:103], v[16:19], a[44:47], v[100:103]\n"
"ds_read_b64_tr_b16 v[14:15], v6 offset:46592\n"
"buffer_load_dwordx4 a[116:119], v3, s[12:15], s22 offen offset:2048 sc1\n"
"s_waitcnt lgkmcnt(12)\n"
"v_mfma_f32_16x16x32_bf16 v[104:107], v[20:23], a[40:43], v[104:107]\n"
"ds_read_b64_tr_b16 v[16:17], v6 offset:47104\n"
"v_mfma_f32_16x16x32_bf16 v[108:111], v[20:23], a[44:47], v[108:111]\n"
"ds_read_b64_tr_b16 v[18:19], v6 offset:47616\n"
"v_mfma_f32_16x16x32_bf16 v[112:115], v[24:27], a[40:43], v[112:115]\n"
"ds_read_b64_tr_b16 v[20:21], v6 offset:48128\n"
"v_mfma_f32_16x16x32_bf16 v[116:119], v[24:27], a[44:47], v[116:119]\n"
"ds_read_b64_tr_b16 v[22:23], v6 offset:48640\n"
"s_add_u32 m0, s20, 131072\n"
"s_mov_b32 s21, 131072\n"
"buffer_load_dwordx4 v1, s[8:11], s21 offen sc1 lds\n"
"s_barrier\n"
"s_waitcnt lgkmcnt(12)\n"
"v_mfma_f32_16x16x32_bf16 v[120:123], v[28:31], a[40:43], v[120:123]\n"
"ds_read_b64_tr_b16 v[24:25], v6 offset:49152\n"
"v_mfma_f32_16x16x32_bf16 v[124:127], v[28:31], a[44:47], v[124:127]\n"
"ds_read_b64_tr_b16 v[26:27], v6 offset:49664\n"
"v_mfma_f32_16x16x32_bf16 v[128:131], v[32:35], a[40:43], v[128:131]\n"
"ds_read_b64_tr_b16 v[28:29], v6 offset:50176\n"
"v_mfma_f32_16x16x32_bf16 v[132:135], v[32:35], a[44:47], v[132:135]\n"
"ds_read_b64_tr_b16 v[30:31], v6 offset:50688\n"
"buffer_load_dwordx4 v1, s[8:11], s21 offen offset:1024 sc1 lds\n"
"s_waitcnt vmcnt(27)\n"
"s_waitcnt lgkmcnt(12)\n"
"v_mfma_f32_16x16x32_bf16 v[80:83], v[36:39], a[48:51], v[80:83]\n"
"ds_read_b64_tr_b16 v[32:33], v6 offset:51200\n"
"v_mfma_f32_16x16x32_bf16 v[84:87], v[36:39], a[52:55], v[84:87]\n"
"ds_read_b64_tr_b16 v[34:35], v6 offset:51712\n"
"v_mfma_f32_16x16x32_bf16 v[88:91], v[40:43], a[48:51], v[88:91]\n"
"ds_read_b64_tr_b16 v[36:37], v6 offset:52224\n"
"v_mfma_f32_16x16x32_bf16 v[92:95], v[40:43], a[52:55], v[92:95]\n"
"ds_read_b64_tr_b16 v[38:39], v6 offset:52736\n"
"buffer_load_dwordx4 v1, s[8:11], s21 offen offset:2048 sc1 lds\n"
"s_waitcnt lgkmcnt(12)\n"
"v_mfma_f32_16x16x32_bf16 v[96:99], v[44:47], a[48:51], v[96:99]\n"
"ds_read_b64_tr_b16 v[40:41], v6 offset:53248\n"
"v_mfma_f32_16x16x32_bf16 v[100:103], v[44:47], a[52:55], v[100:103]\n"
"ds_read_b64_tr_b16 v[42:43], v6 offset:53760\n"
"v_mfma_f32_16x16x32_bf16 v[104:107], v[12:15], a[48:51], v[104:107]\n"
"ds_read_b64_tr_b16 v[44:45], v6 offset:54272\n"
"v_mfma_f32_16x16x32_bf16 v[108:111], v[12:15], a[52:55], v[108:111]\n"
"ds_read_b64_tr_b16 v[46:47], v6 offset:54784\n"
"buffer_load_dwordx4 v1, s[8:11], s21 offen offset:3072 sc1 lds\n"
"s_waitcnt lgkmcnt(12)\n"
"v_mfma_f32_16x16x32_bf16 v[112:115], v[16:19], a[48:51], v[112:115]\n"
"ds_read_b64_tr_b16 v[12:13], v6 offset:55296\n"
"v_mfma_f32_16x16x32_bf16 v[116:119], v[16:19], a[52:55], v[116:119]\n"
"ds_read_b64_tr_b16 v[14:15], v6 offset:55808\n"
"v_mfma_f32_16x16x32_bf16 v[120:123], v[20:23], a[48:51], v[120:123]\n"
"ds_read_b64_tr_b16 v[16:17], v6 offset:56320\n"
"v_mfma_f32_16x16x32_bf16 v[124:127], v[20:23], a[52:55], v[124:127]\n"
"ds_read_b64_tr_b16 v[18:19], v6 offset:56832\n"
"buffer_load_dwordx4 a[120:123], v2, s[12:15], s22 offen offset:3072 sc1\n"
"s_waitcnt lgkmcnt(12)\n"
"v_mfma_f32_16x16x32_bf16 v[128:131], v[24:27], a[48:51], v[128:131]\n"
"ds_read_b64_tr_b16 v[20:21], v6 offset:57344\n"
"v_mfma_f32_16x16x32_bf16 v[132:135], v[24:27], a[52:55], v[132:135]\n"
"ds_read_b64_tr_b16 v[22:23], v6 offset:57856\n"
"s_waitcnt vmcnt(28)\n"
"v_mfma_f32_16x16x32_bf16 v[80:83], v[28:31], a[56:59], v[80:83]\n"
"ds_read_b64_tr_b16 v[24:25], v6 offset:58368\n"
"v_mfma_f32_16x16x32_bf16 v[84:87], v[28:31], a[60:63], v[84:87]\n"
"ds_read_b64_tr_b16 v[26:27], v6 offset:58880\n"
"buffer_load_dwordx4 a[124:127], v3, s[12:15], s22 offen offset:3072 sc1\n"
"s_waitcnt lgkmcnt(12)\n"
"v_mfma_f32_16x16x32_bf16 v[88:91], v[32:35], a[56:59], v[88:91]\n"
"ds_read_b64_tr_b16 v[28:29], v6 offset:59392\n"
"v_mfma_f32_16x16x32_bf16 v[92:95], v[32:35], a[60:63], v[92:95]\n"
"ds_read_b64_tr_b16 v[30:31], v6 offset:59904\n"
"v_mfma_f32_16x16x32_bf16 v[96:99], v[36:39], a[56:59], v[96:99]\n"
"ds_read_b64_tr_b16 v[32:33], v6 offset:60416\n"
"v_mfma_f32_16x16x32_bf16 v[100:103], v[36:39], a[60:63], v[100:103]\n"
"ds_read_b64_tr_b16 v[34:35], v6 offset:60928\n"
"s_waitcnt vmcnt(11)\n"
"s_mov_b32 s53, 2097152\n"
"Lg0_poll_%=:\n"
"v_cmp_gt_u32_e32 vcc, s52, v155\n"
"s_cmp_eq_u64 vcc, 0\n"
"s_cbranch_scc1 Lg0_polld_%=\n"
"global_load_dword v155, v154, s[50:51] sc1\n"
"s_waitcnt vmcnt(0)\n"
"s_sub_u32 s53, s53, 1\n"
"s_cmp_lg_u32 s53, 0\n"
"s_cbranch_scc1 Lg0_poll_%=\n"
"Lg0_polld_%=:\n"
"s_mov_b32 s22, 16384\n"
"buffer_load_dwordx4 a[128:131], v2, s[12:15], s22 offen sc1\n"
"s_waitcnt lgkmcnt(12)\n"
"v_mfma_f32_16x16x32_bf16 v[104:107], v[40:43], a[56:59], v[104:107]\n"
"ds_read_b64_tr_b16 v[36:37], v6 offset:61440\n"
"v_mfma_f32_16x16x32_bf16 v[108:111], v[40:43], a[60:63], v[108:111]\n"
"ds_read_b64_tr_b16 v[38:39], v6 offset:61952\n"
"v_mfma_f32_16x16x32_bf16 v[112:115], v[44:47], a[56:59], v[112:115]\n"
"ds_read_b64_tr_b16 v[40:41], v6 offset:62464\n"
"v_mfma_f32_16x16x32_bf16 v[116:119], v[44:47], a[60:63], v[116:119]\n"
"ds_read_b64_tr_b16 v[42:43], v6 offset:62976\n"
"buffer_load_dwordx4 a[132:135], v3, s[12:15], s22 offen sc1\n"
"s_waitcnt lgkmcnt(12)\n"
"v_mfma_f32_16x16x32_bf16 v[120:123], v[12:15], a[56:59], v[120:123]\n"
"ds_read_b64_tr_b16 v[44:45], v6 offset:63488\n"
"v_mfma_f32_16x16x32_bf16 v[124:127], v[12:15], a[60:63], v[124:127]\n"
"ds_read_b64_tr_b16 v[46:47], v6 offset:64000\n"
"v_mfma_f32_16x16x32_bf16 v[128:131], v[16:19], a[56:59], v[128:131]\n"
"ds_read_b64_tr_b16 v[12:13], v6 offset:64512\n"
"v_mfma_f32_16x16x32_bf16 v[132:135], v[16:19], a[60:63], v[132:135]\n"
"ds_read_b64_tr_b16 v[14:15], v6 offset:65024\n"
"s_add_u32 m0, s20, 0\n"
"s_mov_b32 s21, 147456\n"
"buffer_load_dwordx4 v1, s[8:11], s21 offen sc1 lds\n"
"s_barrier\n"
"s_waitcnt lgkmcnt(12)\n"
"v_mfma_f32_16x16x32_bf16 v[80:83], v[20:23], a[64:67], v[80:83]\n"
"ds_read_b64_tr_b16 v[16:17], v7 offset:0\n"
"v_mfma_f32_16x16x32_bf16 v[84:87], v[20:23], a[68:71], v[84:87]\n"
"ds_read_b64_tr_b16 v[18:19], v7 offset:512\n"
"v_mfma_f32_16x16x32_bf16 v[88:91], v[24:27], a[64:67], v[88:91]\n"
"ds_read_b64_tr_b16 v[20:21], v7 offset:1024\n"
"v_mfma_f32_16x16x32_bf16 v[92:95], v[24:27], a[68:71], v[92:95]\n"
"ds_read_b64_tr_b16 v[22:23], v7 offset:1536\n"
"buffer_load_dwordx4 v1, s[8:11], s21 offen offset:1024 sc1 lds\n"
"s_waitcnt lgkmcnt(12)\n"
"v_mfma_f32_16x16x32_bf16 v[96:99], v[28:31], a[64:67], v[96:99]\n"
"ds_read_b64_tr_b16 v[24:25], v7 offset:2048\n"
"v_mfma_f32_16x16x32_bf16 v[100:103], v[28:31], a[68:71], v[100:103]\n"
"ds_read_b64_tr_b16 v[26:27], v7 offset:2560\n"
"v_mfma_f32_16x16x32_bf16 v[104:107], v[32:35], a[64:67], v[104:107]\n"
"ds_read_b64_tr_b16 v[28:29], v7 offset:3072\n"
"v_mfma_f32_16x16x32_bf16 v[108:111], v[32:35], a[68:71], v[108:111]\n"
"ds_read_b64_tr_b16 v[30:31], v7 offset:3584\n"
"buffer_load_dwordx4 v1, s[8:11], s21 offen offset:2048 sc1 lds\n"
"s_waitcnt lgkmcnt(12)\n"
"v_mfma_f32_16x16x32_bf16 v[112:115], v[36:39], a[64:67], v[112:115]\n"
"ds_read_b64_tr_b16 v[32:33], v7 offset:4096\n"
"v_mfma_f32_16x16x32_bf16 v[116:119], v[36:39], a[68:71], v[116:119]\n"
"ds_read_b64_tr_b16 v[34:35], v7 offset:4608\n"
"v_mfma_f32_16x16x32_bf16 v[120:123], v[40:43], a[64:67], v[120:123]\n"
"ds_read_b64_tr_b16 v[36:37], v7 offset:5120\n"
"v_mfma_f32_16x16x32_bf16 v[124:127], v[40:43], a[68:71], v[124:127]\n"
"ds_read_b64_tr_b16 v[38:39], v7 offset:5632\n"
"buffer_load_dwordx4 v1, s[8:11], s21 offen offset:3072 sc1 lds\n"
"s_waitcnt lgkmcnt(12)\n"
"v_mfma_f32_16x16x32_bf16 v[128:131], v[44:47], a[64:67], v[128:131]\n"
"ds_read_b64_tr_b16 v[40:41], v7 offset:6144\n"
"v_mfma_f32_16x16x32_bf16 v[132:135], v[44:47], a[68:71], v[132:135]\n"
"ds_read_b64_tr_b16 v[42:43], v7 offset:6656\n"
"v_mfma_f32_16x16x32_bf16 v[80:83], v[12:15], a[72:75], v[80:83]\n"
"ds_read_b64_tr_b16 v[44:45], v7 offset:7168\n"
"v_mfma_f32_16x16x32_bf16 v[84:87], v[12:15], a[76:79], v[84:87]\n"
"ds_read_b64_tr_b16 v[46:47], v7 offset:7680\n"
"buffer_load_dwordx4 a[136:139], v2, s[12:15], s22 offen offset:1024 sc1\n"
"s_waitcnt lgkmcnt(12)\n"
"v_mfma_f32_16x16x32_bf16 v[88:91], v[16:19], a[72:75], v[88:91]\n"
"ds_read_b64_tr_b16 v[12:13], v7 offset:8192\n"
"v_mfma_f32_16x16x32_bf16 v[92:95], v[16:19], a[76:79], v[92:95]\n"
"ds_read_b64_tr_b16 v[14:15], v7 offset:8704\n"
"v_mfma_f32_16x16x32_bf16 v[96:99], v[20:23], a[72:75], v[96:99]\n"
"ds_read_b64_tr_b16 v[16:17], v7 offset:9216\n"
"v_mfma_f32_16x16x32_bf16 v[100:103], v[20:23], a[76:79], v[100:103]\n"
"ds_read_b64_tr_b16 v[18:19], v7 offset:9728\n"
"buffer_load_dwordx4 a[140:143], v3, s[12:15], s22 offen offset:1024 sc1\n"
"s_waitcnt lgkmcnt(12)\n"
"v_mfma_f32_16x16x32_bf16 v[104:107], v[24:27], a[72:75], v[104:107]\n"
"ds_read_b64_tr_b16 v[20:21], v7 offset:10240\n"
"v_mfma_f32_16x16x32_bf16 v[108:111], v[24:27], a[76:79], v[108:111]\n"
"ds_read_b64_tr_b16 v[22:23], v7 offset:10752\n"
"v_mfma_f32_16x16x32_bf16 v[112:115], v[28:31], a[72:75], v[112:115]\n"
"ds_read_b64_tr_b16 v[24:25], v7 offset:11264\n"
"v_mfma_f32_16x16x32_bf16 v[116:119], v[28:31], a[76:79], v[116:119]\n"
"ds_read_b64_tr_b16 v[26:27], v7 offset:11776\n"
"buffer_load_dwordx4 a[144:147], v2, s[12:15], s22 offen offset:2048 sc1\n"
"s_waitcnt lgkmcnt(12)\n"
"v_mfma_f32_16x16x32_bf16 v[120:123], v[32:35], a[72:75], v[120:123]\n"
"ds_read_b64_tr_b16 v[28:29], v7 offset:12288\n"
"v_mfma_f32_16x16x32_bf16 v[124:127], v[32:35], a[76:79], v[124:127]\n"
"ds_read_b64_tr_b16 v[30:31], v7 offset:12800\n"
"v_mfma_f32_16x16x32_bf16 v[128:131], v[36:39], a[72:75], v[128:131]\n"
"ds_read_b64_tr_b16 v[32:33], v7 offset:13312\n"
"v_mfma_f32_16x16x32_bf16 v[132:135], v[36:39], a[76:79], v[132:135]\n"
"ds_read_b64_tr_b16 v[34:35], v7 offset:13824\n"
"buffer_load_dwordx4 a[148:151], v3, s[12:15], s22 offen offset:2048 sc1\n"
"s_waitcnt lgkmcnt(12)\n"
"v_mfma_f32_16x16x32_bf16 v[80:83], v[40:43], a[80:83], v[80:83]\n"
"ds_read_b64_tr_b16 v[36:37], v7 offset:14336\n"
"v_mfma_f32_16x16x32_bf16 v[84:87], v[40:43], a[84:87], v[84:87]\n"
"ds_read_b64_tr_b16 v[38:39], v7 offset:14848\n"
"v_mfma_f32_16x16x32_bf16 v[88:91], v[44:47], a[80:83], v[88:91]\n"
"ds_read_b64_tr_b16 v[40:41], v7 offset:15360\n"
"v_mfma_f32_16x16x32_bf16 v[92:95], v[44:47], a[84:87], v[92:95]\n"
"ds_read_b64_tr_b16 v[42:43], v7 offset:15872\n"
"s_add_u32 m0, s20, 16384\n"
"s_mov_b32 s21, 163840\n"
"buffer_load_dwordx4 v1, s[8:11], s21 offen sc1 lds\n"
"s_barrier\n"
"s_waitcnt lgkmcnt(12)\n"
"v_mfma_f32_16x16x32_bf16 v[96:99], v[12:15], a[80:83], v[96:99]\n"
"ds_read_b64_tr_b16 v[44:45], v7 offset:16384\n"
"v_mfma_f32_16x16x32_bf16 v[100:103], v[12:15], a[84:87], v[100:103]\n"
"ds_read_b64_tr_b16 v[46:47], v7 offset:16896\n"
"v_mfma_f32_16x16x32_bf16 v[104:107], v[16:19], a[80:83], v[104:107]\n"
"ds_read_b64_tr_b16 v[12:13], v7 offset:17408\n"
"v_mfma_f32_16x16x32_bf16 v[108:111], v[16:19], a[84:87], v[108:111]\n"
"ds_read_b64_tr_b16 v[14:15], v7 offset:17920\n"
"buffer_load_dwordx4 v1, s[8:11], s21 offen offset:1024 sc1 lds\n"
"s_waitcnt lgkmcnt(12)\n"
"v_mfma_f32_16x16x32_bf16 v[112:115], v[20:23], a[80:83], v[112:115]\n"
"ds_read_b64_tr_b16 v[16:17], v7 offset:18432\n"
"v_mfma_f32_16x16x32_bf16 v[116:119], v[20:23], a[84:87], v[116:119]\n"
"ds_read_b64_tr_b16 v[18:19], v7 offset:18944\n"
"v_mfma_f32_16x16x32_bf16 v[120:123], v[24:27], a[80:83], v[120:123]\n"
"ds_read_b64_tr_b16 v[20:21], v7 offset:19456\n"
"v_mfma_f32_16x16x32_bf16 v[124:127], v[24:27], a[84:87], v[124:127]\n"
"ds_read_b64_tr_b16 v[22:23], v7 offset:19968\n"
"buffer_load_dwordx4 v1, s[8:11], s21 offen offset:2048 sc1 lds\n"
"s_waitcnt lgkmcnt(12)\n"
"v_mfma_f32_16x16x32_bf16 v[128:131], v[28:31], a[80:83], v[128:131]\n"
"ds_read_b64_tr_b16 v[24:25], v7 offset:20480\n"
"v_mfma_f32_16x16x32_bf16 v[132:135], v[28:31], a[84:87], v[132:135]\n"
"ds_read_b64_tr_b16 v[26:27], v7 offset:20992\n"
"v_mfma_f32_16x16x32_bf16 v[80:83], v[32:35], a[88:91], v[80:83]\n"
"ds_read_b64_tr_b16 v[28:29], v7 offset:21504\n"
"v_mfma_f32_16x16x32_bf16 v[84:87], v[32:35], a[92:95], v[84:87]\n"
"ds_read_b64_tr_b16 v[30:31], v7 offset:22016\n"
"buffer_load_dwordx4 v1, s[8:11], s21 offen offset:3072 sc1 lds\n"
"s_waitcnt lgkmcnt(12)\n"
"v_mfma_f32_16x16x32_bf16 v[88:91], v[36:39], a[88:91], v[88:91]\n"
"ds_read_b64_tr_b16 v[32:33], v7 offset:22528\n"
"v_mfma_f32_16x16x32_bf16 v[92:95], v[36:39], a[92:95], v[92:95]\n"
"ds_read_b64_tr_b16 v[34:35], v7 offset:23040\n"
"v_mfma_f32_16x16x32_bf16 v[96:99], v[40:43], a[88:91], v[96:99]\n"
"ds_read_b64_tr_b16 v[36:37], v7 offset:23552\n"
"v_mfma_f32_16x16x32_bf16 v[100:103], v[40:43], a[92:95], v[100:103]\n"
"ds_read_b64_tr_b16 v[38:39], v7 offset:24064\n"
"buffer_load_dwordx4 a[152:155], v2, s[12:15], s22 offen offset:3072 sc1\n"
"s_waitcnt lgkmcnt(12)\n"
"v_mfma_f32_16x16x32_bf16 v[104:107], v[44:47], a[88:91], v[104:107]\n"
"ds_read_b64_tr_b16 v[40:41], v7 offset:24576\n"
"v_mfma_f32_16x16x32_bf16 v[108:111], v[44:47], a[92:95], v[108:111]\n"
"ds_read_b64_tr_b16 v[42:43], v7 offset:25088\n"
"v_mfma_f32_16x16x32_bf16 v[112:115], v[12:15], a[88:91], v[112:115]\n"
"ds_read_b64_tr_b16 v[44:45], v7 offset:25600\n"
"v_mfma_f32_16x16x32_bf16 v[116:119], v[12:15], a[92:95], v[116:119]\n"
"ds_read_b64_tr_b16 v[46:47], v7 offset:26112\n"
"buffer_load_dwordx4 a[156:159], v3, s[12:15], s22 offen offset:3072 sc1\n"
"s_waitcnt lgkmcnt(12)\n"
"v_mfma_f32_16x16x32_bf16 v[120:123], v[16:19], a[88:91], v[120:123]\n"
"ds_read_b64_tr_b16 v[12:13], v7 offset:26624\n"
"v_mfma_f32_16x16x32_bf16 v[124:127], v[16:19], a[92:95], v[124:127]\n"
"ds_read_b64_tr_b16 v[14:15], v7 offset:27136\n"
"v_mfma_f32_16x16x32_bf16 v[128:131], v[20:23], a[88:91], v[128:131]\n"
"ds_read_b64_tr_b16 v[16:17], v7 offset:27648\n"
"v_mfma_f32_16x16x32_bf16 v[132:135], v[20:23], a[92:95], v[132:135]\n"
"ds_read_b64_tr_b16 v[18:19], v7 offset:28160\n"
"s_mov_b32 s22, 20480\n"
"buffer_load_dwordx4 a[160:163], v2, s[12:15], s22 offen sc1\n"
"s_waitcnt lgkmcnt(12)\n"
"v_mfma_f32_16x16x32_bf16 v[80:83], v[24:27], a[96:99], v[80:83]\n"
"ds_read_b64_tr_b16 v[20:21], v7 offset:28672\n"
"v_mfma_f32_16x16x32_bf16 v[84:87], v[24:27], a[100:103], v[84:87]\n"
"ds_read_b64_tr_b16 v[22:23], v7 offset:29184\n"
"v_mfma_f32_16x16x32_bf16 v[88:91], v[28:31], a[96:99], v[88:91]\n"
"ds_read_b64_tr_b16 v[24:25], v7 offset:29696\n"
"v_mfma_f32_16x16x32_bf16 v[92:95], v[28:31], a[100:103], v[92:95]\n"
"ds_read_b64_tr_b16 v[26:27], v7 offset:30208\n"
"buffer_load_dwordx4 a[164:167], v3, s[12:15], s22 offen sc1\n"
"s_waitcnt lgkmcnt(12)\n"
"v_mfma_f32_16x16x32_bf16 v[96:99], v[32:35], a[96:99], v[96:99]\n"
"ds_read_b64_tr_b16 v[28:29], v7 offset:30720\n"
"v_mfma_f32_16x16x32_bf16 v[100:103], v[32:35], a[100:103], v[100:103]\n"
"ds_read_b64_tr_b16 v[30:31], v7 offset:31232\n"
"v_mfma_f32_16x16x32_bf16 v[104:107], v[36:39], a[96:99], v[104:107]\n"
"ds_read_b64_tr_b16 v[32:33], v7 offset:31744\n"
"v_mfma_f32_16x16x32_bf16 v[108:111], v[36:39], a[100:103], v[108:111]\n"
"ds_read_b64_tr_b16 v[34:35], v7 offset:32256\n"
"buffer_load_dwordx4 a[168:171], v2, s[12:15], s22 offen offset:1024 sc1\n"
"s_barrier\n"
"s_waitcnt lgkmcnt(12)\n"
"v_mfma_f32_16x16x32_bf16 v[112:115], v[40:43], a[96:99], v[112:115]\n"
"ds_read_b64_tr_b16 v[36:37], v7 offset:32768\n"
"v_mfma_f32_16x16x32_bf16 v[116:119], v[40:43], a[100:103], v[116:119]\n"
"ds_read_b64_tr_b16 v[38:39], v7 offset:33280\n"
"v_mfma_f32_16x16x32_bf16 v[120:123], v[44:47], a[96:99], v[120:123]\n"
"ds_read_b64_tr_b16 v[40:41], v7 offset:33792\n"
"v_mfma_f32_16x16x32_bf16 v[124:127], v[44:47], a[100:103], v[124:127]\n"
"ds_read_b64_tr_b16 v[42:43], v7 offset:34304\n"
"buffer_load_dwordx4 a[172:175], v3, s[12:15], s22 offen offset:1024 sc1\n"
"s_waitcnt lgkmcnt(12)\n"
"v_mfma_f32_16x16x32_bf16 v[128:131], v[12:15], a[96:99], v[128:131]\n"
"ds_read_b64_tr_b16 v[44:45], v7 offset:34816\n"
"v_mfma_f32_16x16x32_bf16 v[132:135], v[12:15], a[100:103], v[132:135]\n"
"ds_read_b64_tr_b16 v[46:47], v7 offset:35328\n"
"s_waitcnt vmcnt(28)\n"
"v_mfma_f32_16x16x32_bf16 v[80:83], v[16:19], a[104:107], v[80:83]\n"
"ds_read_b64_tr_b16 v[12:13], v7 offset:35840\n"
"v_mfma_f32_16x16x32_bf16 v[84:87], v[16:19], a[108:111], v[84:87]\n"
"ds_read_b64_tr_b16 v[14:15], v7 offset:36352\n"
"s_add_u32 m0, s20, 32768\n"
"s_mov_b32 s21, 180224\n"
"buffer_load_dwordx4 v1, s[8:11], s21 offen sc1 lds\n"
"s_waitcnt lgkmcnt(12)\n"
"v_mfma_f32_16x16x32_bf16 v[88:91], v[20:23], a[104:107], v[88:91]\n"
"ds_read_b64_tr_b16 v[16:17], v7 offset:36864\n"
"v_mfma_f32_16x16x32_bf16 v[92:95], v[20:23], a[108:111], v[92:95]\n"
"ds_read_b64_tr_b16 v[18:19], v7 offset:37376\n"
"v_mfma_f32_16x16x32_bf16 v[96:99], v[24:27], a[104:107], v[96:99]\n"
"ds_read_b64_tr_b16 v[20:21], v7 offset:37888\n"
"v_mfma_f32_16x16x32_bf16 v[100:103], v[24:27], a[108:111], v[100:103]\n"
"ds_read_b64_tr_b16 v[22:23], v7 offset:38400\n"
"buffer_load_dwordx4 v1, s[8:11], s21 offen offset:1024 sc1 lds\n"
"s_waitcnt lgkmcnt(12)\n"
"v_mfma_f32_16x16x32_bf16 v[104:107], v[28:31], a[104:107], v[104:107]\n"
"ds_read_b64_tr_b16 v[24:25], v7 offset:38912\n"
"v_mfma_f32_16x16x32_bf16 v[108:111], v[28:31], a[108:111], v[108:111]\n"
"ds_read_b64_tr_b16 v[26:27], v7 offset:39424\n"
"v_mfma_f32_16x16x32_bf16 v[112:115], v[32:35], a[104:107], v[112:115]\n"
"ds_read_b64_tr_b16 v[28:29], v7 offset:39936\n"
"v_mfma_f32_16x16x32_bf16 v[116:119], v[32:35], a[108:111], v[116:119]\n"
"ds_read_b64_tr_b16 v[30:31], v7 offset:40448\n"
"buffer_load_dwordx4 v1, s[8:11], s21 offen offset:2048 sc1 lds\n"
"s_waitcnt lgkmcnt(12)\n"
"v_mfma_f32_16x16x32_bf16 v[120:123], v[36:39], a[104:107], v[120:123]\n"
"ds_read_b64_tr_b16 v[32:33], v7 offset:40960\n"
"v_mfma_f32_16x16x32_bf16 v[124:127], v[36:39], a[108:111], v[124:127]\n"
"ds_read_b64_tr_b16 v[34:35], v7 offset:41472\n"
"v_mfma_f32_16x16x32_bf16 v[128:131], v[40:43], a[104:107], v[128:131]\n"
"ds_read_b64_tr_b16 v[36:37], v7 offset:41984\n"
"v_mfma_f32_16x16x32_bf16 v[132:135], v[40:43], a[108:111], v[132:135]\n"
"ds_read_b64_tr_b16 v[38:39], v7 offset:42496\n"
"buffer_load_dwordx4 v1, s[8:11], s21 offen offset:3072 sc1 lds\n"
"s_waitcnt vmcnt(30)\n"
"s_waitcnt lgkmcnt(12)\n"
"v_mfma_f32_16x16x32_bf16 v[80:83], v[44:47], a[112:115], v[80:83]\n"
"ds_read_b64_tr_b16 v[40:41], v7 offset:43008\n"
"v_mfma_f32_16x16x32_bf16 v[84:87], v[44:47], a[116:119], v[84:87]\n"
"ds_read_b64_tr_b16 v[42:43], v7 offset:43520\n"
"v_mfma_f32_16x16x32_bf16 v[88:91], v[12:15], a[112:115], v[88:91]\n"
"ds_read_b64_tr_b16 v[44:45], v7 offset:44032\n"
"v_mfma_f32_16x16x32_bf16 v[92:95], v[12:15], a[116:119], v[92:95]\n"
"ds_read_b64_tr_b16 v[46:47], v7 offset:44544\n"
"buffer_load_dwordx4 a[176:179], v2, s[12:15], s22 offen offset:2048 sc1\n"
"s_waitcnt lgkmcnt(12)\n"
"v_mfma_f32_16x16x32_bf16 v[96:99], v[16:19], a[112:115], v[96:99]\n"
"ds_read_b64_tr_b16 v[12:13], v7 offset:45056\n"
"v_mfma_f32_16x16x32_bf16 v[100:103], v[16:19], a[116:119], v[100:103]\n"
"ds_read_b64_tr_b16 v[14:15], v7 offset:45568\n"
"v_mfma_f32_16x16x32_bf16 v[104:107], v[20:23], a[112:115], v[104:107]\n"
"ds_read_b64_tr_b16 v[16:17], v7 offset:46080\n"
"v_mfma_f32_16x16x32_bf16 v[108:111], v[20:23], a[116:119], v[108:111]\n"
"ds_read_b64_tr_b16 v[18:19], v7 offset:46592\n"
"buffer_load_dwordx4 a[180:183], v3, s[12:15], s22 offen offset:2048 sc1\n"
"s_waitcnt lgkmcnt(12)\n"
"v_mfma_f32_16x16x32_bf16 v[112:115], v[24:27], a[112:115], v[112:115]\n"
"ds_read_b64_tr_b16 v[20:21], v7 offset:47104\n"
"v_mfma_f32_16x16x32_bf16 v[116:119], v[24:27], a[116:119], v[116:119]\n"
"ds_read_b64_tr_b16 v[22:23], v7 offset:47616\n"
"v_mfma_f32_16x16x32_bf16 v[120:123], v[28:31], a[112:115], v[120:123]\n"
"ds_read_b64_tr_b16 v[24:25], v7 offset:48128\n"
"v_mfma_f32_16x16x32_bf16 v[124:127], v[28:31], a[116:119], v[124:127]\n"
"ds_read_b64_tr_b16 v[26:27], v7 offset:48640\n"
"buffer_load_dwordx4 a[184:187], v2, s[12:15], s22 offen offset:3072 sc1\n"
"s_barrier\n"
"s_waitcnt lgkmcnt(12)\n"
"v_mfma_f32_16x16x32_bf16 v[128:131], v[32:35], a[112:115], v[128:131]\n"
"ds_read_b64_tr_b16 v[28:29], v7 offset:49152\n"
"v_mfma_f32_16x16x32_bf16 v[132:135], v[32:35], a[116:119], v[132:135]\n"
"ds_read_b64_tr_b16 v[30:31], v7 offset:49664\n"
"s_waitcnt vmcnt(27)\n"
"v_mfma_f32_16x16x32_bf16 v[80:83], v[36:39], a[120:123], v[80:83]\n"
"ds_read_b64_tr_b16 v[32:33], v7 offset:50176\n"
"v_mfma_f32_16x16x32_bf16 v[84:87], v[36:39], a[124:127], v[84:87]\n"
"ds_read_b64_tr_b16 v[34:35], v7 offset:50688\n"
"buffer_load_dwordx4 a[188:191], v3, s[12:15], s22 offen offset:3072 sc1\n"
"s_waitcnt lgkmcnt(12)\n"
"v_mfma_f32_16x16x32_bf16 v[88:91], v[40:43], a[120:123], v[88:91]\n"
"ds_read_b64_tr_b16 v[36:37], v7 offset:51200\n"
"v_mfma_f32_16x16x32_bf16 v[92:95], v[40:43], a[124:127], v[92:95]\n"
"ds_read_b64_tr_b16 v[38:39], v7 offset:51712\n"
"v_mfma_f32_16x16x32_bf16 v[96:99], v[44:47], a[120:123], v[96:99]\n"
"ds_read_b64_tr_b16 v[40:41], v7 offset:52224\n"
"v_mfma_f32_16x16x32_bf16 v[100:103], v[44:47], a[124:127], v[100:103]\n"
"ds_read_b64_tr_b16 v[42:43], v7 offset:52736\n"
"s_add_u32 m0, s20, 49152\n"
"s_mov_b32 s21, 196608\n"
"buffer_load_dwordx4 v1, s[8:11], s21 offen sc1 lds\n"
"s_waitcnt lgkmcnt(12)\n"
"v_mfma_f32_16x16x32_bf16 v[104:107], v[12:15], a[120:123], v[104:107]\n"
"ds_read_b64_tr_b16 v[44:45], v7 offset:53248\n"
"v_mfma_f32_16x16x32_bf16 v[108:111], v[12:15], a[124:127], v[108:111]\n"
"ds_read_b64_tr_b16 v[46:47], v7 offset:53760\n"
"v_mfma_f32_16x16x32_bf16 v[112:115], v[16:19], a[120:123], v[112:115]\n"
"ds_read_b64_tr_b16 v[12:13], v7 offset:54272\n"
"v_mfma_f32_16x16x32_bf16 v[116:119], v[16:19], a[124:127], v[116:119]\n"
"ds_read_b64_tr_b16 v[14:15], v7 offset:54784\n"
"buffer_load_dwordx4 v1, s[8:11], s21 offen offset:1024 sc1 lds\n"
"s_waitcnt lgkmcnt(12)\n"
"v_mfma_f32_16x16x32_bf16 v[120:123], v[20:23], a[120:123], v[120:123]\n"
"ds_read_b64_tr_b16 v[16:17], v7 offset:55296\n"
"v_mfma_f32_16x16x32_bf16 v[124:127], v[20:23], a[124:127], v[124:127]\n"
"ds_read_b64_tr_b16 v[18:19], v7 offset:55808\n"
"v_mfma_f32_16x16x32_bf16 v[128:131], v[24:27], a[120:123], v[128:131]\n"
"ds_read_b64_tr_b16 v[20:21], v7 offset:56320\n"
"v_mfma_f32_16x16x32_bf16 v[132:135], v[24:27], a[124:127], v[132:135]\n"
"ds_read_b64_tr_b16 v[22:23], v7 offset:56832\n"
"buffer_load_dwordx4 v1, s[8:11], s21 offen offset:2048 sc1 lds\n"
"s_waitcnt vmcnt(29)\n"
"s_waitcnt lgkmcnt(12)\n"
"v_mfma_f32_16x16x32_bf16 v[80:83], v[28:31], a[128:131], v[80:83]\n"
"ds_read_b64_tr_b16 v[24:25], v7 offset:57344\n"
"v_mfma_f32_16x16x32_bf16 v[84:87], v[28:31], a[132:135], v[84:87]\n"
"ds_read_b64_tr_b16 v[26:27], v7 offset:57856\n"
"v_mfma_f32_16x16x32_bf16 v[88:91], v[32:35], a[128:131], v[88:91]\n"
"ds_read_b64_tr_b16 v[28:29], v7 offset:58368\n"
"v_mfma_f32_16x16x32_bf16 v[92:95], v[32:35], a[132:135], v[92:95]\n"
"ds_read_b64_tr_b16 v[30:31], v7 offset:58880\n"
"buffer_load_dwordx4 v1, s[8:11], s21 offen offset:3072 sc1 lds\n"
"s_waitcnt lgkmcnt(12)\n"
"v_mfma_f32_16x16x32_bf16 v[96:99], v[36:39], a[128:131], v[96:99]\n"
"ds_read_b64_tr_b16 v[32:33], v7 offset:59392\n"
"v_mfma_f32_16x16x32_bf16 v[100:103], v[36:39], a[132:135], v[100:103]\n"
"ds_read_b64_tr_b16 v[34:35], v7 offset:59904\n"
"v_mfma_f32_16x16x32_bf16 v[104:107], v[40:43], a[128:131], v[104:107]\n"
"ds_read_b64_tr_b16 v[36:37], v7 offset:60416\n"
"v_mfma_f32_16x16x32_bf16 v[108:111], v[40:43], a[132:135], v[108:111]\n"
"ds_read_b64_tr_b16 v[38:39], v7 offset:60928\n"
"s_mov_b32 s22, 24576\n"
"buffer_load_dwordx4 a[192:195], v2, s[12:15], s22 offen sc1\n"
"s_waitcnt lgkmcnt(12)\n"
"v_mfma_f32_16x16x32_bf16 v[112:115], v[44:47], a[128:131], v[112:115]\n"
"ds_read_b64_tr_b16 v[40:41], v7 offset:61440\n"
"v_mfma_f32_16x16x32_bf16 v[116:119], v[44:47], a[132:135], v[116:119]\n"
"ds_read_b64_tr_b16 v[42:43], v7 offset:61952\n"
"v_mfma_f32_16x16x32_bf16 v[120:123], v[12:15], a[128:131], v[120:123]\n"
"ds_read_b64_tr_b16 v[44:45], v7 offset:62464\n"
"v_mfma_f32_16x16x32_bf16 v[124:127], v[12:15], a[132:135], v[124:127]\n"
"ds_read_b64_tr_b16 v[46:47], v7 offset:62976\n"
"buffer_load_dwordx4 a[196:199], v3, s[12:15], s22 offen sc1\n"
"s_waitcnt lgkmcnt(12)\n"
"v_mfma_f32_16x16x32_bf16 v[128:131], v[16:19], a[128:131], v[128:131]\n"
"ds_read_b64_tr_b16 v[12:13], v7 offset:63488\n"
"v_mfma_f32_16x16x32_bf16 v[132:135], v[16:19], a[132:135], v[132:135]\n"
"ds_read_b64_tr_b16 v[14:15], v7 offset:64000\n"
"s_waitcnt vmcnt(26)\n"
"v_mfma_f32_16x16x32_bf16 v[80:83], v[20:23], a[136:139], v[80:83]\n"
"ds_read_b64_tr_b16 v[16:17], v7 offset:64512\n"
"v_mfma_f32_16x16x32_bf16 v[84:87], v[20:23], a[140:143], v[84:87]\n"
"ds_read_b64_tr_b16 v[18:19], v7 offset:65024\n"
"buffer_load_dwordx4 a[200:203], v2, s[12:15], s22 offen offset:1024 sc1\n"
"s_barrier\n"
"s_waitcnt lgkmcnt(12)\n"
"v_mfma_f32_16x16x32_bf16 v[88:91], v[24:27], a[136:139], v[88:91]\n"
"ds_read_b64_tr_b16 v[20:21], v8 offset:0\n"
"v_mfma_f32_16x16x32_bf16 v[92:95], v[24:27], a[140:143], v[92:95]\n"
"ds_read_b64_tr_b16 v[22:23], v8 offset:512\n"
"v_mfma_f32_16x16x32_bf16 v[96:99], v[28:31], a[136:139], v[96:99]\n"
"ds_read_b64_tr_b16 v[24:25], v8 offset:1024\n"
"v_mfma_f32_16x16x32_bf16 v[100:103], v[28:31], a[140:143], v[100:103]\n"
"ds_read_b64_tr_b16 v[26:27], v8 offset:1536\n"
"buffer_load_dwordx4 a[204:207], v3, s[12:15], s22 offen offset:1024 sc1\n"
"s_waitcnt lgkmcnt(12)\n"
"v_mfma_f32_16x16x32_bf16 v[104:107], v[32:35], a[136:139], v[104:107]\n"
"ds_read_b64_tr_b16 v[28:29], v8 offset:2048\n"
"v_mfma_f32_16x16x32_bf16 v[108:111], v[32:35], a[140:143], v[108:111]\n"
"ds_read_b64_tr_b16 v[30:31], v8 offset:2560\n"
"v_mfma_f32_16x16x32_bf16 v[112:115], v[36:39], a[136:139], v[112:115]\n"
"ds_read_b64_tr_b16 v[32:33], v8 offset:3072\n"
"v_mfma_f32_16x16x32_bf16 v[116:119], v[36:39], a[140:143], v[116:119]\n"
"ds_read_b64_tr_b16 v[34:35], v8 offset:3584\n"
"s_add_u32 m0, s20, 65536\n"
"s_mov_b32 s21, 212992\n"
"buffer_load_dwordx4 v1, s[8:11], s21 offen sc1 lds\n"
"s_waitcnt lgkmcnt(12)\n"
"v_mfma_f32_16x16x32_bf16 v[120:123], v[40:43], a[136:139], v[120:123]\n"
"ds_read_b64_tr_b16 v[36:37], v8 offset:4096\n"
"v_mfma_f32_16x16x32_bf16 v[124:127], v[40:43], a[140:143], v[124:127]\n"
"ds_read_b64_tr_b16 v[38:39], v8 offset:4608\n"
"v_mfma_f32_16x16x32_bf16 v[128:131], v[44:47], a[136:139], v[128:131]\n"
"ds_read_b64_tr_b16 v[40:41], v8 offset:5120\n"
"v_mfma_f32_16x16x32_bf16 v[132:135], v[44:47], a[140:143], v[132:135]\n"
"ds_read_b64_tr_b16 v[42:43], v8 offset:5632\n"
"buffer_load_dwordx4 v1, s[8:11], s21 offen offset:1024 sc1 lds\n"
"s_waitcnt vmcnt(28)\n"
"s_waitcnt lgkmcnt(12)\n"
"v_mfma_f32_16x16x32_bf16 v[80:83], v[12:15], a[144:147], v[80:83]\n"
"ds_read_b64_tr_b16 v[44:45], v8 offset:6144\n"
"v_mfma_f32_16x16x32_bf16 v[84:87], v[12:15], a[148:151], v[84:87]\n"
"ds_read_b64_tr_b16 v[46:47], v8 offset:6656\n"
"v_mfma_f32_16x16x32_bf16 v[88:91], v[16:19], a[144:147], v[88:91]\n"
"ds_read_b64_tr_b16 v[12:13], v8 offset:7168\n"
"v_mfma_f32_16x16x32_bf16 v[92:95], v[16:19], a[148:151], v[92:95]\n"
"ds_read_b64_tr_b16 v[14:15], v8 offset:7680\n"
"buffer_load_dwordx4 v1, s[8:11], s21 offen offset:2048 sc1 lds\n"
"s_waitcnt lgkmcnt(12)\n"
"v_mfma_f32_16x16x32_bf16 v[96:99], v[20:23], a[144:147], v[96:99]\n"
"ds_read_b64_tr_b16 v[16:17], v8 offset:8192\n"
"v_mfma_f32_16x16x32_bf16 v[100:103], v[20:23], a[148:151], v[100:103]\n"
"ds_read_b64_tr_b16 v[18:19], v8 offset:8704\n"
"v_mfma_f32_16x16x32_bf16 v[104:107], v[24:27], a[144:147], v[104:107]\n"
"ds_read_b64_tr_b16 v[20:21], v8 offset:9216\n"
"v_mfma_f32_16x16x32_bf16 v[108:111], v[24:27], a[148:151], v[108:111]\n"
"ds_read_b64_tr_b16 v[22:23], v8 offset:9728\n"
"buffer_load_dwordx4 v1, s[8:11], s21 offen offset:3072 sc1 lds\n"
"s_waitcnt lgkmcnt(12)\n"
"v_mfma_f32_16x16x32_bf16 v[112:115], v[28:31], a[144:147], v[112:115]\n"
"ds_read_b64_tr_b16 v[24:25], v8 offset:10240\n"
"v_mfma_f32_16x16x32_bf16 v[116:119], v[28:31], a[148:151], v[116:119]\n"
"ds_read_b64_tr_b16 v[26:27], v8 offset:10752\n"
"v_mfma_f32_16x16x32_bf16 v[120:123], v[32:35], a[144:147], v[120:123]\n"
"ds_read_b64_tr_b16 v[28:29], v8 offset:11264\n"
"v_mfma_f32_16x16x32_bf16 v[124:127], v[32:35], a[148:151], v[124:127]\n"
"ds_read_b64_tr_b16 v[30:31], v8 offset:11776\n"
"buffer_load_dwordx4 a[208:211], v2, s[12:15], s22 offen offset:2048 sc1\n"
"s_waitcnt lgkmcnt(12)\n"
"v_mfma_f32_16x16x32_bf16 v[128:131], v[36:39], a[144:147], v[128:131]\n"
"ds_read_b64_tr_b16 v[32:33], v8 offset:12288\n"
"v_mfma_f32_16x16x32_bf16 v[132:135], v[36:39], a[148:151], v[132:135]\n"
"ds_read_b64_tr_b16 v[34:35], v8 offset:12800\n"
"s_waitcnt vmcnt(25)\n"
"v_mfma_f32_16x16x32_bf16 v[80:83], v[40:43], a[152:155], v[80:83]\n"
"ds_read_b64_tr_b16 v[36:37], v8 offset:13312\n"
"v_mfma_f32_16x16x32_bf16 v[84:87], v[40:43], a[156:159], v[84:87]\n"
"ds_read_b64_tr_b16 v[38:39], v8 offset:13824\n"
"buffer_load_dwordx4 a[212:215], v3, s[12:15], s22 offen offset:2048 sc1\n"
"s_waitcnt lgkmcnt(12)\n"
"v_mfma_f32_16x16x32_bf16 v[88:91], v[44:47], a[152:155], v[88:91]\n"
"ds_read_b64_tr_b16 v[40:41], v8 offset:14336\n"
"v_mfma_f32_16x16x32_bf16 v[92:95], v[44:47], a[156:159], v[92:95]\n"
"ds_read_b64_tr_b16 v[42:43], v8 offset:14848\n"
"v_mfma_f32_16x16x32_bf16 v[96:99], v[12:15], a[152:155], v[96:99]\n"
"ds_read_b64_tr_b16 v[44:45], v8 offset:15360\n"
"v_mfma_f32_16x16x32_bf16 v[100:103], v[12:15], a[156:159], v[100:103]\n"
"ds_read_b64_tr_b16 v[46:47], v8 offset:15872\n"
"buffer_load_dwordx4 a[216:219], v2, s[12:15], s22 offen offset:3072 sc1\n"
"s_barrier\n"
"s_waitcnt lgkmcnt(12)\n"
"v_mfma_f32_16x16x32_bf16 v[104:107], v[16:19], a[152:155], v[104:107]\n"
"ds_read_b64_tr_b16 v[12:13], v6 offset:0\n"
"v_mfma_f32_16x16x32_bf16 v[108:111], v[16:19], a[156:159], v[108:111]\n"
"ds_read_b64_tr_b16 v[14:15], v6 offset:512\n"
"v_mfma_f32_16x16x32_bf16 v[112:115], v[20:23], a[152:155], v[112:115]\n"
"ds_read_b64_tr_b16 v[16:17], v6 offset:1024\n"
"v_mfma_f32_16x16x32_bf16 v[116:119], v[20:23], a[156:159], v[116:119]\n"
"ds_read_b64_tr_b16 v[18:19], v6 offset:1536\n"
"buffer_load_dwordx4 a[220:223], v3, s[12:15], s22 offen offset:3072 sc1\n"
"s_waitcnt lgkmcnt(12)\n"
"v_mfma_f32_16x16x32_bf16 v[120:123], v[24:27], a[152:155], v[120:123]\n"
"ds_read_b64_tr_b16 v[20:21], v6 offset:2048\n"
"v_mfma_f32_16x16x32_bf16 v[124:127], v[24:27], a[156:159], v[124:127]\n"
"ds_read_b64_tr_b16 v[22:23], v6 offset:2560\n"
"v_mfma_f32_16x16x32_bf16 v[128:131], v[28:31], a[152:155], v[128:131]\n"
"ds_read_b64_tr_b16 v[24:25], v6 offset:3072\n"
"v_mfma_f32_16x16x32_bf16 v[132:135], v[28:31], a[156:159], v[132:135]\n"
"ds_read_b64_tr_b16 v[26:27], v6 offset:3584\n"
"s_mov_b32 s22, 28672\n"
"buffer_load_dwordx4 a[224:227], v2, s[12:15], s22 offen sc1\n"
"s_waitcnt vmcnt(27)\n"
"s_waitcnt lgkmcnt(12)\n"
"v_mfma_f32_16x16x32_bf16 v[80:83], v[32:35], a[160:163], v[80:83]\n"
"ds_read_b64_tr_b16 v[28:29], v6 offset:4096\n"
"v_mfma_f32_16x16x32_bf16 v[84:87], v[32:35], a[164:167], v[84:87]\n"
"ds_read_b64_tr_b16 v[30:31], v6 offset:4608\n"
"v_mfma_f32_16x16x32_bf16 v[88:91], v[36:39], a[160:163], v[88:91]\n"
"ds_read_b64_tr_b16 v[32:33], v6 offset:5120\n"
"v_mfma_f32_16x16x32_bf16 v[92:95], v[36:39], a[164:167], v[92:95]\n"
"ds_read_b64_tr_b16 v[34:35], v6 offset:5632\n"
"buffer_load_dwordx4 a[228:231], v3, s[12:15], s22 offen sc1\n"
"s_waitcnt lgkmcnt(12)\n"
"v_mfma_f32_16x16x32_bf16 v[96:99], v[40:43], a[160:163], v[96:99]\n"
"ds_read_b64_tr_b16 v[36:37], v6 offset:6144\n"
"v_mfma_f32_16x16x32_bf16 v[100:103], v[40:43], a[164:167], v[100:103]\n"
"ds_read_b64_tr_b16 v[38:39], v6 offset:6656\n"
"v_mfma_f32_16x16x32_bf16 v[104:107], v[44:47], a[160:163], v[104:107]\n"
"ds_read_b64_tr_b16 v[40:41], v6 offset:7168\n"
"v_mfma_f32_16x16x32_bf16 v[108:111], v[44:47], a[164:167], v[108:111]\n"
"ds_read_b64_tr_b16 v[42:43], v6 offset:7680\n"
"s_add_u32 m0, s20, 81920\n"
"s_mov_b32 s21, 229376\n"
"buffer_load_dwordx4 v1, s[8:11], s21 offen sc1 lds\n"
"s_waitcnt lgkmcnt(12)\n"
"v_mfma_f32_16x16x32_bf16 v[112:115], v[12:15], a[160:163], v[112:115]\n"
"ds_read_b64_tr_b16 v[44:45], v6 offset:8192\n"
"v_mfma_f32_16x16x32_bf16 v[116:119], v[12:15], a[164:167], v[116:119]\n"
"ds_read_b64_tr_b16 v[46:47], v6 offset:8704\n"
"v_mfma_f32_16x16x32_bf16 v[120:123], v[16:19], a[160:163], v[120:123]\n"
"ds_read_b64_tr_b16 v[12:13], v6 offset:9216\n"
"v_mfma_f32_16x16x32_bf16 v[124:127], v[16:19], a[164:167], v[124:127]\n"
"ds_read_b64_tr_b16 v[14:15], v6 offset:9728\n"
"buffer_load_dwordx4 v1, s[8:11], s21 offen offset:1024 sc1 lds\n"
"s_waitcnt lgkmcnt(12)\n"
"v_mfma_f32_16x16x32_bf16 v[128:131], v[20:23], a[160:163], v[128:131]\n"
"ds_read_b64_tr_b16 v[16:17], v6 offset:10240\n"
"v_mfma_f32_16x16x32_bf16 v[132:135], v[20:23], a[164:167], v[132:135]\n"
"ds_read_b64_tr_b16 v[18:19], v6 offset:10752\n"
"s_waitcnt vmcnt(28)\n"
"v_mfma_f32_16x16x32_bf16 v[80:83], v[24:27], a[168:171], v[80:83]\n"
"ds_read_b64_tr_b16 v[20:21], v6 offset:11264\n"
"v_mfma_f32_16x16x32_bf16 v[84:87], v[24:27], a[172:175], v[84:87]\n"
"ds_read_b64_tr_b16 v[22:23], v6 offset:11776\n"
"buffer_load_dwordx4 v1, s[8:11], s21 offen offset:2048 sc1 lds\n"
"s_waitcnt lgkmcnt(12)\n"
"v_mfma_f32_16x16x32_bf16 v[88:91], v[28:31], a[168:171], v[88:91]\n"
"ds_read_b64_tr_b16 v[24:25], v6 offset:12288\n"
"v_mfma_f32_16x16x32_bf16 v[92:95], v[28:31], a[172:175], v[92:95]\n"
"ds_read_b64_tr_b16 v[26:27], v6 offset:12800\n"
"v_mfma_f32_16x16x32_bf16 v[96:99], v[32:35], a[168:171], v[96:99]\n"
"ds_read_b64_tr_b16 v[28:29], v6 offset:13312\n"
"v_mfma_f32_16x16x32_bf16 v[100:103], v[32:35], a[172:175], v[100:103]\n"
"ds_read_b64_tr_b16 v[30:31], v6 offset:13824\n"
"buffer_load_dwordx4 v1, s[8:11], s21 offen offset:3072 sc1 lds\n"
"s_waitcnt lgkmcnt(12)\n"
"v_mfma_f32_16x16x32_bf16 v[104:107], v[36:39], a[168:171], v[104:107]\n"
"ds_read_b64_tr_b16 v[32:33], v6 offset:14336\n"
"v_mfma_f32_16x16x32_bf16 v[108:111], v[36:39], a[172:175], v[108:111]\n"
"ds_read_b64_tr_b16 v[34:35], v6 offset:14848\n"
"v_mfma_f32_16x16x32_bf16 v[112:115], v[40:43], a[168:171], v[112:115]\n"
"ds_read_b64_tr_b16 v[36:37], v6 offset:15360\n"
"v_mfma_f32_16x16x32_bf16 v[116:119], v[40:43], a[172:175], v[116:119]\n"
"ds_read_b64_tr_b16 v[38:39], v6 offset:15872\n"
"buffer_load_dwordx4 a[232:235], v2, s[12:15], s22 offen offset:1024 sc1\n"
"s_barrier\n"
"s_waitcnt lgkmcnt(12)\n"
"v_mfma_f32_16x16x32_bf16 v[120:123], v[44:47], a[168:171], v[120:123]\n"
"ds_read_b64_tr_b16 v[40:41], v6 offset:16384\n"
"v_mfma_f32_16x16x32_bf16 v[124:127], v[44:47], a[172:175], v[124:127]\n"
"ds_read_b64_tr_b16 v[42:43], v6 offset:16896\n"
"v_mfma_f32_16x16x32_bf16 v[128:131], v[12:15], a[168:171], v[128:131]\n"
"ds_read_b64_tr_b16 v[44:45], v6 offset:17408\n"
"v_mfma_f32_16x16x32_bf16 v[132:135], v[12:15], a[172:175], v[132:135]\n"
"ds_read_b64_tr_b16 v[46:47], v6 offset:17920\n"
"buffer_load_dwordx4 a[236:239], v3, s[12:15], s22 offen offset:1024 sc1\n"
"s_waitcnt vmcnt(26)\n"
"s_waitcnt lgkmcnt(12)\n"
"v_mfma_f32_16x16x32_bf16 v[80:83], v[16:19], a[176:179], v[80:83]\n"
"ds_read_b64_tr_b16 v[12:13], v6 offset:18432\n"
"v_mfma_f32_16x16x32_bf16 v[84:87], v[16:19], a[180:183], v[84:87]\n"
"ds_read_b64_tr_b16 v[14:15], v6 offset:18944\n"
"v_mfma_f32_16x16x32_bf16 v[88:91], v[20:23], a[176:179], v[88:91]\n"
"ds_read_b64_tr_b16 v[16:17], v6 offset:19456\n"
"v_mfma_f32_16x16x32_bf16 v[92:95], v[20:23], a[180:183], v[92:95]\n"
"ds_read_b64_tr_b16 v[18:19], v6 offset:19968\n"
"buffer_load_dwordx4 a[240:243], v2, s[12:15], s22 offen offset:2048 sc1\n"
"s_waitcnt lgkmcnt(12)\n"
"v_mfma_f32_16x16x32_bf16 v[96:99], v[24:27], a[176:179], v[96:99]\n"
"ds_read_b64_tr_b16 v[20:21], v6 offset:20480\n"
"v_mfma_f32_16x16x32_bf16 v[100:103], v[24:27], a[180:183], v[100:103]\n"
"ds_read_b64_tr_b16 v[22:23], v6 offset:20992\n"
"v_mfma_f32_16x16x32_bf16 v[104:107], v[28:31], a[176:179], v[104:107]\n"
"ds_read_b64_tr_b16 v[24:25], v6 offset:21504\n"
"v_mfma_f32_16x16x32_bf16 v[108:111], v[28:31], a[180:183], v[108:111]\n"
"ds_read_b64_tr_b16 v[26:27], v6 offset:22016\n"
"buffer_load_dwordx4 a[244:247], v3, s[12:15], s22 offen offset:2048 sc1\n"
"s_waitcnt lgkmcnt(12)\n"
"v_mfma_f32_16x16x32_bf16 v[112:115], v[32:35], a[176:179], v[112:115]\n"
"ds_read_b64_tr_b16 v[28:29], v6 offset:22528\n"
"v_mfma_f32_16x16x32_bf16 v[116:119], v[32:35], a[180:183], v[116:119]\n"
"ds_read_b64_tr_b16 v[30:31], v6 offset:23040\n"
"v_mfma_f32_16x16x32_bf16 v[120:123], v[36:39], a[176:179], v[120:123]\n"
"ds_read_b64_tr_b16 v[32:33], v6 offset:23552\n"
"v_mfma_f32_16x16x32_bf16 v[124:127], v[36:39], a[180:183], v[124:127]\n"
"ds_read_b64_tr_b16 v[34:35], v6 offset:24064\n"
"s_add_u32 m0, s20, 98304\n"
"s_mov_b32 s21, 245760\n"
"buffer_load_dwordx4 v1, s[8:11], s21 offen sc1 lds\n"
"s_waitcnt lgkmcnt(12)\n"
"v_mfma_f32_16x16x32_bf16 v[128:131], v[40:43], a[176:179], v[128:131]\n"
"ds_read_b64_tr_b16 v[36:37], v6 offset:24576\n"
"v_mfma_f32_16x16x32_bf16 v[132:135], v[40:43], a[180:183], v[132:135]\n"
"ds_read_b64_tr_b16 v[38:39], v6 offset:25088\n"
"s_waitcnt vmcnt(27)\n"
"v_mfma_f32_16x16x32_bf16 v[80:83], v[44:47], a[184:187], v[80:83]\n"
"ds_read_b64_tr_b16 v[40:41], v6 offset:25600\n"
"v_mfma_f32_16x16x32_bf16 v[84:87], v[44:47], a[188:191], v[84:87]\n"
"ds_read_b64_tr_b16 v[42:43], v6 offset:26112\n"
"buffer_load_dwordx4 v1, s[8:11], s21 offen offset:1024 sc1 lds\n"
"s_waitcnt lgkmcnt(12)\n"
"v_mfma_f32_16x16x32_bf16 v[88:91], v[12:15], a[184:187], v[88:91]\n"
"ds_read_b64_tr_b16 v[44:45], v6 offset:26624\n"
"v_mfma_f32_16x16x32_bf16 v[92:95], v[12:15], a[188:191], v[92:95]\n"
"ds_read_b64_tr_b16 v[46:47], v6 offset:27136\n"
"v_mfma_f32_16x16x32_bf16 v[96:99], v[16:19], a[184:187], v[96:99]\n"
"ds_read_b64_tr_b16 v[12:13], v6 offset:27648\n"
"v_mfma_f32_16x16x32_bf16 v[100:103], v[16:19], a[188:191], v[100:103]\n"
"ds_read_b64_tr_b16 v[14:15], v6 offset:28160\n"
"buffer_load_dwordx4 v1, s[8:11], s21 offen offset:2048 sc1 lds\n"
"s_waitcnt lgkmcnt(12)\n"
"v_mfma_f32_16x16x32_bf16 v[104:107], v[20:23], a[184:187], v[104:107]\n"
"ds_read_b64_tr_b16 v[16:17], v6 offset:28672\n"
"v_mfma_f32_16x16x32_bf16 v[108:111], v[20:23], a[188:191], v[108:111]\n"
"ds_read_b64_tr_b16 v[18:19], v6 offset:29184\n"
"v_mfma_f32_16x16x32_bf16 v[112:115], v[24:27], a[184:187], v[112:115]\n"
"ds_read_b64_tr_b16 v[20:21], v6 offset:29696\n"
"v_mfma_f32_16x16x32_bf16 v[116:119], v[24:27], a[188:191], v[116:119]\n"
"ds_read_b64_tr_b16 v[22:23], v6 offset:30208\n"
"buffer_load_dwordx4 v1, s[8:11], s21 offen offset:3072 sc1 lds\n"
"s_waitcnt lgkmcnt(12)\n"
"v_mfma_f32_16x16x32_bf16 v[120:123], v[28:31], a[184:187], v[120:123]\n"
"ds_read_b64_tr_b16 v[24:25], v6 offset:30720\n"
"v_mfma_f32_16x16x32_bf16 v[124:127], v[28:31], a[188:191], v[124:127]\n"
"ds_read_b64_tr_b16 v[26:27], v6 offset:31232\n"
"v_mfma_f32_16x16x32_bf16 v[128:131], v[32:35], a[184:187], v[128:131]\n"
"ds_read_b64_tr_b16 v[28:29], v6 offset:31744\n"
"v_mfma_f32_16x16x32_bf16 v[132:135], v[32:35], a[188:191], v[132:135]\n"
"ds_read_b64_tr_b16 v[30:31], v6 offset:32256\n"
"buffer_load_dwordx4 a[248:251], v2, s[12:15], s22 offen offset:3072 sc1\n"
"s_barrier\n"
"s_waitcnt vmcnt(25)\n"
"s_waitcnt lgkmcnt(12)\n"
"v_mfma_f32_16x16x32_bf16 v[80:83], v[36:39], a[192:195], v[80:83]\n"
"ds_read_b64_tr_b16 v[32:33], v6 offset:32768\n"
"v_mfma_f32_16x16x32_bf16 v[84:87], v[36:39], a[196:199], v[84:87]\n"
"ds_read_b64_tr_b16 v[34:35], v6 offset:33280\n"
"v_mfma_f32_16x16x32_bf16 v[88:91], v[40:43], a[192:195], v[88:91]\n"
"ds_read_b64_tr_b16 v[36:37], v6 offset:33792\n"
"v_mfma_f32_16x16x32_bf16 v[92:95], v[40:43], a[196:199], v[92:95]\n"
"ds_read_b64_tr_b16 v[38:39], v6 offset:34304\n"
"buffer_load_dwordx4 a[252:255], v3, s[12:15], s22 offen offset:3072 sc1\n"
"s_waitcnt lgkmcnt(12)\n"
"v_mfma_f32_16x16x32_bf16 v[96:99], v[44:47], a[192:195], v[96:99]\n"
"ds_read_b64_tr_b16 v[40:41], v6 offset:34816\n"
"v_mfma_f32_16x16x32_bf16 v[100:103], v[44:47], a[196:199], v[100:103]\n"
"ds_read_b64_tr_b16 v[42:43], v6 offset:35328\n"
"v_mfma_f32_16x16x32_bf16 v[104:107], v[12:15], a[192:195], v[104:107]\n"
"ds_read_b64_tr_b16 v[44:45], v6 offset:35840\n"
"v_mfma_f32_16x16x32_bf16 v[108:111], v[12:15], a[196:199], v[108:111]\n"
"ds_read_b64_tr_b16 v[46:47], v6 offset:36352\n"
"s_add_u32 m0, s20, 114688\n"
"s_mov_b32 s21, 262144\n"
"buffer_load_dwordx4 v1, s[8:11], s21 offen sc1 lds\n"
"s_waitcnt lgkmcnt(12)\n"
"v_mfma_f32_16x16x32_bf16 v[112:115], v[16:19], a[192:195], v[112:115]\n"
"ds_read_b64_tr_b16 v[12:13], v6 offset:36864\n"
"v_mfma_f32_16x16x32_bf16 v[116:119], v[16:19], a[196:199], v[116:119]\n"
"ds_read_b64_tr_b16 v[14:15], v6 offset:37376\n"
"v_mfma_f32_16x16x32_bf16 v[120:123], v[20:23], a[192:195], v[120:123]\n"
"ds_read_b64_tr_b16 v[16:17], v6 offset:37888\n"
"v_mfma_f32_16x16x32_bf16 v[124:127], v[20:23], a[196:199], v[124:127]\n"
"ds_read_b64_tr_b16 v[18:19], v6 offset:38400\n"
"buffer_load_dwordx4 v1, s[8:11], s21 offen offset:1024 sc1 lds\n"
"s_waitcnt lgkmcnt(12)\n"
"v_mfma_f32_16x16x32_bf16 v[128:131], v[24:27], a[192:195], v[128:131]\n"
"ds_read_b64_tr_b16 v[20:21], v6 offset:38912\n"
"v_mfma_f32_16x16x32_bf16 v[132:135], v[24:27], a[196:199], v[132:135]\n"
"ds_read_b64_tr_b16 v[22:23], v6 offset:39424\n"
"s_waitcnt vmcnt(26)\n"
"v_mfma_f32_16x16x32_bf16 v[80:83], v[28:31], a[200:203], v[80:83]\n"
"ds_read_b64_tr_b16 v[24:25], v6 offset:39936\n"
"v_mfma_f32_16x16x32_bf16 v[84:87], v[28:31], a[204:207], v[84:87]\n"
"ds_read_b64_tr_b16 v[26:27], v6 offset:40448\n"
"buffer_load_dwordx4 v1, s[8:11], s21 offen offset:2048 sc1 lds\n"
"s_waitcnt lgkmcnt(12)\n"
"v_mfma_f32_16x16x32_bf16 v[88:91], v[32:35], a[200:203], v[88:91]\n"
"ds_read_b64_tr_b16 v[28:29], v6 offset:40960\n"
"v_mfma_f32_16x16x32_bf16 v[92:95], v[32:35], a[204:207], v[92:95]\n"
"ds_read_b64_tr_b16 v[30:31], v6 offset:41472\n"
"v_mfma_f32_16x16x32_bf16 v[96:99], v[36:39], a[200:203], v[96:99]\n"
"ds_read_b64_tr_b16 v[32:33], v6 offset:41984\n"
"v_mfma_f32_16x16x32_bf16 v[100:103], v[36:39], a[204:207], v[100:103]\n"
"ds_read_b64_tr_b16 v[34:35], v6 offset:42496\n"
"buffer_load_dwordx4 v1, s[8:11], s21 offen offset:3072 sc1 lds\n"
"s_waitcnt lgkmcnt(12)\n"
"v_mfma_f32_16x16x32_bf16 v[104:107], v[40:43], a[200:203], v[104:107]\n"
"ds_read_b64_tr_b16 v[36:37], v6 offset:43008\n"
"v_mfma_f32_16x16x32_bf16 v[108:111], v[40:43], a[204:207], v[108:111]\n"
"ds_read_b64_tr_b16 v[38:39], v6 offset:43520\n"
"v_mfma_f32_16x16x32_bf16 v[112:115], v[44:47], a[200:203], v[112:115]\n"
"ds_read_b64_tr_b16 v[40:41], v6 offset:44032\n"
"v_mfma_f32_16x16x32_bf16 v[116:119], v[44:47], a[204:207], v[116:119]\n"
"ds_read_b64_tr_b16 v[42:43], v6 offset:44544\n"
"s_add_u32 m0, s20, 131072\n"
"s_mov_b32 s21, 278528\n"
"buffer_load_dwordx4 v1, s[8:11], s21 offen sc1 lds\n"
"s_waitcnt lgkmcnt(12)\n"
"v_mfma_f32_16x16x32_bf16 v[120:123], v[12:15], a[200:203], v[120:123]\n"
"ds_read_b64_tr_b16 v[44:45], v6 offset:45056\n"
"v_mfma_f32_16x16x32_bf16 v[124:127], v[12:15], a[204:207], v[124:127]\n"
"ds_read_b64_tr_b16 v[46:47], v6 offset:45568\n"
"v_mfma_f32_16x16x32_bf16 v[128:131], v[16:19], a[200:203], v[128:131]\n"
"ds_read_b64_tr_b16 v[12:13], v6 offset:46080\n"
"v_mfma_f32_16x16x32_bf16 v[132:135], v[16:19], a[204:207], v[132:135]\n"
"ds_read_b64_tr_b16 v[14:15], v6 offset:46592\n"
"buffer_load_dwordx4 v1, s[8:11], s21 offen offset:1024 sc1 lds\n"
"s_waitcnt vmcnt(24)\n"
"s_waitcnt lgkmcnt(12)\n"
"v_mfma_f32_16x16x32_bf16 v[80:83], v[20:23], a[208:211], v[80:83]\n"
"ds_read_b64_tr_b16 v[16:17], v6 offset:47104\n"
"v_mfma_f32_16x16x32_bf16 v[84:87], v[20:23], a[212:215], v[84:87]\n"
"ds_read_b64_tr_b16 v[18:19], v6 offset:47616\n"
"v_mfma_f32_16x16x32_bf16 v[88:91], v[24:27], a[208:211], v[88:91]\n"
"ds_read_b64_tr_b16 v[20:21], v6 offset:48128\n"
"v_mfma_f32_16x16x32_bf16 v[92:95], v[24:27], a[212:215], v[92:95]\n"
"ds_read_b64_tr_b16 v[22:23], v6 offset:48640\n"
"buffer_load_dwordx4 v1, s[8:11], s21 offen offset:2048 sc1 lds\n"
"s_barrier\n"
"s_waitcnt lgkmcnt(12)\n"
"v_mfma_f32_16x16x32_bf16 v[96:99], v[28:31], a[208:211], v[96:99]\n"
"ds_read_b64_tr_b16 v[24:25], v6 offset:49152\n"
"v_mfma_f32_16x16x32_bf16 v[100:103], v[28:31], a[212:215], v[100:103]\n"
"ds_read_b64_tr_b16 v[26:27], v6 offset:49664\n"
"v_mfma_f32_16x16x32_bf16 v[104:107], v[32:35], a[208:211], v[104:107]\n"
"ds_read_b64_tr_b16 v[28:29], v6 offset:50176\n"
"v_mfma_f32_16x16x32_bf16 v[108:111], v[32:35], a[212:215], v[108:111]\n"
"ds_read_b64_tr_b16 v[30:31], v6 offset:50688\n"
"buffer_load_dwordx4 v1, s[8:11], s21 offen offset:3072 sc1 lds\n"
"s_waitcnt lgkmcnt(12)\n"
"v_mfma_f32_16x16x32_bf16 v[112:115], v[36:39], a[208:211], v[112:115]\n"
"ds_read_b64_tr_b16 v[32:33], v6 offset:51200\n"
"v_mfma_f32_16x16x32_bf16 v[116:119], v[36:39], a[212:215], v[116:119]\n"
"ds_read_b64_tr_b16 v[34:35], v6 offset:51712\n"
"v_mfma_f32_16x16x32_bf16 v[120:123], v[40:43], a[208:211], v[120:123]\n"
"ds_read_b64_tr_b16 v[36:37], v6 offset:52224\n"
"v_mfma_f32_16x16x32_bf16 v[124:127], v[40:43], a[212:215], v[124:127]\n"
"ds_read_b64_tr_b16 v[38:39], v6 offset:52736\n"
"s_add_u32 m0, s20, 0\n"
"s_mov_b32 s21, 0\n"
"buffer_load_dwordx4 v1, s[24:27], s21 offen sc1 lds\n"
"s_waitcnt lgkmcnt(12)\n"
"v_mfma_f32_16x16x32_bf16 v[128:131], v[44:47], a[208:211], v[128:131]\n"
"ds_read_b64_tr_b16 v[40:41], v6 offset:53248\n"
"v_mfma_f32_16x16x32_bf16 v[132:135], v[44:47], a[212:215], v[132:135]\n"
"ds_read_b64_tr_b16 v[42:43], v6 offset:53760\n"
"s_waitcnt vmcnt(25)\n"
"v_mfma_f32_16x16x32_bf16 v[80:83], v[12:15], a[216:219], v[80:83]\n"
"ds_read_b64_tr_b16 v[44:45], v6 offset:54272\n"
"v_mfma_f32_16x16x32_bf16 v[84:87], v[12:15], a[220:223], v[84:87]\n"
"ds_read_b64_tr_b16 v[46:47], v6 offset:54784\n"
"buffer_load_dwordx4 v1, s[24:27], s21 offen offset:1024 sc1 lds\n"
"s_waitcnt lgkmcnt(12)\n"
"v_mfma_f32_16x16x32_bf16 v[88:91], v[16:19], a[216:219], v[88:91]\n"
"ds_read_b64_tr_b16 v[12:13], v6 offset:55296\n"
"v_mfma_f32_16x16x32_bf16 v[92:95], v[16:19], a[220:223], v[92:95]\n"
"ds_read_b64_tr_b16 v[14:15], v6 offset:55808\n"
"v_mfma_f32_16x16x32_bf16 v[96:99], v[20:23], a[216:219], v[96:99]\n"
"ds_read_b64_tr_b16 v[16:17], v6 offset:56320\n"
"v_mfma_f32_16x16x32_bf16 v[100:103], v[20:23], a[220:223], v[100:103]\n"
"ds_read_b64_tr_b16 v[18:19], v6 offset:56832\n"
"buffer_load_dwordx4 v1, s[24:27], s21 offen offset:2048 sc1 lds\n"
"s_waitcnt lgkmcnt(12)\n"
"v_mfma_f32_16x16x32_bf16 v[104:107], v[24:27], a[216:219], v[104:107]\n"
"ds_read_b64_tr_b16 v[20:21], v6 offset:57344\n"
"v_mfma_f32_16x16x32_bf16 v[108:111], v[24:27], a[220:223], v[108:111]\n"
"ds_read_b64_tr_b16 v[22:23], v6 offset:57856\n"
"v_mfma_f32_16x16x32_bf16 v[112:115], v[28:31], a[216:219], v[112:115]\n"
"ds_read_b64_tr_b16 v[24:25], v6 offset:58368\n"
"v_mfma_f32_16x16x32_bf16 v[116:119], v[28:31], a[220:223], v[116:119]\n"
"ds_read_b64_tr_b16 v[26:27], v6 offset:58880\n"
"buffer_load_dwordx4 v1, s[24:27], s21 offen offset:3072 sc1 lds\n"
"s_waitcnt lgkmcnt(12)\n"
"v_mfma_f32_16x16x32_bf16 v[120:123], v[32:35], a[216:219], v[120:123]\n"
"ds_read_b64_tr_b16 v[28:29], v6 offset:59392\n"
"v_mfma_f32_16x16x32_bf16 v[124:127], v[32:35], a[220:223], v[124:127]\n"
"ds_read_b64_tr_b16 v[30:31], v6 offset:59904\n"
"v_mfma_f32_16x16x32_bf16 v[128:131], v[36:39], a[216:219], v[128:131]\n"
"ds_read_b64_tr_b16 v[32:33], v6 offset:60416\n"
"v_mfma_f32_16x16x32_bf16 v[132:135], v[36:39], a[220:223], v[132:135]\n"
"ds_read_b64_tr_b16 v[34:35], v6 offset:60928\n"
"s_add_u32 m0, s20, 16384\n"
"s_mov_b32 s21, 16384\n"
"buffer_load_dwordx4 v1, s[24:27], s21 offen sc1 lds\n"
"s_waitcnt vmcnt(27)\n"
"s_waitcnt lgkmcnt(12)\n"
"v_mfma_f32_16x16x32_bf16 v[80:83], v[40:43], a[224:227], v[80:83]\n"
"ds_read_b64_tr_b16 v[36:37], v6 offset:61440\n"
"v_mfma_f32_16x16x32_bf16 v[84:87], v[40:43], a[228:231], v[84:87]\n"
"ds_read_b64_tr_b16 v[38:39], v6 offset:61952\n"
"v_mfma_f32_16x16x32_bf16 v[88:91], v[44:47], a[224:227], v[88:91]\n"
"ds_read_b64_tr_b16 v[40:41], v6 offset:62464\n"
"v_mfma_f32_16x16x32_bf16 v[92:95], v[44:47], a[228:231], v[92:95]\n"
"ds_read_b64_tr_b16 v[42:43], v6 offset:62976\n"
"buffer_load_dwordx4 v1, s[24:27], s21 offen offset:1024 sc1 lds\n"
"s_waitcnt lgkmcnt(12)\n"
"v_mfma_f32_16x16x32_bf16 v[96:99], v[12:15], a[224:227], v[96:99]\n"
"ds_read_b64_tr_b16 v[44:45], v6 offset:63488\n"
"v_mfma_f32_16x16x32_bf16 v[100:103], v[12:15], a[228:231], v[100:103]\n"
"ds_read_b64_tr_b16 v[46:47], v6 offset:64000\n"
"v_mfma_f32_16x16x32_bf16 v[104:107], v[16:19], a[224:227], v[104:107]\n"
"ds_read_b64_tr_b16 v[12:13], v6 offset:64512\n"
"v_mfma_f32_16x16x32_bf16 v[108:111], v[16:19], a[228:231], v[108:111]\n"
"ds_read_b64_tr_b16 v[14:15], v6 offset:65024\n"
"buffer_load_dwordx4 v1, s[24:27], s21 offen offset:2048 sc1 lds\n"
"s_barrier\n"
"s_waitcnt lgkmcnt(12)\n"
"v_mfma_f32_16x16x32_bf16 v[112:115], v[20:23], a[224:227], v[112:115]\n"
"ds_read_b64_tr_b16 v[16:17], v7 offset:0\n"
"v_mfma_f32_16x16x32_bf16 v[116:119], v[20:23], a[228:231], v[116:119]\n"
"ds_read_b64_tr_b16 v[18:19], v7 offset:512\n"
"v_mfma_f32_16x16x32_bf16 v[120:123], v[24:27], a[224:227], v[120:123]\n"
"ds_read_b64_tr_b16 v[20:21], v7 offset:1024\n"
"v_mfma_f32_16x16x32_bf16 v[124:127], v[24:27], a[228:231], v[124:127]\n"
"ds_read_b64_tr_b16 v[22:23], v7 offset:1536\n"
"buffer_load_dwordx4 v1, s[24:27], s21 offen offset:3072 sc1 lds\n"
"s_waitcnt lgkmcnt(12)\n"
"v_mfma_f32_16x16x32_bf16 v[128:131], v[28:31], a[224:227], v[128:131]\n"
"ds_read_b64_tr_b16 v[24:25], v7 offset:2048\n"
"v_mfma_f32_16x16x32_bf16 v[132:135], v[28:31], a[228:231], v[132:135]\n"
"ds_read_b64_tr_b16 v[26:27], v7 offset:2560\n"
"s_waitcnt vmcnt(24)\n"
"v_mfma_f32_16x16x32_bf16 v[80:83], v[32:35], a[232:235], v[80:83]\n"
"ds_read_b64_tr_b16 v[28:29], v7 offset:3072\n"
"v_mfma_f32_16x16x32_bf16 v[84:87], v[32:35], a[236:239], v[84:87]\n"
"ds_read_b64_tr_b16 v[30:31], v7 offset:3584\n"
"s_add_u32 m0, s20, 32768\n"
"s_mov_b32 s21, 32768\n"
"buffer_load_dwordx4 v1, s[24:27], s21 offen sc1 lds\n"
"s_waitcnt lgkmcnt(12)\n"
"v_mfma_f32_16x16x32_bf16 v[88:91], v[36:39], a[232:235], v[88:91]\n"
"ds_read_b64_tr_b16 v[32:33], v7 offset:4096\n"
"v_mfma_f32_16x16x32_bf16 v[92:95], v[36:39], a[236:239], v[92:95]\n"
"ds_read_b64_tr_b16 v[34:35], v7 offset:4608\n"
"v_mfma_f32_16x16x32_bf16 v[96:99], v[40:43], a[232:235], v[96:99]\n"
"ds_read_b64_tr_b16 v[36:37], v7 offset:5120\n"
"v_mfma_f32_16x16x32_bf16 v[100:103], v[40:43], a[236:239], v[100:103]\n"
"ds_read_b64_tr_b16 v[38:39], v7 offset:5632\n"
"buffer_load_dwordx4 v1, s[24:27], s21 offen offset:1024 sc1 lds\n"
"s_waitcnt lgkmcnt(12)\n"
"v_mfma_f32_16x16x32_bf16 v[104:107], v[44:47], a[232:235], v[104:107]\n"
"ds_read_b64_tr_b16 v[40:41], v7 offset:6144\n"
"v_mfma_f32_16x16x32_bf16 v[108:111], v[44:47], a[236:239], v[108:111]\n"
"ds_read_b64_tr_b16 v[42:43], v7 offset:6656\n"
"v_mfma_f32_16x16x32_bf16 v[112:115], v[12:15], a[232:235], v[112:115]\n"
"ds_read_b64_tr_b16 v[44:45], v7 offset:7168\n"
"v_mfma_f32_16x16x32_bf16 v[116:119], v[12:15], a[236:239], v[116:119]\n"
"ds_read_b64_tr_b16 v[46:47], v7 offset:7680\n"
"buffer_load_dwordx4 v1, s[24:27], s21 offen offset:2048 sc1 lds\n"
"s_waitcnt lgkmcnt(12)\n"
"v_mfma_f32_16x16x32_bf16 v[120:123], v[16:19], a[232:235], v[120:123]\n"
"ds_read_b64_tr_b16 v[12:13], v7 offset:8192\n"
"v_mfma_f32_16x16x32_bf16 v[124:127], v[16:19], a[236:239], v[124:127]\n"
"ds_read_b64_tr_b16 v[14:15], v7 offset:8704\n"
"v_mfma_f32_16x16x32_bf16 v[128:131], v[20:23], a[232:235], v[128:131]\n"
"ds_read_b64_tr_b16 v[16:17], v7 offset:9216\n"
"v_mfma_f32_16x16x32_bf16 v[132:135], v[20:23], a[236:239], v[132:135]\n"
"ds_read_b64_tr_b16 v[18:19], v7 offset:9728\n"
"buffer_load_dwordx4 v1, s[24:27], s21 offen offset:3072 sc1 lds\n"
"s_waitcnt vmcnt(26)\n"
"s_waitcnt lgkmcnt(12)\n"
"v_mfma_f32_16x16x32_bf16 v[80:83], v[24:27], a[240:243], v[80:83]\n"
"ds_read_b64_tr_b16 v[20:21], v7 offset:10240\n"
"v_mfma_f32_16x16x32_bf16 v[84:87], v[24:27], a[244:247], v[84:87]\n"
"ds_read_b64_tr_b16 v[22:23], v7 offset:10752\n"
"v_mfma_f32_16x16x32_bf16 v[88:91], v[28:31], a[240:243], v[88:91]\n"
"ds_read_b64_tr_b16 v[24:25], v7 offset:11264\n"
"v_mfma_f32_16x16x32_bf16 v[92:95], v[28:31], a[244:247], v[92:95]\n"
"ds_read_b64_tr_b16 v[26:27], v7 offset:11776\n"
"s_waitcnt lgkmcnt(12)\n"
"v_mfma_f32_16x16x32_bf16 v[96:99], v[32:35], a[240:243], v[96:99]\n"
"ds_read_b64_tr_b16 v[28:29], v7 offset:12288\n"
"v_mfma_f32_16x16x32_bf16 v[100:103], v[32:35], a[244:247], v[100:103]\n"
"ds_read_b64_tr_b16 v[30:31], v7 offset:12800\n"
"v_mfma_f32_16x16x32_bf16 v[104:107], v[36:39], a[240:243], v[104:107]\n"
"ds_read_b64_tr_b16 v[32:33], v7 offset:13312\n"
"v_mfma_f32_16x16x32_bf16 v[108:111], v[36:39], a[244:247], v[108:111]\n"
"ds_read_b64_tr_b16 v[34:35], v7 offset:13824\n"
"s_waitcnt lgkmcnt(12)\n"
"v_mfma_f32_16x16x32_bf16 v[112:115], v[40:43], a[240:243], v[112:115]\n"
"ds_read_b64_tr_b16 v[36:37], v7 offset:14336\n"
"v_mfma_f32_16x16x32_bf16 v[116:119], v[40:43], a[244:247], v[116:119]\n"
"ds_read_b64_tr_b16 v[38:39], v7 offset:14848\n"
"v_mfma_f32_16x16x32_bf16 v[120:123], v[44:47], a[240:243], v[120:123]\n"
"ds_read_b64_tr_b16 v[40:41], v7 offset:15360\n"
"v_mfma_f32_16x16x32_bf16 v[124:127], v[44:47], a[244:247], v[124:127]\n"
"ds_read_b64_tr_b16 v[42:43], v7 offset:15872\n"
"s_barrier\n"
"s_waitcnt lgkmcnt(12)\n"
"v_mfma_f32_16x16x32_bf16 v[128:131], v[12:15], a[240:243], v[128:131]\n"
"ds_read_b64_tr_b16 v[44:45], v7 offset:16384\n"
"v_mfma_f32_16x16x32_bf16 v[132:135], v[12:15], a[244:247], v[132:135]\n"
"ds_read_b64_tr_b16 v[46:47], v7 offset:16896\n"
"s_waitcnt vmcnt(20)\n"
"v_mfma_f32_16x16x32_bf16 v[80:83], v[16:19], a[248:251], v[80:83]\n"
"ds_read_b64_tr_b16 v[12:13], v7 offset:17408\n"
"v_mfma_f32_16x16x32_bf16 v[84:87], v[16:19], a[252:255], v[84:87]\n"
"ds_read_b64_tr_b16 v[14:15], v7 offset:17920\n"
"s_add_u32 m0, s20, 49152\n"
"s_mov_b32 s21, 49152\n"
"buffer_load_dwordx4 v1, s[24:27], s21 offen sc1 lds\n"
"s_waitcnt lgkmcnt(12)\n"
"v_mfma_f32_16x16x32_bf16 v[88:91], v[20:23], a[248:251], v[88:91]\n"
"ds_read_b64_tr_b16 v[16:17], v7 offset:18432\n"
"v_mfma_f32_16x16x32_bf16 v[92:95], v[20:23], a[252:255], v[92:95]\n"
"ds_read_b64_tr_b16 v[18:19], v7 offset:18944\n"
"v_mfma_f32_16x16x32_bf16 v[96:99], v[24:27], a[248:251], v[96:99]\n"
"ds_read_b64_tr_b16 v[20:21], v7 offset:19456\n"
"v_mfma_f32_16x16x32_bf16 v[100:103], v[24:27], a[252:255], v[100:103]\n"
"ds_read_b64_tr_b16 v[22:23], v7 offset:19968\n"
"buffer_load_dwordx4 v1, s[24:27], s21 offen offset:1024 sc1 lds\n"
"s_waitcnt lgkmcnt(12)\n"
"v_mfma_f32_16x16x32_bf16 v[104:107], v[28:31], a[248:251], v[104:107]\n"
"ds_read_b64_tr_b16 v[24:25], v7 offset:20480\n"
"v_mfma_f32_16x16x32_bf16 v[108:111], v[28:31], a[252:255], v[108:111]\n"
"ds_read_b64_tr_b16 v[26:27], v7 offset:20992\n"
"v_cvt_pk_bf16_f32 v80, v80, v81\n"
"v_mfma_f32_16x16x32_bf16 v[112:115], v[32:35], a[248:251], v[112:115]\n"
"ds_read_b64_tr_b16 v[28:29], v7 offset:21504\n"
"v_mfma_f32_16x16x32_bf16 v[116:119], v[32:35], a[252:255], v[116:119]\n"
"ds_read_b64_tr_b16 v[30:31], v7 offset:22016\n"
"buffer_load_dwordx4 v1, s[24:27], s21 offen offset:2048 sc1 lds\n"
"s_waitcnt lgkmcnt(12)\n"
"v_mfma_f32_16x16x32_bf16 v[120:123], v[36:39], a[248:251], v[120:123]\n"
"ds_read_b64_tr_b16 v[32:33], v7 offset:22528\n"
"v_mfma_f32_16x16x32_bf16 v[124:127], v[36:39], a[252:255], v[124:127]\n"
"ds_read_b64_tr_b16 v[34:35], v7 offset:23040\n"
"v_cvt_pk_bf16_f32 v81, v82, v83\n"
"v_mfma_f32_16x16x32_bf16 v[128:131], v[40:43], a[248:251], v[128:131]\n"
"ds_read_b64_tr_b16 v[36:37], v7 offset:23552\n"
"v_mfma_f32_16x16x32_bf16 v[132:135], v[40:43], a[252:255], v[132:135]\n"
"ds_read_b64_tr_b16 v[38:39], v7 offset:24064\n"
"buffer_load_dwordx4 v1, s[24:27], s21 offen offset:3072 sc1 lds\n"
"s_waitcnt lgkmcnt(12)\n"
"v_mfma_f32_16x16x32_bf16 v[136:139], v[44:47], a[0:3], 0\n"
"ds_read_b64_tr_b16 v[40:41], v7 offset:24576\n"
"v_mfma_f32_16x16x32_bf16 v[140:143], v[44:47], a[4:7], 0\n"
"ds_read_b64_tr_b16 v[42:43], v7 offset:25088\n"
"v_cvt_pk_bf16_f32 v82, v84, v85\n"
"v_mfma_f32_16x16x32_bf16 v[136:139], v[12:15], a[8:11], v[136:139]\n"
"ds_read_b64_tr_b16 v[44:45], v7 offset:25600\n"
"v_mfma_f32_16x16x32_bf16 v[140:143], v[12:15], a[12:15], v[140:143]\n"
"ds_read_b64_tr_b16 v[46:47], v7 offset:26112\n"
"v_cvt_pk_bf16_f32 v83, v86, v87\n"
"s_waitcnt lgkmcnt(12)\n"
"v_mfma_f32_16x16x32_bf16 v[136:139], v[16:19], a[16:19], v[136:139]\n"
"ds_read_b64_tr_b16 v[12:13], v7 offset:26624\n"
"v_mfma_f32_16x16x32_bf16 v[140:143], v[16:19], a[20:23], v[140:143]\n"
"ds_read_b64_tr_b16 v[14:15], v7 offset:27136\n"
"buffer_store_dwordx4 v[80:83], v9, s[16:19], 0 offen sc1\n"
"v_mfma_f32_16x16x32_bf16 v[136:139], v[20:23], a[24:27], v[136:139]\n"
"ds_read_b64_tr_b16 v[16:17], v7 offset:27648\n"
"v_mfma_f32_16x16x32_bf16 v[140:143], v[20:23], a[28:31], v[140:143]\n"
"ds_read_b64_tr_b16 v[18:19], v7 offset:28160\n"
"v_cvt_pk_bf16_f32 v88, v88, v89\n"
"s_waitcnt lgkmcnt(12)\n"
"v_mfma_f32_16x16x32_bf16 v[136:139], v[24:27], a[32:35], v[136:139]\n"
"ds_read_b64_tr_b16 v[20:21], v7 offset:28672\n"
"v_mfma_f32_16x16x32_bf16 v[140:143], v[24:27], a[36:39], v[140:143]\n"
"ds_read_b64_tr_b16 v[22:23], v7 offset:29184\n"
"v_cvt_pk_bf16_f32 v89, v90, v91\n"
"v_mfma_f32_16x16x32_bf16 v[136:139], v[28:31], a[40:43], v[136:139]\n"
"ds_read_b64_tr_b16 v[24:25], v7 offset:29696\n"
"v_mfma_f32_16x16x32_bf16 v[140:143], v[28:31], a[44:47], v[140:143]\n"
"ds_read_b64_tr_b16 v[26:27], v7 offset:30208\n"
"v_cvt_pk_bf16_f32 v90, v92, v93\n"
"s_waitcnt lgkmcnt(12)\n"
"v_mfma_f32_16x16x32_bf16 v[136:139], v[32:35], a[48:51], v[136:139]\n"
"ds_read_b64_tr_b16 v[28:29], v7 offset:30720\n"
"v_mfma_f32_16x16x32_bf16 v[140:143], v[32:35], a[52:55], v[140:143]\n"
"ds_read_b64_tr_b16 v[30:31], v7 offset:31232\n"
"v_cvt_pk_bf16_f32 v91, v94, v95\n"
"v_mfma_f32_16x16x32_bf16 v[136:139], v[36:39], a[56:59], v[136:139]\n"
"ds_read_b64_tr_b16 v[32:33], v7 offset:31744\n"
"v_mfma_f32_16x16x32_bf16 v[140:143], v[36:39], a[60:63], v[140:143]\n"
"ds_read_b64_tr_b16 v[34:35], v7 offset:32256\n"
"s_barrier\n"
"s_waitcnt lgkmcnt(12)\n"
"v_mfma_f32_16x16x32_bf16 v[136:139], v[40:43], a[64:67], v[136:139]\n"
"ds_read_b64_tr_b16 v[36:37], v7 offset:32768\n"
"v_mfma_f32_16x16x32_bf16 v[140:143], v[40:43], a[68:71], v[140:143]\n"
"ds_read_b64_tr_b16 v[38:39], v7 offset:33280\n"
"v_cvt_pk_bf16_f32 v96, v96, v97\n"
"v_mfma_f32_16x16x32_bf16 v[136:139], v[44:47], a[72:75], v[136:139]\n"
"ds_read_b64_tr_b16 v[40:41], v7 offset:33792\n"
"v_mfma_f32_16x16x32_bf16 v[140:143], v[44:47], a[76:79], v[140:143]\n"
"ds_read_b64_tr_b16 v[42:43], v7 offset:34304\n"
"s_add_u32 m0, s20, 65536\n"
"s_mov_b32 s21, 65536\n"
"buffer_load_dwordx4 v1, s[24:27], s21 offen sc1 lds\n"
"s_waitcnt lgkmcnt(12)\n"
"v_mfma_f32_16x16x32_bf16 v[136:139], v[12:15], a[80:83], v[136:139]\n"
"ds_read_b64_tr_b16 v[44:45], v7 offset:34816\n"
"v_mfma_f32_16x16x32_bf16 v[140:143], v[12:15], a[84:87], v[140:143]\n"
"ds_read_b64_tr_b16 v[46:47], v7 offset:35328\n"
"v_cvt_pk_bf16_f32 v97, v98, v99\n"
"buffer_store_dwordx4 v[88:91], v9, s[16:19], 0 offen offset:256 sc1\n"
"v_mfma_f32_16x16x32_bf16 v[136:139], v[16:19], a[88:91], v[136:139]\n"
"ds_read_b64_tr_b16 v[12:13], v7 offset:35840\n"
"v_mfma_f32_16x16x32_bf16 v[140:143], v[16:19], a[92:95], v[140:143]\n"
"ds_read_b64_tr_b16 v[14:15], v7 offset:36352\n"
"buffer_load_dwordx4 v1, s[24:27], s21 offen offset:1024 sc1 lds\n"
"s_waitcnt lgkmcnt(12)\n"
"v_mfma_f32_16x16x32_bf16 v[136:139], v[20:23], a[96:99], v[136:139]\n"
"ds_read_b64_tr_b16 v[16:17], v7 offset:36864\n"
"v_mfma_f32_16x16x32_bf16 v[140:143], v[20:23], a[100:103], v[140:143]\n"
"ds_read_b64_tr_b16 v[18:19], v7 offset:37376\n"
"v_cvt_pk_bf16_f32 v98, v100, v101\n"
"v_mfma_f32_16x16x32_bf16 v[136:139], v[24:27], a[104:107], v[136:139]\n"
"ds_read_b64_tr_b16 v[20:21], v7 offset:37888\n"
"v_mfma_f32_16x16x32_bf16 v[140:143], v[24:27], a[108:111], v[140:143]\n"
"ds_read_b64_tr_b16 v[22:23], v7 offset:38400\n"
"buffer_load_dwordx4 v1, s[24:27], s21 offen offset:2048 sc1 lds\n"
"s_waitcnt lgkmcnt(12)\n"
"v_mfma_f32_16x16x32_bf16 v[136:139], v[28:31], a[112:115], v[136:139]\n"
"ds_read_b64_tr_b16 v[24:25], v7 offset:38912\n"
"v_mfma_f32_16x16x32_bf16 v[140:143], v[28:31], a[116:119], v[140:143]\n"
"ds_read_b64_tr_b16 v[26:27], v7 offset:39424\n"
"v_cvt_pk_bf16_f32 v99, v102, v103\n"
"v_mfma_f32_16x16x32_bf16 v[136:139], v[32:35], a[120:123], v[136:139]\n"
"ds_read_b64_tr_b16 v[28:29], v7 offset:39936\n"
"v_mfma_f32_16x16x32_bf16 v[140:143], v[32:35], a[124:127], v[140:143]\n"
"ds_read_b64_tr_b16 v[30:31], v7 offset:40448\n"
"buffer_load_dwordx4 v1, s[24:27], s21 offen offset:3072 sc1 lds\n"
"s_waitcnt lgkmcnt(12)\n"
"v_mfma_f32_16x16x32_bf16 v[136:139], v[36:39], a[128:131], v[136:139]\n"
"ds_read_b64_tr_b16 v[32:33], v7 offset:40960\n"
"v_mfma_f32_16x16x32_bf16 v[140:143], v[36:39], a[132:135], v[140:143]\n"
"ds_read_b64_tr_b16 v[34:35], v7 offset:41472\n"
"v_mfma_f32_16x16x32_bf16 v[136:139], v[40:43], a[136:139], v[136:139]\n"
"ds_read_b64_tr_b16 v[36:37], v7 offset:41984\n"
"v_mfma_f32_16x16x32_bf16 v[140:143], v[40:43], a[140:143], v[140:143]\n"
"ds_read_b64_tr_b16 v[38:39], v7 offset:42496\n"
"v_cvt_pk_bf16_f32 v104, v104, v105\n"
"s_waitcnt lgkmcnt(12)\n"
"v_mfma_f32_16x16x32_bf16 v[136:139], v[44:47], a[144:147], v[136:139]\n"
"ds_read_b64_tr_b16 v[40:41], v7 offset:43008\n"
"v_mfma_f32_16x16x32_bf16 v[140:143], v[44:47], a[148:151], v[140:143]\n"
"ds_read_b64_tr_b16 v[42:43], v7 offset:43520\n"
"v_cvt_pk_bf16_f32 v105, v106, v107\n"
"buffer_store_dwordx4 v[96:99], v9, s[16:19], 0 offen offset:512 sc1\n"
"v_mfma_f32_16x16x32_bf16 v[136:139], v[12:15], a[152:155], v[136:139]\n"
"ds_read_b64_tr_b16 v[44:45], v7 offset:44032\n"
"v_mfma_f32_16x16x32_bf16 v[140:143], v[12:15], a[156:159], v[140:143]\n"
"ds_read_b64_tr_b16 v[46:47], v7 offset:44544\n"
"v_cvt_pk_bf16_f32 v106, v108, v109\n"
"s_waitcnt lgkmcnt(12)\n"
"v_mfma_f32_16x16x32_bf16 v[136:139], v[16:19], a[160:163], v[136:139]\n"
"ds_read_b64_tr_b16 v[12:13], v7 offset:45056\n"
"v_mfma_f32_16x16x32_bf16 v[140:143], v[16:19], a[164:167], v[140:143]\n"
"ds_read_b64_tr_b16 v[14:15], v7 offset:45568\n"
"v_cvt_pk_bf16_f32 v107, v110, v111\n"
"v_mfma_f32_16x16x32_bf16 v[136:139], v[20:23], a[168:171], v[136:139]\n"
"ds_read_b64_tr_b16 v[16:17], v7 offset:46080\n"
"v_mfma_f32_16x16x32_bf16 v[140:143], v[20:23], a[172:175], v[140:143]\n"
"ds_read_b64_tr_b16 v[18:19], v7 offset:46592\n"
"s_waitcnt lgkmcnt(12)\n"
"v_mfma_f32_16x16x32_bf16 v[136:139], v[24:27], a[176:179], v[136:139]\n"
"ds_read_b64_tr_b16 v[20:21], v7 offset:47104\n"
"v_mfma_f32_16x16x32_bf16 v[140:143], v[24:27], a[180:183], v[140:143]\n"
"ds_read_b64_tr_b16 v[22:23], v7 offset:47616\n"
"v_cvt_pk_bf16_f32 v112, v112, v113\n"
"v_mfma_f32_16x16x32_bf16 v[136:139], v[28:31], a[184:187], v[136:139]\n"
"ds_read_b64_tr_b16 v[24:25], v7 offset:48128\n"
"v_mfma_f32_16x16x32_bf16 v[140:143], v[28:31], a[188:191], v[140:143]\n"
"ds_read_b64_tr_b16 v[26:27], v7 offset:48640\n"
"v_cvt_pk_bf16_f32 v113, v114, v115\n"
"s_waitcnt vmcnt(27)\n"
"s_barrier\n"
"s_waitcnt lgkmcnt(12)\n"
"v_mfma_f32_16x16x32_bf16 v[136:139], v[32:35], a[192:195], v[136:139]\n"
"ds_read_b64_tr_b16 v[28:29], v7 offset:49152\n"
"v_mfma_f32_16x16x32_bf16 v[140:143], v[32:35], a[196:199], v[140:143]\n"
"ds_read_b64_tr_b16 v[30:31], v7 offset:49664\n"
"v_cvt_pk_bf16_f32 v114, v116, v117\n"
"v_mfma_f32_16x16x32_bf16 v[136:139], v[36:39], a[200:203], v[136:139]\n"
"ds_read_b64_tr_b16 v[32:33], v7 offset:50176\n"
"v_mfma_f32_16x16x32_bf16 v[140:143], v[36:39], a[204:207], v[140:143]\n"
"ds_read_b64_tr_b16 v[34:35], v7 offset:50688\n"
"v_cvt_pk_bf16_f32 v115, v118, v119\n"
"s_waitcnt lgkmcnt(12)\n"
"v_mfma_f32_16x16x32_bf16 v[136:139], v[40:43], a[208:211], v[136:139]\n"
"ds_read_b64_tr_b16 v[36:37], v7 offset:51200\n"
"v_mfma_f32_16x16x32_bf16 v[140:143], v[40:43], a[212:215], v[140:143]\n"
"ds_read_b64_tr_b16 v[38:39], v7 offset:51712\n"
"buffer_store_dwordx4 v[104:107], v9, s[16:19], 0 offen offset:768 sc1\n"
"v_mfma_f32_16x16x32_bf16 v[136:139], v[44:47], a[216:219], v[136:139]\n"
"ds_read_b64_tr_b16 v[40:41], v7 offset:52224\n"
"v_mfma_f32_16x16x32_bf16 v[140:143], v[44:47], a[220:223], v[140:143]\n"
"ds_read_b64_tr_b16 v[42:43], v7 offset:52736\n"
"v_cvt_pk_bf16_f32 v120, v120, v121\n"
"s_waitcnt lgkmcnt(12)\n"
"v_mfma_f32_16x16x32_bf16 v[136:139], v[12:15], a[224:227], v[136:139]\n"
"ds_read_b64_tr_b16 v[44:45], v7 offset:53248\n"
"v_mfma_f32_16x16x32_bf16 v[140:143], v[12:15], a[228:231], v[140:143]\n"
"ds_read_b64_tr_b16 v[46:47], v7 offset:53760\n"
"v_cvt_pk_bf16_f32 v121, v122, v123\n"
"v_mfma_f32_16x16x32_bf16 v[136:139], v[16:19], a[232:235], v[136:139]\n"
"ds_read_b64_tr_b16 v[12:13], v7 offset:54272\n"
"v_mfma_f32_16x16x32_bf16 v[140:143], v[16:19], a[236:239], v[140:143]\n"
"ds_read_b64_tr_b16 v[14:15], v7 offset:54784\n"
"v_cvt_pk_bf16_f32 v122, v124, v125\n"
"s_waitcnt lgkmcnt(12)\n"
"v_mfma_f32_16x16x32_bf16 v[136:139], v[20:23], a[240:243], v[136:139]\n"
"ds_read_b64_tr_b16 v[16:17], v7 offset:55296\n"
"v_mfma_f32_16x16x32_bf16 v[140:143], v[20:23], a[244:247], v[140:143]\n"
"ds_read_b64_tr_b16 v[18:19], v7 offset:55808\n"
"v_cvt_pk_bf16_f32 v123, v126, v127\n"
"v_mfma_f32_16x16x32_bf16 v[136:139], v[24:27], a[248:251], v[136:139]\n"
"ds_read_b64_tr_b16 v[20:21], v7 offset:56320\n"
"v_mfma_f32_16x16x32_bf16 v[140:143], v[24:27], a[252:255], v[140:143]\n"
"ds_read_b64_tr_b16 v[22:23], v7 offset:56832\n"
"s_waitcnt lgkmcnt(12)\n"
"v_mfma_f32_16x16x32_bf16 v[144:147], v[28:31], a[0:3], 0\n"
"ds_read_b64_tr_b16 v[24:25], v7 offset:57344\n"
"v_mfma_f32_16x16x32_bf16 v[148:151], v[28:31], a[4:7], 0\n"
"ds_read_b64_tr_b16 v[26:27], v7 offset:57856\n"
"v_cvt_pk_bf16_f32 v128, v128, v129\n"
"v_mfma_f32_16x16x32_bf16 v[144:147], v[32:35], a[8:11], v[144:147]\n"
"ds_read_b64_tr_b16 v[28:29], v7 offset:58368\n"
"v_mfma_f32_16x16x32_bf16 v[148:151], v[32:35], a[12:15], v[148:151]\n"
"ds_read_b64_tr_b16 v[30:31], v7 offset:58880\n"
"v_cvt_pk_bf16_f32 v129, v130, v131\n"
"s_waitcnt lgkmcnt(12)\n"
"v_mfma_f32_16x16x32_bf16 v[144:147], v[36:39], a[16:19], v[144:147]\n"
"ds_read_b64_tr_b16 v[32:33], v7 offset:59392\n"
"v_mfma_f32_16x16x32_bf16 v[148:151], v[36:39], a[20:23], v[148:151]\n"
"ds_read_b64_tr_b16 v[34:35], v7 offset:59904\n"
"v_cvt_pk_bf16_f32 v130, v132, v133\n"
"buffer_store_dwordx4 v[112:115], v9, s[16:19], 0 offen offset:1024 sc1\n"
"v_mfma_f32_16x16x32_bf16 v[144:147], v[40:43], a[24:27], v[144:147]\n"
"ds_read_b64_tr_b16 v[36:37], v7 offset:60416\n"
"v_mfma_f32_16x16x32_bf16 v[148:151], v[40:43], a[28:31], v[148:151]\n"
"ds_read_b64_tr_b16 v[38:39], v7 offset:60928\n"
"v_cvt_pk_bf16_f32 v131, v134, v135\n"
"s_waitcnt lgkmcnt(12)\n"
"v_mfma_f32_16x16x32_bf16 v[144:147], v[44:47], a[32:35], v[144:147]\n"
"ds_read_b64_tr_b16 v[40:41], v7 offset:61440\n"
"v_mfma_f32_16x16x32_bf16 v[148:151], v[44:47], a[36:39], v[148:151]\n"
"ds_read_b64_tr_b16 v[42:43], v7 offset:61952\n"
"v_mfma_f32_16x16x32_bf16 v[144:147], v[12:15], a[40:43], v[144:147]\n"
"ds_read_b64_tr_b16 v[44:45], v7 offset:62464\n"
"v_mfma_f32_16x16x32_bf16 v[148:151], v[12:15], a[44:47], v[148:151]\n"
"ds_read_b64_tr_b16 v[46:47], v7 offset:62976\n"
"v_cvt_pk_bf16_f32 v136, v136, v137\n"
"s_waitcnt lgkmcnt(12)\n"
"v_mfma_f32_16x16x32_bf16 v[144:147], v[16:19], a[48:51], v[144:147]\n"
"ds_read_b64_tr_b16 v[12:13], v7 offset:63488\n"
"v_mfma_f32_16x16x32_bf16 v[148:151], v[16:19], a[52:55], v[148:151]\n"
"ds_read_b64_tr_b16 v[14:15], v7 offset:64000\n"
"v_cvt_pk_bf16_f32 v137, v138, v139\n"
"buffer_store_dwordx4 v[120:123], v9, s[16:19], 0 offen offset:1280 sc1\n"
"v_mfma_f32_16x16x32_bf16 v[144:147], v[20:23], a[56:59], v[144:147]\n"
"ds_read_b64_tr_b16 v[16:17], v7 offset:64512\n"
"v_mfma_f32_16x16x32_bf16 v[148:151], v[20:23], a[60:63], v[148:151]\n"
"ds_read_b64_tr_b16 v[18:19], v7 offset:65024\n"
"v_cvt_pk_bf16_f32 v138, v140, v141\n"
"s_waitcnt vmcnt(26)\n"
"s_barrier\n"
"s_waitcnt lgkmcnt(12)\n"
"v_mfma_f32_16x16x32_bf16 v[144:147], v[24:27], a[64:67], v[144:147]\n"
"ds_read_b64_tr_b16 v[20:21], v8 offset:0\n"
"v_mfma_f32_16x16x32_bf16 v[148:151], v[24:27], a[68:71], v[148:151]\n"
"ds_read_b64_tr_b16 v[22:23], v8 offset:512\n"
"v_cvt_pk_bf16_f32 v139, v142, v143\n"
"v_mfma_f32_16x16x32_bf16 v[144:147], v[28:31], a[72:75], v[144:147]\n"
"ds_read_b64_tr_b16 v[24:25], v8 offset:1024\n"
"v_mfma_f32_16x16x32_bf16 v[148:151], v[28:31], a[76:79], v[148:151]\n"
"ds_read_b64_tr_b16 v[26:27], v8 offset:1536\n"
"s_waitcnt lgkmcnt(12)\n"
"v_mfma_f32_16x16x32_bf16 v[144:147], v[32:35], a[80:83], v[144:147]\n"
"ds_read_b64_tr_b16 v[28:29], v8 offset:2048\n"
"v_mfma_f32_16x16x32_bf16 v[148:151], v[32:35], a[84:87], v[148:151]\n"
"ds_read_b64_tr_b16 v[30:31], v8 offset:2560\n"
"buffer_store_dwordx4 v[128:131], v9, s[16:19], 0 offen offset:1536 sc1\n"
"v_mfma_f32_16x16x32_bf16 v[144:147], v[36:39], a[88:91], v[144:147]\n"
"ds_read_b64_tr_b16 v[32:33], v8 offset:3072\n"
"v_mfma_f32_16x16x32_bf16 v[148:151], v[36:39], a[92:95], v[148:151]\n"
"ds_read_b64_tr_b16 v[34:35], v8 offset:3584\n"
"s_waitcnt lgkmcnt(12)\n"
"v_mfma_f32_16x16x32_bf16 v[144:147], v[40:43], a[96:99], v[144:147]\n"
"ds_read_b64_tr_b16 v[36:37], v8 offset:4096\n"
"v_mfma_f32_16x16x32_bf16 v[148:151], v[40:43], a[100:103], v[148:151]\n"
"ds_read_b64_tr_b16 v[38:39], v8 offset:4608\n"
"v_mfma_f32_16x16x32_bf16 v[144:147], v[44:47], a[104:107], v[144:147]\n"
"ds_read_b64_tr_b16 v[40:41], v8 offset:5120\n"
"v_mfma_f32_16x16x32_bf16 v[148:151], v[44:47], a[108:111], v[148:151]\n"
"ds_read_b64_tr_b16 v[42:43], v8 offset:5632\n"
"s_waitcnt lgkmcnt(12)\n"
"v_mfma_f32_16x16x32_bf16 v[144:147], v[12:15], a[112:115], v[144:147]\n"
"ds_read_b64_tr_b16 v[44:45], v8 offset:6144\n"
"v_mfma_f32_16x16x32_bf16 v[148:151], v[12:15], a[116:119], v[148:151]\n"
"ds_read_b64_tr_b16 v[46:47], v8 offset:6656\n"
"buffer_store_dwordx4 v[136:139], v9, s[16:19], 0 offen offset:1792 sc1\n"
"v_mfma_f32_16x16x32_bf16 v[144:147], v[16:19], a[120:123], v[144:147]\n"
"ds_read_b64_tr_b16 v[12:13], v8 offset:7168\n"
"v_mfma_f32_16x16x32_bf16 v[148:151], v[16:19], a[124:127], v[148:151]\n"
"ds_read_b64_tr_b16 v[14:15], v8 offset:7680\n"
"s_waitcnt lgkmcnt(12)\n"
"v_mfma_f32_16x16x32_bf16 v[144:147], v[20:23], a[128:131], v[144:147]\n"
"ds_read_b64_tr_b16 v[16:17], v8 offset:8192\n"
"v_mfma_f32_16x16x32_bf16 v[148:151], v[20:23], a[132:135], v[148:151]\n"
"ds_read_b64_tr_b16 v[18:19], v8 offset:8704\n"
"v_mfma_f32_16x16x32_bf16 v[144:147], v[24:27], a[136:139], v[144:147]\n"
"ds_read_b64_tr_b16 v[20:21], v8 offset:9216\n"
"v_mfma_f32_16x16x32_bf16 v[148:151], v[24:27], a[140:143], v[148:151]\n"
"ds_read_b64_tr_b16 v[22:23], v8 offset:9728\n"
"s_waitcnt lgkmcnt(12)\n"
"v_mfma_f32_16x16x32_bf16 v[144:147], v[28:31], a[144:147], v[144:147]\n"
"ds_read_b64_tr_b16 v[24:25], v8 offset:10240\n"
"v_mfma_f32_16x16x32_bf16 v[148:151], v[28:31], a[148:151], v[148:151]\n"
"ds_read_b64_tr_b16 v[26:27], v8 offset:10752\n"
"v_mfma_f32_16x16x32_bf16 v[144:147], v[32:35], a[152:155], v[144:147]\n"
"ds_read_b64_tr_b16 v[28:29], v8 offset:11264\n"
"v_mfma_f32_16x16x32_bf16 v[148:151], v[32:35], a[156:159], v[148:151]\n"
"ds_read_b64_tr_b16 v[30:31], v8 offset:11776\n"
"s_waitcnt lgkmcnt(12)\n"
"v_mfma_f32_16x16x32_bf16 v[144:147], v[36:39], a[160:163], v[144:147]\n"
"ds_read_b64_tr_b16 v[32:33], v8 offset:12288\n"
"v_mfma_f32_16x16x32_bf16 v[148:151], v[36:39], a[164:167], v[148:151]\n"
"ds_read_b64_tr_b16 v[34:35], v8 offset:12800\n"
"v_mfma_f32_16x16x32_bf16 v[144:147], v[40:43], a[168:171], v[144:147]\n"
"ds_read_b64_tr_b16 v[36:37], v8 offset:13312\n"
"v_mfma_f32_16x16x32_bf16 v[148:151], v[40:43], a[172:175], v[148:151]\n"
"ds_read_b64_tr_b16 v[38:39], v8 offset:13824\n"
"s_waitcnt lgkmcnt(12)\n"
"v_mfma_f32_16x16x32_bf16 v[144:147], v[44:47], a[176:179], v[144:147]\n"
"ds_read_b64_tr_b16 v[40:41], v8 offset:14336\n"
"v_mfma_f32_16x16x32_bf16 v[148:151], v[44:47], a[180:183], v[148:151]\n"
"ds_read_b64_tr_b16 v[42:43], v8 offset:14848\n"
"v_mfma_f32_16x16x32_bf16 v[144:147], v[12:15], a[184:187], v[144:147]\n"
"ds_read_b64_tr_b16 v[44:45], v8 offset:15360\n"
"v_mfma_f32_16x16x32_bf16 v[148:151], v[12:15], a[188:191], v[148:151]\n"
"ds_read_b64_tr_b16 v[46:47], v8 offset:15872\n"
"s_waitcnt lgkmcnt(12)\n"
"v_mfma_f32_16x16x32_bf16 v[144:147], v[16:19], a[192:195], v[144:147]\n"
"v_mfma_f32_16x16x32_bf16 v[148:151], v[16:19], a[196:199], v[148:151]\n"
"v_mfma_f32_16x16x32_bf16 v[144:147], v[20:23], a[200:203], v[144:147]\n"
"v_mfma_f32_16x16x32_bf16 v[148:151], v[20:23], a[204:207], v[148:151]\n"
"s_waitcnt lgkmcnt(8)\n"
"v_mfma_f32_16x16x32_bf16 v[144:147], v[24:27], a[208:211], v[144:147]\n"
"v_mfma_f32_16x16x32_bf16 v[148:151], v[24:27], a[212:215], v[148:151]\n"
"v_mfma_f32_16x16x32_bf16 v[144:147], v[28:31], a[216:219], v[144:147]\n"
"v_mfma_f32_16x16x32_bf16 v[148:151], v[28:31], a[220:223], v[148:151]\n"
"s_waitcnt lgkmcnt(4)\n"
"v_mfma_f32_16x16x32_bf16 v[144:147], v[32:35], a[224:227], v[144:147]\n"
"v_mfma_f32_16x16x32_bf16 v[148:151], v[32:35], a[228:231], v[148:151]\n"
"v_mfma_f32_16x16x32_bf16 v[144:147], v[36:39], a[232:235], v[144:147]\n"
"v_mfma_f32_16x16x32_bf16 v[148:151], v[36:39], a[236:239], v[148:151]\n"
"s_waitcnt lgkmcnt(0)\n"
"v_mfma_f32_16x16x32_bf16 v[144:147], v[40:43], a[240:243], v[144:147]\n"
"v_mfma_f32_16x16x32_bf16 v[148:151], v[40:43], a[244:247], v[148:151]\n"
"v_mfma_f32_16x16x32_bf16 v[144:147], v[44:47], a[248:251], v[144:147]\n"
"v_mfma_f32_16x16x32_bf16 v[148:151], v[44:47], a[252:255], v[148:151]\n"
"s_nop 7\n"
"v_cvt_pk_bf16_f32 v144, v144, v145\n"
"v_cvt_pk_bf16_f32 v145, v146, v147\n"
"v_cvt_pk_bf16_f32 v146, v148, v149\n"
"v_cvt_pk_bf16_f32 v147, v150, v151\n"
"buffer_store_dwordx4 v[144:147], v9, s[16:19], 0 offen offset:2048 sc1\n"
"s_waitcnt vmcnt(0)\n"
"s_waitcnt lgkmcnt(0)\n"
      :: GEMM_IN(a) : "v12","v13","v14","v15","v16","v17","v18","v19","v20","v21","v22","v23","v24","v25","v26","v27","v28","v29","v30","v31","v32","v33","v34","v35","v36","v37","v38","v39","v40","v41","v42","v43","v44","v45","v46","v47","v48","v49","v50","v51","v52","v53","v54","v55","v56","v57","v58","v59","v60","v61","v62","v63","v64","v65","v66","v67","v68","v69","v70","v71","v72","v73","v74","v75","v76","v77","v78","v79","v80","v81","v82","v83","v84","v85","v86","v87","v88","v89","v90","v91","v92","v93","v94","v95","v96","v97","v98","v99","v100","v101","v102","v103","v104","v105","v106","v107","v108","v109","v110","v111","v112","v113","v114","v115","v116","v117","v118","v119","v120","v121","v122","v123","v124","v125","v126","v127","v128","v129","v130","v131","v132","v133","v134","v135","v136","v137","v138","v139","v140","v141","v142","v143","v144","v145","v146","v147","v148","v149","v150","v151","v152","v153","v154","v155","v156","v157","v158","v159","a0","a1","a2","a3","a4","a5","a6","a7","a8","a9","a10","a11","a12","a13","a14","a15","a16","a17","a18","a19","a20","a21","a22","a23","a24","a25","a26","a27","a28","a29","a30","a31","a32","a33","a34","a35","a36","a37","a38","a39","a40","a41","a42","a43","a44","a45","a46","a47","a48","a49","a50","a51","a52","a53","a54","a55","a56","a57","a58","a59","a60","a61","a62","a63","a64","a65","a66","a67","a68","a69","a70","a71","a72","a73","a74","a75","a76","a77","a78","a79","a80","a81","a82","a83","a84","a85","a86","a87","a88","a89","a90","a91","a92","a93","a94","a95","a96","a97","a98","a99","a100","a101","a102","a103","a104","a105","a106","a107","a108","a109","a110","a111","a112","a113","a114","a115","a116","a117","a118","a119","a120","a121","a122","a123","a124","a125","a126","a127","a128","a129","a130","a131","a132","a133","a134","a135","a136","a137","a138","a139","a140","a141","a142","a143","a144","a145","a146","a147","a148","a149","a150","a151","a152","a153","a154","a155","a156","a157","a158","a159","a160","a161","a162","a163","a164","a165","a166","a167","a168","a169","a170","a171","a172","a173","a174","a175","a176","a177","a178","a179","a180","a181","a182","a183","a184","a185","a186","a187","a188","a189","a190","a191","a192","a193","a194","a195","a196","a197","a198","a199","a200","a201","a202","a203","a204","a205","a206","a207","a208","a209","a210","a211","a212","a213","a214","a215","a216","a217","a218","a219","a220","a221","a222","a223","a224","a225","a226","a227","a228","a229","a230","a231","a232","a233","a234","a235","a236","a237","a238","a239","a240","a241","a242","a243","a244","a245","a246","a247","a248","a249","a250","a251","a252","a253","a254","a255","s21","s22","s53","scc","vcc","memory");

    CODE_LABEL("Lm_C");
    signal_cnt<false, 2>(cnt + (CNT_B + rb) * 64u, wid);
#pragma unroll
    for (int i = 0; i < 3; ++i) asm volatile("" : "+v"(xw[i][0]), "+v"(xw[i][1]), "+v"(sc[i]));

  }
  {
    const unsigned lane = lane_id();
    const unsigned jj = split * 4u + cb, f4 = lane >> 4, g2 = (lane >> 2) & 3u, r4 = lane & 3u;
    const unsigned tok16 = rb * 8u + 2u * (jj >> 2), rr = 4u * (jj & 3u) + r4;
    const bool fo = (f4 & 1u) != 0;
    const __amdgpu_buffer_rsrc_t rs = mk_rsrc(ws + WS_SLAB + (size_t)rb * 589824u + jj * 36864u, 36864u);
    const __amdgpu_buffer_rsrc_t rx = mk_rsrc(ws + WS_X1 + (size_t)tok16 * 36864u, 2u * 36864u), rh = mk_rsrc(ws + WS_H2F + (size_t)tok16 * 18432u, 2u * 18432u);
    unsigned lo = wid * 1024u + lane * 16u;
    unsigned xo = (4u * wid + f4) * 1024u + (16u * g2 + rr) * 16u;
    unsigned ho = (fo ? 18432u : 0u) + (2u * wid + (f4 >> 1)) * 1024u + (16u * g2 + rr) * 16u;
    f32x4 xv[3][2];
#pragma unroll
    for (int i = 0; i < 3; ++i) {
#pragma unroll
      for (int tb = 0; tb < 2; ++tb) xv[i][tb] = f32x4{bflo(xw[i][tb][0]), bfhi(xw[i][tb][0]), bflo(xw[i][tb][1]), bfhi(xw[i][tb][1])};
      sc[i] = f32x4{rbf(1.f + sc[i][0]), rbf(1.f + sc[i][1]), rbf(1.f + sc[i][2]), rbf(1.f + sc[i][3])};
      asm volatile("" : "+v"(xv[i][0]), "+v"(xv[i][1]), "+v"(sc[i]));
    }
    asm volatile("" : "+v"(lo), "+v"(xo), "+v"(ho));
    wait_cnt(cnt + (CNT_B + rb) * 64u + 32u * (cb & 1u), 16u, wid);

    u32x4 pw[3][4];
#pragma unroll
    for (int i = 0; i < 3; ++i)
#pragma unroll
      for (int sp = 0; sp < 4; ++sp) pw[i][sp] = __builtin_amdgcn_raw_buffer_load_b128(rs, lo, (i < 2 || three) ? sp * 9216u + i * 4096u : 0x7fff0000u, 16);
    float sum[2] = {0.f, 0.f};
#pragma unroll
    for (int i = 0; i < 3; ++i)
#pragma unroll
      for (int tb = 0; tb < 2; ++tb) {
        float s[4] = {0.f, 0.f, 0.f, 0.f};
#pragma unroll
        for (int sp = 0; sp < 4; ++sp) unpack4(s, pw[i][sp][2 * tb], pw[i][sp][2 * tb + 1]);
#pragma unroll
        for (int e = 0; e < 4; ++e) xv[i][tb][e] += ga[i][e] * s[e];
        const float part = (xv[i][tb][0] + xv[i][tb][1]) + (xv[i][tb][2] + xv[i][tb][3]);
        sum[tb] += (i < 2 || three) ? part : 0.f;
      }
    const float rnw = three ? (1.f / 192.f) : (1.f / 128.f);
    float m2[2];
#pragma unroll
    for (int tb = 0; tb < 2; ++tb) {
      sum[tb] = red_r4(sum[tb]);
      const float mw = sum[tb] * rnw;
      float q = 0.f, q3 = 0.f;
#pragma unroll
      for (int e = 0; e < 4; ++e) { const float d0 = xv[0][tb][e] - mw, d1 = xv[1][tb][e] - mw, d2 = xv[2][tb][e] - mw; q += d0 * d0; q += d1 * d1; q3 += d2 * d2; }
      m2[tb] = red_r4(three ? q + q3 : q);
    }
    if (lane < 4u) *(LAS f32x4*)(red + (wid * 4u + lane) * 4u) = f32x4{sum[0], sum[1], m2[0], m2[1]};
#pragma unroll
    for (int i = 0; i < 3; ++i) if (i < 2 || three) {
#pragma unroll
      for (int tb = 0; tb < 2; ++tb) __builtin_amdgcn_raw_buffer_store_b128(__builtin_bit_cast(u32x4, xv[i][tb]), rx, xo, i * 16384u + tb * 36864u, 16);
    }
    asm volatile("s_waitcnt lgkmcnt(0)\n\ts_barrier" ::: "memory");
    float mean[2], rstd[2];
    {
      f32x4 pw4[4];
#pragma unroll
      for (int w2 = 0; w2 < 4; ++w2) pw4[w2] = *(const LAS f32x4*)(red + (w2 * 4u + r4) * 4u);
#pragma unroll
      for (int tb = 0; tb < 2; ++tb) {
        mean[tb] = ((pw4[0][tb] + pw4[1][tb]) + (pw4[2][tb] + pw4[3][tb])) * (1.f / DM);
        float M2 = 0.f;
#pragma unroll
        for (int w2 = 0; w2 < 4; ++w2) { const float n2 = w2 ? 128.f : 192.f, dm = pw4[w2][tb] * (1.f / n2) - mean[tb]; M2 += pw4[w2][2 + tb] + n2 * dm * dm; }
        rstd[tb] = __builtin_amdgcn_rsqf(M2 * (1.f / DM) + 1e-5f);
      }
    }
#pragma unroll
    for (int i = 0; i < 3; ++i) if (i < 2 || three) {
      unsigned p[2][2];
#pragma unroll
      for (int tb = 0; tb < 2; ++tb) {
        float h[4];
#pragma unroll
        for (int e = 0; e < 4; ++e) h[e] = (xv[i][tb][e] - mean[tb]) * (rstd[tb] * sc[i][e]) + sh[i][e];
        p[tb][0] = cvtpk(h[0], h[1]); p[tb][1] = cvtpk(h[2], h[3]);
      }
      const auto s0 = __builtin_amdgcn_permlane16_swap(p[0][0], p[1][0], false, false), s1 = __builtin_amdgcn_permlane16_swap(p[0][1], p[1][1], false, false);
      __builtin_amdgcn_raw_buffer_store_b128(u32x4{s0[0], s1[0], s0[1], s1[1]}, rh, ho, i * 8192u, 16);
    }
    signal_cnt<true, 4>(cnt + (CNT_C2 + 2u * (rb >> 1)) * 64u, wid);

  }
  {
    GemmArgs a1 = gemm_args<1>(ws + WS_H2F, ws + WS_WGT + (size_t)ft * 331776u, rb2 * 16u + wid * 4u, 0u, ldsb, wid, lane_id());
    const unsigned tp = (ft & 7u) >> 1, par = ft & 1u, kb = 36u * split + 7u * tp;
    a1.srdO = make_srd(ws + WS_HHF + ((size_t)(rb2 * 16u + wid * 4u) * 144u + kb) * 1024u, (4u * 144u - kb) * 1024u);
    const unsigned lane = lane_id();
    a1.srdN = make_srd(ws + WS_WDT + (size_t)(cb * 4u + split) * 331776u, 331776u);
    a1.vo0 = 3u * par * 1024u + lane * 16u; a1.vo1 = 6u * 1024u + lane * 16u + 8u * par; a1.vo2 = (28u - 5u * tp + par) * 1024u + lane * 16u;
    a1.sig = cnt + (CNT_D + rb2 * 4u + split) * 64u; a1.poll = a1.sig;
    wait_cnt(cnt + (CNT_C2 + 2u * rb2) * 64u + 32u * (ft & 3u), 32u, wid);

    asm volatile(
"s_nop 4\n"
"s_mov_b32 s22, 0\n"
"buffer_load_dwordx4 a[0:3], v2, s[12:15], s22 offen sc1\n"
"buffer_load_dwordx4 a[4:7], v3, s[12:15], s22 offen sc1\n"
"buffer_load_dwordx4 a[8:11], v4, s[12:15], s22 offen sc1\n"
"buffer_load_dwordx4 a[12:15], v5, s[12:15], s22 offen sc1\n"
"buffer_load_dwordx4 a[16:19], v2, s[12:15], s22 offen offset:1024 sc1\n"
"buffer_load_dwordx4 a[20:23], v3, s[12:15], s22 offen offset:1024 sc1\n"
"buffer_load_dwordx4 a[24:27], v4, s[12:15], s22 offen offset:1024 sc1\n"
"buffer_load_dwordx4 a[28:31], v5, s[12:15], s22 offen offset:1024 sc1\n"
"buffer_load_dwordx4 a[32:35], v2, s[12:15], s22 offen offset:2048 sc1\n"
"buffer_load_dwordx4 a[36:39], v3, s[12:15], s22 offen offset:2048 sc1\n"
"buffer_load_dwordx4 a[40:43], v4, s[12:15], s22 offen offset:2048 sc1\n"
"buffer_load_dwordx4 a[44:47], v5, s[12:15], s22 offen offset:2048 sc1\n"
"buffer_load_dwordx4 a[48:51], v2, s[12:15], s22 offen offset:3072 sc1\n"
"buffer_load_dwordx4 a[52:55], v3, s[12:15], s22 offen offset:3072 sc1\n"
"buffer_load_dwordx4 a[56:59], v4, s[12:15], s22 offen offset:3072 sc1\n"
"buffer_load_dwordx4 a[60:63], v5, s[12:15], s22 offen offset:3072 sc1\n"
"s_mov_b32 s22, 4096\n"
"buffer_load_dwordx4 a[64:67], v2, s[12:15], s22 offen sc1\n"
"buffer_load_dwordx4 a[68:71], v3, s[12:15], s22 offen sc1\n"
"buffer_load_dwordx4 a[72:75], v4, s[12:15], s22 offen sc1\n"
"buffer_load_dwordx4 a[76:79], v5, s[12:15], s22 offen sc1\n"
"buffer_load_dwordx4 a[80:83], v2, s[12:15], s22 offen offset:1024 sc1\n"
"buffer_load_dwordx4 a[84:87], v3, s[12:15], s22 offen offset:1024 sc1\n"
"buffer_load_dwordx4 a[88:91], v4, s[12:15], s22 offen offset:1024 sc1\n"
"buffer_load_dwordx4 a[92:95], v5, s[12:15], s22 offen offset:1024 sc1\n"
"ds_read_b64_tr_b16 v[12:13], v6 offset:0\n"
"ds_read_b64_tr_b16 v[14:15], v6 offset:512\n"
"ds_read_b64_tr_b16 v[16:17], v6 offset:1024\n"
"ds_read_b64_tr_b16 v[18:19], v6 offset:1536\n"
"ds_read_b64_tr_b16 v[20:21], v6 offset:2048\n"
"ds_read_b64_tr_b16 v[22:23], v6 offset:2560\n"
"ds_read_b64_tr_b16 v[24:25], v6 offset:3072\n"
"ds_read_b64_tr_b16 v[26:27], v6 offset:3584\n"
"ds_read_b64_tr_b16 v[28:29], v6 offset:4096\n"
"ds_read_b64_tr_b16 v[30:31], v6 offset:4608\n"
"ds_read_b64_tr_b16 v[32:33], v6 offset:5120\n"
"ds_read_b64_tr_b16 v[34:35], v6 offset:5632\n"
"ds_read_b64_tr_b16 v[36:37], v6 offset:6144\n"
"ds_read_b64_tr_b16 v[38:39], v6 offset:6656\n"
"ds_read_b64_tr_b16 v[40:41], v6 offset:7168\n"
"ds_read_b64_tr_b16 v[42:43], v6 offset:7680\n"
"s_waitcnt vmcnt(20)\n"
"s_waitcnt lgkmcnt(12)\n"
"v_mfma_f32_16x16x32_bf16 v[80:83], v[12:15], a[0:3], 0\n"
"ds_read_b64_tr_b16 v[44:45], v6 offset:8192\n"
"v_mfma_f32_16x16x32_bf16 v[84:87], v[12:15], a[4:7], 0\n"
"ds_read_b64_tr_b16 v[46:47], v6 offset:8704\n"
"v_mfma_f32_16x16x32_bf16 v[88:91], v[12:15], a[8:11], 0\n"
"buffer_load_dwordx4 a[96:99], v2, s[12:15], s22 offen offset:2048 sc1\n"
"v_mfma_f32_16x16x32_bf16 v[92:95], v[12:15], a[12:15], 0\n"
"v_mfma_f32_16x16x32_bf16 v[96:99], v[16:19], a[0:3], 0\n"
"ds_read_b64_tr_b16 v[12:13], v6 offset:9216\n"
"v_mfma_f32_16x16x32_bf16 v[100:103], v[16:19], a[4:7], 0\n"
"ds_read_b64_tr_b16 v[14:15], v6 offset:9728\n"
"v_mfma_f32_16x16x32_bf16 v[104:107], v[16:19], a[8:11], 0\n"
"buffer_load_dwordx4 a[100:103], v3, s[12:15], s22 offen offset:2048 sc1\n"
"v_mfma_f32_16x16x32_bf16 v[108:111], v[16:19], a[12:15], 0\n"
"s_waitcnt lgkmcnt(12)\n"
"v_mfma_f32_16x16x32_bf16 v[112:115], v[20:23], a[0:3], 0\n"
"ds_read_b64_tr_b16 v[16:17], v6 offset:10240\n"
"v_mfma_f32_16x16x32_bf16 v[116:119], v[20:23], a[4:7], 0\n"
"ds_read_b64_tr_b16 v[18:19], v6 offset:10752\n"
"v_mfma_f32_16x16x32_bf16 v[120:123], v[20:23], a[8:11], 0\n"
"buffer_load_dwordx4 a[104:107], v4, s[12:15], s22 offen offset:2048 sc1\n"
"v_mfma_f32_16x16x32_bf16 v[124:127], v[20:23], a[12:15], 0\n"
"v_mfma_f32_16x16x32_bf16 v[128:131], v[24:27], a[0:3], 0\n"
"ds_read_b64_tr_b16 v[20:21], v6 offset:11264\n"
"v_mfma_f32_16x16x32_bf16 v[132:135], v[24:27], a[4:7], 0\n"
"ds_read_b64_tr_b16 v[22:23], v6 offset:11776\n"
"v_mfma_f32_16x16x32_bf16 v[136:139], v[24:27], a[8:11], 0\n"
"buffer_load_dwordx4 a[108:111], v5, s[12:15], s22 offen offset:2048 sc1\n"
"v_mfma_f32_16x16x32_bf16 v[140:143], v[24:27], a[12:15], 0\n"
"s_waitcnt lgkmcnt(12)\n"
"v_mfma_f32_16x16x32_bf16 v[144:147], v[28:31], a[0:3], 0\n"
"ds_read_b64_tr_b16 v[24:25], v6 offset:12288\n"
"v_mfma_f32_16x16x32_bf16 v[148:151], v[28:31], a[4:7], 0\n"
"ds_read_b64_tr_b16 v[26:27], v6 offset:12800\n"
"v_mfma_f32_16x16x32_bf16 v[152:155], v[28:31], a[8:11], 0\n"
"buffer_load_dwordx4 a[112:115], v2, s[12:15], s22 offen offset:3072 sc1\n"
"v_mfma_f32_16x16x32_bf16 v[156:159], v[28:31], a[12:15], 0\n"
"v_mfma_f32_16x16x32_bf16 v[160:163], v[32:35], a[0:3], 0\n"
"ds_read_b64_tr_b16 v[28:29], v6 offset:13312\n"
"v_mfma_f32_16x16x32_bf16 v[164:167], v[32:35], a[4:7], 0\n"
"ds_read_b64_tr_b16 v[30:31], v6 offset:13824\n"
"v_mfma_f32_16x16x32_bf16 v[168:171], v[32:35], a[8:11], 0\n"
"buffer_load_dwordx4 a[116:119], v3, s[12:15], s22 offen offset:3072 sc1\n"
"v_mfma_f32_16x16x32_bf16 v[172:175], v[32:35], a[12:15], 0\n"
"s_waitcnt vmcnt(22)\n"
"s_waitcnt lgkmcnt(12)\n"
"v_mfma_f32_16x16x32_bf16 v[80:83], v[36:39], a[16:19], v[80:83]\n"
"ds_read_b64_tr_b16 v[32:33], v6 offset:14336\n"
"v_mfma_f32_16x16x32_bf16 v[84:87], v[36:39], a[20:23], v[84:87]\n"
"ds_read_b64_tr_b16 v[34:35], v6 offset:14848\n"
"v_mfma_f32_16x16x32_bf16 v[88:91], v[36:39], a[24:27], v[88:91]\n"
"buffer_load_dwordx4 a[120:123], v4, s[12:15], s22 offen offset:3072 sc1\n"
"v_mfma_f32_16x16x32_bf16 v[92:95], v[36:39], a[28:31], v[92:95]\n"
"v_mfma_f32_16x16x32_bf16 v[96:99], v[40:43], a[16:19], v[96:99]\n"
"ds_read_b64_tr_b16 v[36:37], v6 offset:15360\n"
"v_mfma_f32_16x16x32_bf16 v[100:103], v[40:43], a[20:23], v[100:103]\n"
"ds_read_b64_tr_b16 v[38:39], v6 offset:15872\n"
"v_mfma_f32_16x16x32_bf16 v[104:107], v[40:43], a[24:27], v[104:107]\n"
"buffer_load_dwordx4 a[124:127], v5, s[12:15], s22 offen offset:3072 sc1\n"
"v_mfma_f32_16x16x32_bf16 v[108:111], v[40:43], a[28:31], v[108:111]\n"
"s_waitcnt lgkmcnt(12)\n"
"v_mfma_f32_16x16x32_bf16 v[112:115], v[44:47], a[16:19], v[112:115]\n"
"ds_read_b64_tr_b16 v[40:41], v6 offset:16384\n"
"v_mfma_f32_16x16x32_bf16 v[116:119], v[44:47], a[20:23], v[116:119]\n"
"ds_read_b64_tr_b16 v[42:43], v6 offset:16896\n"
"v_mfma_f32_16x16x32_bf16 v[120:123], v[44:47], a[24:27], v[120:123]\n"
"s_mov_b32 s22, 8192\n"
"buffer_load_dwordx4 a[128:131], v2, s[12:15], s22 offen sc1\n"
"v_mfma_f32_16x16x32_bf16 v[124:127], v[44:47], a[28:31], v[124:127]\n"
"v_mfma_f32_16x16x32_bf16 v[128:131], v[12:15], a[16:19], v[128:131]\n"
"ds_read_b64_tr_b16 v[44:45], v6 offset:17408\n"
"v_mfma_f32_16x16x32_bf16 v[132:135], v[12:15], a[20:23], v[132:135]\n"
"ds_read_b64_tr_b16 v[46:47], v6 offset:17920\n"
"v_mfma_f32_16x16x32_bf16 v[136:139], v[12:15], a[24:27], v[136:139]\n"
"buffer_load_dwordx4 a[132:135], v3, s[12:15], s22 offen sc1\n"
"v_mfma_f32_16x16x32_bf16 v[140:143], v[12:15], a[28:31], v[140:143]\n"
"s_waitcnt lgkmcnt(12)\n"
"v_mfma_f32_16x16x32_bf16 v[144:147], v[16:19], a[16:19], v[144:147]\n"
"ds_read_b64_tr_b16 v[12:13], v6 offset:18432\n"
"v_mfma_f32_16x16x32_bf16 v[148:151], v[16:19], a[20:23], v[148:151]\n"
"ds_read_b64_tr_b16 v[14:15], v6 offset:18944\n"
"v_mfma_f32_16x16x32_bf16 v[152:155], v[16:19], a[24:27], v[152:155]\n"
"buffer_load_dwordx4 a[136:139], v4, s[12:15], s22 offen sc1\n"
"v_mfma_f32_16x16x32_bf16 v[156:159], v[16:19], a[28:31], v[156:159]\n"
"v_mfma_f32_16x16x32_bf16 v[160:163], v[20:23], a[16:19], v[160:163]\n"
"ds_read_b64_tr_b16 v[16:17], v6 offset:19456\n"
"v_mfma_f32_16x16x32_bf16 v[164:167], v[20:23], a[20:23], v[164:167]\n"
"ds_read_b64_tr_b16 v[18:19], v6 offset:19968\n"
"v_mfma_f32_16x16x32_bf16 v[168:171], v[20:23], a[24:27], v[168:171]\n"
"buffer_load_dwordx4 a[140:143], v5, s[12:15], s22 offen sc1\n"
"v_mfma_f32_16x16x32_bf16 v[172:175], v[20:23], a[28:31], v[172:175]\n"
"s_waitcnt vmcnt(24)\n"
"s_waitcnt lgkmcnt(12)\n"
"v_mfma_f32_16x16x32_bf16 v[80:83], v[24:27], a[32:35], v[80:83]\n"
"ds_read_b64_tr_b16 v[20:21], v6 offset:20480\n"
"v_mfma_f32_16x16x32_bf16 v[84:87], v[24:27], a[36:39], v[84:87]\n"
"ds_read_b64_tr_b16 v[22:23], v6 offset:20992\n"
"v_mfma_f32_16x16x32_bf16 v[88:91], v[24:27], a[40:43], v[88:91]\n"
"buffer_load_dwordx4 a[144:147], v2, s[12:15], s22 offen offset:1024 sc1\n"
"v_mfma_f32_16x16x32_bf16 v[92:95], v[24:27], a[44:47], v[92:95]\n"
"v_mfma_f32_16x16x32_bf16 v[96:99], v[28:31], a[32:35], v[96:99]\n"
"ds_read_b64_tr_b16 v[24:25], v6 offset:21504\n"
"v_mfma_f32_16x16x32_bf16 v[100:103], v[28:31], a[36:39], v[100:103]\n"
"ds_read_b64_tr_b16 v[26:27], v6 offset:22016\n"
"v_mfma_f32_16x16x32_bf16 v[104:107], v[28:31], a[40:43], v[104:107]\n"
"buffer_load_dwordx4 a[148:151], v3, s[12:15], s22 offen offset:1024 sc1\n"
"v_mfma_f32_16x16x32_bf16 v[108:111], v[28:31], a[44:47], v[108:111]\n"
"s_waitcnt lgkmcnt(12)\n"
"v_mfma_f32_16x16x32_bf16 v[112:115], v[32:35], a[32:35], v[112:115]\n"
"ds_read_b64_tr_b16 v[28:29], v6 offset:22528\n"
"v_mfma_f32_16x16x32_bf16 v[116:119], v[32:35], a[36:39], v[116:119]\n"
"ds_read_b64_tr_b16 v[30:31], v6 offset:23040\n"
"v_mfma_f32_16x16x32_bf16 v[120:123], v[32:35], a[40:43], v[120:123]\n"
"buffer_load_dwordx4 a[152:155], v4, s[12:15], s22 offen offset:1024 sc1\n"
"v_mfma_f32_16x16x32_bf16 v[124:127], v[32:35], a[44:47], v[124:127]\n"
"v_mfma_f32_16x16x32_bf16 v[128:131], v[36:39], a[32:35], v[128:131]\n"
"ds_read_b64_tr_b16 v[32:33], v6 offset:23552\n"
"v_mfma_f32_16x16x32_bf16 v[132:135], v[36:39], a[36:39], v[132:135]\n"
"ds_read_b64_tr_b16 v[34:35], v6 offset:24064\n"
"v_mfma_f32_16x16x32_bf16 v[136:139], v[36:39], a[40:43], v[136:139]\n"
"buffer_load_dwordx4 a[156:159], v5, s[12:15], s22 offen offset:1024 sc1\n"
"v_mfma_f32_16x16x32_bf16 v[140:143], v[36:39], a[44:47], v[140:143]\n"
"s_waitcnt lgkmcnt(12)\n"
"v_mfma_f32_16x16x32_bf16 v[144:147], v[40:43], a[32:35], v[144:147]\n"
"ds_read_b64_tr_b16 v[36:37], v6 offset:24576\n"
"v_mfma_f32_16x16x32_bf16 v[148:151], v[40:43], a[36:39], v[148:151]\n"
"ds_read_b64_tr_b16 v[38:39], v6 offset:25088\n"
"v_mfma_f32_16x16x32_bf16 v[152:155], v[40:43], a[40:43], v[152:155]\n"
"buffer_load_dwordx4 a[160:163], v2, s[12:15], s22 offen offset:2048 sc1\n"
"v_mfma_f32_16x16x32_bf16 v[156:159], v[40:43], a[44:47], v[156:159]\n"
"v_mfma_f32_16x16x32_bf16 v[160:163], v[44:47], a[32:35], v[160:163]\n"
"ds_read_b64_tr_b16 v[40:41], v6 offset:25600\n"
"v_mfma_f32_16x16x32_bf16 v[164:167], v[44:47], a[36:39], v[164:167]\n"
"ds_read_b64_tr_b16 v[42:43], v6 offset:26112\n"
"v_mfma_f32_16x16x32_bf16 v[168:171], v[44:47], a[40:43], v[168:171]\n"
"buffer_load_dwordx4 a[164:167], v3, s[12:15], s22 offen offset:2048 sc1\n"
"v_mfma_f32_16x16x32_bf16 v[172:175], v[44:47], a[44:47], v[172:175]\n"
"s_waitcnt vmcnt(26)\n"
"s_waitcnt lgkmcnt(12)\n"
"v_mfma_f32_16x16x32_bf16 v[80:83], v[12:15], a[48:51], v[80:83]\n"
"ds_read_b64_tr_b16 v[44:45], v6 offset:26624\n"
"v_mfma_f32_16x16x32_bf16 v[84:87], v[12:15], a[52:55], v[84:87]\n"
"ds_read_b64_tr_b16 v[46:47], v6 offset:27136\n"
"v_mfma_f32_16x16x32_bf16 v[88:91], v[12:15], a[56:59], v[88:91]\n"
"buffer_load_dwordx4 a[168:171], v4, s[12:15], s22 offen offset:2048 sc1\n"
"v_mfma_f32_16x16x32_bf16 v[92:95], v[12:15], a[60:63], v[92:95]\n"
"v_mfma_f32_16x16x32_bf16 v[96:99], v[16:19], a[48:51], v[96:99]\n"
"ds_read_b64_tr_b16 v[12:13], v6 offset:27648\n"
"v_mfma_f32_16x16x32_bf16 v[100:103], v[16:19], a[52:55], v[100:103]\n"
"ds_read_b64_tr_b16 v[14:15], v6 offset:28160\n"
"v_mfma_f32_16x16x32_bf16 v[104:107], v[16:19], a[56:59], v[104:107]\n"
"buffer_load_dwordx4 a[172:175], v5, s[12:15], s22 offen offset:2048 sc1\n"
"v_mfma_f32_16x16x32_bf16 v[108:111], v[16:19], a[60:63], v[108:111]\n"
"s_waitcnt lgkmcnt(12)\n"
"v_mfma_f32_16x16x32_bf16 v[112:115], v[20:23], a[48:51], v[112:115]\n"
"ds_read_b64_tr_b16 v[16:17], v6 offset:28672\n"
"v_mfma_f32_16x16x32_bf16 v[116:119], v[20:23], a[52:55], v[116:119]\n"
"ds_read_b64_tr_b16 v[18:19], v6 offset:29184\n"
"v_mfma_f32_16x16x32_bf16 v[120:123], v[20:23], a[56:59], v[120:123]\n"
"s_add_u32 m0, s20, 81920\n"
"s_mov_b32 s21, 81920\n"
"buffer_load_dwordx4 v1, s[8:11], s21 offen sc1 lds\n"
"v_mfma_f32_16x16x32_bf16 v[124:127], v[20:23], a[60:63], v[124:127]\n"
"v_mfma_f32_16x16x32_bf16 v[128:131], v[24:27], a[48:51], v[128:131]\n"
"ds_read_b64_tr_b16 v[20:21], v6 offset:29696\n"
"v_mfma_f32_16x16x32_bf16 v[132:135], v[24:27], a[52:55], v[132:135]\n"
"ds_read_b64_tr_b16 v[22:23], v6 offset:30208\n"
"v_mfma_f32_16x16x32_bf16 v[136:139], v[24:27], a[56:59], v[136:139]\n"
"buffer_load_dwordx4 v1, s[8:11], s21 offen offset:1024 sc1 lds\n"
"v_mfma_f32_16x16x32_bf16 v[140:143], v[24:27], a[60:63], v[140:143]\n"
"s_waitcnt lgkmcnt(12)\n"
"v_mfma_f32_16x16x32_bf16 v[144:147], v[28:31], a[48:51], v[144:147]\n"
"ds_read_b64_tr_b16 v[24:25], v6 offset:30720\n"
"v_mfma_f32_16x16x32_bf16 v[148:151], v[28:31], a[52:55], v[148:151]\n"
"ds_read_b64_tr_b16 v[26:27], v6 offset:31232\n"
"v_mfma_f32_16x16x32_bf16 v[152:155], v[28:31], a[56:59], v[152:155]\n"
"buffer_load_dwordx4 v1, s[8:11], s21 offen offset:2048 sc1 lds\n"
"v_mfma_f32_16x16x32_bf16 v[156:159], v[28:31], a[60:63], v[156:159]\n"
"v_mfma_f32_16x16x32_bf16 v[160:163], v[32:35], a[48:51], v[160:163]\n"
"ds_read_b64_tr_b16 v[28:29], v6 offset:31744\n"
"v_mfma_f32_16x16x32_bf16 v[164:167], v[32:35], a[52:55], v[164:167]\n"
"ds_read_b64_tr_b16 v[30:31], v6 offset:32256\n"
"v_mfma_f32_16x16x32_bf16 v[168:171], v[32:35], a[56:59], v[168:171]\n"
"buffer_load_dwordx4 v1, s[8:11], s21 offen offset:3072 sc1 lds\n"
"v_mfma_f32_16x16x32_bf16 v[172:175], v[32:35], a[60:63], v[172:175]\n"
"s_barrier\n"
"s_waitcnt vmcnt(28)\n"
"s_waitcnt lgkmcnt(12)\n"
"v_mfma_f32_16x16x32_bf16 v[80:83], v[36:39], a[64:67], v[80:83]\n"
"ds_read_b64_tr_b16 v[32:33], v6 offset:32768\n"
"v_mfma_f32_16x16x32_bf16 v[84:87], v[36:39], a[68:71], v[84:87]\n"
"ds_read_b64_tr_b16 v[34:35], v6 offset:33280\n"
"v_mfma_f32_16x16x32_bf16 v[88:91], v[36:39], a[72:75], v[88:91]\n"
"buffer_load_dwordx4 a[176:179], v2, s[12:15], s22 offen offset:3072 sc1\n"
"v_mfma_f32_16x16x32_bf16 v[92:95], v[36:39], a[76:79], v[92:95]\n"
"v_mfma_f32_16x16x32_bf16 v[96:99], v[40:43], a[64:67], v[96:99]\n"
"ds_read_b64_tr_b16 v[36:37], v6 offset:33792\n"
"v_mfma_f32_16x16x32_bf16 v[100:103], v[40:43], a[68:71], v[100:103]\n"
"ds_read_b64_tr_b16 v[38:39], v6 offset:34304\n"
"v_mfma_f32_16x16x32_bf16 v[104:107], v[40:43], a[72:75], v[104:107]\n"
"buffer_load_dwordx4 a[180:183], v3, s[12:15], s22 offen offset:3072 sc1\n"
"v_mfma_f32_16x16x32_bf16 v[108:111], v[40:43], a[76:79], v[108:111]\n"
"s_waitcnt lgkmcnt(12)\n"
"v_mfma_f32_16x16x32_bf16 v[112:115], v[44:47], a[64:67], v[112:115]\n"
"ds_read_b64_tr_b16 v[40:41], v6 offset:34816\n"
"v_mfma_f32_16x16x32_bf16 v[116:119], v[44:47], a[68:71], v[116:119]\n"
"ds_read_b64_tr_b16 v[42:43], v6 offset:35328\n"
"v_mfma_f32_16x16x32_bf16 v[120:123], v[44:47], a[72:75], v[120:123]\n"
"buffer_load_dwordx4 a[184:187], v4, s[12:15], s22 offen offset:3072 sc1\n"
"v_mfma_f32_16x16x32_bf16 v[124:127], v[44:47], a[76:79], v[124:127]\n"
"v_mfma_f32_16x16x32_bf16 v[128:131], v[12:15], a[64:67], v[128:131]\n"
"ds_read_b64_tr_b16 v[44:45], v6 offset:35840\n"
"v_mfma_f32_16x16x32_bf16 v[132:135], v[12:15], a[68:71], v[132:135]\n"
"ds_read_b64_tr_b16 v[46:47], v6 offset:36352\n"
"v_mfma_f32_16x16x32_bf16 v[136:139], v[12:15], a[72:75], v[136:139]\n"
"buffer_load_dwordx4 a[188:191], v5, s[12:15], s22 offen offset:3072 sc1\n"
"v_mfma_f32_16x16x32_bf16 v[140:143], v[12:15], a[76:79], v[140:143]\n"
"s_waitcnt lgkmcnt(12)\n"
"v_mfma_f32_16x16x32_bf16 v[144:147], v[16:19], a[64:67], v[144:147]\n"
"ds_read_b64_tr_b16 v[12:13], v6 offset:36864\n"
"v_mfma_f32_16x16x32_bf16 v[148:151], v[16:19], a[68:71], v[148:151]\n"
"ds_read_b64_tr_b16 v[14:15], v6 offset:37376\n"
"v_mfma_f32_16x16x32_bf16 v[152:155], v[16:19], a[72:75], v[152:155]\n"
"s_mov_b32 s22, 12288\n"
"buffer_load_dwordx4 a[192:195], v2, s[12:15], s22 offen sc1\n"
"v_mfma_f32_16x16x32_bf16 v[156:159], v[16:19], a[76:79], v[156:159]\n"
"v_mfma_f32_16x16x32_bf16 v[160:163], v[20:23], a[64:67], v[160:163]\n"
"ds_read_b64_tr_b16 v[16:17], v6 offset:37888\n"
"v_mfma_f32_16x16x32_bf16 v[164:167], v[20:23], a[68:71], v[164:167]\n"
"ds_read_b64_tr_b16 v[18:19], v6 offset:38400\n"
"v_mfma_f32_16x16x32_bf16 v[168:171], v[20:23], a[72:75], v[168:171]\n"
"buffer_load_dwordx4 a[196:199], v3, s[12:15], s22 offen sc1\n"
"v_mfma_f32_16x16x32_bf16 v[172:175], v[20:23], a[76:79], v[172:175]\n"
"s_waitcnt vmcnt(30)\n"
"s_waitcnt lgkmcnt(12)\n"
"v_mfma_f32_16x16x32_bf16 v[80:83], v[24:27], a[80:83], v[80:83]\n"
"ds_read_b64_tr_b16 v[20:21], v6 offset:38912\n"
"v_mfma_f32_16x16x32_bf16 v[84:87], v[24:27], a[84:87], v[84:87]\n"
"ds_read_b64_tr_b16 v[22:23], v6 offset:39424\n"
"v_mfma_f32_16x16x32_bf16 v[88:91], v[24:27], a[88:91], v[88:91]\n"
"buffer_load_dwordx4 a[200:203], v4, s[12:15], s22 offen sc1\n"
"v_mfma_f32_16x16x32_bf16 v[92:95], v[24:27], a[92:95], v[92:95]\n"
"v_mfma_f32_16x16x32_bf16 v[96:99], v[28:31], a[80:83], v[96:99]\n"
"ds_read_b64_tr_b16 v[24:25], v6 offset:39936\n"
"v_mfma_f32_16x16x32_bf16 v[100:103], v[28:31], a[84:87], v[100:103]\n"
"ds_read_b64_tr_b16 v[26:27], v6 offset:40448\n"
"v_mfma_f32_16x16x32_bf16 v[104:107], v[28:31], a[88:91], v[104:107]\n"
"buffer_load_dwordx4 a[204:207], v5, s[12:15], s22 offen sc1\n"
"v_mfma_f32_16x16x32_bf16 v[108:111], v[28:31], a[92:95], v[108:111]\n"
"s_waitcnt lgkmcnt(12)\n"
"v_mfma_f32_16x16x32_bf16 v[112:115], v[32:35], a[80:83], v[112:115]\n"
"ds_read_b64_tr_b16 v[28:29], v6 offset:40960\n"
"v_mfma_f32_16x16x32_bf16 v[116:119], v[32:35], a[84:87], v[116:119]\n"
"ds_read_b64_tr_b16 v[30:31], v6 offset:41472\n"
"v_mfma_f32_16x16x32_bf16 v[120:123], v[32:35], a[88:91], v[120:123]\n"
"buffer_load_dwordx4 a[208:211], v2, s[12:15], s22 offen offset:1024 sc1\n"
"v_mfma_f32_16x16x32_bf16 v[124:127], v[32:35], a[92:95], v[124:127]\n"
"v_mfma_f32_16x16x32_bf16 v[128:131], v[36:39], a[80:83], v[128:131]\n"
"ds_read_b64_tr_b16 v[32:33], v6 offset:41984\n"
"v_mfma_f32_16x16x32_bf16 v[132:135], v[36:39], a[84:87], v[132:135]\n"
"ds_read_b64_tr_b16 v[34:35], v6 offset:42496\n"
"v_mfma_f32_16x16x32_bf16 v[136:139], v[36:39], a[88:91], v[136:139]\n"
"buffer_load_dwordx4 a[212:215], v3, s[12:15], s22 offen offset:1024 sc1\n"
"v_mfma_f32_16x16x32_bf16 v[140:143], v[36:39], a[92:95], v[140:143]\n"
"s_waitcnt lgkmcnt(12)\n"
"v_mfma_f32_16x16x32_bf16 v[144:147], v[40:43], a[80:83], v[144:147]\n"
"ds_read_b64_tr_b16 v[36:37], v6 offset:43008\n"
"v_mfma_f32_16x16x32_bf16 v[148:151], v[40:43], a[84:87], v[148:151]\n"
"ds_read_b64_tr_b16 v[38:39], v6 offset:43520\n"
"v_mfma_f32_16x16x32_bf16 v[152:155], v[40:43], a[88:91], v[152:155]\n"
"buffer_load_dwordx4 a[216:219], v4, s[12:15], s22 offen offset:1024 sc1\n"
"v_mfma_f32_16x16x32_bf16 v[156:159], v[40:43], a[92:95], v[156:159]\n"
"v_mfma_f32_16x16x32_bf16 v[160:163], v[44:47], a[80:83], v[160:163]\n"
"ds_read_b64_tr_b16 v[40:41], v6 offset:44032\n"
"v_mfma_f32_16x16x32_bf16 v[164:167], v[44:47], a[84:87], v[164:167]\n"
"ds_read_b64_tr_b16 v[42:43], v6 offset:44544\n"
"v_mfma_f32_16x16x32_bf16 v[168:171], v[44:47], a[88:91], v[168:171]\n"
"buffer_load_dwordx4 a[220:223], v5, s[12:15], s22 offen offset:1024 sc1\n"
"v_mfma_f32_16x16x32_bf16 v[172:175], v[44:47], a[92:95], v[172:175]\n"
"s_waitcnt vmcnt(32)\n"
"s_waitcnt lgkmcnt(12)\n"
"v_mfma_f32_16x16x32_bf16 v[80:83], v[12:15], a[96:99], v[80:83]\n"
"ds_read_b64_tr_b16 v[44:45], v6 offset:45056\n"
"v_mfma_f32_16x16x32_bf16 v[84:87], v[12:15], a[100:103], v[84:87]\n"
"ds_read_b64_tr_b16 v[46:47], v6 offset:45568\n"
"v_mfma_f32_16x16x32_bf16 v[88:91], v[12:15], a[104:107], v[88:91]\n"
"s_add_u32 m0, s20, 98304\n"
"s_mov_b32 s21, 98304\n"
"buffer_load_dwordx4 v1, s[8:11], s21 offen sc1 lds\n"
"v_mfma_f32_16x16x32_bf16 v[92:95], v[12:15], a[108:111], v[92:95]\n"
"v_mfma_f32_16x16x32_bf16 v[96:99], v[16:19], a[96:99], v[96:99]\n"
"ds_read_b64_tr_b16 v[12:13], v6 offset:46080\n"
"v_mfma_f32_16x16x32_bf16 v[100:103], v[16:19], a[100:103], v[100:103]\n"
"ds_read_b64_tr_b16 v[14:15], v6 offset:46592\n"
"v_mfma_f32_16x16x32_bf16 v[104:107], v[16:19], a[104:107], v[104:107]\n"
"buffer_load_dwordx4 v1, s[8:11], s21 offen offset:1024 sc1 lds\n"
"v_mfma_f32_16x16x32_bf16 v[108:111], v[16:19], a[108:111], v[108:111]\n"
"s_waitcnt lgkmcnt(12)\n"
"v_mfma_f32_16x16x32_bf16 v[112:115], v[20:23], a[96:99], v[112:115]\n"
"ds_read_b64_tr_b16 v[16:17], v6 offset:47104\n"
"v_mfma_f32_16x16x32_bf16 v[116:119], v[20:23], a[100:103], v[116:119]\n"
"ds_read_b64_tr_b16 v[18:19], v6 offset:47616\n"
"v_mfma_f32_16x16x32_bf16 v[120:123], v[20:23], a[104:107], v[120:123]\n"
"buffer_load_dwordx4 v1, s[8:11], s21 offen offset:2048 sc1 lds\n"
"v_mfma_f32_16x16x32_bf16 v[124:127], v[20:23], a[108:111], v[124:127]\n"
"v_mfma_f32_16x16x32_bf16 v[128:131], v[24:27], a[96:99], v[128:131]\n"
"ds_read_b64_tr_b16 v[20:21], v6 offset:48128\n"
"v_mfma_f32_16x16x32_bf16 v[132:135], v[24:27], a[100:103], v[132:135]\n"
"ds_read_b64_tr_b16 v[22:23], v6 offset:48640\n"
"v_mfma_f32_16x16x32_bf16 v[136:139], v[24:27], a[104:107], v[136:139]\n"
"buffer_load_dwordx4 v1, s[8:11], s21 offen offset:3072 sc1 lds\n"
"v_mfma_f32_16x16x32_bf16 v[140:143], v[24:27], a[108:111], v[140:143]\n"
"s_barrier\n"
"s_waitcnt lgkmcnt(12)\n"
"v_mfma_f32_16x16x32_bf16 v[144:147], v[28:31], a[96:99], v[144:147]\n"
"ds_read_b64_tr_b16 v[24:25], v6 offset:49152\n"
"v_mfma_f32_16x16x32_bf16 v[148:151], v[28:31], a[100:103], v[148:151]\n"
"ds_read_b64_tr_b16 v[26:27], v6 offset:49664\n"
"v_mfma_f32_16x16x32_bf16 v[152:155], v[28:31], a[104:107], v[152:155]\n"
"buffer_load_dwordx4 a[224:227], v2, s[12:15], s22 offen offset:2048 sc1\n"
"v_mfma_f32_16x16x32_bf16 v[156:159], v[28:31], a[108:111], v[156:159]\n"
"v_mfma_f32_16x16x32_bf16 v[160:163], v[32:35], a[96:99], v[160:163]\n"
"ds_read_b64_tr_b16 v[28:29], v6 offset:50176\n"
"v_mfma_f32_16x16x32_bf16 v[164:167], v[32:35], a[100:103], v[164:167]\n"
"ds_read_b64_tr_b16 v[30:31], v6 offset:50688\n"
"v_mfma_f32_16x16x32_bf16 v[168:171], v[32:35], a[104:107], v[168:171]\n"
"buffer_load_dwordx4 a[228:231], v3, s[12:15], s22 offen offset:2048 sc1\n"
"v_mfma_f32_16x16x32_bf16 v[172:175], v[32:35], a[108:111], v[172:175]\n"
"s_waitcnt vmcnt(34)\n"
"s_waitcnt lgkmcnt(12)\n"
"v_mfma_f32_16x16x32_bf16 v[80:83], v[36:39], a[112:115], v[80:83]\n"
"ds_read_b64_tr_b16 v[32:33], v6 offset:51200\n"
"v_mfma_f32_16x16x32_bf16 v[84:87], v[36:39], a[116:119], v[84:87]\n"
"ds_read_b64_tr_b16 v[34:35], v6 offset:51712\n"
"v_mfma_f32_16x16x32_bf16 v[88:91], v[36:39], a[120:123], v[88:91]\n"
"buffer_load_dwordx4 a[232:235], v4, s[12:15], s22 offen offset:2048 sc1\n"
"v_mfma_f32_16x16x32_bf16 v[92:95], v[36:39], a[124:127], v[92:95]\n"
"v_mfma_f32_16x16x32_bf16 v[96:99], v[40:43], a[112:115], v[96:99]\n"
"ds_read_b64_tr_b16 v[36:37], v6 offset:52224\n"
"v_mfma_f32_16x16x32_bf16 v[100:103], v[40:43], a[116:119], v[100:103]\n"
"ds_read_b64_tr_b16 v[38:39], v6 offset:52736\n"
"v_mfma_f32_16x16x32_bf16 v[104:107], v[40:43], a[120:123], v[104:107]\n"
"buffer_load_dwordx4 a[236:239], v5, s[12:15], s22 offen offset:2048 sc1\n"
"v_mfma_f32_16x16x32_bf16 v[108:111], v[40:43], a[124:127], v[108:111]\n"
"s_waitcnt lgkmcnt(12)\n"
"v_mfma_f32_16x16x32_bf16 v[112:115], v[44:47], a[112:115], v[112:115]\n"
"ds_read_b64_tr_b16 v[40:41], v6 offset:53248\n"
"v_mfma_f32_16x16x32_bf16 v[116:119], v[44:47], a[116:119], v[116:119]\n"
"ds_read_b64_tr_b16 v[42:43], v6 offset:53760\n"
"v_mfma_f32_16x16x32_bf16 v[120:123], v[44:47], a[120:123], v[120:123]\n"
"buffer_load_dwordx4 a[240:243], v2, s[12:15], s22 offen offset:3072 sc1\n"
"v_mfma_f32_16x16x32_bf16 v[124:127], v[44:47], a[124:127], v[124:127]\n"
"v_mfma_f32_16x16x32_bf16 v[128:131], v[12:15], a[112:115], v[128:131]\n"
"ds_read_b64_tr_b16 v[44:45], v6 offset:54272\n"
"v_mfma_f32_16x16x32_bf16 v[132:135], v[12:15], a[116:119], v[132:135]\n"
"ds_read_b64_tr_b16 v[46:47], v6 offset:54784\n"
"v_mfma_f32_16x16x32_bf16 v[136:139], v[12:15], a[120:123], v[136:139]\n"
"buffer_load_dwordx4 a[244:247], v3, s[12:15], s22 offen offset:3072 sc1\n"
"v_mfma_f32_16x16x32_bf16 v[140:143], v[12:15], a[124:127], v[140:143]\n"
"s_waitcnt lgkmcnt(12)\n"
"v_mfma_f32_16x16x32_bf16 v[144:147], v[16:19], a[112:115], v[144:147]\n"
"ds_read_b64_tr_b16 v[12:13], v6 offset:55296\n"
"v_mfma_f32_16x16x32_bf16 v[148:151], v[16:19], a[116:119], v[148:151]\n"
"ds_read_b64_tr_b16 v[14:15], v6 offset:55808\n"
"v_mfma_f32_16x16x32_bf16 v[152:155], v[16:19], a[120:123], v[152:155]\n"
"buffer_load_dwordx4 a[248:251], v4, s[12:15], s22 offen offset:3072 sc1\n"
"v_mfma_f32_16x16x32_bf16 v[156:159], v[16:19], a[124:127], v[156:159]\n"
"v_mfma_f32_16x16x32_bf16 v[160:163], v[20:23], a[112:115], v[160:163]\n"
"ds_read_b64_tr_b16 v[16:17], v6 offset:56320\n"
"v_mfma_f32_16x16x32_bf16 v[164:167], v[20:23], a[116:119], v[164:167]\n"
"ds_read_b64_tr_b16 v[18:19], v6 offset:56832\n"
"v_mfma_f32_16x16x32_bf16 v[168:171], v[20:23], a[120:123], v[168:171]\n"
"buffer_load_dwordx4 a[252:255], v5, s[12:15], s22 offen offset:3072 sc1\n"
"v_mfma_f32_16x16x32_bf16 v[172:175], v[20:23], a[124:127], v[172:175]\n"
"s_waitcnt vmcnt(36)\n"
"s_waitcnt lgkmcnt(12)\n"
"v_mfma_f32_16x16x32_bf16 v[80:83], v[24:27], a[128:131], v[80:83]\n"
"ds_read_b64_tr_b16 v[20:21], v6 offset:57344\n"
"v_mfma_f32_16x16x32_bf16 v[84:87], v[24:27], a[132:135], v[84:87]\n"
"ds_read_b64_tr_b16 v[22:23], v6 offset:57856\n"
"v_mfma_f32_16x16x32_bf16 v[88:91], v[24:27], a[136:139], v[88:91]\n"
"s_add_u32 m0, s20, 114688\n"
"s_mov_b32 s21, 114688\n"
"buffer_load_dwordx4 v1, s[8:11], s21 offen sc1 lds\n"
"v_mfma_f32_16x16x32_bf16 v[92:95], v[24:27], a[140:143], v[92:95]\n"
"v_mfma_f32_16x16x32_bf16 v[96:99], v[28:31], a[128:131], v[96:99]\n"
"ds_read_b64_tr_b16 v[24:25], v6 offset:58368\n"
"v_mfma_f32_16x16x32_bf16 v[100:103], v[28:31], a[132:135], v[100:103]\n"
"ds_read_b64_tr_b16 v[26:27], v6 offset:58880\n"
"v_mfma_f32_16x16x32_bf16 v[104:107], v[28:31], a[136:139], v[104:107]\n"
"buffer_load_dwordx4 v1, s[8:11], s21 offen offset:1024 sc1 lds\n"
"v_mfma_f32_16x16x32_bf16 v[108:111], v[28:31], a[140:143], v[108:111]\n"
"s_waitcnt lgkmcnt(12)\n"
"v_mfma_f32_16x16x32_bf16 v[112:115], v[32:35], a[128:131], v[112:115]\n"
"ds_read_b64_tr_b16 v[28:29], v6 offset:59392\n"
"v_mfma_f32_16x16x32_bf16 v[116:119], v[32:35], a[132:135], v[116:119]\n"
"ds_read_b64_tr_b16 v[30:31], v6 offset:59904\n"
"v_mfma_f32_16x16x32_bf16 v[120:123], v[32:35], a[136:139], v[120:123]\n"
"buffer_load_dwordx4 v1, s[8:11], s21 offen offset:2048 sc1 lds\n"
"v_mfma_f32_16x16x32_bf16 v[124:127], v[32:35], a[140:143], v[124:127]\n"
"v_mfma_f32_16x16x32_bf16 v[128:131], v[36:39], a[128:131], v[128:131]\n"
"ds_read_b64_tr_b16 v[32:33], v6 offset:60416\n"
"v_mfma_f32_16x16x32_bf16 v[132:135], v[36:39], a[132:135], v[132:135]\n"
"ds_read_b64_tr_b16 v[34:35], v6 offset:60928\n"
"v_mfma_f32_16x16x32_bf16 v[136:139], v[36:39], a[136:139], v[136:139]\n"
"buffer_load_dwordx4 v1, s[8:11], s21 offen offset:3072 sc1 lds\n"
"v_mfma_f32_16x16x32_bf16 v[140:143], v[36:39], a[140:143], v[140:143]\n"
"s_waitcnt lgkmcnt(12)\n"
"v_mfma_f32_16x16x32_bf16 v[144:147], v[40:43], a[128:131], v[144:147]\n"
"ds_read_b64_tr_b16 v[36:37], v6 offset:61440\n"
"v_mfma_f32_16x16x32_bf16 v[148:151], v[40:43], a[132:135], v[148:151]\n"
"ds_read_b64_tr_b16 v[38:39], v6 offset:61952\n"
"v_mfma_f32_16x16x32_bf16 v[152:155], v[40:43], a[136:139], v[152:155]\n"
"s_mov_b32 s22, 16384\n"
"buffer_load_dwordx4 v[48:51], v2, s[12:15], s22 offen sc1\n"
"v_mfma_f32_16x16x32_bf16 v[156:159], v[40:43], a[140:143], v[156:159]\n"
"v_mfma_f32_16x16x32_bf16 v[160:163], v[44:47], a[128:131], v[160:163]\n"
"ds_read_b64_tr_b16 v[40:41], v6 offset:62464\n"
"v_mfma_f32_16x16x32_bf16 v[164:167], v[44:47], a[132:135], v[164:167]\n"
"ds_read_b64_tr_b16 v[42:43], v6 offset:62976\n"
"v_mfma_f32_16x16x32_bf16 v[168:171], v[44:47], a[136:139], v[168:171]\n"
"buffer_load_dwordx4 v[52:55], v3, s[12:15], s22 offen sc1\n"
"v_mfma_f32_16x16x32_bf16 v[172:175], v[44:47], a[140:143], v[172:175]\n"
"s_waitcnt vmcnt(38)\n"
"s_waitcnt lgkmcnt(12)\n"
"v_mfma_f32_16x16x32_bf16 v[80:83], v[12:15], a[144:147], v[80:83]\n"
"ds_read_b64_tr_b16 v[44:45], v6 offset:63488\n"
"v_mfma_f32_16x16x32_bf16 v[84:87], v[12:15], a[148:151], v[84:87]\n"
"ds_read_b64_tr_b16 v[46:47], v6 offset:64000\n"
"v_mfma_f32_16x16x32_bf16 v[88:91], v[12:15], a[152:155], v[88:91]\n"
"buffer_load_dwordx4 v[56:59], v4, s[12:15], s22 offen sc1\n"
"v_mfma_f32_16x16x32_bf16 v[92:95], v[12:15], a[156:159], v[92:95]\n"
"v_mfma_f32_16x16x32_bf16 v[96:99], v[16:19], a[144:147], v[96:99]\n"
"ds_read_b64_tr_b16 v[12:13], v6 offset:64512\n"
"v_mfma_f32_16x16x32_bf16 v[100:103], v[16:19], a[148:151], v[100:103]\n"
"ds_read_b64_tr_b16 v[14:15], v6 offset:65024\n"
"v_mfma_f32_16x16x32_bf16 v[104:107], v[16:19], a[152:155], v[104:107]\n"
"buffer_load_dwordx4 v[60:63], v5, s[12:15], s22 offen sc1\n"
"v_mfma_f32_16x16x32_bf16 v[108:111], v[16:19], a[156:159], v[108:111]\n"
"s_barrier\n"
"s_waitcnt lgkmcnt(12)\n"
"v_mfma_f32_16x16x32_bf16 v[112:115], v[20:23], a[144:147], v[112:115]\n"
"ds_read_b64_tr_b16 v[16:17], v7 offset:0\n"
"v_mfma_f32_16x16x32_bf16 v[116:119], v[20:23], a[148:151], v[116:119]\n"
"ds_read_b64_tr_b16 v[18:19], v7 offset:512\n"
"v_mfma_f32_16x16x32_bf16 v[120:123], v[20:23], a[152:155], v[120:123]\n"
"buffer_load_dwordx4 v[64:67], v2, s[12:15], s22 offen offset:1024 sc1\n"
"v_mfma_f32_16x16x32_bf16 v[124:127], v[20:23], a[156:159], v[124:127]\n"
"v_mfma_f32_16x16x32_bf16 v[128:131], v[24:27], a[144:147], v[128:131]\n"
"ds_read_b64_tr_b16 v[20:21], v7 offset:1024\n"
"v_mfma_f32_16x16x32_bf16 v[132:135], v[24:27], a[148:151], v[132:135]\n"
"ds_read_b64_tr_b16 v[22:23], v7 offset:1536\n"
"v_mfma_f32_16x16x32_bf16 v[136:139], v[24:27], a[152:155], v[136:139]\n"
"buffer_load_dwordx4 v[68:71], v3, s[12:15], s22 offen offset:1024 sc1\n"
"v_mfma_f32_16x16x32_bf16 v[140:143], v[24:27], a[156:159], v[140:143]\n"
"s_waitcnt lgkmcnt(12)\n"
"v_mfma_f32_16x16x32_bf16 v[144:147], v[28:31], a[144:147], v[144:147]\n"
"ds_read_b64_tr_b16 v[24:25], v7 offset:2048\n"
"v_mfma_f32_16x16x32_bf16 v[148:151], v[28:31], a[148:151], v[148:151]\n"
"ds_read_b64_tr_b16 v[26:27], v7 offset:2560\n"
"v_mfma_f32_16x16x32_bf16 v[152:155], v[28:31], a[152:155], v[152:155]\n"
"buffer_load_dwordx4 v[72:75], v4, s[12:15], s22 offen offset:1024 sc1\n"
"v_mfma_f32_16x16x32_bf16 v[156:159], v[28:31], a[156:159], v[156:159]\n"
"v_mfma_f32_16x16x32_bf16 v[160:163], v[32:35], a[144:147], v[160:163]\n"
"ds_read_b64_tr_b16 v[28:29], v7 offset:3072\n"
"v_mfma_f32_16x16x32_bf16 v[164:167], v[32:35], a[148:151], v[164:167]\n"
"ds_read_b64_tr_b16 v[30:31], v7 offset:3584\n"
"v_mfma_f32_16x16x32_bf16 v[168:171], v[32:35], a[152:155], v[168:171]\n"
"buffer_load_dwordx4 v[76:79], v5, s[12:15], s22 offen offset:1024 sc1\n"
"v_mfma_f32_16x16x32_bf16 v[172:175], v[32:35], a[156:159], v[172:175]\n"
"s_waitcnt vmcnt(40)\n"
"s_waitcnt lgkmcnt(12)\n"
"v_mfma_f32_16x16x32_bf16 v[80:83], v[36:39], a[160:163], v[80:83]\n"
"ds_read_b64_tr_b16 v[32:33], v7 offset:4096\n"
"v_mfma_f32_16x16x32_bf16 v[84:87], v[36:39], a[164:167], v[84:87]\n"
"ds_read_b64_tr_b16 v[34:35], v7 offset:4608\n"
"v_mfma_f32_16x16x32_bf16 v[88:91], v[36:39], a[168:171], v[88:91]\n"
"s_add_u32 m0, s20, 131072\n"
"s_mov_b32 s21, 131072\n"
"buffer_load_dwordx4 v1, s[8:11], s21 offen sc1 lds\n"
"v_mfma_f32_16x16x32_bf16 v[92:95], v[36:39], a[172:175], v[92:95]\n"
"v_mfma_f32_16x16x32_bf16 v[96:99], v[40:43], a[160:163], v[96:99]\n"
"ds_read_b64_tr_b16 v[36:37], v7 offset:5120\n"
"v_mfma_f32_16x16x32_bf16 v[100:103], v[40:43], a[164:167], v[100:103]\n"
"ds_read_b64_tr_b16 v[38:39], v7 offset:5632\n"
"v_mfma_f32_16x16x32_bf16 v[104:107], v[40:43], a[168:171], v[104:107]\n"
"buffer_load_dwordx4 v1, s[8:11], s21 offen offset:1024 sc1 lds\n"
"v_mfma_f32_16x16x32_bf16 v[108:111], v[40:43], a[172:175], v[108:111]\n"
"s_waitcnt lgkmcnt(12)\n"
"v_mfma_f32_16x16x32_bf16 v[112:115], v[44:47], a[160:163], v[112:115]\n"
"ds_read_b64_tr_b16 v[40:41], v7 offset:6144\n"
"v_mfma_f32_16x16x32_bf16 v[116:119], v[44:47], a[164:167], v[116:119]\n"
"ds_read_b64_tr_b16 v[42:43], v7 offset:6656\n"
"v_mfma_f32_16x16x32_bf16 v[120:123], v[44:47], a[168:171], v[120:123]\n"
"buffer_load_dwordx4 v1, s[8:11], s21 offen offset:2048 sc1 lds\n"
"v_mfma_f32_16x16x32_bf16 v[124:127], v[44:47], a[172:175], v[124:127]\n"
"v_mfma_f32_16x16x32_bf16 v[128:131], v[12:15], a[160:163], v[128:131]\n"
"ds_read_b64_tr_b16 v[44:45], v7 offset:7168\n"
"v_mfma_f32_16x16x32_bf16 v[132:135], v[12:15], a[164:167], v[132:135]\n"
"ds_read_b64_tr_b16 v[46:47], v7 offset:7680\n"
"v_mfma_f32_16x16x32_bf16 v[136:139], v[12:15], a[168:171], v[136:139]\n"
"buffer_load_dwordx4 v1, s[8:11], s21 offen offset:3072 sc1 lds\n"
"v_mfma_f32_16x16x32_bf16 v[140:143], v[12:15], a[172:175], v[140:143]\n"
"s_waitcnt lgkmcnt(12)\n"
"v_mfma_f32_16x16x32_bf16 v[144:147], v[16:19], a[160:163], v[144:147]\n"
"ds_read_b64_tr_b16 v[12:13], v7 offset:8192\n"
"v_mfma_f32_16x16x32_bf16 v[148:151], v[16:19], a[164:167], v[148:151]\n"
"ds_read_b64_tr_b16 v[14:15], v7 offset:8704\n"
"v_mfma_f32_16x16x32_bf16 v[152:155], v[16:19], a[168:171], v[152:155]\n"
"s_add_u32 m0, s20, 0\n"
"s_mov_b32 s21, 147456\n"
"buffer_load_dwordx4 v1, s[8:11], s21 offen sc1 lds\n"
"v_mfma_f32_16x16x32_bf16 v[156:159], v[16:19], a[172:175], v[156:159]\n"
"v_mfma_f32_16x16x32_bf16 v[160:163], v[20:23], a[160:163], v[160:163]\n"
"ds_read_b64_tr_b16 v[16:17], v7 offset:9216\n"
"v_mfma_f32_16x16x32_bf16 v[164:167], v[20:23], a[164:167], v[164:167]\n"
"ds_read_b64_tr_b16 v[18:19], v7 offset:9728\n"
"v_mfma_f32_16x16x32_bf16 v[168:171], v[20:23], a[168:171], v[168:171]\n"
"buffer_load_dwordx4 v1, s[8:11], s21 offen offset:1024 sc1 lds\n"
"v_mfma_f32_16x16x32_bf16 v[172:175], v[20:23], a[172:175], v[172:175]\n"
"s_waitcnt vmcnt(38)\n"
"s_waitcnt lgkmcnt(12)\n"
"v_mfma_f32_16x16x32_bf16 v[80:83], v[24:27], a[176:179], v[80:83]\n"
"ds_read_b64_tr_b16 v[20:21], v7 offset:10240\n"
"v_mfma_f32_16x16x32_bf16 v[84:87], v[24:27], a[180:183], v[84:87]\n"
"ds_read_b64_tr_b16 v[22:23], v7 offset:10752\n"
"v_mfma_f32_16x16x32_bf16 v[88:91], v[24:27], a[184:187], v[88:91]\n"
"buffer_load_dwordx4 v1, s[8:11], s21 offen offset:2048 sc1 lds\n"
"v_mfma_f32_16x16x32_bf16 v[92:95], v[24:27], a[188:191], v[92:95]\n"
"v_mfma_f32_16x16x32_bf16 v[96:99], v[28:31], a[176:179], v[96:99]\n"
"ds_read_b64_tr_b16 v[24:25], v7 offset:11264\n"
"v_mfma_f32_16x16x32_bf16 v[100:103], v[28:31], a[180:183], v[100:103]\n"
"ds_read_b64_tr_b16 v[26:27], v7 offset:11776\n"
"v_mfma_f32_16x16x32_bf16 v[104:107], v[28:31], a[184:187], v[104:107]\n"
"buffer_load_dwordx4 v1, s[8:11], s21 offen offset:3072 sc1 lds\n"
"v_mfma_f32_16x16x32_bf16 v[108:111], v[28:31], a[188:191], v[108:111]\n"
"s_waitcnt lgkmcnt(12)\n"
"v_mfma_f32_16x16x32_bf16 v[112:115], v[32:35], a[176:179], v[112:115]\n"
"ds_read_b64_tr_b16 v[28:29], v7 offset:12288\n"
"v_mfma_f32_16x16x32_bf16 v[116:119], v[32:35], a[180:183], v[116:119]\n"
"ds_read_b64_tr_b16 v[30:31], v7 offset:12800\n"
"v_mfma_f32_16x16x32_bf16 v[120:123], v[32:35], a[184:187], v[120:123]\n"
"s_add_u32 m0, s20, 16384\n"
"s_mov_b32 s21, 163840\n"
"buffer_load_dwordx4 v1, s[8:11], s21 offen sc1 lds\n"
"v_mfma_f32_16x16x32_bf16 v[124:127], v[32:35], a[188:191], v[124:127]\n"
"v_mfma_f32_16x16x32_bf16 v[128:131], v[36:39], a[176:179], v[128:131]\n"
"ds_read_b64_tr_b16 v[32:33], v7 offset:13312\n"
"v_mfma_f32_16x16x32_bf16 v[132:135], v[36:39], a[180:183], v[132:135]\n"
"ds_read_b64_tr_b16 v[34:35], v7 offset:13824\n"
"v_mfma_f32_16x16x32_bf16 v[136:139], v[36:39], a[184:187], v[136:139]\n"
"buffer_load_dwordx4 v1, s[8:11], s21 offen offset:1024 sc1 lds\n"
"v_mfma_f32_16x16x32_bf16 v[140:143], v[36:39], a[188:191], v[140:143]\n"
"s_waitcnt lgkmcnt(12)\n"
"v_mfma_f32_16x16x32_bf16 v[144:147], v[40:43], a[176:179], v[144:147]\n"
"ds_read_b64_tr_b16 v[36:37], v7 offset:14336\n"
"v_mfma_f32_16x16x32_bf16 v[148:151], v[40:43], a[180:183], v[148:151]\n"
"ds_read_b64_tr_b16 v[38:39], v7 offset:14848\n"
"v_mfma_f32_16x16x32_bf16 v[152:155], v[40:43], a[184:187], v[152:155]\n"
"buffer_load_dwordx4 v1, s[8:11], s21 offen offset:2048 sc1 lds\n"
"v_mfma_f32_16x16x32_bf16 v[156:159], v[40:43], a[188:191], v[156:159]\n"
"v_mfma_f32_16x16x32_bf16 v[160:163], v[44:47], a[176:179], v[160:163]\n"
"ds_read_b64_tr_b16 v[40:41], v7 offset:15360\n"
"v_mfma_f32_16x16x32_bf16 v[164:167], v[44:47], a[180:183], v[164:167]\n"
"ds_read_b64_tr_b16 v[42:43], v7 offset:15872\n"
"v_mfma_f32_16x16x32_bf16 v[168:171], v[44:47], a[184:187], v[168:171]\n"
"buffer_load_dwordx4 v1, s[8:11], s21 offen offset:3072 sc1 lds\n"
"v_mfma_f32_16x16x32_bf16 v[172:175], v[44:47], a[188:191], v[172:175]\n"
"s_barrier\n"
"s_waitcnt vmcnt(40)\n"
"s_waitcnt lgkmcnt(12)\n"
"v_mfma_f32_16x16x32_bf16 v[80:83], v[12:15], a[192:195], v[80:83]\n"
"ds_read_b64_tr_b16 v[44:45], v7 offset:16384\n"
"v_mfma_f32_16x16x32_bf16 v[84:87], v[12:15], a[196:199], v[84:87]\n"
"ds_read_b64_tr_b16 v[46:47], v7 offset:16896\n"
"v_mfma_f32_16x16x32_bf16 v[88:91], v[12:15], a[200:203], v[88:91]\n"
"s_add_u32 m0, s20, 32768\n"
"s_mov_b32 s21, 180224\n"
"buffer_load_dwordx4 v1, s[8:11], s21 offen sc1 lds\n"
"v_mfma_f32_16x16x32_bf16 v[92:95], v[12:15], a[204:207], v[92:95]\n"
"v_mfma_f32_16x16x32_bf16 v[96:99], v[16:19], a[192:195], v[96:99]\n"
"ds_read_b64_tr_b16 v[12:13], v7 offset:17408\n"
"v_mfma_f32_16x16x32_bf16 v[100:103], v[16:19], a[196:199], v[100:103]\n"
"ds_read_b64_tr_b16 v[14:15], v7 offset:17920\n"
"v_mfma_f32_16x16x32_bf16 v[104:107], v[16:19], a[200:203], v[104:107]\n"
"buffer_load_dwordx4 v1, s[8:11], s21 offen offset:1024 sc1 lds\n"
"v_mfma_f32_16x16x32_bf16 v[108:111], v[16:19], a[204:207], v[108:111]\n"
"s_waitcnt lgkmcnt(12)\n"
"v_mfma_f32_16x16x32_bf16 v[112:115], v[20:23], a[192:195], v[112:115]\n"
"ds_read_b64_tr_b16 v[16:17], v7 offset:18432\n"
"v_mfma_f32_16x16x32_bf16 v[116:119], v[20:23], a[196:199], v[116:119]\n"
"ds_read_b64_tr_b16 v[18:19], v7 offset:18944\n"
"v_mfma_f32_16x16x32_bf16 v[120:123], v[20:23], a[200:203], v[120:123]\n"
"buffer_load_dwordx4 v1, s[8:11], s21 offen offset:2048 sc1 lds\n"
"v_mfma_f32_16x16x32_bf16 v[124:127], v[20:23], a[204:207], v[124:127]\n"
"v_mfma_f32_16x16x32_bf16 v[128:131], v[24:27], a[192:195], v[128:131]\n"
"ds_read_b64_tr_b16 v[20:21], v7 offset:19456\n"
"v_mfma_f32_16x16x32_bf16 v[132:135], v[24:27], a[196:199], v[132:135]\n"
"ds_read_b64_tr_b16 v[22:23], v7 offset:19968\n"
"v_mfma_f32_16x16x32_bf16 v[136:139], v[24:27], a[200:203], v[136:139]\n"
"buffer_load_dwordx4 v1, s[8:11], s21 offen offset:3072 sc1 lds\n"
"v_mfma_f32_16x16x32_bf16 v[140:143], v[24:27], a[204:207], v[140:143]\n"
"s_waitcnt lgkmcnt(12)\n"
"v_mfma_f32_16x16x32_bf16 v[144:147], v[28:31], a[192:195], v[144:147]\n"
"ds_read_b64_tr_b16 v[24:25], v7 offset:20480\n"
"v_mfma_f32_16x16x32_bf16 v[148:151], v[28:31], a[196:199], v[148:151]\n"
"ds_read_b64_tr_b16 v[26:27], v7 offset:20992\n"
"v_mfma_f32_16x16x32_bf16 v[152:155], v[28:31], a[200:203], v[152:155]\n"
"s_add_u32 m0, s20, 49152\n"
"s_mov_b32 s21, 196608\n"
"buffer_load_dwordx4 v1, s[8:11], s21 offen sc1 lds\n"
"v_mfma_f32_16x16x32_bf16 v[156:159], v[28:31], a[204:207], v[156:159]\n"
"v_mfma_f32_16x16x32_bf16 v[160:163], v[32:35], a[192:195], v[160:163]\n"
"ds_read_b64_tr_b16 v[28:29], v7 offset:21504\n"
"v_mfma_f32_16x16x32_bf16 v[164:167], v[32:35], a[196:199], v[164:167]\n"
"ds_read_b64_tr_b16 v[30:31], v7 offset:22016\n"
"v_mfma_f32_16x16x32_bf16 v[168:171], v[32:35], a[200:203], v[168:171]\n"
"buffer_load_dwordx4 v1, s[8:11], s21 offen offset:1024 sc1 lds\n"
"v_mfma_f32_16x16x32_bf16 v[172:175], v[32:35], a[204:207], v[172:175]\n"
"s_waitcnt vmcnt(42)\n"
"s_waitcnt lgkmcnt(12)\n"
"v_mfma_f32_16x16x32_bf16 v[80:83], v[36:39], a[208:211], v[80:83]\n"
"ds_read_b64_tr_b16 v[32:33], v7 offset:22528\n"
"v_mfma_f32_16x16x32_bf16 v[84:87], v[36:39], a[212:215], v[84:87]\n"
"ds_read_b64_tr_b16 v[34:35], v7 offset:23040\n"
"v_mfma_f32_16x16x32_bf16 v[88:91], v[36:39], a[216:219], v[88:91]\n"
"buffer_load_dwordx4 v1, s[8:11], s21 offen offset:2048 sc1 lds\n"
"v_mfma_f32_16x16x32_bf16 v[92:95], v[36:39], a[220:223], v[92:95]\n"
"v_mfma_f32_16x16x32_bf16 v[96:99], v[40:43], a[208:211], v[96:99]\n"
"ds_read_b64_tr_b16 v[36:37], v7 offset:23552\n"
"v_mfma_f32_16x16x32_bf16 v[100:103], v[40:43], a[212:215], v[100:103]\n"
"ds_read_b64_tr_b16 v[38:39], v7 offset:24064\n"
"v_mfma_f32_16x16x32_bf16 v[104:107], v[40:43], a[216:219], v[104:107]\n"
"buffer_load_dwordx4 v1, s[8:11], s21 offen offset:3072 sc1 lds\n"
"v_mfma_f32_16x16x32_bf16 v[108:111], v[40:43], a[220:223], v[108:111]\n"
"s_waitcnt lgkmcnt(12)\n"
"v_mfma_f32_16x16x32_bf16 v[112:115], v[44:47], a[208:211], v[112:115]\n"
"ds_read_b64_tr_b16 v[40:41], v7 offset:24576\n"
"v_mfma_f32_16x16x32_bf16 v[116:119], v[44:47], a[212:215], v[116:119]\n"
"ds_read_b64_tr_b16 v[42:43], v7 offset:25088\n"
"v_mfma_f32_16x16x32_bf16 v[120:123], v[44:47], a[216:219], v[120:123]\n"
"v_mfma_f32_16x16x32_bf16 v[124:127], v[44:47], a[220:223], v[124:127]\n"
"v_mfma_f32_16x16x32_bf16 v[128:131], v[12:15], a[208:211], v[128:131]\n"
"ds_read_b64_tr_b16 v[44:45], v7 offset:25600\n"
"v_mfma_f32_16x16x32_bf16 v[132:135], v[12:15], a[212:215], v[132:135]\n"
"ds_read_b64_tr_b16 v[46:47], v7 offset:26112\n"
"v_mfma_f32_16x16x32_bf16 v[136:139], v[12:15], a[216:219], v[136:139]\n"
"v_mfma_f32_16x16x32_bf16 v[140:143], v[12:15], a[220:223], v[140:143]\n"
"s_waitcnt lgkmcnt(12)\n"
"v_mfma_f32_16x16x32_bf16 v[144:147], v[16:19], a[208:211], v[144:147]\n"
"ds_read_b64_tr_b16 v[12:13], v7 offset:26624\n"
"v_mfma_f32_16x16x32_bf16 v[148:151], v[16:19], a[212:215], v[148:151]\n"
"ds_read_b64_tr_b16 v[14:15], v7 offset:27136\n"
"v_mfma_f32_16x16x32_bf16 v[152:155], v[16:19], a[216:219], v[152:155]\n"
"v_mfma_f32_16x16x32_bf16 v[156:159], v[16:19], a[220:223], v[156:159]\n"
"v_mfma_f32_16x16x32_bf16 v[160:163], v[20:23], a[208:211], v[160:163]\n"
"ds_read_b64_tr_b16 v[16:17], v7 offset:27648\n"
"v_mfma_f32_16x16x32_bf16 v[164:167], v[20:23], a[212:215], v[164:167]\n"
"ds_read_b64_tr_b16 v[18:19], v7 offset:28160\n"
"v_mfma_f32_16x16x32_bf16 v[168:171], v[20:23], a[216:219], v[168:171]\n"
"v_mfma_f32_16x16x32_bf16 v[172:175], v[20:23], a[220:223], v[172:175]\n"
"s_waitcnt vmcnt(36)\n"
"s_waitcnt lgkmcnt(12)\n"
"v_mfma_f32_16x16x32_bf16 v[80:83], v[24:27], a[224:227], v[80:83]\n"
"ds_read_b64_tr_b16 v[20:21], v7 offset:28672\n"
"v_mfma_f32_16x16x32_bf16 v[84:87], v[24:27], a[228:231], v[84:87]\n"
"ds_read_b64_tr_b16 v[22:23], v7 offset:29184\n"
"v_mfma_f32_16x16x32_bf16 v[88:91], v[24:27], a[232:235], v[88:91]\n"
"v_mfma_f32_16x16x32_bf16 v[92:95], v[24:27], a[236:239], v[92:95]\n"
"v_mfma_f32_16x16x32_bf16 v[96:99], v[28:31], a[224:227], v[96:99]\n"
"ds_read_b64_tr_b16 v[24:25], v7 offset:29696\n"
"v_mfma_f32_16x16x32_bf16 v[100:103], v[28:31], a[228:231], v[100:103]\n"
"ds_read_b64_tr_b16 v[26:27], v7 offset:30208\n"
"v_mfma_f32_16x16x32_bf16 v[104:107], v[28:31], a[232:235], v[104:107]\n"
"v_mfma_f32_16x16x32_bf16 v[108:111], v[28:31], a[236:239], v[108:111]\n"
"s_waitcnt lgkmcnt(12)\n"
"v_mfma_f32_16x16x32_bf16 v[112:115], v[32:35], a[224:227], v[112:115]\n"
"ds_read_b64_tr_b16 v[28:29], v7 offset:30720\n"
"v_mfma_f32_16x16x32_bf16 v[116:119], v[32:35], a[228:231], v[116:119]\n"
"ds_read_b64_tr_b16 v[30:31], v7 offset:31232\n"
"v_mfma_f32_16x16x32_bf16 v[120:123], v[32:35], a[232:235], v[120:123]\n"
"v_mfma_f32_16x16x32_bf16 v[124:127], v[32:35], a[236:239], v[124:127]\n"
"v_mfma_f32_16x16x32_bf16 v[128:131], v[36:39], a[224:227], v[128:131]\n"
"ds_read_b64_tr_b16 v[32:33], v7 offset:31744\n"
"v_mfma_f32_16x16x32_bf16 v[132:135], v[36:39], a[228:231], v[132:135]\n"
"ds_read_b64_tr_b16 v[34:35], v7 offset:32256\n"
"v_mfma_f32_16x16x32_bf16 v[136:139], v[36:39], a[232:235], v[136:139]\n"
"v_mfma_f32_16x16x32_bf16 v[140:143], v[36:39], a[236:239], v[140:143]\n"
"s_barrier\n"
"s_waitcnt lgkmcnt(12)\n"
"v_mfma_f32_16x16x32_bf16 v[144:147], v[40:43], a[224:227], v[144:147]\n"
"ds_read_b64_tr_b16 v[36:37], v7 offset:32768\n"
"v_mfma_f32_16x16x32_bf16 v[148:151], v[40:43], a[228:231], v[148:151]\n"
"ds_read_b64_tr_b16 v[38:39], v7 offset:33280\n"
"v_mfma_f32_16x16x32_bf16 v[152:155], v[40:43], a[232:235], v[152:155]\n"
"s_add_u32 m0, s20, 65536\n"
"s_mov_b32 s21, 212992\n"
"buffer_load_dwordx4 v1, s[8:11], s21 offen sc1 lds\n"
"v_mfma_f32_16x16x32_bf16 v[156:159], v[40:43], a[236:239], v[156:159]\n"
"v_mfma_f32_16x16x32_bf16 v[160:163], v[44:47], a[224:227], v[160:163]\n"
"ds_read_b64_tr_b16 v[40:41], v7 offset:33792\n"
"v_mfma_f32_16x16x32_bf16 v[164:167], v[44:47], a[228:231], v[164:167]\n"
"ds_read_b64_tr_b16 v[42:43], v7 offset:34304\n"
"v_mfma_f32_16x16x32_bf16 v[168:171], v[44:47], a[232:235], v[168:171]\n"
"buffer_load_dwordx4 v1, s[8:11], s21 offen offset:1024 sc1 lds\n"
"v_mfma_f32_16x16x32_bf16 v[172:175], v[44:47], a[236:239], v[172:175]\n"
"s_waitcnt vmcnt(34)\n"
"s_waitcnt lgkmcnt(12)\n"
"v_mfma_f32_16x16x32_bf16 v[80:83], v[12:15], a[240:243], v[80:83]\n"
"ds_read_b64_tr_b16 v[44:45], v7 offset:34816\n"
"v_mfma_f32_16x16x32_bf16 v[84:87], v[12:15], a[244:247], v[84:87]\n"
"ds_read_b64_tr_b16 v[46:47], v7 offset:35328\n"
"v_mfma_f32_16x16x32_bf16 v[88:91], v[12:15], a[248:251], v[88:91]\n"
"buffer_load_dwordx4 v1, s[8:11], s21 offen offset:2048 sc1 lds\n"
"v_mfma_f32_16x16x32_bf16 v[92:95], v[12:15], a[252:255], v[92:95]\n"
"v_mfma_f32_16x16x32_bf16 v[96:99], v[16:19], a[240:243], v[96:99]\n"
"ds_read_b64_tr_b16 v[12:13], v7 offset:35840\n"
"v_mfma_f32_16x16x32_bf16 v[100:103], v[16:19], a[244:247], v[100:103]\n"
"ds_read_b64_tr_b16 v[14:15], v7 offset:36352\n"
"v_mfma_f32_16x16x32_bf16 v[104:107], v[16:19], a[248:251], v[104:107]\n"
"buffer_load_dwordx4 v1, s[8:11], s21 offen offset:3072 sc1 lds\n"
"v_mfma_f32_16x16x32_bf16 v[108:111], v[16:19], a[252:255], v[108:111]\n"
"s_waitcnt lgkmcnt(12)\n"
"v_mfma_f32_16x16x32_bf16 v[112:115], v[20:23], a[240:243], v[112:115]\n"
"ds_read_b64_tr_b16 v[16:17], v7 offset:36864\n"
"v_mfma_f32_16x16x32_bf16 v[116:119], v[20:23], a[244:247], v[116:119]\n"
"ds_read_b64_tr_b16 v[18:19], v7 offset:37376\n"
"v_mfma_f32_16x16x32_bf16 v[120:123], v[20:23], a[248:251], v[120:123]\n"
"v_mfma_f32_16x16x32_bf16 v[124:127], v[20:23], a[252:255], v[124:127]\n"
"v_mfma_f32_16x16x32_bf16 v[128:131], v[24:27], a[240:243], v[128:131]\n"
"ds_read_b64_tr_b16 v[20:21], v7 offset:37888\n"
"v_mfma_f32_16x16x32_bf16 v[132:135], v[24:27], a[244:247], v[132:135]\n"
"ds_read_b64_tr_b16 v[22:23], v7 offset:38400\n"
"v_mfma_f32_16x16x32_bf16 v[136:139], v[24:27], a[248:251], v[136:139]\n"
"v_mfma_f32_16x16x32_bf16 v[140:143], v[24:27], a[252:255], v[140:143]\n"
"s_waitcnt lgkmcnt(12)\n"
"v_mfma_f32_16x16x32_bf16 v[144:147], v[28:31], a[240:243], v[144:147]\n"
"ds_read_b64_tr_b16 v[24:25], v7 offset:38912\n"
"v_mfma_f32_16x16x32_bf16 v[148:151], v[28:31], a[244:247], v[148:151]\n"
"ds_read_b64_tr_b16 v[26:27], v7 offset:39424\n"
"v_mfma_f32_16x16x32_bf16 v[152:155], v[28:31], a[248:251], v[152:155]\n"
"v_mfma_f32_16x16x32_bf16 v[156:159], v[28:31], a[252:255], v[156:159]\n"
"v_mfma_f32_16x16x32_bf16 v[160:163], v[32:35], a[240:243], v[160:163]\n"
"ds_read_b64_tr_b16 v[28:29], v7 offset:39936\n"
"v_mfma_f32_16x16x32_bf16 v[164:167], v[32:35], a[244:247], v[164:167]\n"
"ds_read_b64_tr_b16 v[30:31], v7 offset:40448\n"
"v_mfma_f32_16x16x32_bf16 v[168:171], v[32:35], a[248:251], v[168:171]\n"
"v_mfma_f32_16x16x32_bf16 v[172:175], v[32:35], a[252:255], v[172:175]\n"
"s_waitcnt vmcnt(28)\n"
"s_waitcnt lgkmcnt(12)\n"
"v_mfma_f32_16x16x32_bf16 v[80:83], v[36:39], v[48:51], v[80:83]\n"
"ds_read_b64_tr_b16 v[32:33], v7 offset:40960\n"
"v_mfma_f32_16x16x32_bf16 v[84:87], v[36:39], v[52:55], v[84:87]\n"
"ds_read_b64_tr_b16 v[34:35], v7 offset:41472\n"
"v_mfma_f32_16x16x32_bf16 v[88:91], v[36:39], v[56:59], v[88:91]\n"
"v_mfma_f32_16x16x32_bf16 v[92:95], v[36:39], v[60:63], v[92:95]\n"
"v_mfma_f32_16x16x32_bf16 v[96:99], v[40:43], v[48:51], v[96:99]\n"
"ds_read_b64_tr_b16 v[36:37], v7 offset:41984\n"
"v_mfma_f32_16x16x32_bf16 v[100:103], v[40:43], v[52:55], v[100:103]\n"
"ds_read_b64_tr_b16 v[38:39], v7 offset:42496\n"
"v_mfma_f32_16x16x32_bf16 v[104:107], v[40:43], v[56:59], v[104:107]\n"
"v_mfma_f32_16x16x32_bf16 v[108:111], v[40:43], v[60:63], v[108:111]\n"
"s_waitcnt lgkmcnt(12)\n"
"v_mfma_f32_16x16x32_bf16 v[112:115], v[44:47], v[48:51], v[112:115]\n"
"ds_read_b64_tr_b16 v[40:41], v7 offset:43008\n"
"v_mfma_f32_16x16x32_bf16 v[116:119], v[44:47], v[52:55], v[116:119]\n"
"ds_read_b64_tr_b16 v[42:43], v7 offset:43520\n"
"v_mfma_f32_16x16x32_bf16 v[120:123], v[44:47], v[56:59], v[120:123]\n"
"v_mfma_f32_16x16x32_bf16 v[124:127], v[44:47], v[60:63], v[124:127]\n"
"v_mfma_f32_16x16x32_bf16 v[128:131], v[12:15], v[48:51], v[128:131]\n"
"ds_read_b64_tr_b16 v[44:45], v7 offset:44032\n"
"v_mfma_f32_16x16x32_bf16 v[132:135], v[12:15], v[52:55], v[132:135]\n"
"ds_read_b64_tr_b16 v[46:47], v7 offset:44544\n"
"v_mfma_f32_16x16x32_bf16 v[136:139], v[12:15], v[56:59], v[136:139]\n"
"v_mfma_f32_16x16x32_bf16 v[140:143], v[12:15], v[60:63], v[140:143]\n"
"s_waitcnt lgkmcnt(12)\n"
"v_mfma_f32_16x16x32_bf16 v[144:147], v[16:19], v[48:51], v[144:147]\n"
"ds_read_b64_tr_b16 v[12:13], v7 offset:45056\n"
"v_mfma_f32_16x16x32_bf16 v[148:151], v[16:19], v[52:55], v[148:151]\n"
"ds_read_b64_tr_b16 v[14:15], v7 offset:45568\n"
"v_mfma_f32_16x16x32_bf16 v[152:155], v[16:19], v[56:59], v[152:155]\n"
"v_mfma_f32_16x16x32_bf16 v[156:159], v[16:19], v[60:63], v[156:159]\n"
"v_mfma_f32_16x16x32_bf16 v[160:163], v[20:23], v[48:51], v[160:163]\n"
"ds_read_b64_tr_b16 v[16:17], v7 offset:46080\n"
"v_mfma_f32_16x16x32_bf16 v[164:167], v[20:23], v[52:55], v[164:167]\n"
"ds_read_b64_tr_b16 v[18:19], v7 offset:46592\n"
"v_mfma_f32_16x16x32_bf16 v[168:171], v[20:23], v[56:59], v[168:171]\n"
"v_mfma_f32_16x16x32_bf16 v[172:175], v[20:23], v[60:63], v[172:175]\n"
"s_waitcnt vmcnt(24)\n"
"s_waitcnt lgkmcnt(12)\n"
"v_mfma_f32_16x16x32_bf16 v[80:83], v[24:27], v[64:67], v[80:83]\n"
"ds_read_b64_tr_b16 v[20:21], v7 offset:47104\n"
"v_mfma_f32_16x16x32_bf16 v[84:87], v[24:27], v[68:71], v[84:87]\n"
"ds_read_b64_tr_b16 v[22:23], v7 offset:47616\n"
"v_mfma_f32_16x16x32_bf16 v[88:91], v[24:27], v[72:75], v[88:91]\n"
"v_mfma_f32_16x16x32_bf16 v[92:95], v[24:27], v[76:79], v[92:95]\n"
"v_mfma_f32_16x16x32_bf16 v[96:99], v[28:31], v[64:67], v[96:99]\n"
"ds_read_b64_tr_b16 v[24:25], v7 offset:48128\n"
"v_mfma_f32_16x16x32_bf16 v[100:103], v[28:31], v[68:71], v[100:103]\n"
"ds_read_b64_tr_b16 v[26:27], v7 offset:48640\n"
"v_mfma_f32_16x16x32_bf16 v[104:107], v[28:31], v[72:75], v[104:107]\n"
"v_mfma_f32_16x16x32_bf16 v[108:111], v[28:31], v[76:79], v[108:111]\n"
"s_barrier\n"
"s_waitcnt lgkmcnt(12)\n"
"v_mfma_f32_16x16x32_bf16 v[112:115], v[32:35], v[64:67], v[112:115]\n"
"ds_read_b64_tr_b16 v[28:29], v7 offset:49152\n"
"v_mfma_f32_16x16x32_bf16 v[116:119], v[32:35], v[68:71], v[116:119]\n"
"ds_read_b64_tr_b16 v[30:31], v7 offset:49664\n"
"v_mfma_f32_16x16x32_bf16 v[120:123], v[32:35], v[72:75], v[120:123]\n"
"s_add_u32 m0, s20, 81920\n"
"s_mov_b32 s21, 229376\n"
"buffer_load_dwordx4 v1, s[8:11], s21 offen sc1 lds\n"
"v_mfma_f32_16x16x32_bf16 v[124:127], v[32:35], v[76:79], v[124:127]\n"
"v_mfma_f32_16x16x32_bf16 v[128:131], v[36:39], v[64:67], v[128:131]\n"
"ds_read_b64_tr_b16 v[32:33], v7 offset:50176\n"
"v_max_f32_e32 v80, 0, v80\n"
"v_mfma_f32_16x16x32_bf16 v[132:135], v[36:39], v[68:71], v[132:135]\n"
"ds_read_b64_tr_b16 v[34:35], v7 offset:50688\n"
"v_max_f32_e32 v81, 0, v81\n"
"v_mfma_f32_16x16x32_bf16 v[136:139], v[36:39], v[72:75], v[136:139]\n"
"buffer_load_dwordx4 v1, s[8:11], s21 offen offset:1024 sc1 lds\n"
"v_max_f32_e32 v82, 0, v82\n"
"v_mfma_f32_16x16x32_bf16 v[140:143], v[36:39], v[76:79], v[140:143]\n"
"v_max_f32_e32 v83, 0, v83\n"
"v_mul_f32_e32 v96, v80, v96\n"
"s_waitcnt lgkmcnt(12)\n"
"v_mfma_f32_16x16x32_bf16 v[144:147], v[40:43], v[64:67], v[144:147]\n"
"ds_read_b64_tr_b16 v[36:37], v7 offset:51200\n"
"v_mul_f32_e32 v97, v81, v97\n"
"v_mfma_f32_16x16x32_bf16 v[148:151], v[40:43], v[68:71], v[148:151]\n"
"ds_read_b64_tr_b16 v[38:39], v7 offset:51712\n"
"v_mul_f32_e32 v98, v82, v98\n"
"v_mfma_f32_16x16x32_bf16 v[152:155], v[40:43], v[72:75], v[152:155]\n"
"buffer_load_dwordx4 v1, s[8:11], s21 offen offset:2048 sc1 lds\n"
"v_mul_f32_e32 v99, v83, v99\n"
"v_mfma_f32_16x16x32_bf16 v[156:159], v[40:43], v[76:79], v[156:159]\n"
"v_cvt_pk_bf16_f32 v80, v96, v97\n"
"v_cvt_pk_bf16_f32 v81, v98, v99\n"
"v_mfma_f32_16x16x32_bf16 v[160:163], v[44:47], v[64:67], v[160:163]\n"
"ds_read_b64_tr_b16 v[40:41], v7 offset:52224\n"
"v_mfma_f32_16x16x32_bf16 v[164:167], v[44:47], v[68:71], v[164:167]\n"
"ds_read_b64_tr_b16 v[42:43], v7 offset:52736\n"
"v_max_f32_e32 v84, 0, v84\n"
"v_mfma_f32_16x16x32_bf16 v[168:171], v[44:47], v[72:75], v[168:171]\n"
"buffer_load_dwordx4 v1, s[8:11], s21 offen offset:3072 sc1 lds\n"
"v_max_f32_e32 v85, 0, v85\n"
"v_mfma_f32_16x16x32_bf16 v[172:175], v[44:47], v[76:79], v[172:175]\n"
"v_max_f32_e32 v86, 0, v86\n"
"v_max_f32_e32 v87, 0, v87\n"
"s_mov_b32 s22, 0\n"
"buffer_store_dwordx2 v[80:81], v10, s[16:19], s22 offen sc1\n"
"s_waitcnt lgkmcnt(12)\n"
"v_mfma_f32_16x16x32_bf16 v[176:179], v[12:15], a[0:3], 0\n"
"ds_read_b64_tr_b16 v[44:45], v7 offset:53248\n"
"v_mul_f32_e32 v100, v84, v100\n"
"v_mfma_f32_16x16x32_bf16 v[180:183], v[12:15], a[4:7], 0\n"
"ds_read_b64_tr_b16 v[46:47], v7 offset:53760\n"
"v_mul_f32_e32 v101, v85, v101\n"
"v_mfma_f32_16x16x32_bf16 v[184:187], v[12:15], a[8:11], 0\n"
"v_mul_f32_e32 v102, v86, v102\n"
"v_mul_f32_e32 v103, v87, v103\n"
"v_mfma_f32_16x16x32_bf16 v[188:191], v[12:15], a[12:15], 0\n"
"v_cvt_pk_bf16_f32 v84, v100, v101\n"
"v_cvt_pk_bf16_f32 v85, v102, v103\n"
"v_mfma_f32_16x16x32_bf16 v[192:195], v[16:19], a[0:3], 0\n"
"ds_read_b64_tr_b16 v[12:13], v7 offset:54272\n"
"v_mfma_f32_16x16x32_bf16 v[196:199], v[16:19], a[4:7], 0\n"
"ds_read_b64_tr_b16 v[14:15], v7 offset:54784\n"
"v_max_f32_e32 v88, 0, v88\n"
"v_mfma_f32_16x16x32_bf16 v[200:203], v[16:19], a[8:11], 0\n"
"v_max_f32_e32 v89, 0, v89\n"
"v_max_f32_e32 v90, 0, v90\n"
"v_mfma_f32_16x16x32_bf16 v[204:207], v[16:19], a[12:15], 0\n"
"v_max_f32_e32 v91, 0, v91\n"
"v_mul_f32_e32 v104, v88, v104\n"
"s_waitcnt lgkmcnt(12)\n"
"v_mfma_f32_16x16x32_bf16 v[176:179], v[20:23], a[16:19], v[176:179]\n"
"ds_read_b64_tr_b16 v[16:17], v7 offset:55296\n"
"v_mul_f32_e32 v105, v89, v105\n"
"v_mfma_f32_16x16x32_bf16 v[180:183], v[20:23], a[20:23], v[180:183]\n"
"ds_read_b64_tr_b16 v[18:19], v7 offset:55808\n"
"v_mul_f32_e32 v106, v90, v106\n"
"v_mfma_f32_16x16x32_bf16 v[184:187], v[20:23], a[24:27], v[184:187]\n"
"v_mul_f32_e32 v107, v91, v107\n"
"v_cvt_pk_bf16_f32 v88, v104, v105\n"
"v_mfma_f32_16x16x32_bf16 v[188:191], v[20:23], a[28:31], v[188:191]\n"
"v_cvt_pk_bf16_f32 v89, v106, v107\n"
"v_mfma_f32_16x16x32_bf16 v[192:195], v[24:27], a[16:19], v[192:195]\n"
"ds_read_b64_tr_b16 v[20:21], v7 offset:56320\n"
"v_max_f32_e32 v92, 0, v92\n"
"v_mfma_f32_16x16x32_bf16 v[196:199], v[24:27], a[20:23], v[196:199]\n"
"ds_read_b64_tr_b16 v[22:23], v7 offset:56832\n"
"v_max_f32_e32 v93, 0, v93\n"
"v_mfma_f32_16x16x32_bf16 v[200:203], v[24:27], a[24:27], v[200:203]\n"
"v_max_f32_e32 v94, 0, v94\n"
"v_max_f32_e32 v95, 0, v95\n"
"v_mfma_f32_16x16x32_bf16 v[204:207], v[24:27], a[28:31], v[204:207]\n"
"v_mul_f32_e32 v108, v92, v108\n"
"v_mul_f32_e32 v109, v93, v109\n"
"s_waitcnt lgkmcnt(12)\n"
"v_mfma_f32_16x16x32_bf16 v[176:179], v[28:31], a[32:35], v[176:179]\n"
"ds_read_b64_tr_b16 v[24:25], v7 offset:57344\n"
"v_mul_f32_e32 v110, v94, v110\n"
"v_mfma_f32_16x16x32_bf16 v[180:183], v[28:31], a[36:39], v[180:183]\n"
"ds_read_b64_tr_b16 v[26:27], v7 offset:57856\n"
"v_mul_f32_e32 v111, v95, v111\n"
"v_mfma_f32_16x16x32_bf16 v[184:187], v[28:31], a[40:43], v[184:187]\n"
"v_cvt_pk_bf16_f32 v92, v108, v109\n"
"v_cvt_pk_bf16_f32 v93, v110, v111\n"
"v_mfma_f32_16x16x32_bf16 v[188:191], v[28:31], a[44:47], v[188:191]\n"
"v_max_f32_e32 v112, 0, v112\n"
"v_mfma_f32_16x16x32_bf16 v[192:195], v[32:35], a[32:35], v[192:195]\n"
"ds_read_b64_tr_b16 v[28:29], v7 offset:58368\n"
"v_max_f32_e32 v113, 0, v113\n"
"v_mfma_f32_16x16x32_bf16 v[196:199], v[32:35], a[36:39], v[196:199]\n"
"ds_read_b64_tr_b16 v[30:31], v7 offset:58880\n"
"v_max_f32_e32 v114, 0, v114\n"
"v_mfma_f32_16x16x32_bf16 v[200:203], v[32:35], a[40:43], v[200:203]\n"
"v_max_f32_e32 v115, 0, v115\n"
"v_mul_f32_e32 v128, v112, v128\n"
"v_mfma_f32_16x16x32_bf16 v[204:207], v[32:35], a[44:47], v[204:207]\n"
"v_mul_f32_e32 v129, v113, v129\n"
"v_mul_f32_e32 v130, v114, v130\n"
"s_waitcnt lgkmcnt(12)\n"
"v_mfma_f32_16x16x32_bf16 v[176:179], v[36:39], a[48:51], v[176:179]\n"
"ds_read_b64_tr_b16 v[32:33], v7 offset:59392\n"
"v_mul_f32_e32 v131, v115, v131\n"
"v_mfma_f32_16x16x32_bf16 v[180:183], v[36:39], a[52:55], v[180:183]\n"
"ds_read_b64_tr_b16 v[34:35], v7 offset:59904\n"
"v_cvt_pk_bf16_f32 v112, v128, v129\n"
"v_mfma_f32_16x16x32_bf16 v[184:187], v[36:39], a[56:59], v[184:187]\n"
"v_cvt_pk_bf16_f32 v113, v130, v131\n"
"v_max_f32_e32 v116, 0, v116\n"
"v_mfma_f32_16x16x32_bf16 v[188:191], v[36:39], a[60:63], v[188:191]\n"
"v_max_f32_e32 v117, 0, v117\n"
"v_max_f32_e32 v118, 0, v118\n"
"v_mfma_f32_16x16x32_bf16 v[192:195], v[40:43], a[48:51], v[192:195]\n"
"ds_read_b64_tr_b16 v[36:37], v7 offset:60416\n"
"v_max_f32_e32 v119, 0, v119\n"
"v_mfma_f32_16x16x32_bf16 v[196:199], v[40:43], a[52:55], v[196:199]\n"
"ds_read_b64_tr_b16 v[38:39], v7 offset:60928\n"
"v_mul_f32_e32 v132, v116, v132\n"
"v_mfma_f32_16x16x32_bf16 v[200:203], v[40:43], a[56:59], v[200:203]\n"
"v_mul_f32_e32 v133, v117, v133\n"
"v_mul_f32_e32 v134, v118, v134\n"
"v_mfma_f32_16x16x32_bf16 v[204:207], v[40:43], a[60:63], v[204:207]\n"
"v_mul_f32_e32 v135, v119, v135\n"
"v_cvt_pk_bf16_f32 v116, v132, v133\n"
"s_waitcnt lgkmcnt(12)\n"
"v_mfma_f32_16x16x32_bf16 v[176:179], v[44:47], a[64:67], v[176:179]\n"
"ds_read_b64_tr_b16 v[40:41], v7 offset:61440\n"
"v_cvt_pk_bf16_f32 v117, v134, v135\n"
"v_mfma_f32_16x16x32_bf16 v[180:183], v[44:47], a[68:71], v[180:183]\n"
"ds_read_b64_tr_b16 v[42:43], v7 offset:61952\n"
"v_max_f32_e32 v120, 0, v120\n"
"v_mfma_f32_16x16x32_bf16 v[184:187], v[44:47], a[72:75], v[184:187]\n"
"v_max_f32_e32 v121, 0, v121\n"
"v_max_f32_e32 v122, 0, v122\n"
"v_mfma_f32_16x16x32_bf16 v[188:191], v[44:47], a[76:79], v[188:191]\n"
"v_max_f32_e32 v123, 0, v123\n"
"v_mul_f32_e32 v136, v120, v136\n"
"v_mfma_f32_16x16x32_bf16 v[192:195], v[12:15], a[64:67], v[192:195]\n"
"ds_read_b64_tr_b16 v[44:45], v7 offset:62464\n"
"v_mul_f32_e32 v137, v121, v137\n"
"v_mfma_f32_16x16x32_bf16 v[196:199], v[12:15], a[68:71], v[196:199]\n"
"ds_read_b64_tr_b16 v[46:47], v7 offset:62976\n"
"v_mul_f32_e32 v138, v122, v138\n"
"v_mfma_f32_16x16x32_bf16 v[200:203], v[12:15], a[72:75], v[200:203]\n"
"v_mul_f32_e32 v139, v123, v139\n"
"v_cvt_pk_bf16_f32 v120, v136, v137\n"
"v_mfma_f32_16x16x32_bf16 v[204:207], v[12:15], a[76:79], v[204:207]\n"
"v_cvt_pk_bf16_f32 v121, v138, v139\n"
"v_max_f32_e32 v124, 0, v124\n"
"s_waitcnt lgkmcnt(12)\n"
"v_mfma_f32_16x16x32_bf16 v[176:179], v[16:19], a[80:83], v[176:179]\n"
"ds_read_b64_tr_b16 v[12:13], v7 offset:63488\n"
"v_max_f32_e32 v125, 0, v125\n"
"v_mfma_f32_16x16x32_bf16 v[180:183], v[16:19], a[84:87], v[180:183]\n"
"ds_read_b64_tr_b16 v[14:15], v7 offset:64000\n"
"v_max_f32_e32 v126, 0, v126\n"
"v_mfma_f32_16x16x32_bf16 v[184:187], v[16:19], a[88:91], v[184:187]\n"
"v_max_f32_e32 v127, 0, v127\n"
"v_mul_f32_e32 v140, v124, v140\n"
"v_mfma_f32_16x16x32_bf16 v[188:191], v[16:19], a[92:95], v[188:191]\n"
"v_mul_f32_e32 v141, v125, v141\n"
"v_mul_f32_e32 v142, v126, v142\n"
"v_mfma_f32_16x16x32_bf16 v[192:195], v[20:23], a[80:83], v[192:195]\n"
"ds_read_b64_tr_b16 v[16:17], v7 offset:64512\n"
"v_mul_f32_e32 v143, v127, v143\n"
"v_mfma_f32_16x16x32_bf16 v[196:199], v[20:23], a[84:87], v[196:199]\n"
"ds_read_b64_tr_b16 v[18:19], v7 offset:65024\n"
"v_cvt_pk_bf16_f32 v124, v140, v141\n"
"v_mfma_f32_16x16x32_bf16 v[200:203], v[20:23], a[88:91], v[200:203]\n"
"v_cvt_pk_bf16_f32 v125, v142, v143\n"
"v_max_f32_e32 v144, 0, v144\n"
"v_mfma_f32_16x16x32_bf16 v[204:207], v[20:23], a[92:95], v[204:207]\n"
"v_max_f32_e32 v145, 0, v145\n"
"v_max_f32_e32 v146, 0, v146\n"
"s_mov_b32 s22, 147456\n"
"buffer_store_dwordx2 v[84:85], v10, s[16:19], s22 offen sc1\n"
"s_waitcnt vmcnt(26)\n"
"s_barrier\n"
"s_waitcnt lgkmcnt(12)\n"
"v_mfma_f32_16x16x32_bf16 v[176:179], v[24:27], a[96:99], v[176:179]\n"
"ds_read_b64_tr_b16 v[20:21], v8 offset:0\n"
"v_max_f32_e32 v147, 0, v147\n"
"v_mfma_f32_16x16x32_bf16 v[180:183], v[24:27], a[100:103], v[180:183]\n"
"ds_read_b64_tr_b16 v[22:23], v8 offset:512\n"
"v_mul_f32_e32 v160, v144, v160\n"
"v_mfma_f32_16x16x32_bf16 v[184:187], v[24:27], a[104:107], v[184:187]\n"
"s_add_u32 m0, s20, 98304\n"
"s_mov_b32 s21, 245760\n"
"buffer_load_dwordx4 v1, s[8:11], s21 offen sc1 lds\n"
"v_mul_f32_e32 v161, v145, v161\n"
"v_mfma_f32_16x16x32_bf16 v[188:191], v[24:27], a[108:111], v[188:191]\n"
"v_mul_f32_e32 v162, v146, v162\n"
"v_mul_f32_e32 v163, v147, v163\n"
"v_mfma_f32_16x16x32_bf16 v[192:195], v[28:31], a[96:99], v[192:195]\n"
"ds_read_b64_tr_b16 v[24:25], v8 offset:1024\n"
"v_cvt_pk_bf16_f32 v114, v160, v161\n"
"v_mfma_f32_16x16x32_bf16 v[196:199], v[28:31], a[100:103], v[196:199]\n"
"ds_read_b64_tr_b16 v[26:27], v8 offset:1536\n"
"v_cvt_pk_bf16_f32 v115, v162, v163\n"
"v_mfma_f32_16x16x32_bf16 v[200:203], v[28:31], a[104:107], v[200:203]\n"
"buffer_load_dwordx4 v1, s[8:11], s21 offen offset:1024 sc1 lds\n"
"v_mfma_f32_16x16x32_bf16 v[204:207], v[28:31], a[108:111], v[204:207]\n"
"v_max_f32_e32 v148, 0, v148\n"
"v_max_f32_e32 v149, 0, v149\n"
"s_waitcnt lgkmcnt(12)\n"
"v_mfma_f32_16x16x32_bf16 v[176:179], v[32:35], a[112:115], v[176:179]\n"
"ds_read_b64_tr_b16 v[28:29], v8 offset:2048\n"
"v_max_f32_e32 v150, 0, v150\n"
"v_mfma_f32_16x16x32_bf16 v[180:183], v[32:35], a[116:119], v[180:183]\n"
"ds_read_b64_tr_b16 v[30:31], v8 offset:2560\n"
"v_max_f32_e32 v151, 0, v151\n"
"v_mfma_f32_16x16x32_bf16 v[184:187], v[32:35], a[120:123], v[184:187]\n"
"buffer_load_dwordx4 v1, s[8:11], s21 offen offset:2048 sc1 lds\n"
"v_mul_f32_e32 v164, v148, v164\n"
"v_mfma_f32_16x16x32_bf16 v[188:191], v[32:35], a[124:127], v[188:191]\n"
"v_mul_f32_e32 v165, v149, v165\n"
"v_mul_f32_e32 v166, v150, v166\n"
"v_mfma_f32_16x16x32_bf16 v[192:195], v[36:39], a[112:115], v[192:195]\n"
"ds_read_b64_tr_b16 v[32:33], v8 offset:3072\n"
"v_mul_f32_e32 v167, v151, v167\n"
"v_mfma_f32_16x16x32_bf16 v[196:199], v[36:39], a[116:119], v[196:199]\n"
"ds_read_b64_tr_b16 v[34:35], v8 offset:3584\n"
"v_cvt_pk_bf16_f32 v118, v164, v165\n"
"v_mfma_f32_16x16x32_bf16 v[200:203], v[36:39], a[120:123], v[200:203]\n"
"buffer_load_dwordx4 v1, s[8:11], s21 offen offset:3072 sc1 lds\n"
"v_cvt_pk_bf16_f32 v119, v166, v167\n"
"v_mfma_f32_16x16x32_bf16 v[204:207], v[36:39], a[124:127], v[204:207]\n"
"v_max_f32_e32 v152, 0, v152\n"
"s_waitcnt lgkmcnt(12)\n"
"v_mfma_f32_16x16x32_bf16 v[176:179], v[40:43], a[128:131], v[176:179]\n"
"ds_read_b64_tr_b16 v[36:37], v8 offset:4096\n"
"v_max_f32_e32 v153, 0, v153\n"
"v_mfma_f32_16x16x32_bf16 v[180:183], v[40:43], a[132:135], v[180:183]\n"
"ds_read_b64_tr_b16 v[38:39], v8 offset:4608\n"
"v_max_f32_e32 v154, 0, v154\n"
"v_mfma_f32_16x16x32_bf16 v[184:187], v[40:43], a[136:139], v[184:187]\n"
"v_max_f32_e32 v155, 0, v155\n"
"v_mul_f32_e32 v168, v152, v168\n"
"v_mfma_f32_16x16x32_bf16 v[188:191], v[40:43], a[140:143], v[188:191]\n"
"v_mul_f32_e32 v169, v153, v169\n"
"v_mul_f32_e32 v170, v154, v170\n"
"v_mfma_f32_16x16x32_bf16 v[192:195], v[44:47], a[128:131], v[192:195]\n"
"ds_read_b64_tr_b16 v[40:41], v8 offset:5120\n"
"v_mul_f32_e32 v171, v155, v171\n"
"v_mfma_f32_16x16x32_bf16 v[196:199], v[44:47], a[132:135], v[196:199]\n"
"ds_read_b64_tr_b16 v[42:43], v8 offset:5632\n"
"v_cvt_pk_bf16_f32 v122, v168, v169\n"
"v_mfma_f32_16x16x32_bf16 v[200:203], v[44:47], a[136:139], v[200:203]\n"
"v_cvt_pk_bf16_f32 v123, v170, v171\n"
"v_mfma_f32_16x16x32_bf16 v[204:207], v[44:47], a[140:143], v[204:207]\n"
"v_max_f32_e32 v156, 0, v156\n"
"v_max_f32_e32 v157, 0, v157\n"
"s_waitcnt lgkmcnt(12)\n"
"v_mfma_f32_16x16x32_bf16 v[176:179], v[12:15], a[144:147], v[176:179]\n"
"ds_read_b64_tr_b16 v[44:45], v8 offset:6144\n"
"v_max_f32_e32 v158, 0, v158\n"
"v_mfma_f32_16x16x32_bf16 v[180:183], v[12:15], a[148:151], v[180:183]\n"
"ds_read_b64_tr_b16 v[46:47], v8 offset:6656\n"
"v_max_f32_e32 v159, 0, v159\n"
"v_mfma_f32_16x16x32_bf16 v[184:187], v[12:15], a[152:155], v[184:187]\n"
"v_mul_f32_e32 v172, v156, v172\n"
"v_mul_f32_e32 v173, v157, v173\n"
"v_mfma_f32_16x16x32_bf16 v[188:191], v[12:15], a[156:159], v[188:191]\n"
"v_mul_f32_e32 v174, v158, v174\n"
"v_mul_f32_e32 v175, v159, v175\n"
"v_mfma_f32_16x16x32_bf16 v[192:195], v[16:19], a[144:147], v[192:195]\n"
"ds_read_b64_tr_b16 v[12:13], v8 offset:7168\n"
"v_cvt_pk_bf16_f32 v126, v172, v173\n"
"v_mfma_f32_16x16x32_bf16 v[196:199], v[16:19], a[148:151], v[196:199]\n"
"ds_read_b64_tr_b16 v[14:15], v8 offset:7680\n"
"v_cvt_pk_bf16_f32 v127, v174, v175\n"
"v_mfma_f32_16x16x32_bf16 v[200:203], v[16:19], a[152:155], v[200:203]\n"
"v_mfma_f32_16x16x32_bf16 v[204:207], v[16:19], a[156:159], v[204:207]\n"
"s_waitcnt lgkmcnt(12)\n"
"v_mfma_f32_16x16x32_bf16 v[176:179], v[20:23], a[160:163], v[176:179]\n"
"ds_read_b64_tr_b16 v[16:17], v8 offset:8192\n"
"v_mfma_f32_16x16x32_bf16 v[180:183], v[20:23], a[164:167], v[180:183]\n"
"ds_read_b64_tr_b16 v[18:19], v8 offset:8704\n"
"v_mfma_f32_16x16x32_bf16 v[184:187], v[20:23], a[168:171], v[184:187]\n"
"v_mfma_f32_16x16x32_bf16 v[188:191], v[20:23], a[172:175], v[188:191]\n"
"v_mfma_f32_16x16x32_bf16 v[192:195], v[24:27], a[160:163], v[192:195]\n"
"ds_read_b64_tr_b16 v[20:21], v8 offset:9216\n"
"v_mfma_f32_16x16x32_bf16 v[196:199], v[24:27], a[164:167], v[196:199]\n"
"ds_read_b64_tr_b16 v[22:23], v8 offset:9728\n"
"v_mfma_f32_16x16x32_bf16 v[200:203], v[24:27], a[168:171], v[200:203]\n"
"v_mfma_f32_16x16x32_bf16 v[204:207], v[24:27], a[172:175], v[204:207]\n"
"s_waitcnt lgkmcnt(12)\n"
"v_mfma_f32_16x16x32_bf16 v[176:179], v[28:31], a[176:179], v[176:179]\n"
"ds_read_b64_tr_b16 v[24:25], v8 offset:10240\n"
"v_mfma_f32_16x16x32_bf16 v[180:183], v[28:31], a[180:183], v[180:183]\n"
"ds_read_b64_tr_b16 v[26:27], v8 offset:10752\n"
"v_mfma_f32_16x16x32_bf16 v[184:187], v[28:31], a[184:187], v[184:187]\n"
"v_mfma_f32_16x16x32_bf16 v[188:191], v[28:31], a[188:191], v[188:191]\n"
"v_mfma_f32_16x16x32_bf16 v[192:195], v[32:35], a[176:179], v[192:195]\n"
"ds_read_b64_tr_b16 v[28:29], v8 offset:11264\n"
"v_mfma_f32_16x16x32_bf16 v[196:199], v[32:35], a[180:183], v[196:199]\n"
"ds_read_b64_tr_b16 v[30:31], v8 offset:11776\n"
"v_mfma_f32_16x16x32_bf16 v[200:203], v[32:35], a[184:187], v[200:203]\n"
"v_mfma_f32_16x16x32_bf16 v[204:207], v[32:35], a[188:191], v[204:207]\n"
"s_mov_b32 s22, 294912\n"
"buffer_store_dwordx2 v[88:89], v10, s[16:19], s22 offen sc1\n"
"s_waitcnt lgkmcnt(12)\n"
"v_mfma_f32_16x16x32_bf16 v[176:179], v[36:39], a[192:195], v[176:179]\n"
"ds_read_b64_tr_b16 v[32:33], v8 offset:12288\n"
"v_mfma_f32_16x16x32_bf16 v[180:183], v[36:39], a[196:199], v[180:183]\n"
"ds_read_b64_tr_b16 v[34:35], v8 offset:12800\n"
"v_mfma_f32_16x16x32_bf16 v[184:187], v[36:39], a[200:203], v[184:187]\n"
"v_mfma_f32_16x16x32_bf16 v[188:191], v[36:39], a[204:207], v[188:191]\n"
"v_mfma_f32_16x16x32_bf16 v[192:195], v[40:43], a[192:195], v[192:195]\n"
"ds_read_b64_tr_b16 v[36:37], v8 offset:13312\n"
"v_mfma_f32_16x16x32_bf16 v[196:199], v[40:43], a[196:199], v[196:199]\n"
"ds_read_b64_tr_b16 v[38:39], v8 offset:13824\n"
"v_mfma_f32_16x16x32_bf16 v[200:203], v[40:43], a[200:203], v[200:203]\n"
"v_mfma_f32_16x16x32_bf16 v[204:207], v[40:43], a[204:207], v[204:207]\n"
"s_waitcnt lgkmcnt(12)\n"
"v_mfma_f32_16x16x32_bf16 v[176:179], v[44:47], a[208:211], v[176:179]\n"
"ds_read_b64_tr_b16 v[40:41], v8 offset:14336\n"
"v_mfma_f32_16x16x32_bf16 v[180:183], v[44:47], a[212:215], v[180:183]\n"
"ds_read_b64_tr_b16 v[42:43], v8 offset:14848\n"
"v_mfma_f32_16x16x32_bf16 v[184:187], v[44:47], a[216:219], v[184:187]\n"
"v_mfma_f32_16x16x32_bf16 v[188:191], v[44:47], a[220:223], v[188:191]\n"
"v_mfma_f32_16x16x32_bf16 v[192:195], v[12:15], a[208:211], v[192:195]\n"
"ds_read_b64_tr_b16 v[44:45], v8 offset:15360\n"
"v_mfma_f32_16x16x32_bf16 v[196:199], v[12:15], a[212:215], v[196:199]\n"
"ds_read_b64_tr_b16 v[46:47], v8 offset:15872\n"
"v_mfma_f32_16x16x32_bf16 v[200:203], v[12:15], a[216:219], v[200:203]\n"
"v_mfma_f32_16x16x32_bf16 v[204:207], v[12:15], a[220:223], v[204:207]\n"
"s_waitcnt vmcnt(27)\n"
"s_barrier\n"
"s_waitcnt lgkmcnt(12)\n"
"v_mfma_f32_16x16x32_bf16 v[176:179], v[16:19], a[224:227], v[176:179]\n"
"ds_read_b64_tr_b16 v[12:13], v6 offset:0\n"
"v_mfma_f32_16x16x32_bf16 v[180:183], v[16:19], a[228:231], v[180:183]\n"
"ds_read_b64_tr_b16 v[14:15], v6 offset:512\n"
"v_mfma_f32_16x16x32_bf16 v[184:187], v[16:19], a[232:235], v[184:187]\n"
"s_add_u32 m0, s20, 114688\n"
"s_mov_b32 s21, 262144\n"
"buffer_load_dwordx4 v1, s[8:11], s21 offen sc1 lds\n"
"v_mfma_f32_16x16x32_bf16 v[188:191], v[16:19], a[236:239], v[188:191]\n"
"v_mfma_f32_16x16x32_bf16 v[192:195], v[20:23], a[224:227], v[192:195]\n"
"ds_read_b64_tr_b16 v[16:17], v6 offset:1024\n"
"v_mfma_f32_16x16x32_bf16 v[196:199], v[20:23], a[228:231], v[196:199]\n"
"ds_read_b64_tr_b16 v[18:19], v6 offset:1536\n"
"v_mfma_f32_16x16x32_bf16 v[200:203], v[20:23], a[232:235], v[200:203]\n"
"buffer_load_dwordx4 v1, s[8:11], s21 offen offset:1024 sc1 lds\n"
"v_mfma_f32_16x16x32_bf16 v[204:207], v[20:23], a[236:239], v[204:207]\n"
"s_waitcnt lgkmcnt(12)\n"
"v_mfma_f32_16x16x32_bf16 v[176:179], v[24:27], a[240:243], v[176:179]\n"
"ds_read_b64_tr_b16 v[20:21], v6 offset:2048\n"
"v_mfma_f32_16x16x32_bf16 v[180:183], v[24:27], a[244:247], v[180:183]\n"
"ds_read_b64_tr_b16 v[22:23], v6 offset:2560\n"
"v_mfma_f32_16x16x32_bf16 v[184:187], v[24:27], a[248:251], v[184:187]\n"
"buffer_load_dwordx4 v1, s[8:11], s21 offen offset:2048 sc1 lds\n"
"v_mfma_f32_16x16x32_bf16 v[188:191], v[24:27], a[252:255], v[188:191]\n"
"v_mfma_f32_16x16x32_bf16 v[192:195], v[28:31], a[240:243], v[192:195]\n"
"ds_read_b64_tr_b16 v[24:25], v6 offset:3072\n"
"v_mfma_f32_16x16x32_bf16 v[196:199], v[28:31], a[244:247], v[196:199]\n"
"ds_read_b64_tr_b16 v[26:27], v6 offset:3584\n"
"v_mfma_f32_16x16x32_bf16 v[200:203], v[28:31], a[248:251], v[200:203]\n"
"buffer_load_dwordx4 v1, s[8:11], s21 offen offset:3072 sc1 lds\n"
"v_mfma_f32_16x16x32_bf16 v[204:207], v[28:31], a[252:255], v[204:207]\n"
"s_waitcnt lgkmcnt(12)\n"
"v_mfma_f32_16x16x32_bf16 v[176:179], v[32:35], v[48:51], v[176:179]\n"
"ds_read_b64_tr_b16 v[28:29], v6 offset:4096\n"
"v_mfma_f32_16x16x32_bf16 v[180:183], v[32:35], v[52:55], v[180:183]\n"
"ds_read_b64_tr_b16 v[30:31], v6 offset:4608\n"
"v_mfma_f32_16x16x32_bf16 v[184:187], v[32:35], v[56:59], v[184:187]\n"
"v_mfma_f32_16x16x32_bf16 v[188:191], v[32:35], v[60:63], v[188:191]\n"
"v_mfma_f32_16x16x32_bf16 v[192:195], v[36:39], v[48:51], v[192:195]\n"
"ds_read_b64_tr_b16 v[32:33], v6 offset:5120\n"
"v_mfma_f32_16x16x32_bf16 v[196:199], v[36:39], v[52:55], v[196:199]\n"
"ds_read_b64_tr_b16 v[34:35], v6 offset:5632\n"
"v_mfma_f32_16x16x32_bf16 v[200:203], v[36:39], v[56:59], v[200:203]\n"
"v_mfma_f32_16x16x32_bf16 v[204:207], v[36:39], v[60:63], v[204:207]\n"
"s_waitcnt lgkmcnt(12)\n"
"v_mfma_f32_16x16x32_bf16 v[176:179], v[40:43], v[64:67], v[176:179]\n"
"ds_read_b64_tr_b16 v[36:37], v6 offset:6144\n"
"v_mfma_f32_16x16x32_bf16 v[180:183], v[40:43], v[68:71], v[180:183]\n"
"ds_read_b64_tr_b16 v[38:39], v6 offset:6656\n"
"v_mfma_f32_16x16x32_bf16 v[184:187], v[40:43], v[72:75], v[184:187]\n"
"v_mfma_f32_16x16x32_bf16 v[188:191], v[40:43], v[76:79], v[188:191]\n"
"v_mfma_f32_16x16x32_bf16 v[192:195], v[44:47], v[64:67], v[192:195]\n"
"ds_read_b64_tr_b16 v[40:41], v6 offset:7168\n"
"v_mfma_f32_16x16x32_bf16 v[196:199], v[44:47], v[68:71], v[196:199]\n"
"ds_read_b64_tr_b16 v[42:43], v6 offset:7680\n"
"v_mfma_f32_16x16x32_bf16 v[200:203], v[44:47], v[72:75], v[200:203]\n"
"v_mfma_f32_16x16x32_bf16 v[204:207], v[44:47], v[76:79], v[204:207]\n"
"s_mov_b32 s22, 442368\n"
"buffer_store_dwordx2 v[92:93], v10, s[16:19], s22 offen sc1\n"
"s_waitcnt lgkmcnt(12)\n"
"v_mfma_f32_16x16x32_bf16 v[208:211], v[12:15], a[0:3], 0\n"
"ds_read_b64_tr_b16 v[44:45], v6 offset:8192\n"
"v_mfma_f32_16x16x32_bf16 v[212:215], v[12:15], a[4:7], 0\n"
"ds_read_b64_tr_b16 v[46:47], v6 offset:8704\n"
"v_mfma_f32_16x16x32_bf16 v[216:219], v[12:15], a[8:11], 0\n"
"v_mfma_f32_16x16x32_bf16 v[220:223], v[12:15], a[12:15], 0\n"
"v_mfma_f32_16x16x32_bf16 v[224:227], v[16:19], a[0:3], 0\n"
"ds_read_b64_tr_b16 v[12:13], v6 offset:9216\n"
"v_max_f32_e32 v176, 0, v176\n"
"v_mfma_f32_16x16x32_bf16 v[228:231], v[16:19], a[4:7], 0\n"
"ds_read_b64_tr_b16 v[14:15], v6 offset:9728\n"
"v_max_f32_e32 v177, 0, v177\n"
"v_mfma_f32_16x16x32_bf16 v[232:235], v[16:19], a[8:11], 0\n"
"v_max_f32_e32 v178, 0, v178\n"
"v_max_f32_e32 v179, 0, v179\n"
"v_mfma_f32_16x16x32_bf16 v[236:239], v[16:19], a[12:15], 0\n"
"v_mul_f32_e32 v192, v176, v192\n"
"v_mul_f32_e32 v193, v177, v193\n"
"s_waitcnt lgkmcnt(12)\n"
"v_mfma_f32_16x16x32_bf16 v[208:211], v[20:23], a[16:19], v[208:211]\n"
"ds_read_b64_tr_b16 v[16:17], v6 offset:10240\n"
"v_mul_f32_e32 v194, v178, v194\n"
"v_mfma_f32_16x16x32_bf16 v[212:215], v[20:23], a[20:23], v[212:215]\n"
"ds_read_b64_tr_b16 v[18:19], v6 offset:10752\n"
"v_mul_f32_e32 v195, v179, v195\n"
"v_mfma_f32_16x16x32_bf16 v[216:219], v[20:23], a[24:27], v[216:219]\n"
"v_cvt_pk_bf16_f32 v176, v192, v193\n"
"v_cvt_pk_bf16_f32 v177, v194, v195\n"
"v_mfma_f32_16x16x32_bf16 v[220:223], v[20:23], a[28:31], v[220:223]\n"
"v_max_f32_e32 v180, 0, v180\n"
"v_max_f32_e32 v181, 0, v181\n"
"v_mfma_f32_16x16x32_bf16 v[224:227], v[24:27], a[16:19], v[224:227]\n"
"ds_read_b64_tr_b16 v[20:21], v6 offset:11264\n"
"v_max_f32_e32 v182, 0, v182\n"
"v_mfma_f32_16x16x32_bf16 v[228:231], v[24:27], a[20:23], v[228:231]\n"
"ds_read_b64_tr_b16 v[22:23], v6 offset:11776\n"
"v_max_f32_e32 v183, 0, v183\n"
"v_mfma_f32_16x16x32_bf16 v[232:235], v[24:27], a[24:27], v[232:235]\n"
"v_mul_f32_e32 v196, v180, v196\n"
"v_mul_f32_e32 v197, v181, v197\n"
"v_mfma_f32_16x16x32_bf16 v[236:239], v[24:27], a[28:31], v[236:239]\n"
"v_mul_f32_e32 v198, v182, v198\n"
"v_mul_f32_e32 v199, v183, v199\n"
"s_waitcnt lgkmcnt(12)\n"
"v_mfma_f32_16x16x32_bf16 v[208:211], v[28:31], a[32:35], v[208:211]\n"
"ds_read_b64_tr_b16 v[24:25], v6 offset:12288\n"
"v_cvt_pk_bf16_f32 v180, v196, v197\n"
"v_mfma_f32_16x16x32_bf16 v[212:215], v[28:31], a[36:39], v[212:215]\n"
"ds_read_b64_tr_b16 v[26:27], v6 offset:12800\n"
"v_cvt_pk_bf16_f32 v181, v198, v199\n"
"v_mfma_f32_16x16x32_bf16 v[216:219], v[28:31], a[40:43], v[216:219]\n"
"v_max_f32_e32 v184, 0, v184\n"
"v_max_f32_e32 v185, 0, v185\n"
"v_mfma_f32_16x16x32_bf16 v[220:223], v[28:31], a[44:47], v[220:223]\n"
"v_max_f32_e32 v186, 0, v186\n"
"v_max_f32_e32 v187, 0, v187\n"
"v_mfma_f32_16x16x32_bf16 v[224:227], v[32:35], a[32:35], v[224:227]\n"
"ds_read_b64_tr_b16 v[28:29], v6 offset:13312\n"
"v_mul_f32_e32 v200, v184, v200\n"
"v_mfma_f32_16x16x32_bf16 v[228:231], v[32:35], a[36:39], v[228:231]\n"
"ds_read_b64_tr_b16 v[30:31], v6 offset:13824\n"
"v_mul_f32_e32 v201, v185, v201\n"
"v_mfma_f32_16x16x32_bf16 v[232:235], v[32:35], a[40:43], v[232:235]\n"
"v_mul_f32_e32 v202, v186, v202\n"
"v_mul_f32_e32 v203, v187, v203\n"
"v_mfma_f32_16x16x32_bf16 v[236:239], v[32:35], a[44:47], v[236:239]\n"
"v_cvt_pk_bf16_f32 v184, v200, v201\n"
"v_cvt_pk_bf16_f32 v185, v202, v203\n"
"s_waitcnt lgkmcnt(12)\n"
"v_mfma_f32_16x16x32_bf16 v[208:211], v[36:39], a[48:51], v[208:211]\n"
"ds_read_b64_tr_b16 v[32:33], v6 offset:14336\n"
"v_max_f32_e32 v188, 0, v188\n"
"v_mfma_f32_16x16x32_bf16 v[212:215], v[36:39], a[52:55], v[212:215]\n"
"ds_read_b64_tr_b16 v[34:35], v6 offset:14848\n"
"v_max_f32_e32 v189, 0, v189\n"
"v_mfma_f32_16x16x32_bf16 v[216:219], v[36:39], a[56:59], v[216:219]\n"
"v_max_f32_e32 v190, 0, v190\n"
"v_max_f32_e32 v191, 0, v191\n"
"v_mfma_f32_16x16x32_bf16 v[220:223], v[36:39], a[60:63], v[220:223]\n"
"v_mul_f32_e32 v204, v188, v204\n"
"v_mul_f32_e32 v205, v189, v205\n"
"v_mfma_f32_16x16x32_bf16 v[224:227], v[40:43], a[48:51], v[224:227]\n"
"ds_read_b64_tr_b16 v[36:37], v6 offset:15360\n"
"v_mul_f32_e32 v206, v190, v206\n"
"v_mfma_f32_16x16x32_bf16 v[228:231], v[40:43], a[52:55], v[228:231]\n"
"ds_read_b64_tr_b16 v[38:39], v6 offset:15872\n"
"v_mul_f32_e32 v207, v191, v207\n"
"v_mfma_f32_16x16x32_bf16 v[232:235], v[40:43], a[56:59], v[232:235]\n"
"v_cvt_pk_bf16_f32 v188, v204, v205\n"
"v_cvt_pk_bf16_f32 v189, v206, v207\n"
"v_mfma_f32_16x16x32_bf16 v[236:239], v[40:43], a[60:63], v[236:239]\n"
"s_waitcnt vmcnt(28)\n"
"s_barrier\n"
"s_waitcnt lgkmcnt(12)\n"
"v_mfma_f32_16x16x32_bf16 v[208:211], v[44:47], a[64:67], v[208:211]\n"
"ds_read_b64_tr_b16 v[40:41], v6 offset:16384\n"
"v_mfma_f32_16x16x32_bf16 v[212:215], v[44:47], a[68:71], v[212:215]\n"
"ds_read_b64_tr_b16 v[42:43], v6 offset:16896\n"
"v_mfma_f32_16x16x32_bf16 v[216:219], v[44:47], a[72:75], v[216:219]\n"
"s_add_u32 m0, s20, 131072\n"
"s_mov_b32 s21, 278528\n"
"buffer_load_dwordx4 v1, s[8:11], s21 offen sc1 lds\n"
"v_mfma_f32_16x16x32_bf16 v[220:223], v[44:47], a[76:79], v[220:223]\n"
"v_mfma_f32_16x16x32_bf16 v[224:227], v[12:15], a[64:67], v[224:227]\n"
"ds_read_b64_tr_b16 v[44:45], v6 offset:17408\n"
"v_mfma_f32_16x16x32_bf16 v[228:231], v[12:15], a[68:71], v[228:231]\n"
"ds_read_b64_tr_b16 v[46:47], v6 offset:17920\n"
"v_mfma_f32_16x16x32_bf16 v[232:235], v[12:15], a[72:75], v[232:235]\n"
"buffer_load_dwordx4 v1, s[8:11], s21 offen offset:1024 sc1 lds\n"
"v_mfma_f32_16x16x32_bf16 v[236:239], v[12:15], a[76:79], v[236:239]\n"
"s_waitcnt lgkmcnt(12)\n"
"v_mfma_f32_16x16x32_bf16 v[208:211], v[16:19], a[80:83], v[208:211]\n"
"ds_read_b64_tr_b16 v[12:13], v6 offset:18432\n"
"v_mfma_f32_16x16x32_bf16 v[212:215], v[16:19], a[84:87], v[212:215]\n"
"ds_read_b64_tr_b16 v[14:15], v6 offset:18944\n"
"v_mfma_f32_16x16x32_bf16 v[216:219], v[16:19], a[88:91], v[216:219]\n"
"buffer_load_dwordx4 v1, s[8:11], s21 offen offset:2048 sc1 lds\n"
"v_mfma_f32_16x16x32_bf16 v[220:223], v[16:19], a[92:95], v[220:223]\n"
"v_mfma_f32_16x16x32_bf16 v[224:227], v[20:23], a[80:83], v[224:227]\n"
"ds_read_b64_tr_b16 v[16:17], v6 offset:19456\n"
"v_mfma_f32_16x16x32_bf16 v[228:231], v[20:23], a[84:87], v[228:231]\n"
"ds_read_b64_tr_b16 v[18:19], v6 offset:19968\n"
"v_mfma_f32_16x16x32_bf16 v[232:235], v[20:23], a[88:91], v[232:235]\n"
"buffer_load_dwordx4 v1, s[8:11], s21 offen offset:3072 sc1 lds\n"
"v_mfma_f32_16x16x32_bf16 v[236:239], v[20:23], a[92:95], v[236:239]\n"
"s_mov_b32 s22, 0\n"
"buffer_store_dwordx4 v[112:115], v9, s[16:19], s22 offen sc1\n"
"s_waitcnt lgkmcnt(12)\n"
"v_mfma_f32_16x16x32_bf16 v[208:211], v[24:27], a[96:99], v[208:211]\n"
"ds_read_b64_tr_b16 v[20:21], v6 offset:20480\n"
"v_mfma_f32_16x16x32_bf16 v[212:215], v[24:27], a[100:103], v[212:215]\n"
"ds_read_b64_tr_b16 v[22:23], v6 offset:20992\n"
"v_mfma_f32_16x16x32_bf16 v[216:219], v[24:27], a[104:107], v[216:219]\n"
"v_mfma_f32_16x16x32_bf16 v[220:223], v[24:27], a[108:111], v[220:223]\n"
"v_mfma_f32_16x16x32_bf16 v[224:227], v[28:31], a[96:99], v[224:227]\n"
"ds_read_b64_tr_b16 v[24:25], v6 offset:21504\n"
"v_mfma_f32_16x16x32_bf16 v[228:231], v[28:31], a[100:103], v[228:231]\n"
"ds_read_b64_tr_b16 v[26:27], v6 offset:22016\n"
"v_mfma_f32_16x16x32_bf16 v[232:235], v[28:31], a[104:107], v[232:235]\n"
"v_mfma_f32_16x16x32_bf16 v[236:239], v[28:31], a[108:111], v[236:239]\n"
"s_waitcnt lgkmcnt(12)\n"
"v_mfma_f32_16x16x32_bf16 v[208:211], v[32:35], a[112:115], v[208:211]\n"
"ds_read_b64_tr_b16 v[28:29], v6 offset:22528\n"
"v_mfma_f32_16x16x32_bf16 v[212:215], v[32:35], a[116:119], v[212:215]\n"
"ds_read_b64_tr_b16 v[30:31], v6 offset:23040\n"
"v_mfma_f32_16x16x32_bf16 v[216:219], v[32:35], a[120:123], v[216:219]\n"
"v_mfma_f32_16x16x32_bf16 v[220:223], v[32:35], a[124:127], v[220:223]\n"
"v_mfma_f32_16x16x32_bf16 v[224:227], v[36:39], a[112:115], v[224:227]\n"
"ds_read_b64_tr_b16 v[32:33], v6 offset:23552\n"
"v_mfma_f32_16x16x32_bf16 v[228:231], v[36:39], a[116:119], v[228:231]\n"
"ds_read_b64_tr_b16 v[34:35], v6 offset:24064\n"
"v_mfma_f32_16x16x32_bf16 v[232:235], v[36:39], a[120:123], v[232:235]\n"
"v_mfma_f32_16x16x32_bf16 v[236:239], v[36:39], a[124:127], v[236:239]\n"
"s_waitcnt lgkmcnt(12)\n"
"v_mfma_f32_16x16x32_bf16 v[208:211], v[40:43], a[128:131], v[208:211]\n"
"ds_read_b64_tr_b16 v[36:37], v6 offset:24576\n"
"v_mfma_f32_16x16x32_bf16 v[212:215], v[40:43], a[132:135], v[212:215]\n"
"ds_read_b64_tr_b16 v[38:39], v6 offset:25088\n"
"v_mfma_f32_16x16x32_bf16 v[216:219], v[40:43], a[136:139], v[216:219]\n"
"v_mfma_f32_16x16x32_bf16 v[220:223], v[40:43], a[140:143], v[220:223]\n"
"v_mfma_f32_16x16x32_bf16 v[224:227], v[44:47], a[128:131], v[224:227]\n"
"ds_read_b64_tr_b16 v[40:41], v6 offset:25600\n"
"v_mfma_f32_16x16x32_bf16 v[228:231], v[44:47], a[132:135], v[228:231]\n"
"ds_read_b64_tr_b16 v[42:43], v6 offset:26112\n"
"v_mfma_f32_16x16x32_bf16 v[232:235], v[44:47], a[136:139], v[232:235]\n"
"v_mfma_f32_16x16x32_bf16 v[236:239], v[44:47], a[140:143], v[236:239]\n"
"s_waitcnt lgkmcnt(12)\n"
"v_mfma_f32_16x16x32_bf16 v[208:211], v[12:15], a[144:147], v[208:211]\n"
"ds_read_b64_tr_b16 v[44:45], v6 offset:26624\n"
"v_mfma_f32_16x16x32_bf16 v[212:215], v[12:15], a[148:151], v[212:215]\n"
"ds_read_b64_tr_b16 v[46:47], v6 offset:27136\n"
"v_mfma_f32_16x16x32_bf16 v[216:219], v[12:15], a[152:155], v[216:219]\n"
"v_mfma_f32_16x16x32_bf16 v[220:223], v[12:15], a[156:159], v[220:223]\n"
"v_mfma_f32_16x16x32_bf16 v[224:227], v[16:19], a[144:147], v[224:227]\n"
"ds_read_b64_tr_b16 v[12:13], v6 offset:27648\n"
"v_mfma_f32_16x16x32_bf16 v[228:231], v[16:19], a[148:151], v[228:231]\n"
"ds_read_b64_tr_b16 v[14:15], v6 offset:28160\n"
"v_mfma_f32_16x16x32_bf16 v[232:235], v[16:19], a[152:155], v[232:235]\n"
"v_mfma_f32_16x16x32_bf16 v[236:239], v[16:19], a[156:159], v[236:239]\n"
"s_waitcnt lgkmcnt(12)\n"
"v_mfma_f32_16x16x32_bf16 v[208:211], v[20:23], a[160:163], v[208:211]\n"
"ds_read_b64_tr_b16 v[16:17], v6 offset:28672\n"
"v_mfma_f32_16x16x32_bf16 v[212:215], v[20:23], a[164:167], v[212:215]\n"
"ds_read_b64_tr_b16 v[18:19], v6 offset:29184\n"
"v_mfma_f32_16x16x32_bf16 v[216:219], v[20:23], a[168:171], v[216:219]\n"
"v_mfma_f32_16x16x32_bf16 v[220:223], v[20:23], a[172:175], v[220:223]\n"
"v_mfma_f32_16x16x32_bf16 v[224:227], v[24:27], a[160:163], v[224:227]\n"
"ds_read_b64_tr_b16 v[20:21], v6 offset:29696\n"
"v_mfma_f32_16x16x32_bf16 v[228:231], v[24:27], a[164:167], v[228:231]\n"
"ds_read_b64_tr_b16 v[22:23], v6 offset:30208\n"
"v_mfma_f32_16x16x32_bf16 v[232:235], v[24:27], a[168:171], v[232:235]\n"
"v_mfma_f32_16x16x32_bf16 v[236:239], v[24:27], a[172:175], v[236:239]\n"
"s_waitcnt lgkmcnt(12)\n"
"v_mfma_f32_16x16x32_bf16 v[208:211], v[28:31], a[176:179], v[208:211]\n"
"ds_read_b64_tr_b16 v[24:25], v6 offset:30720\n"
"v_mfma_f32_16x16x32_bf16 v[212:215], v[28:31], a[180:183], v[212:215]\n"
"ds_read_b64_tr_b16 v[26:27], v6 offset:31232\n"
"v_mfma_f32_16x16x32_bf16 v[216:219], v[28:31], a[184:187], v[216:219]\n"
"v_mfma_f32_16x16x32_bf16 v[220:223], v[28:31], a[188:191], v[220:223]\n"
"v_mfma_f32_16x16x32_bf16 v[224:227], v[32:35], a[176:179], v[224:227]\n"
"ds_read_b64_tr_b16 v[28:29], v6 offset:31744\n"
"v_mfma_f32_16x16x32_bf16 v[228:231], v[32:35], a[180:183], v[228:231]\n"
"ds_read_b64_tr_b16 v[30:31], v6 offset:32256\n"
"v_mfma_f32_16x16x32_bf16 v[232:235], v[32:35], a[184:187], v[232:235]\n"
"v_mfma_f32_16x16x32_bf16 v[236:239], v[32:35], a[188:191], v[236:239]\n"
"s_mov_b32 s22, 147456\n"
"buffer_store_dwordx4 v[116:119], v9, s[16:19], s22 offen sc1\n"
"s_waitcnt vmcnt(30)\n"
"s_barrier\n"
"s_waitcnt lgkmcnt(12)\n"
"v_mfma_f32_16x16x32_bf16 v[208:211], v[36:39], a[192:195], v[208:211]\n"
"ds_read_b64_tr_b16 v[32:33], v6 offset:32768\n"
"v_mfma_f32_16x16x32_bf16 v[212:215], v[36:39], a[196:199], v[212:215]\n"
"ds_read_b64_tr_b16 v[34:35], v6 offset:33280\n"
"v_mfma_f32_16x16x32_bf16 v[216:219], v[36:39], a[200:203], v[216:219]\n"
"s_add_u32 m0, s20, 0\n"
"s_mov_b32 s21, 294912\n"
"buffer_load_dwordx4 v1, s[8:11], s21 offen sc1 lds\n"
"v_mfma_f32_16x16x32_bf16 v[220:223], v[36:39], a[204:207], v[220:223]\n"
"v_mfma_f32_16x16x32_bf16 v[224:227], v[40:43], a[192:195], v[224:227]\n"
"ds_read_b64_tr_b16 v[36:37], v6 offset:33792\n"
"v_mfma_f32_16x16x32_bf16 v[228:231], v[40:43], a[196:199], v[228:231]\n"
"ds_read_b64_tr_b16 v[38:39], v6 offset:34304\n"
"v_mfma_f32_16x16x32_bf16 v[232:235], v[40:43], a[200:203], v[232:235]\n"
"buffer_load_dwordx4 v1, s[8:11], s21 offen offset:1024 sc1 lds\n"
"v_mfma_f32_16x16x32_bf16 v[236:239], v[40:43], a[204:207], v[236:239]\n"
"s_waitcnt lgkmcnt(12)\n"
"v_mfma_f32_16x16x32_bf16 v[208:211], v[44:47], a[208:211], v[208:211]\n"
"ds_read_b64_tr_b16 v[40:41], v6 offset:34816\n"
"v_mfma_f32_16x16x32_bf16 v[212:215], v[44:47], a[212:215], v[212:215]\n"
"ds_read_b64_tr_b16 v[42:43], v6 offset:35328\n"
"v_mfma_f32_16x16x32_bf16 v[216:219], v[44:47], a[216:219], v[216:219]\n"
"buffer_load_dwordx4 v1, s[8:11], s21 offen offset:2048 sc1 lds\n"
"v_mfma_f32_16x16x32_bf16 v[220:223], v[44:47], a[220:223], v[220:223]\n"
"v_mfma_f32_16x16x32_bf16 v[224:227], v[12:15], a[208:211], v[224:227]\n"
"ds_read_b64_tr_b16 v[44:45], v6 offset:35840\n"
"v_mfma_f32_16x16x32_bf16 v[228:231], v[12:15], a[212:215], v[228:231]\n"
"ds_read_b64_tr_b16 v[46:47], v6 offset:36352\n"
"v_mfma_f32_16x16x32_bf16 v[232:235], v[12:15], a[216:219], v[232:235]\n"
"buffer_load_dwordx4 v1, s[8:11], s21 offen offset:3072 sc1 lds\n"
"v_mfma_f32_16x16x32_bf16 v[236:239], v[12:15], a[220:223], v[236:239]\n"
"s_waitcnt lgkmcnt(12)\n"
"v_mfma_f32_16x16x32_bf16 v[208:211], v[16:19], a[224:227], v[208:211]\n"
"ds_read_b64_tr_b16 v[12:13], v6 offset:36864\n"
"v_mfma_f32_16x16x32_bf16 v[212:215], v[16:19], a[228:231], v[212:215]\n"
"ds_read_b64_tr_b16 v[14:15], v6 offset:37376\n"
"v_mfma_f32_16x16x32_bf16 v[216:219], v[16:19], a[232:235], v[216:219]\n"
"v_mfma_f32_16x16x32_bf16 v[220:223], v[16:19], a[236:239], v[220:223]\n"
"v_mfma_f32_16x16x32_bf16 v[224:227], v[20:23], a[224:227], v[224:227]\n"
"ds_read_b64_tr_b16 v[16:17], v6 offset:37888\n"
"v_mfma_f32_16x16x32_bf16 v[228:231], v[20:23], a[228:231], v[228:231]\n"
"ds_read_b64_tr_b16 v[18:19], v6 offset:38400\n"
"v_mfma_f32_16x16x32_bf16 v[232:235], v[20:23], a[232:235], v[232:235]\n"
"v_mfma_f32_16x16x32_bf16 v[236:239], v[20:23], a[236:239], v[236:239]\n"
"s_waitcnt lgkmcnt(12)\n"
"v_mfma_f32_16x16x32_bf16 v[208:211], v[24:27], a[240:243], v[208:211]\n"
"ds_read_b64_tr_b16 v[20:21], v6 offset:38912\n"
"v_mfma_f32_16x16x32_bf16 v[212:215], v[24:27], a[244:247], v[212:215]\n"
"ds_read_b64_tr_b16 v[22:23], v6 offset:39424\n"
"v_mfma_f32_16x16x32_bf16 v[216:219], v[24:27], a[248:251], v[216:219]\n"
"v_mfma_f32_16x16x32_bf16 v[220:223], v[24:27], a[252:255], v[220:223]\n"
"v_mfma_f32_16x16x32_bf16 v[224:227], v[28:31], a[240:243], v[224:227]\n"
"ds_read_b64_tr_b16 v[24:25], v6 offset:39936\n"
"v_mfma_f32_16x16x32_bf16 v[228:231], v[28:31], a[244:247], v[228:231]\n"
"ds_read_b64_tr_b16 v[26:27], v6 offset:40448\n"
"v_mfma_f32_16x16x32_bf16 v[232:235], v[28:31], a[248:251], v[232:235]\n"
"v_mfma_f32_16x16x32_bf16 v[236:239], v[28:31], a[252:255], v[236:239]\n"
"s_waitcnt lgkmcnt(12)\n"
"v_mfma_f32_16x16x32_bf16 v[208:211], v[32:35], v[48:51], v[208:211]\n"
"ds_read_b64_tr_b16 v[28:29], v6 offset:40960\n"
"v_mfma_f32_16x16x32_bf16 v[212:215], v[32:35], v[52:55], v[212:215]\n"
"ds_read_b64_tr_b16 v[30:31], v6 offset:41472\n"
"v_mfma_f32_16x16x32_bf16 v[216:219], v[32:35], v[56:59], v[216:219]\n"
"v_mfma_f32_16x16x32_bf16 v[220:223], v[32:35], v[60:63], v[220:223]\n"
"v_mfma_f32_16x16x32_bf16 v[224:227], v[36:39], v[48:51], v[224:227]\n"
"ds_read_b64_tr_b16 v[32:33], v6 offset:41984\n"
"v_mfma_f32_16x16x32_bf16 v[228:231], v[36:39], v[52:55], v[228:231]\n"
"ds_read_b64_tr_b16 v[34:35], v6 offset:42496\n"
"v_mfma_f32_16x16x32_bf16 v[232:235], v[36:39], v[56:59], v[232:235]\n"
"v_mfma_f32_16x16x32_bf16 v[236:239], v[36:39], v[60:63], v[236:239]\n"
"s_waitcnt lgkmcnt(12)\n"
"v_mfma_f32_16x16x32_bf16 v[208:211], v[40:43], v[64:67], v[208:211]\n"
"ds_read_b64_tr_b16 v[36:37], v6 offset:43008\n"
"v_mfma_f32_16x16x32_bf16 v[212:215], v[40:43], v[68:71], v[212:215]\n"
"ds_read_b64_tr_b16 v[38:39], v6 offset:43520\n"
"v_mfma_f32_16x16x32_bf16 v[216:219], v[40:43], v[72:75], v[216:219]\n"
"v_mfma_f32_16x16x32_bf16 v[220:223], v[40:43], v[76:79], v[220:223]\n"
"v_mfma_f32_16x16x32_bf16 v[224:227], v[44:47], v[64:67], v[224:227]\n"
"ds_read_b64_tr_b16 v[40:41], v6 offset:44032\n"
"v_mfma_f32_16x16x32_bf16 v[228:231], v[44:47], v[68:71], v[228:231]\n"
"ds_read_b64_tr_b16 v[42:43], v6 offset:44544\n"
"v_mfma_f32_16x16x32_bf16 v[232:235], v[44:47], v[72:75], v[232:235]\n"
"v_mfma_f32_16x16x32_bf16 v[236:239], v[44:47], v[76:79], v[236:239]\n"
"s_mov_b32 s22, 294912\n"
"buffer_store_dwordx4 v[120:123], v9, s[16:19], s22 offen sc1\n"
"s_waitcnt lgkmcnt(12)\n"
"v_mfma_f32_16x16x32_bf16 v[80:83], v[12:15], a[0:3], 0\n"
"ds_read_b64_tr_b16 v[44:45], v6 offset:45056\n"
"v_mfma_f32_16x16x32_bf16 v[84:87], v[12:15], a[4:7], 0\n"
"ds_read_b64_tr_b16 v[46:47], v6 offset:45568\n"
"v_mfma_f32_16x16x32_bf16 v[88:91], v[12:15], a[8:11], 0\n"
"v_mfma_f32_16x16x32_bf16 v[92:95], v[12:15], a[12:15], 0\n"
"v_mfma_f32_16x16x32_bf16 v[96:99], v[16:19], a[0:3], 0\n"
"ds_read_b64_tr_b16 v[12:13], v6 offset:46080\n"
"v_max_f32_e32 v208, 0, v208\n"
"v_mfma_f32_16x16x32_bf16 v[100:103], v[16:19], a[4:7], 0\n"
"ds_read_b64_tr_b16 v[14:15], v6 offset:46592\n"
"v_max_f32_e32 v209, 0, v209\n"
"v_mfma_f32_16x16x32_bf16 v[104:107], v[16:19], a[8:11], 0\n"
"v_max_f32_e32 v210, 0, v210\n"
"v_max_f32_e32 v211, 0, v211\n"
"v_mfma_f32_16x16x32_bf16 v[108:111], v[16:19], a[12:15], 0\n"
"v_mul_f32_e32 v224, v208, v224\n"
"v_mul_f32_e32 v225, v209, v225\n"
"s_waitcnt lgkmcnt(12)\n"
"v_mfma_f32_16x16x32_bf16 v[80:83], v[20:23], a[16:19], v[80:83]\n"
"ds_read_b64_tr_b16 v[16:17], v6 offset:47104\n"
"v_mul_f32_e32 v226, v210, v226\n"
"v_mfma_f32_16x16x32_bf16 v[84:87], v[20:23], a[20:23], v[84:87]\n"
"ds_read_b64_tr_b16 v[18:19], v6 offset:47616\n"
"v_mul_f32_e32 v227, v211, v227\n"
"v_mfma_f32_16x16x32_bf16 v[88:91], v[20:23], a[24:27], v[88:91]\n"
"v_cvt_pk_bf16_f32 v178, v224, v225\n"
"v_cvt_pk_bf16_f32 v179, v226, v227\n"
"v_mfma_f32_16x16x32_bf16 v[92:95], v[20:23], a[28:31], v[92:95]\n"
"v_max_f32_e32 v212, 0, v212\n"
"v_mfma_f32_16x16x32_bf16 v[96:99], v[24:27], a[16:19], v[96:99]\n"
"ds_read_b64_tr_b16 v[20:21], v6 offset:48128\n"
"v_max_f32_e32 v213, 0, v213\n"
"v_mfma_f32_16x16x32_bf16 v[100:103], v[24:27], a[20:23], v[100:103]\n"
"ds_read_b64_tr_b16 v[22:23], v6 offset:48640\n"
"v_max_f32_e32 v214, 0, v214\n"
"v_mfma_f32_16x16x32_bf16 v[104:107], v[24:27], a[24:27], v[104:107]\n"
"v_max_f32_e32 v215, 0, v215\n"
"v_mul_f32_e32 v228, v212, v228\n"
"v_mfma_f32_16x16x32_bf16 v[108:111], v[24:27], a[28:31], v[108:111]\n"
"v_mul_f32_e32 v229, v213, v229\n"
"v_mul_f32_e32 v230, v214, v230\n"
"s_waitcnt vmcnt(31)\n"
"s_barrier\n"
"s_waitcnt lgkmcnt(12)\n"
"v_mfma_f32_16x16x32_bf16 v[80:83], v[28:31], a[32:35], v[80:83]\n"
"ds_read_b64_tr_b16 v[24:25], v6 offset:49152\n"
"v_mul_f32_e32 v231, v215, v231\n"
"v_mfma_f32_16x16x32_bf16 v[84:87], v[28:31], a[36:39], v[84:87]\n"
"ds_read_b64_tr_b16 v[26:27], v6 offset:49664\n"
"v_cvt_pk_bf16_f32 v182, v228, v229\n"
"v_mfma_f32_16x16x32_bf16 v[88:91], v[28:31], a[40:43], v[88:91]\n"
"s_add_u32 m0, s20, 16384\n"
"s_mov_b32 s21, 311296\n"
"buffer_load_dwordx4 v1, s[8:11], s21 offen sc1 lds\n"
"v_cvt_pk_bf16_f32 v183, v230, v231\n"
"v_mfma_f32_16x16x32_bf16 v[92:95], v[28:31], a[44:47], v[92:95]\n"
"v_max_f32_e32 v216, 0, v216\n"
"v_mfma_f32_16x16x32_bf16 v[96:99], v[32:35], a[32:35], v[96:99]\n"
"ds_read_b64_tr_b16 v[28:29], v6 offset:50176\n"
"v_max_f32_e32 v217, 0, v217\n"
"v_mfma_f32_16x16x32_bf16 v[100:103], v[32:35], a[36:39], v[100:103]\n"
"ds_read_b64_tr_b16 v[30:31], v6 offset:50688\n"
"v_max_f32_e32 v218, 0, v218\n"
"v_mfma_f32_16x16x32_bf16 v[104:107], v[32:35], a[40:43], v[104:107]\n"
"buffer_load_dwordx4 v1, s[8:11], s21 offen offset:1024 sc1 lds\n"
"v_max_f32_e32 v219, 0, v219\n"
"v_mfma_f32_16x16x32_bf16 v[108:111], v[32:35], a[44:47], v[108:111]\n"
"v_mul_f32_e32 v232, v216, v232\n"
"v_mul_f32_e32 v233, v217, v233\n"
"s_waitcnt lgkmcnt(12)\n"
"v_mfma_f32_16x16x32_bf16 v[80:83], v[36:39], a[48:51], v[80:83]\n"
"ds_read_b64_tr_b16 v[32:33], v6 offset:51200\n"
"v_mul_f32_e32 v234, v218, v234\n"
"v_mfma_f32_16x16x32_bf16 v[84:87], v[36:39], a[52:55], v[84:87]\n"
"ds_read_b64_tr_b16 v[34:35], v6 offset:51712\n"
"v_mul_f32_e32 v235, v219, v235\n"
"v_mfma_f32_16x16x32_bf16 v[88:91], v[36:39], a[56:59], v[88:91]\n"
"buffer_load_dwordx4 v1, s[8:11], s21 offen offset:2048 sc1 lds\n"
"v_cvt_pk_bf16_f32 v186, v232, v233\n"
"v_mfma_f32_16x16x32_bf16 v[92:95], v[36:39], a[60:63], v[92:95]\n"
"v_cvt_pk_bf16_f32 v187, v234, v235\n"
"v_mfma_f32_16x16x32_bf16 v[96:99], v[40:43], a[48:51], v[96:99]\n"
"ds_read_b64_tr_b16 v[36:37], v6 offset:52224\n"
"v_max_f32_e32 v220, 0, v220\n"
"v_mfma_f32_16x16x32_bf16 v[100:103], v[40:43], a[52:55], v[100:103]\n"
"ds_read_b64_tr_b16 v[38:39], v6 offset:52736\n"
"v_max_f32_e32 v221, 0, v221\n"
"v_mfma_f32_16x16x32_bf16 v[104:107], v[40:43], a[56:59], v[104:107]\n"
"buffer_load_dwordx4 v1, s[8:11], s21 offen offset:3072 sc1 lds\n"
"v_max_f32_e32 v222, 0, v222\n"
"v_mfma_f32_16x16x32_bf16 v[108:111], v[40:43], a[60:63], v[108:111]\n"
"v_max_f32_e32 v223, 0, v223\n"
"v_mul_f32_e32 v236, v220, v236\n"
"s_waitcnt lgkmcnt(12)\n"
"v_mfma_f32_16x16x32_bf16 v[80:83], v[44:47], a[64:67], v[80:83]\n"
"ds_read_b64_tr_b16 v[40:41], v6 offset:53248\n"
"v_mul_f32_e32 v237, v221, v237\n"
"v_mfma_f32_16x16x32_bf16 v[84:87], v[44:47], a[68:71], v[84:87]\n"
"ds_read_b64_tr_b16 v[42:43], v6 offset:53760\n"
"v_mul_f32_e32 v238, v222, v238\n"
"v_mfma_f32_16x16x32_bf16 v[88:91], v[44:47], a[72:75], v[88:91]\n"
"v_mul_f32_e32 v239, v223, v239\n"
"v_cvt_pk_bf16_f32 v190, v236, v237\n"
"v_mfma_f32_16x16x32_bf16 v[92:95], v[44:47], a[76:79], v[92:95]\n"
"v_cvt_pk_bf16_f32 v191, v238, v239\n"
"v_mfma_f32_16x16x32_bf16 v[96:99], v[12:15], a[64:67], v[96:99]\n"
"ds_read_b64_tr_b16 v[44:45], v6 offset:54272\n"
"v_mfma_f32_16x16x32_bf16 v[100:103], v[12:15], a[68:71], v[100:103]\n"
"ds_read_b64_tr_b16 v[46:47], v6 offset:54784\n"
"v_mfma_f32_16x16x32_bf16 v[104:107], v[12:15], a[72:75], v[104:107]\n"
"v_mfma_f32_16x16x32_bf16 v[108:111], v[12:15], a[76:79], v[108:111]\n"
"s_waitcnt lgkmcnt(12)\n"
"v_mfma_f32_16x16x32_bf16 v[80:83], v[16:19], a[80:83], v[80:83]\n"
"ds_read_b64_tr_b16 v[12:13], v6 offset:55296\n"
"v_mfma_f32_16x16x32_bf16 v[84:87], v[16:19], a[84:87], v[84:87]\n"
"ds_read_b64_tr_b16 v[14:15], v6 offset:55808\n"
"v_mfma_f32_16x16x32_bf16 v[88:91], v[16:19], a[88:91], v[88:91]\n"
"v_mfma_f32_16x16x32_bf16 v[92:95], v[16:19], a[92:95], v[92:95]\n"
"v_mfma_f32_16x16x32_bf16 v[96:99], v[20:23], a[80:83], v[96:99]\n"
"ds_read_b64_tr_b16 v[16:17], v6 offset:56320\n"
"v_mfma_f32_16x16x32_bf16 v[100:103], v[20:23], a[84:87], v[100:103]\n"
"ds_read_b64_tr_b16 v[18:19], v6 offset:56832\n"
"v_mfma_f32_16x16x32_bf16 v[104:107], v[20:23], a[88:91], v[104:107]\n"
"v_mfma_f32_16x16x32_bf16 v[108:111], v[20:23], a[92:95], v[108:111]\n"
"s_mov_b32 s22, 442368\n"
"buffer_store_dwordx4 v[124:127], v9, s[16:19], s22 offen sc1\n"
"s_waitcnt lgkmcnt(12)\n"
"v_mfma_f32_16x16x32_bf16 v[80:83], v[24:27], a[96:99], v[80:83]\n"
"ds_read_b64_tr_b16 v[20:21], v6 offset:57344\n"
"v_mfma_f32_16x16x32_bf16 v[84:87], v[24:27], a[100:103], v[84:87]\n"
"ds_read_b64_tr_b16 v[22:23], v6 offset:57856\n"
"v_mfma_f32_16x16x32_bf16 v[88:91], v[24:27], a[104:107], v[88:91]\n"
"v_mfma_f32_16x16x32_bf16 v[92:95], v[24:27], a[108:111], v[92:95]\n"
"v_mfma_f32_16x16x32_bf16 v[96:99], v[28:31], a[96:99], v[96:99]\n"
"ds_read_b64_tr_b16 v[24:25], v6 offset:58368\n"
"v_mfma_f32_16x16x32_bf16 v[100:103], v[28:31], a[100:103], v[100:103]\n"
"ds_read_b64_tr_b16 v[26:27], v6 offset:58880\n"
"v_mfma_f32_16x16x32_bf16 v[104:107], v[28:31], a[104:107], v[104:107]\n"
"v_mfma_f32_16x16x32_bf16 v[108:111], v[28:31], a[108:111], v[108:111]\n"
"s_waitcnt lgkmcnt(12)\n"
"v_mfma_f32_16x16x32_bf16 v[80:83], v[32:35], a[112:115], v[80:83]\n"
"ds_read_b64_tr_b16 v[28:29], v6 offset:59392\n"
"v_mfma_f32_16x16x32_bf16 v[84:87], v[32:35], a[116:119], v[84:87]\n"
"ds_read_b64_tr_b16 v[30:31], v6 offset:59904\n"
"v_mfma_f32_16x16x32_bf16 v[88:91], v[32:35], a[120:123], v[88:91]\n"
"v_mfma_f32_16x16x32_bf16 v[92:95], v[32:35], a[124:127], v[92:95]\n"
"v_mfma_f32_16x16x32_bf16 v[96:99], v[36:39], a[112:115], v[96:99]\n"
"ds_read_b64_tr_b16 v[32:33], v6 offset:60416\n"
"v_mfma_f32_16x16x32_bf16 v[100:103], v[36:39], a[116:119], v[100:103]\n"
"ds_read_b64_tr_b16 v[34:35], v6 offset:60928\n"
"v_mfma_f32_16x16x32_bf16 v[104:107], v[36:39], a[120:123], v[104:107]\n"
"v_mfma_f32_16x16x32_bf16 v[108:111], v[36:39], a[124:127], v[108:111]\n"
"s_waitcnt lgkmcnt(12)\n"
"v_mfma_f32_16x16x32_bf16 v[80:83], v[40:43], a[128:131], v[80:83]\n"
"ds_read_b64_tr_b16 v[36:37], v6 offset:61440\n"
"v_mfma_f32_16x16x32_bf16 v[84:87], v[40:43], a[132:135], v[84:87]\n"
"ds_read_b64_tr_b16 v[38:39], v6 offset:61952\n"
"v_mfma_f32_16x16x32_bf16 v[88:91], v[40:43], a[136:139], v[88:91]\n"
"v_mfma_f32_16x16x32_bf16 v[92:95], v[40:43], a[140:143], v[92:95]\n"
"v_mfma_f32_16x16x32_bf16 v[96:99], v[44:47], a[128:131], v[96:99]\n"
"ds_read_b64_tr_b16 v[40:41], v6 offset:62464\n"
"v_mfma_f32_16x16x32_bf16 v[100:103], v[44:47], a[132:135], v[100:103]\n"
"ds_read_b64_tr_b16 v[42:43], v6 offset:62976\n"
"v_mfma_f32_16x16x32_bf16 v[104:107], v[44:47], a[136:139], v[104:107]\n"
"v_mfma_f32_16x16x32_bf16 v[108:111], v[44:47], a[140:143], v[108:111]\n"
"s_waitcnt lgkmcnt(12)\n"
"v_mfma_f32_16x16x32_bf16 v[80:83], v[12:15], a[144:147], v[80:83]\n"
"ds_read_b64_tr_b16 v[44:45], v6 offset:63488\n"
"v_mfma_f32_16x16x32_bf16 v[84:87], v[12:15], a[148:151], v[84:87]\n"
"ds_read_b64_tr_b16 v[46:47], v6 offset:64000\n"
"v_mfma_f32_16x16x32_bf16 v[88:91], v[12:15], a[152:155], v[88:91]\n"
"v_mfma_f32_16x16x32_bf16 v[92:95], v[12:15], a[156:159], v[92:95]\n"
"v_mfma_f32_16x16x32_bf16 v[96:99], v[16:19], a[144:147], v[96:99]\n"
"ds_read_b64_tr_b16 v[12:13], v6 offset:64512\n"
"v_mfma_f32_16x16x32_bf16 v[100:103], v[16:19], a[148:151], v[100:103]\n"
"ds_read_b64_tr_b16 v[14:15], v6 offset:65024\n"
"v_mfma_f32_16x16x32_bf16 v[104:107], v[16:19], a[152:155], v[104:107]\n"
"v_mfma_f32_16x16x32_bf16 v[108:111], v[16:19], a[156:159], v[108:111]\n"
"s_waitcnt vmcnt(32)\n"
"s_barrier\n"
"s_waitcnt lgkmcnt(12)\n"
"v_mfma_f32_16x16x32_bf16 v[80:83], v[20:23], a[160:163], v[80:83]\n"
"ds_read_b64_tr_b16 v[16:17], v7 offset:0\n"
"v_mfma_f32_16x16x32_bf16 v[84:87], v[20:23], a[164:167], v[84:87]\n"
"ds_read_b64_tr_b16 v[18:19], v7 offset:512\n"
"v_mfma_f32_16x16x32_bf16 v[88:91], v[20:23], a[168:171], v[88:91]\n"
"s_add_u32 m0, s20, 32768\n"
"s_mov_b32 s21, 327680\n"
"buffer_load_dwordx4 v1, s[8:11], s21 offen sc1 lds\n"
"v_mfma_f32_16x16x32_bf16 v[92:95], v[20:23], a[172:175], v[92:95]\n"
"v_mfma_f32_16x16x32_bf16 v[96:99], v[24:27], a[160:163], v[96:99]\n"
"ds_read_b64_tr_b16 v[20:21], v7 offset:1024\n"
"v_mfma_f32_16x16x32_bf16 v[100:103], v[24:27], a[164:167], v[100:103]\n"
"ds_read_b64_tr_b16 v[22:23], v7 offset:1536\n"
"v_mfma_f32_16x16x32_bf16 v[104:107], v[24:27], a[168:171], v[104:107]\n"
"buffer_load_dwordx4 v1, s[8:11], s21 offen offset:1024 sc1 lds\n"
"v_mfma_f32_16x16x32_bf16 v[108:111], v[24:27], a[172:175], v[108:111]\n"
"s_waitcnt lgkmcnt(12)\n"
"v_mfma_f32_16x16x32_bf16 v[80:83], v[28:31], a[176:179], v[80:83]\n"
"ds_read_b64_tr_b16 v[24:25], v7 offset:2048\n"
"v_mfma_f32_16x16x32_bf16 v[84:87], v[28:31], a[180:183], v[84:87]\n"
"ds_read_b64_tr_b16 v[26:27], v7 offset:2560\n"
"v_mfma_f32_16x16x32_bf16 v[88:91], v[28:31], a[184:187], v[88:91]\n"
"buffer_load_dwordx4 v1, s[8:11], s21 offen offset:2048 sc1 lds\n"
"v_mfma_f32_16x16x32_bf16 v[92:95], v[28:31], a[188:191], v[92:95]\n"
"v_mfma_f32_16x16x32_bf16 v[96:99], v[32:35], a[176:179], v[96:99]\n"
"ds_read_b64_tr_b16 v[28:29], v7 offset:3072\n"
"v_mfma_f32_16x16x32_bf16 v[100:103], v[32:35], a[180:183], v[100:103]\n"
"ds_read_b64_tr_b16 v[30:31], v7 offset:3584\n"
"v_mfma_f32_16x16x32_bf16 v[104:107], v[32:35], a[184:187], v[104:107]\n"
"buffer_load_dwordx4 v1, s[8:11], s21 offen offset:3072 sc1 lds\n"
"v_mfma_f32_16x16x32_bf16 v[108:111], v[32:35], a[188:191], v[108:111]\n"
"s_mov_b32 s22, 1024\n"
"buffer_store_dwordx4 v[176:179], v9, s[16:19], s22 offen sc1\n"
"s_waitcnt lgkmcnt(12)\n"
"v_mfma_f32_16x16x32_bf16 v[80:83], v[36:39], a[192:195], v[80:83]\n"
"ds_read_b64_tr_b16 v[32:33], v7 offset:4096\n"
"v_mfma_f32_16x16x32_bf16 v[84:87], v[36:39], a[196:199], v[84:87]\n"
"ds_read_b64_tr_b16 v[34:35], v7 offset:4608\n"
"v_mfma_f32_16x16x32_bf16 v[88:91], v[36:39], a[200:203], v[88:91]\n"
"v_mfma_f32_16x16x32_bf16 v[92:95], v[36:39], a[204:207], v[92:95]\n"
"v_mfma_f32_16x16x32_bf16 v[96:99], v[40:43], a[192:195], v[96:99]\n"
"ds_read_b64_tr_b16 v[36:37], v7 offset:5120\n"
"v_mfma_f32_16x16x32_bf16 v[100:103], v[40:43], a[196:199], v[100:103]\n"
"ds_read_b64_tr_b16 v[38:39], v7 offset:5632\n"
"v_mfma_f32_16x16x32_bf16 v[104:107], v[40:43], a[200:203], v[104:107]\n"
"v_mfma_f32_16x16x32_bf16 v[108:111], v[40:43], a[204:207], v[108:111]\n"
"s_waitcnt lgkmcnt(12)\n"
"v_mfma_f32_16x16x32_bf16 v[80:83], v[44:47], a[208:211], v[80:83]\n"
"ds_read_b64_tr_b16 v[40:41], v7 offset:6144\n"
"v_mfma_f32_16x16x32_bf16 v[84:87], v[44:47], a[212:215], v[84:87]\n"
"ds_read_b64_tr_b16 v[42:43], v7 offset:6656\n"
"v_mfma_f32_16x16x32_bf16 v[88:91], v[44:47], a[216:219], v[88:91]\n"
"v_mfma_f32_16x16x32_bf16 v[92:95], v[44:47], a[220:223], v[92:95]\n"
"v_mfma_f32_16x16x32_bf16 v[96:99], v[12:15], a[208:211], v[96:99]\n"
"ds_read_b64_tr_b16 v[44:45], v7 offset:7168\n"
"v_mfma_f32_16x16x32_bf16 v[100:103], v[12:15], a[212:215], v[100:103]\n"
"ds_read_b64_tr_b16 v[46:47], v7 offset:7680\n"
"v_mfma_f32_16x16x32_bf16 v[104:107], v[12:15], a[216:219], v[104:107]\n"
"v_mfma_f32_16x16x32_bf16 v[108:111], v[12:15], a[220:223], v[108:111]\n"
"s_waitcnt lgkmcnt(12)\n"
"v_mfma_f32_16x16x32_bf16 v[80:83], v[16:19], a[224:227], v[80:83]\n"
"ds_read_b64_tr_b16 v[12:13], v7 offset:8192\n"
"v_mfma_f32_16x16x32_bf16 v[84:87], v[16:19], a[228:231], v[84:87]\n"
"ds_read_b64_tr_b16 v[14:15], v7 offset:8704\n"
"v_mfma_f32_16x16x32_bf16 v[88:91], v[16:19], a[232:235], v[88:91]\n"
"v_mfma_f32_16x16x32_bf16 v[92:95], v[16:19], a[236:239], v[92:95]\n"
"v_mfma_f32_16x16x32_bf16 v[96:99], v[20:23], a[224:227], v[96:99]\n"
"ds_read_b64_tr_b16 v[16:17], v7 offset:9216\n"
"v_mfma_f32_16x16x32_bf16 v[100:103], v[20:23], a[228:231], v[100:103]\n"
"ds_read_b64_tr_b16 v[18:19], v7 offset:9728\n"
"v_mfma_f32_16x16x32_bf16 v[104:107], v[20:23], a[232:235], v[104:107]\n"
"v_mfma_f32_16x16x32_bf16 v[108:111], v[20:23], a[236:239], v[108:111]\n"
"s_waitcnt lgkmcnt(12)\n"
"v_mfma_f32_16x16x32_bf16 v[80:83], v[24:27], a[240:243], v[80:83]\n"
"ds_read_b64_tr_b16 v[20:21], v7 offset:10240\n"
"v_mfma_f32_16x16x32_bf16 v[84:87], v[24:27], a[244:247], v[84:87]\n"
"ds_read_b64_tr_b16 v[22:23], v7 offset:10752\n"
"v_mfma_f32_16x16x32_bf16 v[88:91], v[24:27], a[248:251], v[88:91]\n"
"v_mfma_f32_16x16x32_bf16 v[92:95], v[24:27], a[252:255], v[92:95]\n"
"v_mfma_f32_16x16x32_bf16 v[96:99], v[28:31], a[240:243], v[96:99]\n"
"ds_read_b64_tr_b16 v[24:25], v7 offset:11264\n"
"v_mfma_f32_16x16x32_bf16 v[100:103], v[28:31], a[244:247], v[100:103]\n"
"ds_read_b64_tr_b16 v[26:27], v7 offset:11776\n"
"v_mfma_f32_16x16x32_bf16 v[104:107], v[28:31], a[248:251], v[104:107]\n"
"v_mfma_f32_16x16x32_bf16 v[108:111], v[28:31], a[252:255], v[108:111]\n"
"s_waitcnt lgkmcnt(12)\n"
"v_mfma_f32_16x16x32_bf16 v[80:83], v[32:35], v[48:51], v[80:83]\n"
"ds_read_b64_tr_b16 v[28:29], v7 offset:12288\n"
"v_mfma_f32_16x16x32_bf16 v[84:87], v[32:35], v[52:55], v[84:87]\n"
"ds_read_b64_tr_b16 v[30:31], v7 offset:12800\n"
"v_mfma_f32_16x16x32_bf16 v[88:91], v[32:35], v[56:59], v[88:91]\n"
"v_mfma_f32_16x16x32_bf16 v[92:95], v[32:35], v[60:63], v[92:95]\n"
"v_mfma_f32_16x16x32_bf16 v[96:99], v[36:39], v[48:51], v[96:99]\n"
"ds_read_b64_tr_b16 v[32:33], v7 offset:13312\n"
"v_mfma_f32_16x16x32_bf16 v[100:103], v[36:39], v[52:55], v[100:103]\n"
"ds_read_b64_tr_b16 v[34:35], v7 offset:13824\n"
"v_mfma_f32_16x16x32_bf16 v[104:107], v[36:39], v[56:59], v[104:107]\n"
"v_mfma_f32_16x16x32_bf16 v[108:111], v[36:39], v[60:63], v[108:111]\n"
"s_waitcnt lgkmcnt(12)\n"
"v_mfma_f32_16x16x32_bf16 v[80:83], v[40:43], v[64:67], v[80:83]\n"
"ds_read_b64_tr_b16 v[36:37], v7 offset:14336\n"
"v_mfma_f32_16x16x32_bf16 v[84:87], v[40:43], v[68:71], v[84:87]\n"
"ds_read_b64_tr_b16 v[38:39], v7 offset:14848\n"
"v_mfma_f32_16x16x32_bf16 v[88:91], v[40:43], v[72:75], v[88:91]\n"
"v_mfma_f32_16x16x32_bf16 v[92:95], v[40:43], v[76:79], v[92:95]\n"
"v_mfma_f32_16x16x32_bf16 v[96:99], v[44:47], v[64:67], v[96:99]\n"
"ds_read_b64_tr_b16 v[40:41], v7 offset:15360\n"
"v_mfma_f32_16x16x32_bf16 v[100:103], v[44:47], v[68:71], v[100:103]\n"
"ds_read_b64_tr_b16 v[42:43], v7 offset:15872\n"
"v_mfma_f32_16x16x32_bf16 v[104:107], v[44:47], v[72:75], v[104:107]\n"
"v_mfma_f32_16x16x32_bf16 v[108:111], v[44:47], v[76:79], v[108:111]\n"
"s_mov_b32 s22, 148480\n"
"buffer_store_dwordx4 v[180:183], v9, s[16:19], s22 offen sc1\n"
"s_waitcnt vmcnt(34)\n"
"s_barrier\n"
"s_waitcnt lgkmcnt(12)\n"
"v_mfma_f32_16x16x32_bf16 v[112:115], v[12:15], a[0:3], 0\n"
"ds_read_b64_tr_b16 v[44:45], v7 offset:16384\n"
"v_mfma_f32_16x16x32_bf16 v[116:119], v[12:15], a[4:7], 0\n"
"ds_read_b64_tr_b16 v[46:47], v7 offset:16896\n"
"v_mfma_f32_16x16x32_bf16 v[120:123], v[12:15], a[8:11], 0\n"
"s_add_u32 m0, s20, 49152\n"
"s_mov_b32 s21, 0\n"
"buffer_load_dwordx4 v1, s[24:27], s21 offen sc1 lds\n"
"v_mfma_f32_16x16x32_bf16 v[124:127], v[12:15], a[12:15], 0\n"
"v_mfma_f32_16x16x32_bf16 v[128:131], v[16:19], a[0:3], 0\n"
"ds_read_b64_tr_b16 v[12:13], v7 offset:17408\n"
"v_max_f32_e32 v80, 0, v80\n"
"v_mfma_f32_16x16x32_bf16 v[132:135], v[16:19], a[4:7], 0\n"
"ds_read_b64_tr_b16 v[14:15], v7 offset:17920\n"
"v_max_f32_e32 v81, 0, v81\n"
"v_mfma_f32_16x16x32_bf16 v[136:139], v[16:19], a[8:11], 0\n"
"buffer_load_dwordx4 v1, s[24:27], s21 offen offset:1024 sc1 lds\n"
"v_max_f32_e32 v82, 0, v82\n"
"v_mfma_f32_16x16x32_bf16 v[140:143], v[16:19], a[12:15], 0\n"
"v_max_f32_e32 v83, 0, v83\n"
"v_mul_f32_e32 v96, v80, v96\n"
"s_waitcnt lgkmcnt(12)\n"
"v_mfma_f32_16x16x32_bf16 v[112:115], v[20:23], a[16:19], v[112:115]\n"
"ds_read_b64_tr_b16 v[16:17], v7 offset:18432\n"
"v_mul_f32_e32 v97, v81, v97\n"
"v_mfma_f32_16x16x32_bf16 v[116:119], v[20:23], a[20:23], v[116:119]\n"
"ds_read_b64_tr_b16 v[18:19], v7 offset:18944\n"
"v_mul_f32_e32 v98, v82, v98\n"
"v_mfma_f32_16x16x32_bf16 v[120:123], v[20:23], a[24:27], v[120:123]\n"
"buffer_load_dwordx4 v1, s[24:27], s21 offen offset:2048 sc1 lds\n"
"v_mul_f32_e32 v99, v83, v99\n"
"v_mfma_f32_16x16x32_bf16 v[124:127], v[20:23], a[28:31], v[124:127]\n"
"v_cvt_pk_bf16_f32 v80, v96, v97\n"
"v_cvt_pk_bf16_f32 v81, v98, v99\n"
"v_mfma_f32_16x16x32_bf16 v[128:131], v[24:27], a[16:19], v[128:131]\n"
"ds_read_b64_tr_b16 v[20:21], v7 offset:19456\n"
"v_max_f32_e32 v84, 0, v84\n"
"v_mfma_f32_16x16x32_bf16 v[132:135], v[24:27], a[20:23], v[132:135]\n"
"ds_read_b64_tr_b16 v[22:23], v7 offset:19968\n"
"v_max_f32_e32 v85, 0, v85\n"
"v_mfma_f32_16x16x32_bf16 v[136:139], v[24:27], a[24:27], v[136:139]\n"
"buffer_load_dwordx4 v1, s[24:27], s21 offen offset:3072 sc1 lds\n"
"v_max_f32_e32 v86, 0, v86\n"
"v_mfma_f32_16x16x32_bf16 v[140:143], v[24:27], a[28:31], v[140:143]\n"
"v_max_f32_e32 v87, 0, v87\n"
"v_mul_f32_e32 v100, v84, v100\n"
"s_waitcnt lgkmcnt(12)\n"
"v_mfma_f32_16x16x32_bf16 v[112:115], v[28:31], a[32:35], v[112:115]\n"
"ds_read_b64_tr_b16 v[24:25], v7 offset:20480\n"
"v_mul_f32_e32 v101, v85, v101\n"
"v_mfma_f32_16x16x32_bf16 v[116:119], v[28:31], a[36:39], v[116:119]\n"
"ds_read_b64_tr_b16 v[26:27], v7 offset:20992\n"
"v_mul_f32_e32 v102, v86, v102\n"
"v_mfma_f32_16x16x32_bf16 v[120:123], v[28:31], a[40:43], v[120:123]\n"
"v_mul_f32_e32 v103, v87, v103\n"
"v_cvt_pk_bf16_f32 v84, v100, v101\n"
"v_mfma_f32_16x16x32_bf16 v[124:127], v[28:31], a[44:47], v[124:127]\n"
"v_cvt_pk_bf16_f32 v85, v102, v103\n"
"v_max_f32_e32 v88, 0, v88\n"
"v_mfma_f32_16x16x32_bf16 v[128:131], v[32:35], a[32:35], v[128:131]\n"
"ds_read_b64_tr_b16 v[28:29], v7 offset:21504\n"
"v_max_f32_e32 v89, 0, v89\n"
"v_mfma_f32_16x16x32_bf16 v[132:135], v[32:35], a[36:39], v[132:135]\n"
"ds_read_b64_tr_b16 v[30:31], v7 offset:22016\n"
"v_max_f32_e32 v90, 0, v90\n"
"v_mfma_f32_16x16x32_bf16 v[136:139], v[32:35], a[40:43], v[136:139]\n"
"v_max_f32_e32 v91, 0, v91\n"
"v_mul_f32_e32 v104, v88, v104\n"
"v_mfma_f32_16x16x32_bf16 v[140:143], v[32:35], a[44:47], v[140:143]\n"
"v_mul_f32_e32 v105, v89, v105\n"
"v_mul_f32_e32 v106, v90, v106\n"
"s_waitcnt lgkmcnt(12)\n"
"v_mfma_f32_16x16x32_bf16 v[112:115], v[36:39], a[48:51], v[112:115]\n"
"ds_read_b64_tr_b16 v[32:33], v7 offset:22528\n"
"v_mul_f32_e32 v107, v91, v107\n"
"v_mfma_f32_16x16x32_bf16 v[116:119], v[36:39], a[52:55], v[116:119]\n"
"ds_read_b64_tr_b16 v[34:35], v7 offset:23040\n"
"v_cvt_pk_bf16_f32 v88, v104, v105\n"
"v_mfma_f32_16x16x32_bf16 v[120:123], v[36:39], a[56:59], v[120:123]\n"
"v_cvt_pk_bf16_f32 v89, v106, v107\n"
"v_max_f32_e32 v92, 0, v92\n"
"v_mfma_f32_16x16x32_bf16 v[124:127], v[36:39], a[60:63], v[124:127]\n"
"v_max_f32_e32 v93, 0, v93\n"
"v_max_f32_e32 v94, 0, v94\n"
"v_mfma_f32_16x16x32_bf16 v[128:131], v[40:43], a[48:51], v[128:131]\n"
"ds_read_b64_tr_b16 v[36:37], v7 offset:23552\n"
"v_max_f32_e32 v95, 0, v95\n"
"v_mfma_f32_16x16x32_bf16 v[132:135], v[40:43], a[52:55], v[132:135]\n"
"ds_read_b64_tr_b16 v[38:39], v7 offset:24064\n"
"v_mul_f32_e32 v108, v92, v108\n"
"v_mfma_f32_16x16x32_bf16 v[136:139], v[40:43], a[56:59], v[136:139]\n"
"v_mul_f32_e32 v109, v93, v109\n"
"v_mul_f32_e32 v110, v94, v110\n"
"v_mfma_f32_16x16x32_bf16 v[140:143], v[40:43], a[60:63], v[140:143]\n"
"v_mul_f32_e32 v111, v95, v111\n"
"v_cvt_pk_bf16_f32 v92, v108, v109\n"
"s_waitcnt lgkmcnt(12)\n"
"v_mfma_f32_16x16x32_bf16 v[112:115], v[44:47], a[64:67], v[112:115]\n"
"ds_read_b64_tr_b16 v[40:41], v7 offset:24576\n"
"v_cvt_pk_bf16_f32 v93, v110, v111\n"
"v_mfma_f32_16x16x32_bf16 v[116:119], v[44:47], a[68:71], v[116:119]\n"
"ds_read_b64_tr_b16 v[42:43], v7 offset:25088\n"
"v_mfma_f32_16x16x32_bf16 v[120:123], v[44:47], a[72:75], v[120:123]\n"
"v_mfma_f32_16x16x32_bf16 v[124:127], v[44:47], a[76:79], v[124:127]\n"
"v_mfma_f32_16x16x32_bf16 v[128:131], v[12:15], a[64:67], v[128:131]\n"
"ds_read_b64_tr_b16 v[44:45], v7 offset:25600\n"
"v_mfma_f32_16x16x32_bf16 v[132:135], v[12:15], a[68:71], v[132:135]\n"
"ds_read_b64_tr_b16 v[46:47], v7 offset:26112\n"
"v_mfma_f32_16x16x32_bf16 v[136:139], v[12:15], a[72:75], v[136:139]\n"
"v_mfma_f32_16x16x32_bf16 v[140:143], v[12:15], a[76:79], v[140:143]\n"
"s_waitcnt lgkmcnt(12)\n"
"v_mfma_f32_16x16x32_bf16 v[112:115], v[16:19], a[80:83], v[112:115]\n"
"ds_read_b64_tr_b16 v[12:13], v7 offset:26624\n"
"v_mfma_f32_16x16x32_bf16 v[116:119], v[16:19], a[84:87], v[116:119]\n"
"ds_read_b64_tr_b16 v[14:15], v7 offset:27136\n"
"v_mfma_f32_16x16x32_bf16 v[120:123], v[16:19], a[88:91], v[120:123]\n"
"v_mfma_f32_16x16x32_bf16 v[124:127], v[16:19], a[92:95], v[124:127]\n"
"v_mfma_f32_16x16x32_bf16 v[128:131], v[20:23], a[80:83], v[128:131]\n"
"ds_read_b64_tr_b16 v[16:17], v7 offset:27648\n"
"v_mfma_f32_16x16x32_bf16 v[132:135], v[20:23], a[84:87], v[132:135]\n"
"ds_read_b64_tr_b16 v[18:19], v7 offset:28160\n"
"v_mfma_f32_16x16x32_bf16 v[136:139], v[20:23], a[88:91], v[136:139]\n"
"v_mfma_f32_16x16x32_bf16 v[140:143], v[20:23], a[92:95], v[140:143]\n"
"s_mov_b32 s22, 295936\n"
"buffer_store_dwordx4 v[184:187], v9, s[16:19], s22 offen sc1\n"
"s_waitcnt lgkmcnt(12)\n"
"v_mfma_f32_16x16x32_bf16 v[112:115], v[24:27], a[96:99], v[112:115]\n"
"ds_read_b64_tr_b16 v[20:21], v7 offset:28672\n"
"v_mfma_f32_16x16x32_bf16 v[116:119], v[24:27], a[100:103], v[116:119]\n"
"ds_read_b64_tr_b16 v[22:23], v7 offset:29184\n"
"v_mfma_f32_16x16x32_bf16 v[120:123], v[24:27], a[104:107], v[120:123]\n"
"v_mfma_f32_16x16x32_bf16 v[124:127], v[24:27], a[108:111], v[124:127]\n"
"v_mfma_f32_16x16x32_bf16 v[128:131], v[28:31], a[96:99], v[128:131]\n"
"ds_read_b64_tr_b16 v[24:25], v7 offset:29696\n"
"v_mfma_f32_16x16x32_bf16 v[132:135], v[28:31], a[100:103], v[132:135]\n"
"ds_read_b64_tr_b16 v[26:27], v7 offset:30208\n"
"v_mfma_f32_16x16x32_bf16 v[136:139], v[28:31], a[104:107], v[136:139]\n"
"v_mfma_f32_16x16x32_bf16 v[140:143], v[28:31], a[108:111], v[140:143]\n"
"s_waitcnt lgkmcnt(12)\n"
"v_mfma_f32_16x16x32_bf16 v[112:115], v[32:35], a[112:115], v[112:115]\n"
"ds_read_b64_tr_b16 v[28:29], v7 offset:30720\n"
"v_mfma_f32_16x16x32_bf16 v[116:119], v[32:35], a[116:119], v[116:119]\n"
"ds_read_b64_tr_b16 v[30:31], v7 offset:31232\n"
"v_mfma_f32_16x16x32_bf16 v[120:123], v[32:35], a[120:123], v[120:123]\n"
"v_mfma_f32_16x16x32_bf16 v[124:127], v[32:35], a[124:127], v[124:127]\n"
"v_mfma_f32_16x16x32_bf16 v[128:131], v[36:39], a[112:115], v[128:131]\n"
"ds_read_b64_tr_b16 v[32:33], v7 offset:31744\n"
"v_mfma_f32_16x16x32_bf16 v[132:135], v[36:39], a[116:119], v[132:135]\n"
"ds_read_b64_tr_b16 v[34:35], v7 offset:32256\n"
"v_mfma_f32_16x16x32_bf16 v[136:139], v[36:39], a[120:123], v[136:139]\n"
"v_mfma_f32_16x16x32_bf16 v[140:143], v[36:39], a[124:127], v[140:143]\n"
"s_waitcnt vmcnt(33)\n"
"s_barrier\n"
"s_waitcnt lgkmcnt(12)\n"
"v_mfma_f32_16x16x32_bf16 v[112:115], v[40:43], a[128:131], v[112:115]\n"
"ds_read_b64_tr_b16 v[36:37], v7 offset:32768\n"
"v_mfma_f32_16x16x32_bf16 v[116:119], v[40:43], a[132:135], v[116:119]\n"
"ds_read_b64_tr_b16 v[38:39], v7 offset:33280\n"
"v_mfma_f32_16x16x32_bf16 v[120:123], v[40:43], a[136:139], v[120:123]\n"
"s_add_u32 m0, s20, 65536\n"
"s_mov_b32 s21, 16384\n"
"buffer_load_dwordx4 v1, s[24:27], s21 offen sc1 lds\n"
"v_mfma_f32_16x16x32_bf16 v[124:127], v[40:43], a[140:143], v[124:127]\n"
"v_mfma_f32_16x16x32_bf16 v[128:131], v[44:47], a[128:131], v[128:131]\n"
"ds_read_b64_tr_b16 v[40:41], v7 offset:33792\n"
"v_mfma_f32_16x16x32_bf16 v[132:135], v[44:47], a[132:135], v[132:135]\n"
"ds_read_b64_tr_b16 v[42:43], v7 offset:34304\n"
"v_mfma_f32_16x16x32_bf16 v[136:139], v[44:47], a[136:139], v[136:139]\n"
"buffer_load_dwordx4 v1, s[24:27], s21 offen offset:1024 sc1 lds\n"
"v_mfma_f32_16x16x32_bf16 v[140:143], v[44:47], a[140:143], v[140:143]\n"
"s_waitcnt lgkmcnt(12)\n"
"v_mfma_f32_16x16x32_bf16 v[112:115], v[12:15], a[144:147], v[112:115]\n"
"ds_read_b64_tr_b16 v[44:45], v7 offset:34816\n"
"v_mfma_f32_16x16x32_bf16 v[116:119], v[12:15], a[148:151], v[116:119]\n"
"ds_read_b64_tr_b16 v[46:47], v7 offset:35328\n"
"v_mfma_f32_16x16x32_bf16 v[120:123], v[12:15], a[152:155], v[120:123]\n"
"buffer_load_dwordx4 v1, s[24:27], s21 offen offset:2048 sc1 lds\n"
"v_mfma_f32_16x16x32_bf16 v[124:127], v[12:15], a[156:159], v[124:127]\n"
"v_mfma_f32_16x16x32_bf16 v[128:131], v[16:19], a[144:147], v[128:131]\n"
"ds_read_b64_tr_b16 v[12:13], v7 offset:35840\n"
"v_mfma_f32_16x16x32_bf16 v[132:135], v[16:19], a[148:151], v[132:135]\n"
"ds_read_b64_tr_b16 v[14:15], v7 offset:36352\n"
"v_mfma_f32_16x16x32_bf16 v[136:139], v[16:19], a[152:155], v[136:139]\n"
"buffer_load_dwordx4 v1, s[24:27], s21 offen offset:3072 sc1 lds\n"
"v_mfma_f32_16x16x32_bf16 v[140:143], v[16:19], a[156:159], v[140:143]\n"
"s_waitcnt lgkmcnt(12)\n"
"v_mfma_f32_16x16x32_bf16 v[112:115], v[20:23], a[160:163], v[112:115]\n"
"ds_read_b64_tr_b16 v[16:17], v7 offset:36864\n"
"v_mfma_f32_16x16x32_bf16 v[116:119], v[20:23], a[164:167], v[116:119]\n"
"ds_read_b64_tr_b16 v[18:19], v7 offset:37376\n"
"v_mfma_f32_16x16x32_bf16 v[120:123], v[20:23], a[168:171], v[120:123]\n"
"v_mfma_f32_16x16x32_bf16 v[124:127], v[20:23], a[172:175], v[124:127]\n"
"v_mfma_f32_16x16x32_bf16 v[128:131], v[24:27], a[160:163], v[128:131]\n"
"ds_read_b64_tr_b16 v[20:21], v7 offset:37888\n"
"v_mfma_f32_16x16x32_bf16 v[132:135], v[24:27], a[164:167], v[132:135]\n"
"ds_read_b64_tr_b16 v[22:23], v7 offset:38400\n"
"v_mfma_f32_16x16x32_bf16 v[136:139], v[24:27], a[168:171], v[136:139]\n"
"v_mfma_f32_16x16x32_bf16 v[140:143], v[24:27], a[172:175], v[140:143]\n"
"s_waitcnt lgkmcnt(12)\n"
"v_mfma_f32_16x16x32_bf16 v[112:115], v[28:31], a[176:179], v[112:115]\n"
"ds_read_b64_tr_b16 v[24:25], v7 offset:38912\n"
"v_mfma_f32_16x16x32_bf16 v[116:119], v[28:31], a[180:183], v[116:119]\n"
"ds_read_b64_tr_b16 v[26:27], v7 offset:39424\n"
"v_mfma_f32_16x16x32_bf16 v[120:123], v[28:31], a[184:187], v[120:123]\n"
"v_mfma_f32_16x16x32_bf16 v[124:127], v[28:31], a[188:191], v[124:127]\n"
"v_mfma_f32_16x16x32_bf16 v[128:131], v[32:35], a[176:179], v[128:131]\n"
"ds_read_b64_tr_b16 v[28:29], v7 offset:39936\n"
"v_mfma_f32_16x16x32_bf16 v[132:135], v[32:35], a[180:183], v[132:135]\n"
"ds_read_b64_tr_b16 v[30:31], v7 offset:40448\n"
"v_mfma_f32_16x16x32_bf16 v[136:139], v[32:35], a[184:187], v[136:139]\n"
"v_mfma_f32_16x16x32_bf16 v[140:143], v[32:35], a[188:191], v[140:143]\n"
"s_mov_b32 s22, 443392\n"
"buffer_store_dwordx4 v[188:191], v9, s[16:19], s22 offen sc1\n"
"s_waitcnt lgkmcnt(12)\n"
"v_mfma_f32_16x16x32_bf16 v[112:115], v[36:39], a[192:195], v[112:115]\n"
"ds_read_b64_tr_b16 v[32:33], v7 offset:40960\n"
"v_mfma_f32_16x16x32_bf16 v[116:119], v[36:39], a[196:199], v[116:119]\n"
"ds_read_b64_tr_b16 v[34:35], v7 offset:41472\n"
"v_mfma_f32_16x16x32_bf16 v[120:123], v[36:39], a[200:203], v[120:123]\n"
"v_mfma_f32_16x16x32_bf16 v[124:127], v[36:39], a[204:207], v[124:127]\n"
"v_mfma_f32_16x16x32_bf16 v[128:131], v[40:43], a[192:195], v[128:131]\n"
"ds_read_b64_tr_b16 v[36:37], v7 offset:41984\n"
"v_mfma_f32_16x16x32_bf16 v[132:135], v[40:43], a[196:199], v[132:135]\n"
"ds_read_b64_tr_b16 v[38:39], v7 offset:42496\n"
"v_mfma_f32_16x16x32_bf16 v[136:139], v[40:43], a[200:203], v[136:139]\n"
"v_mfma_f32_16x16x32_bf16 v[140:143], v[40:43], a[204:207], v[140:143]\n"
"s_waitcnt lgkmcnt(12)\n"
"v_mfma_f32_16x16x32_bf16 v[112:115], v[44:47], a[208:211], v[112:115]\n"
"ds_read_b64_tr_b16 v[40:41], v7 offset:43008\n"
"v_mfma_f32_16x16x32_bf16 v[116:119], v[44:47], a[212:215], v[116:119]\n"
"ds_read_b64_tr_b16 v[42:43], v7 offset:43520\n"
"v_mfma_f32_16x16x32_bf16 v[120:123], v[44:47], a[216:219], v[120:123]\n"
"v_mfma_f32_16x16x32_bf16 v[124:127], v[44:47], a[220:223], v[124:127]\n"
"v_mfma_f32_16x16x32_bf16 v[128:131], v[12:15], a[208:211], v[128:131]\n"
"ds_read_b64_tr_b16 v[44:45], v7 offset:44032\n"
"v_mfma_f32_16x16x32_bf16 v[132:135], v[12:15], a[212:215], v[132:135]\n"
"ds_read_b64_tr_b16 v[46:47], v7 offset:44544\n"
"v_mfma_f32_16x16x32_bf16 v[136:139], v[12:15], a[216:219], v[136:139]\n"
"v_mfma_f32_16x16x32_bf16 v[140:143], v[12:15], a[220:223], v[140:143]\n"
"s_waitcnt lgkmcnt(12)\n"
"v_mfma_f32_16x16x32_bf16 v[112:115], v[16:19], a[224:227], v[112:115]\n"
"ds_read_b64_tr_b16 v[12:13], v7 offset:45056\n"
"v_mfma_f32_16x16x32_bf16 v[116:119], v[16:19], a[228:231], v[116:119]\n"
"ds_read_b64_tr_b16 v[14:15], v7 offset:45568\n"
"v_mfma_f32_16x16x32_bf16 v[120:123], v[16:19], a[232:235], v[120:123]\n"
"v_mfma_f32_16x16x32_bf16 v[124:127], v[16:19], a[236:239], v[124:127]\n"
"v_mfma_f32_16x16x32_bf16 v[128:131], v[20:23], a[224:227], v[128:131]\n"
"ds_read_b64_tr_b16 v[16:17], v7 offset:46080\n"
"v_mfma_f32_16x16x32_bf16 v[132:135], v[20:23], a[228:231], v[132:135]\n"
"ds_read_b64_tr_b16 v[18:19], v7 offset:46592\n"
"v_mfma_f32_16x16x32_bf16 v[136:139], v[20:23], a[232:235], v[136:139]\n"
"v_mfma_f32_16x16x32_bf16 v[140:143], v[20:23], a[236:239], v[140:143]\n"
"s_waitcnt lgkmcnt(12)\n"
"v_mfma_f32_16x16x32_bf16 v[112:115], v[24:27], a[240:243], v[112:115]\n"
"ds_read_b64_tr_b16 v[20:21], v7 offset:47104\n"
"v_mfma_f32_16x16x32_bf16 v[116:119], v[24:27], a[244:247], v[116:119]\n"
"ds_read_b64_tr_b16 v[22:23], v7 offset:47616\n"
"v_mfma_f32_16x16x32_bf16 v[120:123], v[24:27], a[248:251], v[120:123]\n"
"v_mfma_f32_16x16x32_bf16 v[124:127], v[24:27], a[252:255], v[124:127]\n"
"v_mfma_f32_16x16x32_bf16 v[128:131], v[28:31], a[240:243], v[128:131]\n"
"ds_read_b64_tr_b16 v[24:25], v7 offset:48128\n"
"v_mfma_f32_16x16x32_bf16 v[132:135], v[28:31], a[244:247], v[132:135]\n"
"ds_read_b64_tr_b16 v[26:27], v7 offset:48640\n"
"v_mfma_f32_16x16x32_bf16 v[136:139], v[28:31], a[248:251], v[136:139]\n"
"v_mfma_f32_16x16x32_bf16 v[140:143], v[28:31], a[252:255], v[140:143]\n"
"s_waitcnt vmcnt(33)\n"
"s_barrier\n"
"s_waitcnt lgkmcnt(12)\n"
"v_mfma_f32_16x16x32_bf16 v[112:115], v[32:35], v[48:51], v[112:115]\n"
"ds_read_b64_tr_b16 v[28:29], v7 offset:49152\n"
"v_mfma_f32_16x16x32_bf16 v[116:119], v[32:35], v[52:55], v[116:119]\n"
"ds_read_b64_tr_b16 v[30:31], v7 offset:49664\n"
"v_mfma_f32_16x16x32_bf16 v[120:123], v[32:35], v[56:59], v[120:123]\n"
"s_add_u32 m0, s20, 81920\n"
"s_mov_b32 s21, 32768\n"
"buffer_load_dwordx4 v1, s[24:27], s21 offen sc1 lds\n"
"v_mfma_f32_16x16x32_bf16 v[124:127], v[32:35], v[60:63], v[124:127]\n"
"v_mfma_f32_16x16x32_bf16 v[128:131], v[36:39], v[48:51], v[128:131]\n"
"ds_read_b64_tr_b16 v[32:33], v7 offset:50176\n"
"v_mfma_f32_16x16x32_bf16 v[132:135], v[36:39], v[52:55], v[132:135]\n"
"ds_read_b64_tr_b16 v[34:35], v7 offset:50688\n"
"v_mfma_f32_16x16x32_bf16 v[136:139], v[36:39], v[56:59], v[136:139]\n"
"buffer_load_dwordx4 v1, s[24:27], s21 offen offset:1024 sc1 lds\n"
"v_mfma_f32_16x16x32_bf16 v[140:143], v[36:39], v[60:63], v[140:143]\n"
"s_waitcnt lgkmcnt(12)\n"
"v_mfma_f32_16x16x32_bf16 v[112:115], v[40:43], v[64:67], v[112:115]\n"
"ds_read_b64_tr_b16 v[36:37], v7 offset:51200\n"
"v_mfma_f32_16x16x32_bf16 v[116:119], v[40:43], v[68:71], v[116:119]\n"
"ds_read_b64_tr_b16 v[38:39], v7 offset:51712\n"
"v_mfma_f32_16x16x32_bf16 v[120:123], v[40:43], v[72:75], v[120:123]\n"
"buffer_load_dwordx4 v1, s[24:27], s21 offen offset:2048 sc1 lds\n"
"v_mfma_f32_16x16x32_bf16 v[124:127], v[40:43], v[76:79], v[124:127]\n"
"v_mfma_f32_16x16x32_bf16 v[128:131], v[44:47], v[64:67], v[128:131]\n"
"ds_read_b64_tr_b16 v[40:41], v7 offset:52224\n"
"v_mfma_f32_16x16x32_bf16 v[132:135], v[44:47], v[68:71], v[132:135]\n"
"ds_read_b64_tr_b16 v[42:43], v7 offset:52736\n"
"v_mfma_f32_16x16x32_bf16 v[136:139], v[44:47], v[72:75], v[136:139]\n"
"buffer_load_dwordx4 v1, s[24:27], s21 offen offset:3072 sc1 lds\n"
"v_mfma_f32_16x16x32_bf16 v[140:143], v[44:47], v[76:79], v[140:143]\n"
"s_waitcnt lgkmcnt(12)\n"
"v_mfma_f32_16x16x32_bf16 v[144:147], v[12:15], a[0:3], 0\n"
"ds_read_b64_tr_b16 v[44:45], v7 offset:53248\n"
"v_mfma_f32_16x16x32_bf16 v[148:151], v[12:15], a[4:7], 0\n"
"ds_read_b64_tr_b16 v[46:47], v7 offset:53760\n"
"v_mfma_f32_16x16x32_bf16 v[152:155], v[12:15], a[8:11], 0\n"
"v_mfma_f32_16x16x32_bf16 v[156:159], v[12:15], a[12:15], 0\n"
"v_mfma_f32_16x16x32_bf16 v[160:163], v[16:19], a[0:3], 0\n"
"ds_read_b64_tr_b16 v[12:13], v7 offset:54272\n"
"v_max_f32_e32 v112, 0, v112\n"
"v_mfma_f32_16x16x32_bf16 v[164:167], v[16:19], a[4:7], 0\n"
"ds_read_b64_tr_b16 v[14:15], v7 offset:54784\n"
"v_max_f32_e32 v113, 0, v113\n"
"v_mfma_f32_16x16x32_bf16 v[168:171], v[16:19], a[8:11], 0\n"
"v_max_f32_e32 v114, 0, v114\n"
"v_max_f32_e32 v115, 0, v115\n"
"v_mfma_f32_16x16x32_bf16 v[172:175], v[16:19], a[12:15], 0\n"
"v_mul_f32_e32 v128, v112, v128\n"
"v_mul_f32_e32 v129, v113, v129\n"
"s_waitcnt lgkmcnt(12)\n"
"v_mfma_f32_16x16x32_bf16 v[144:147], v[20:23], a[16:19], v[144:147]\n"
"ds_read_b64_tr_b16 v[16:17], v7 offset:55296\n"
"v_mul_f32_e32 v130, v114, v130\n"
"v_mfma_f32_16x16x32_bf16 v[148:151], v[20:23], a[20:23], v[148:151]\n"
"ds_read_b64_tr_b16 v[18:19], v7 offset:55808\n"
"v_mul_f32_e32 v131, v115, v131\n"
"v_mfma_f32_16x16x32_bf16 v[152:155], v[20:23], a[24:27], v[152:155]\n"
"v_cvt_pk_bf16_f32 v82, v128, v129\n"
"v_cvt_pk_bf16_f32 v83, v130, v131\n"
"v_mfma_f32_16x16x32_bf16 v[156:159], v[20:23], a[28:31], v[156:159]\n"
"v_max_f32_e32 v116, 0, v116\n"
"s_mov_b32 s22, 2048\n"
"buffer_store_dwordx4 v[80:83], v9, s[16:19], s22 offen sc1\n"
"v_mfma_f32_16x16x32_bf16 v[160:163], v[24:27], a[16:19], v[160:163]\n"
"ds_read_b64_tr_b16 v[20:21], v7 offset:56320\n"
"v_max_f32_e32 v117, 0, v117\n"
"v_mfma_f32_16x16x32_bf16 v[164:167], v[24:27], a[20:23], v[164:167]\n"
"ds_read_b64_tr_b16 v[22:23], v7 offset:56832\n"
"v_max_f32_e32 v118, 0, v118\n"
"v_mfma_f32_16x16x32_bf16 v[168:171], v[24:27], a[24:27], v[168:171]\n"
"v_max_f32_e32 v119, 0, v119\n"
"v_mul_f32_e32 v132, v116, v132\n"
"v_mfma_f32_16x16x32_bf16 v[172:175], v[24:27], a[28:31], v[172:175]\n"
"v_mul_f32_e32 v133, v117, v133\n"
"v_mul_f32_e32 v134, v118, v134\n"
"s_waitcnt lgkmcnt(12)\n"
"v_mfma_f32_16x16x32_bf16 v[144:147], v[28:31], a[32:35], v[144:147]\n"
"ds_read_b64_tr_b16 v[24:25], v7 offset:57344\n"
"v_mul_f32_e32 v135, v119, v135\n"
"v_mfma_f32_16x16x32_bf16 v[148:151], v[28:31], a[36:39], v[148:151]\n"
"ds_read_b64_tr_b16 v[26:27], v7 offset:57856\n"
"v_cvt_pk_bf16_f32 v86, v132, v133\n"
"v_mfma_f32_16x16x32_bf16 v[152:155], v[28:31], a[40:43], v[152:155]\n"
"v_cvt_pk_bf16_f32 v87, v134, v135\n"
"v_mfma_f32_16x16x32_bf16 v[156:159], v[28:31], a[44:47], v[156:159]\n"
"v_max_f32_e32 v120, 0, v120\n"
"v_max_f32_e32 v121, 0, v121\n"
"v_mfma_f32_16x16x32_bf16 v[160:163], v[32:35], a[32:35], v[160:163]\n"
"ds_read_b64_tr_b16 v[28:29], v7 offset:58368\n"
"v_max_f32_e32 v122, 0, v122\n"
"v_mfma_f32_16x16x32_bf16 v[164:167], v[32:35], a[36:39], v[164:167]\n"
"ds_read_b64_tr_b16 v[30:31], v7 offset:58880\n"
"v_max_f32_e32 v123, 0, v123\n"
"v_mfma_f32_16x16x32_bf16 v[168:171], v[32:35], a[40:43], v[168:171]\n"
"v_mul_f32_e32 v136, v120, v136\n"
"v_mul_f32_e32 v137, v121, v137\n"
"v_mfma_f32_16x16x32_bf16 v[172:175], v[32:35], a[44:47], v[172:175]\n"
"v_mul_f32_e32 v138, v122, v138\n"
"v_mul_f32_e32 v139, v123, v139\n"
"s_waitcnt lgkmcnt(12)\n"
"v_mfma_f32_16x16x32_bf16 v[144:147], v[36:39], a[48:51], v[144:147]\n"
"ds_read_b64_tr_b16 v[32:33], v7 offset:59392\n"
"v_cvt_pk_bf16_f32 v90, v136, v137\n"
"v_mfma_f32_16x16x32_bf16 v[148:151], v[36:39], a[52:55], v[148:151]\n"
"ds_read_b64_tr_b16 v[34:35], v7 offset:59904\n"
"v_cvt_pk_bf16_f32 v91, v138, v139\n"
"v_mfma_f32_16x16x32_bf16 v[152:155], v[36:39], a[56:59], v[152:155]\n"
"v_max_f32_e32 v124, 0, v124\n"
"v_mfma_f32_16x16x32_bf16 v[156:159], v[36:39], a[60:63], v[156:159]\n"
"v_max_f32_e32 v125, 0, v125\n"
"v_max_f32_e32 v126, 0, v126\n"
"v_mfma_f32_16x16x32_bf16 v[160:163], v[40:43], a[48:51], v[160:163]\n"
"ds_read_b64_tr_b16 v[36:37], v7 offset:60416\n"
"v_max_f32_e32 v127, 0, v127\n"
"v_mfma_f32_16x16x32_bf16 v[164:167], v[40:43], a[52:55], v[164:167]\n"
"ds_read_b64_tr_b16 v[38:39], v7 offset:60928\n"
"v_mul_f32_e32 v140, v124, v140\n"
"v_mfma_f32_16x16x32_bf16 v[168:171], v[40:43], a[56:59], v[168:171]\n"
"v_mul_f32_e32 v141, v125, v141\n"
"v_mul_f32_e32 v142, v126, v142\n"
"v_mfma_f32_16x16x32_bf16 v[172:175], v[40:43], a[60:63], v[172:175]\n"
"v_mul_f32_e32 v143, v127, v143\n"
"v_cvt_pk_bf16_f32 v94, v140, v141\n"
"s_waitcnt lgkmcnt(12)\n"
"v_mfma_f32_16x16x32_bf16 v[144:147], v[44:47], a[64:67], v[144:147]\n"
"ds_read_b64_tr_b16 v[40:41], v7 offset:61440\n"
"v_cvt_pk_bf16_f32 v95, v142, v143\n"
"v_mfma_f32_16x16x32_bf16 v[148:151], v[44:47], a[68:71], v[148:151]\n"
"ds_read_b64_tr_b16 v[42:43], v7 offset:61952\n"
"v_mfma_f32_16x16x32_bf16 v[152:155], v[44:47], a[72:75], v[152:155]\n"
"v_mfma_f32_16x16x32_bf16 v[156:159], v[44:47], a[76:79], v[156:159]\n"
"s_mov_b32 s22, 149504\n"
"buffer_store_dwordx4 v[84:87], v9, s[16:19], s22 offen sc1\n"
"v_mfma_f32_16x16x32_bf16 v[160:163], v[12:15], a[64:67], v[160:163]\n"
"ds_read_b64_tr_b16 v[44:45], v7 offset:62464\n"
"v_mfma_f32_16x16x32_bf16 v[164:167], v[12:15], a[68:71], v[164:167]\n"
"ds_read_b64_tr_b16 v[46:47], v7 offset:62976\n"
"v_mfma_f32_16x16x32_bf16 v[168:171], v[12:15], a[72:75], v[168:171]\n"
"v_mfma_f32_16x16x32_bf16 v[172:175], v[12:15], a[76:79], v[172:175]\n"
"s_waitcnt lgkmcnt(12)\n"
"v_mfma_f32_16x16x32_bf16 v[144:147], v[16:19], a[80:83], v[144:147]\n"
"ds_read_b64_tr_b16 v[12:13], v7 offset:63488\n"
"v_mfma_f32_16x16x32_bf16 v[148:151], v[16:19], a[84:87], v[148:151]\n"
"ds_read_b64_tr_b16 v[14:15], v7 offset:64000\n"
"v_mfma_f32_16x16x32_bf16 v[152:155], v[16:19], a[88:91], v[152:155]\n"
"v_mfma_f32_16x16x32_bf16 v[156:159], v[16:19], a[92:95], v[156:159]\n"
"v_mfma_f32_16x16x32_bf16 v[160:163], v[20:23], a[80:83], v[160:163]\n"
"ds_read_b64_tr_b16 v[16:17], v7 offset:64512\n"
"v_mfma_f32_16x16x32_bf16 v[164:167], v[20:23], a[84:87], v[164:167]\n"
"ds_read_b64_tr_b16 v[18:19], v7 offset:65024\n"
"v_mfma_f32_16x16x32_bf16 v[168:171], v[20:23], a[88:91], v[168:171]\n"
"v_mfma_f32_16x16x32_bf16 v[172:175], v[20:23], a[92:95], v[172:175]\n"
"s_waitcnt vmcnt(34)\n"
"s_barrier\n"
"s_waitcnt lgkmcnt(12)\n"
"v_mfma_f32_16x16x32_bf16 v[144:147], v[24:27], a[96:99], v[144:147]\n"
"ds_read_b64_tr_b16 v[20:21], v8 offset:0\n"
"v_mfma_f32_16x16x32_bf16 v[148:151], v[24:27], a[100:103], v[148:151]\n"
"ds_read_b64_tr_b16 v[22:23], v8 offset:512\n"
"v_mfma_f32_16x16x32_bf16 v[152:155], v[24:27], a[104:107], v[152:155]\n"
"s_add_u32 m0, s20, 98304\n"
"s_mov_b32 s21, 49152\n"
"buffer_load_dwordx4 v1, s[24:27], s21 offen sc1 lds\n"
"v_mfma_f32_16x16x32_bf16 v[156:159], v[24:27], a[108:111], v[156:159]\n"
"v_mfma_f32_16x16x32_bf16 v[160:163], v[28:31], a[96:99], v[160:163]\n"
"ds_read_b64_tr_b16 v[24:25], v8 offset:1024\n"
"v_mfma_f32_16x16x32_bf16 v[164:167], v[28:31], a[100:103], v[164:167]\n"
"ds_read_b64_tr_b16 v[26:27], v8 offset:1536\n"
"v_mfma_f32_16x16x32_bf16 v[168:171], v[28:31], a[104:107], v[168:171]\n"
"buffer_load_dwordx4 v1, s[24:27], s21 offen offset:1024 sc1 lds\n"
"v_mfma_f32_16x16x32_bf16 v[172:175], v[28:31], a[108:111], v[172:175]\n"
"s_waitcnt lgkmcnt(12)\n"
"v_mfma_f32_16x16x32_bf16 v[144:147], v[32:35], a[112:115], v[144:147]\n"
"ds_read_b64_tr_b16 v[28:29], v8 offset:2048\n"
"v_mfma_f32_16x16x32_bf16 v[148:151], v[32:35], a[116:119], v[148:151]\n"
"ds_read_b64_tr_b16 v[30:31], v8 offset:2560\n"
"v_mfma_f32_16x16x32_bf16 v[152:155], v[32:35], a[120:123], v[152:155]\n"
"buffer_load_dwordx4 v1, s[24:27], s21 offen offset:2048 sc1 lds\n"
"v_mfma_f32_16x16x32_bf16 v[156:159], v[32:35], a[124:127], v[156:159]\n"
"s_mov_b32 s22, 296960\n"
"buffer_store_dwordx4 v[88:91], v9, s[16:19], s22 offen sc1\n"
"v_mfma_f32_16x16x32_bf16 v[160:163], v[36:39], a[112:115], v[160:163]\n"
"ds_read_b64_tr_b16 v[32:33], v8 offset:3072\n"
"v_mfma_f32_16x16x32_bf16 v[164:167], v[36:39], a[116:119], v[164:167]\n"
"ds_read_b64_tr_b16 v[34:35], v8 offset:3584\n"
"v_mfma_f32_16x16x32_bf16 v[168:171], v[36:39], a[120:123], v[168:171]\n"
"buffer_load_dwordx4 v1, s[24:27], s21 offen offset:3072 sc1 lds\n"
"v_mfma_f32_16x16x32_bf16 v[172:175], v[36:39], a[124:127], v[172:175]\n"
"s_waitcnt lgkmcnt(12)\n"
"v_mfma_f32_16x16x32_bf16 v[144:147], v[40:43], a[128:131], v[144:147]\n"
"ds_read_b64_tr_b16 v[36:37], v8 offset:4096\n"
"v_mfma_f32_16x16x32_bf16 v[148:151], v[40:43], a[132:135], v[148:151]\n"
"ds_read_b64_tr_b16 v[38:39], v8 offset:4608\n"
"v_mfma_f32_16x16x32_bf16 v[152:155], v[40:43], a[136:139], v[152:155]\n"
"v_mfma_f32_16x16x32_bf16 v[156:159], v[40:43], a[140:143], v[156:159]\n"
"v_mfma_f32_16x16x32_bf16 v[160:163], v[44:47], a[128:131], v[160:163]\n"
"ds_read_b64_tr_b16 v[40:41], v8 offset:5120\n"
"v_mfma_f32_16x16x32_bf16 v[164:167], v[44:47], a[132:135], v[164:167]\n"
"ds_read_b64_tr_b16 v[42:43], v8 offset:5632\n"
"v_mfma_f32_16x16x32_bf16 v[168:171], v[44:47], a[136:139], v[168:171]\n"
"v_mfma_f32_16x16x32_bf16 v[172:175], v[44:47], a[140:143], v[172:175]\n"
"s_waitcnt lgkmcnt(12)\n"
"v_mfma_f32_16x16x32_bf16 v[144:147], v[12:15], a[144:147], v[144:147]\n"
"ds_read_b64_tr_b16 v[44:45], v8 offset:6144\n"
"v_mfma_f32_16x16x32_bf16 v[148:151], v[12:15], a[148:151], v[148:151]\n"
"ds_read_b64_tr_b16 v[46:47], v8 offset:6656\n"
"v_mfma_f32_16x16x32_bf16 v[152:155], v[12:15], a[152:155], v[152:155]\n"
"v_mfma_f32_16x16x32_bf16 v[156:159], v[12:15], a[156:159], v[156:159]\n"
"v_mfma_f32_16x16x32_bf16 v[160:163], v[16:19], a[144:147], v[160:163]\n"
"ds_read_b64_tr_b16 v[12:13], v8 offset:7168\n"
"v_mfma_f32_16x16x32_bf16 v[164:167], v[16:19], a[148:151], v[164:167]\n"
"ds_read_b64_tr_b16 v[14:15], v8 offset:7680\n"
"v_mfma_f32_16x16x32_bf16 v[168:171], v[16:19], a[152:155], v[168:171]\n"
"v_mfma_f32_16x16x32_bf16 v[172:175], v[16:19], a[156:159], v[172:175]\n"
"s_waitcnt lgkmcnt(12)\n"
"v_mfma_f32_16x16x32_bf16 v[144:147], v[20:23], a[160:163], v[144:147]\n"
"ds_read_b64_tr_b16 v[16:17], v8 offset:8192\n"
"v_mfma_f32_16x16x32_bf16 v[148:151], v[20:23], a[164:167], v[148:151]\n"
"ds_read_b64_tr_b16 v[18:19], v8 offset:8704\n"
"v_mfma_f32_16x16x32_bf16 v[152:155], v[20:23], a[168:171], v[152:155]\n"
"v_mfma_f32_16x16x32_bf16 v[156:159], v[20:23], a[172:175], v[156:159]\n"
"s_mov_b32 s22, 444416\n"
"buffer_store_dwordx4 v[92:95], v9, s[16:19], s22 offen sc1\n"
"v_mfma_f32_16x16x32_bf16 v[160:163], v[24:27], a[160:163], v[160:163]\n"
"ds_read_b64_tr_b16 v[20:21], v8 offset:9216\n"
"v_mfma_f32_16x16x32_bf16 v[164:167], v[24:27], a[164:167], v[164:167]\n"
"ds_read_b64_tr_b16 v[22:23], v8 offset:9728\n"
"v_mfma_f32_16x16x32_bf16 v[168:171], v[24:27], a[168:171], v[168:171]\n"
"v_mfma_f32_16x16x32_bf16 v[172:175], v[24:27], a[172:175], v[172:175]\n"
"s_waitcnt lgkmcnt(12)\n"
"v_mfma_f32_16x16x32_bf16 v[144:147], v[28:31], a[176:179], v[144:147]\n"
"ds_read_b64_tr_b16 v[24:25], v8 offset:10240\n"
"v_mfma_f32_16x16x32_bf16 v[148:151], v[28:31], a[180:183], v[148:151]\n"
"ds_read_b64_tr_b16 v[26:27], v8 offset:10752\n"
"v_mfma_f32_16x16x32_bf16 v[152:155], v[28:31], a[184:187], v[152:155]\n"
"v_mfma_f32_16x16x32_bf16 v[156:159], v[28:31], a[188:191], v[156:159]\n"
"v_mfma_f32_16x16x32_bf16 v[160:163], v[32:35], a[176:179], v[160:163]\n"
"ds_read_b64_tr_b16 v[28:29], v8 offset:11264\n"
"v_mfma_f32_16x16x32_bf16 v[164:167], v[32:35], a[180:183], v[164:167]\n"
"ds_read_b64_tr_b16 v[30:31], v8 offset:11776\n"
"v_mfma_f32_16x16x32_bf16 v[168:171], v[32:35], a[184:187], v[168:171]\n"
"v_mfma_f32_16x16x32_bf16 v[172:175], v[32:35], a[188:191], v[172:175]\n"
"s_waitcnt lgkmcnt(12)\n"
"v_mfma_f32_16x16x32_bf16 v[144:147], v[36:39], a[192:195], v[144:147]\n"
"ds_read_b64_tr_b16 v[32:33], v8 offset:12288\n"
"v_mfma_f32_16x16x32_bf16 v[148:151], v[36:39], a[196:199], v[148:151]\n"
"ds_read_b64_tr_b16 v[34:35], v8 offset:12800\n"
"v_mfma_f32_16x16x32_bf16 v[152:155], v[36:39], a[200:203], v[152:155]\n"
"v_mfma_f32_16x16x32_bf16 v[156:159], v[36:39], a[204:207], v[156:159]\n"
"v_mfma_f32_16x16x32_bf16 v[160:163], v[40:43], a[192:195], v[160:163]\n"
"ds_read_b64_tr_b16 v[36:37], v8 offset:13312\n"
"v_mfma_f32_16x16x32_bf16 v[164:167], v[40:43], a[196:199], v[164:167]\n"
"ds_read_b64_tr_b16 v[38:39], v8 offset:13824\n"
"v_mfma_f32_16x16x32_bf16 v[168:171], v[40:43], a[200:203], v[168:171]\n"
"v_mfma_f32_16x16x32_bf16 v[172:175], v[40:43], a[204:207], v[172:175]\n"
"s_waitcnt lgkmcnt(12)\n"
"v_mfma_f32_16x16x32_bf16 v[144:147], v[44:47], a[208:211], v[144:147]\n"
"ds_read_b64_tr_b16 v[40:41], v8 offset:14336\n"
"v_mfma_f32_16x16x32_bf16 v[148:151], v[44:47], a[212:215], v[148:151]\n"
"ds_read_b64_tr_b16 v[42:43], v8 offset:14848\n"
"v_mfma_f32_16x16x32_bf16 v[152:155], v[44:47], a[216:219], v[152:155]\n"
"v_mfma_f32_16x16x32_bf16 v[156:159], v[44:47], a[220:223], v[156:159]\n"
"v_mfma_f32_16x16x32_bf16 v[160:163], v[12:15], a[208:211], v[160:163]\n"
"ds_read_b64_tr_b16 v[44:45], v8 offset:15360\n"
"v_mfma_f32_16x16x32_bf16 v[164:167], v[12:15], a[212:215], v[164:167]\n"
"ds_read_b64_tr_b16 v[46:47], v8 offset:15872\n"
"v_mfma_f32_16x16x32_bf16 v[168:171], v[12:15], a[216:219], v[168:171]\n"
"v_mfma_f32_16x16x32_bf16 v[172:175], v[12:15], a[220:223], v[172:175]\n"
"s_waitcnt vmcnt(34)\n"
"s_barrier\n"
"s_waitcnt lgkmcnt(12)\n"
"v_mfma_f32_16x16x32_bf16 v[144:147], v[16:19], a[224:227], v[144:147]\n"
"ds_read_b64_tr_b16 v[12:13], v6 offset:0\n"
"v_mfma_f32_16x16x32_bf16 v[148:151], v[16:19], a[228:231], v[148:151]\n"
"ds_read_b64_tr_b16 v[14:15], v6 offset:512\n"
"v_mfma_f32_16x16x32_bf16 v[152:155], v[16:19], a[232:235], v[152:155]\n"
"s_add_u32 m0, s20, 114688\n"
"s_mov_b32 s21, 65536\n"
"buffer_load_dwordx4 v1, s[24:27], s21 offen sc1 lds\n"
"v_mfma_f32_16x16x32_bf16 v[156:159], v[16:19], a[236:239], v[156:159]\n"
"v_mfma_f32_16x16x32_bf16 v[160:163], v[20:23], a[224:227], v[160:163]\n"
"ds_read_b64_tr_b16 v[16:17], v6 offset:1024\n"
"v_mfma_f32_16x16x32_bf16 v[164:167], v[20:23], a[228:231], v[164:167]\n"
"ds_read_b64_tr_b16 v[18:19], v6 offset:1536\n"
"v_mfma_f32_16x16x32_bf16 v[168:171], v[20:23], a[232:235], v[168:171]\n"
"buffer_load_dwordx4 v1, s[24:27], s21 offen offset:1024 sc1 lds\n"
"v_mfma_f32_16x16x32_bf16 v[172:175], v[20:23], a[236:239], v[172:175]\n"
"s_waitcnt lgkmcnt(12)\n"
"v_mfma_f32_16x16x32_bf16 v[144:147], v[24:27], a[240:243], v[144:147]\n"
"ds_read_b64_tr_b16 v[20:21], v6 offset:2048\n"
"v_mfma_f32_16x16x32_bf16 v[148:151], v[24:27], a[244:247], v[148:151]\n"
"ds_read_b64_tr_b16 v[22:23], v6 offset:2560\n"
"v_mfma_f32_16x16x32_bf16 v[152:155], v[24:27], a[248:251], v[152:155]\n"
"buffer_load_dwordx4 v1, s[24:27], s21 offen offset:2048 sc1 lds\n"
"v_mfma_f32_16x16x32_bf16 v[156:159], v[24:27], a[252:255], v[156:159]\n"
"v_mfma_f32_16x16x32_bf16 v[160:163], v[28:31], a[240:243], v[160:163]\n"
"ds_read_b64_tr_b16 v[24:25], v6 offset:3072\n"
"v_mfma_f32_16x16x32_bf16 v[164:167], v[28:31], a[244:247], v[164:167]\n"
"ds_read_b64_tr_b16 v[26:27], v6 offset:3584\n"
"v_mfma_f32_16x16x32_bf16 v[168:171], v[28:31], a[248:251], v[168:171]\n"
"buffer_load_dwordx4 v1, s[24:27], s21 offen offset:3072 sc1 lds\n"
"v_mfma_f32_16x16x32_bf16 v[172:175], v[28:31], a[252:255], v[172:175]\n"
"s_waitcnt lgkmcnt(12)\n"
"v_mfma_f32_16x16x32_bf16 v[144:147], v[32:35], v[48:51], v[144:147]\n"
"ds_read_b64_tr_b16 v[28:29], v6 offset:4096\n"
"v_mfma_f32_16x16x32_bf16 v[148:151], v[32:35], v[52:55], v[148:151]\n"
"ds_read_b64_tr_b16 v[30:31], v6 offset:4608\n"
"v_mfma_f32_16x16x32_bf16 v[152:155], v[32:35], v[56:59], v[152:155]\n"
"v_mfma_f32_16x16x32_bf16 v[156:159], v[32:35], v[60:63], v[156:159]\n"
"v_mfma_f32_16x16x32_bf16 v[160:163], v[36:39], v[48:51], v[160:163]\n"
"ds_read_b64_tr_b16 v[32:33], v6 offset:5120\n"
"v_mfma_f32_16x16x32_bf16 v[164:167], v[36:39], v[52:55], v[164:167]\n"
"ds_read_b64_tr_b16 v[34:35], v6 offset:5632\n"
"v_mfma_f32_16x16x32_bf16 v[168:171], v[36:39], v[56:59], v[168:171]\n"
"v_mfma_f32_16x16x32_bf16 v[172:175], v[36:39], v[60:63], v[172:175]\n"
"s_waitcnt lgkmcnt(12)\n"
"v_mfma_f32_16x16x32_bf16 v[144:147], v[40:43], v[64:67], v[144:147]\n"
"ds_read_b64_tr_b16 v[36:37], v6 offset:6144\n"
"v_mfma_f32_16x16x32_bf16 v[148:151], v[40:43], v[68:71], v[148:151]\n"
"ds_read_b64_tr_b16 v[38:39], v6 offset:6656\n"
"v_mfma_f32_16x16x32_bf16 v[152:155], v[40:43], v[72:75], v[152:155]\n"
"v_mfma_f32_16x16x32_bf16 v[156:159], v[40:43], v[76:79], v[156:159]\n"
"v_mfma_f32_16x16x32_bf16 v[160:163], v[44:47], v[64:67], v[160:163]\n"
"ds_read_b64_tr_b16 v[40:41], v6 offset:7168\n"
"v_mfma_f32_16x16x32_bf16 v[164:167], v[44:47], v[68:71], v[164:167]\n"
"ds_read_b64_tr_b16 v[42:43], v6 offset:7680\n"
"v_mfma_f32_16x16x32_bf16 v[168:171], v[44:47], v[72:75], v[168:171]\n"
"v_mfma_f32_16x16x32_bf16 v[172:175], v[44:47], v[76:79], v[172:175]\n"
"s_waitcnt lgkmcnt(12)\n"
"v_mfma_f32_16x16x32_bf16 v[176:179], v[12:15], a[0:3], 0\n"
"ds_read_b64_tr_b16 v[44:45], v6 offset:8192\n"
"v_mfma_f32_16x16x32_bf16 v[180:183], v[12:15], a[4:7], 0\n"
"ds_read_b64_tr_b16 v[46:47], v6 offset:8704\n"
"v_mfma_f32_16x16x32_bf16 v[184:187], v[12:15], a[8:11], 0\n"
"v_mfma_f32_16x16x32_bf16 v[188:191], v[12:15], a[12:15], 0\n"
"v_mfma_f32_16x16x32_bf16 v[192:195], v[16:19], a[0:3], 0\n"
"ds_read_b64_tr_b16 v[12:13], v6 offset:9216\n"
"v_max_f32_e32 v144, 0, v144\n"
"v_mfma_f32_16x16x32_bf16 v[196:199], v[16:19], a[4:7], 0\n"
"ds_read_b64_tr_b16 v[14:15], v6 offset:9728\n"
"v_max_f32_e32 v145, 0, v145\n"
"v_mfma_f32_16x16x32_bf16 v[200:203], v[16:19], a[8:11], 0\n"
"v_max_f32_e32 v146, 0, v146\n"
"v_max_f32_e32 v147, 0, v147\n"
"v_mfma_f32_16x16x32_bf16 v[204:207], v[16:19], a[12:15], 0\n"
"v_mul_f32_e32 v160, v144, v160\n"
"v_mul_f32_e32 v161, v145, v161\n"
"s_waitcnt lgkmcnt(12)\n"
"v_mfma_f32_16x16x32_bf16 v[176:179], v[20:23], a[16:19], v[176:179]\n"
"ds_read_b64_tr_b16 v[16:17], v6 offset:10240\n"
"v_mul_f32_e32 v162, v146, v162\n"
"v_mfma_f32_16x16x32_bf16 v[180:183], v[20:23], a[20:23], v[180:183]\n"
"ds_read_b64_tr_b16 v[18:19], v6 offset:10752\n"
"v_mul_f32_e32 v163, v147, v163\n"
"v_mfma_f32_16x16x32_bf16 v[184:187], v[20:23], a[24:27], v[184:187]\n"
"v_cvt_pk_bf16_f32 v144, v160, v161\n"
"v_cvt_pk_bf16_f32 v145, v162, v163\n"
"v_mfma_f32_16x16x32_bf16 v[188:191], v[20:23], a[28:31], v[188:191]\n"
"v_max_f32_e32 v148, 0, v148\n"
"v_max_f32_e32 v149, 0, v149\n"
"v_mfma_f32_16x16x32_bf16 v[192:195], v[24:27], a[16:19], v[192:195]\n"
"ds_read_b64_tr_b16 v[20:21], v6 offset:11264\n"
"v_max_f32_e32 v150, 0, v150\n"
"v_mfma_f32_16x16x32_bf16 v[196:199], v[24:27], a[20:23], v[196:199]\n"
"ds_read_b64_tr_b16 v[22:23], v6 offset:11776\n"
"v_max_f32_e32 v151, 0, v151\n"
"v_mfma_f32_16x16x32_bf16 v[200:203], v[24:27], a[24:27], v[200:203]\n"
"v_mul_f32_e32 v164, v148, v164\n"
"v_mul_f32_e32 v165, v149, v165\n"
"v_mfma_f32_16x16x32_bf16 v[204:207], v[24:27], a[28:31], v[204:207]\n"
"v_mul_f32_e32 v166, v150, v166\n"
"v_mul_f32_e32 v167, v151, v167\n"
"s_waitcnt lgkmcnt(12)\n"
"v_mfma_f32_16x16x32_bf16 v[176:179], v[28:31], a[32:35], v[176:179]\n"
"ds_read_b64_tr_b16 v[24:25], v6 offset:12288\n"
"v_cvt_pk_bf16_f32 v148, v164, v165\n"
"v_mfma_f32_16x16x32_bf16 v[180:183], v[28:31], a[36:39], v[180:183]\n"
"ds_read_b64_tr_b16 v[26:27], v6 offset:12800\n"
"v_cvt_pk_bf16_f32 v149, v166, v167\n"
"v_mfma_f32_16x16x32_bf16 v[184:187], v[28:31], a[40:43], v[184:187]\n"
"v_max_f32_e32 v152, 0, v152\n"
"v_max_f32_e32 v153, 0, v153\n"
"v_mfma_f32_16x16x32_bf16 v[188:191], v[28:31], a[44:47], v[188:191]\n"
"v_max_f32_e32 v154, 0, v154\n"
"v_max_f32_e32 v155, 0, v155\n"
"v_mfma_f32_16x16x32_bf16 v[192:195], v[32:35], a[32:35], v[192:195]\n"
"ds_read_b64_tr_b16 v[28:29], v6 offset:13312\n"
"v_mul_f32_e32 v168, v152, v168\n"
"v_mfma_f32_16x16x32_bf16 v[196:199], v[32:35], a[36:39], v[196:199]\n"
"ds_read_b64_tr_b16 v[30:31], v6 offset:13824\n"
"v_mul_f32_e32 v169, v153, v169\n"
"v_mfma_f32_16x16x32_bf16 v[200:203], v[32:35], a[40:43], v[200:203]\n"
"v_mul_f32_e32 v170, v154, v170\n"
"v_mul_f32_e32 v171, v155, v171\n"
"v_mfma_f32_16x16x32_bf16 v[204:207], v[32:35], a[44:47], v[204:207]\n"
"v_cvt_pk_bf16_f32 v152, v168, v169\n"
"v_cvt_pk_bf16_f32 v153, v170, v171\n"
"s_waitcnt lgkmcnt(12)\n"
"v_mfma_f32_16x16x32_bf16 v[176:179], v[36:39], a[48:51], v[176:179]\n"
"ds_read_b64_tr_b16 v[32:33], v6 offset:14336\n"
"v_max_f32_e32 v156, 0, v156\n"
"v_mfma_f32_16x16x32_bf16 v[180:183], v[36:39], a[52:55], v[180:183]\n"
"ds_read_b64_tr_b16 v[34:35], v6 offset:14848\n"
"v_max_f32_e32 v157, 0, v157\n"
"v_mfma_f32_16x16x32_bf16 v[184:187], v[36:39], a[56:59], v[184:187]\n"
"v_max_f32_e32 v158, 0, v158\n"
"v_max_f32_e32 v159, 0, v159\n"
"v_mfma_f32_16x16x32_bf16 v[188:191], v[36:39], a[60:63], v[188:191]\n"
"v_mul_f32_e32 v172, v156, v172\n"
"v_mul_f32_e32 v173, v157, v173\n"
"v_mfma_f32_16x16x32_bf16 v[192:195], v[40:43], a[48:51], v[192:195]\n"
"ds_read_b64_tr_b16 v[36:37], v6 offset:15360\n"
"v_mul_f32_e32 v174, v158, v174\n"
"v_mfma_f32_16x16x32_bf16 v[196:199], v[40:43], a[52:55], v[196:199]\n"
"ds_read_b64_tr_b16 v[38:39], v6 offset:15872\n"
"v_mul_f32_e32 v175, v159, v175\n"
"v_mfma_f32_16x16x32_bf16 v[200:203], v[40:43], a[56:59], v[200:203]\n"
"v_cvt_pk_bf16_f32 v156, v172, v173\n"
"v_cvt_pk_bf16_f32 v157, v174, v175\n"
"v_mfma_f32_16x16x32_bf16 v[204:207], v[40:43], a[60:63], v[204:207]\n"
"s_waitcnt vmcnt(33)\n"
"s_waitcnt vmcnt(4)\n"
"s_barrier\n"
"s_cmp_eq_u32 s23, 0\n"
"s_cbranch_scc0 Lg1_sig_%=\n"
"v_mov_b32_e32 v240, 0\n"
"v_mov_b32_e32 v241, 1\n"
"s_mov_b64 exec, 1\n"
"global_atomic_add v240, v241, s[48:49]\n"
"s_mov_b64 exec, -1\n"
"Lg1_sig_%=:\n"
"s_waitcnt lgkmcnt(12)\n"
"v_mfma_f32_16x16x32_bf16 v[176:179], v[44:47], a[64:67], v[176:179]\n"
"ds_read_b64_tr_b16 v[40:41], v6 offset:16384\n"
"v_mfma_f32_16x16x32_bf16 v[180:183], v[44:47], a[68:71], v[180:183]\n"
"ds_read_b64_tr_b16 v[42:43], v6 offset:16896\n"
"v_mfma_f32_16x16x32_bf16 v[184:187], v[44:47], a[72:75], v[184:187]\n"
"s_add_u32 m0, s20, 131072\n"
"s_mov_b32 s21, 81920\n"
"buffer_load_dwordx4 v1, s[24:27], s21 offen sc1 lds\n"
"v_mfma_f32_16x16x32_bf16 v[188:191], v[44:47], a[76:79], v[188:191]\n"
"v_mfma_f32_16x16x32_bf16 v[192:195], v[12:15], a[64:67], v[192:195]\n"
"ds_read_b64_tr_b16 v[44:45], v6 offset:17408\n"
"v_mfma_f32_16x16x32_bf16 v[196:199], v[12:15], a[68:71], v[196:199]\n"
"ds_read_b64_tr_b16 v[46:47], v6 offset:17920\n"
"v_mfma_f32_16x16x32_bf16 v[200:203], v[12:15], a[72:75], v[200:203]\n"
"buffer_load_dwordx4 v1, s[24:27], s21 offen offset:1024 sc1 lds\n"
"v_mfma_f32_16x16x32_bf16 v[204:207], v[12:15], a[76:79], v[204:207]\n"
"s_waitcnt lgkmcnt(12)\n"
"v_mfma_f32_16x16x32_bf16 v[176:179], v[16:19], a[80:83], v[176:179]\n"
"ds_read_b64_tr_b16 v[12:13], v6 offset:18432\n"
"v_mfma_f32_16x16x32_bf16 v[180:183], v[16:19], a[84:87], v[180:183]\n"
"ds_read_b64_tr_b16 v[14:15], v6 offset:18944\n"
"v_mfma_f32_16x16x32_bf16 v[184:187], v[16:19], a[88:91], v[184:187]\n"
"buffer_load_dwordx4 v1, s[24:27], s21 offen offset:2048 sc1 lds\n"
"v_mfma_f32_16x16x32_bf16 v[188:191], v[16:19], a[92:95], v[188:191]\n"
"v_mfma_f32_16x16x32_bf16 v[192:195], v[20:23], a[80:83], v[192:195]\n"
"ds_read_b64_tr_b16 v[16:17], v6 offset:19456\n"
"v_mfma_f32_16x16x32_bf16 v[196:199], v[20:23], a[84:87], v[196:199]\n"
"ds_read_b64_tr_b16 v[18:19], v6 offset:19968\n"
"v_mfma_f32_16x16x32_bf16 v[200:203], v[20:23], a[88:91], v[200:203]\n"
"buffer_load_dwordx4 v1, s[24:27], s21 offen offset:3072 sc1 lds\n"
"v_mfma_f32_16x16x32_bf16 v[204:207], v[20:23], a[92:95], v[204:207]\n"
"s_waitcnt lgkmcnt(12)\n"
"v_mfma_f32_16x16x32_bf16 v[176:179], v[24:27], a[96:99], v[176:179]\n"
"ds_read_b64_tr_b16 v[20:21], v6 offset:20480\n"
"v_mfma_f32_16x16x32_bf16 v[180:183], v[24:27], a[100:103], v[180:183]\n"
"ds_read_b64_tr_b16 v[22:23], v6 offset:20992\n"
"v_mfma_f32_16x16x32_bf16 v[184:187], v[24:27], a[104:107], v[184:187]\n"
"v_mfma_f32_16x16x32_bf16 v[188:191], v[24:27], a[108:111], v[188:191]\n"
"v_mfma_f32_16x16x32_bf16 v[192:195], v[28:31], a[96:99], v[192:195]\n"
"ds_read_b64_tr_b16 v[24:25], v6 offset:21504\n"
"v_mfma_f32_16x16x32_bf16 v[196:199], v[28:31], a[100:103], v[196:199]\n"
"ds_read_b64_tr_b16 v[26:27], v6 offset:22016\n"
"v_mfma_f32_16x16x32_bf16 v[200:203], v[28:31], a[104:107], v[200:203]\n"
"v_mfma_f32_16x16x32_bf16 v[204:207], v[28:31], a[108:111], v[204:207]\n"
"s_waitcnt lgkmcnt(12)\n"
"v_mfma_f32_16x16x32_bf16 v[176:179], v[32:35], a[112:115], v[176:179]\n"
"ds_read_b64_tr_b16 v[28:29], v6 offset:22528\n"
"v_mfma_f32_16x16x32_bf16 v[180:183], v[32:35], a[116:119], v[180:183]\n"
"ds_read_b64_tr_b16 v[30:31], v6 offset:23040\n"
"v_mfma_f32_16x16x32_bf16 v[184:187], v[32:35], a[120:123], v[184:187]\n"
"v_mfma_f32_16x16x32_bf16 v[188:191], v[32:35], a[124:127], v[188:191]\n"
"v_mfma_f32_16x16x32_bf16 v[192:195], v[36:39], a[112:115], v[192:195]\n"
"ds_read_b64_tr_b16 v[32:33], v6 offset:23552\n"
"v_mfma_f32_16x16x32_bf16 v[196:199], v[36:39], a[116:119], v[196:199]\n"
"ds_read_b64_tr_b16 v[34:35], v6 offset:24064\n"
"v_mfma_f32_16x16x32_bf16 v[200:203], v[36:39], a[120:123], v[200:203]\n"
"v_mfma_f32_16x16x32_bf16 v[204:207], v[36:39], a[124:127], v[204:207]\n"
"s_waitcnt lgkmcnt(12)\n"
"v_mfma_f32_16x16x32_bf16 v[176:179], v[40:43], a[128:131], v[176:179]\n"
"ds_read_b64_tr_b16 v[36:37], v6 offset:24576\n"
"v_mfma_f32_16x16x32_bf16 v[180:183], v[40:43], a[132:135], v[180:183]\n"
"ds_read_b64_tr_b16 v[38:39], v6 offset:25088\n"
"v_mfma_f32_16x16x32_bf16 v[184:187], v[40:43], a[136:139], v[184:187]\n"
"v_mfma_f32_16x16x32_bf16 v[188:191], v[40:43], a[140:143], v[188:191]\n"
"v_mfma_f32_16x16x32_bf16 v[192:195], v[44:47], a[128:131], v[192:195]\n"
"ds_read_b64_tr_b16 v[40:41], v6 offset:25600\n"
"v_mfma_f32_16x16x32_bf16 v[196:199], v[44:47], a[132:135], v[196:199]\n"
"ds_read_b64_tr_b16 v[42:43], v6 offset:26112\n"
"v_mfma_f32_16x16x32_bf16 v[200:203], v[44:47], a[136:139], v[200:203]\n"
"v_mfma_f32_16x16x32_bf16 v[204:207], v[44:47], a[140:143], v[204:207]\n"
"s_waitcnt lgkmcnt(12)\n"
"v_mfma_f32_16x16x32_bf16 v[176:179], v[12:15], a[144:147], v[176:179]\n"
"ds_read_b64_tr_b16 v[44:45], v6 offset:26624\n"
"v_mfma_f32_16x16x32_bf16 v[180:183], v[12:15], a[148:151], v[180:183]\n"
"ds_read_b64_tr_b16 v[46:47], v6 offset:27136\n"
"v_mfma_f32_16x16x32_bf16 v[184:187], v[12:15], a[152:155], v[184:187]\n"
"v_mfma_f32_16x16x32_bf16 v[188:191], v[12:15], a[156:159], v[188:191]\n"
"v_mfma_f32_16x16x32_bf16 v[192:195], v[16:19], a[144:147], v[192:195]\n"
"ds_read_b64_tr_b16 v[12:13], v6 offset:27648\n"
"v_mfma_f32_16x16x32_bf16 v[196:199], v[16:19], a[148:151], v[196:199]\n"
"ds_read_b64_tr_b16 v[14:15], v6 offset:28160\n"
"v_mfma_f32_16x16x32_bf16 v[200:203], v[16:19], a[152:155], v[200:203]\n"
"v_mfma_f32_16x16x32_bf16 v[204:207], v[16:19], a[156:159], v[204:207]\n"
"s_waitcnt lgkmcnt(12)\n"
"v_mfma_f32_16x16x32_bf16 v[176:179], v[20:23], a[160:163], v[176:179]\n"
"ds_read_b64_tr_b16 v[16:17], v6 offset:28672\n"
"v_mfma_f32_16x16x32_bf16 v[180:183], v[20:23], a[164:167], v[180:183]\n"
"ds_read_b64_tr_b16 v[18:19], v6 offset:29184\n"
"v_mfma_f32_16x16x32_bf16 v[184:187], v[20:23], a[168:171], v[184:187]\n"
"v_mfma_f32_16x16x32_bf16 v[188:191], v[20:23], a[172:175], v[188:191]\n"
"v_mfma_f32_16x16x32_bf16 v[192:195], v[24:27], a[160:163], v[192:195]\n"
"ds_read_b64_tr_b16 v[20:21], v6 offset:29696\n"
"v_mfma_f32_16x16x32_bf16 v[196:199], v[24:27], a[164:167], v[196:199]\n"
"ds_read_b64_tr_b16 v[22:23], v6 offset:30208\n"
"v_mfma_f32_16x16x32_bf16 v[200:203], v[24:27], a[168:171], v[200:203]\n"
"v_mfma_f32_16x16x32_bf16 v[204:207], v[24:27], a[172:175], v[204:207]\n"
"s_waitcnt lgkmcnt(12)\n"
"v_mfma_f32_16x16x32_bf16 v[176:179], v[28:31], a[176:179], v[176:179]\n"
"ds_read_b64_tr_b16 v[24:25], v6 offset:30720\n"
"v_mfma_f32_16x16x32_bf16 v[180:183], v[28:31], a[180:183], v[180:183]\n"
"ds_read_b64_tr_b16 v[26:27], v6 offset:31232\n"
"v_mfma_f32_16x16x32_bf16 v[184:187], v[28:31], a[184:187], v[184:187]\n"
"v_mfma_f32_16x16x32_bf16 v[188:191], v[28:31], a[188:191], v[188:191]\n"
"v_mfma_f32_16x16x32_bf16 v[192:195], v[32:35], a[176:179], v[192:195]\n"
"ds_read_b64_tr_b16 v[28:29], v6 offset:31744\n"
"v_mfma_f32_16x16x32_bf16 v[196:199], v[32:35], a[180:183], v[196:199]\n"
"ds_read_b64_tr_b16 v[30:31], v6 offset:32256\n"
"v_mfma_f32_16x16x32_bf16 v[200:203], v[32:35], a[184:187], v[200:203]\n"
"v_mfma_f32_16x16x32_bf16 v[204:207], v[32:35], a[188:191], v[204:207]\n"
"s_barrier\n"
"s_waitcnt lgkmcnt(12)\n"
"v_mfma_f32_16x16x32_bf16 v[176:179], v[36:39], a[192:195], v[176:179]\n"
"ds_read_b64_tr_b16 v[32:33], v6 offset:32768\n"
"v_mfma_f32_16x16x32_bf16 v[180:183], v[36:39], a[196:199], v[180:183]\n"
"ds_read_b64_tr_b16 v[34:35], v6 offset:33280\n"
"v_mfma_f32_16x16x32_bf16 v[184:187], v[36:39], a[200:203], v[184:187]\n"
"s_add_u32 m0, s20, 0\n"
"s_mov_b32 s21, 98304\n"
"buffer_load_dwordx4 v1, s[24:27], s21 offen sc1 lds\n"
"v_mfma_f32_16x16x32_bf16 v[188:191], v[36:39], a[204:207], v[188:191]\n"
"v_mfma_f32_16x16x32_bf16 v[192:195], v[40:43], a[192:195], v[192:195]\n"
"ds_read_b64_tr_b16 v[36:37], v6 offset:33792\n"
"v_mfma_f32_16x16x32_bf16 v[196:199], v[40:43], a[196:199], v[196:199]\n"
"ds_read_b64_tr_b16 v[38:39], v6 offset:34304\n"
"v_mfma_f32_16x16x32_bf16 v[200:203], v[40:43], a[200:203], v[200:203]\n"
"buffer_load_dwordx4 v1, s[24:27], s21 offen offset:1024 sc1 lds\n"
"v_mfma_f32_16x16x32_bf16 v[204:207], v[40:43], a[204:207], v[204:207]\n"
"s_waitcnt lgkmcnt(12)\n"
"v_mfma_f32_16x16x32_bf16 v[176:179], v[44:47], a[208:211], v[176:179]\n"
"ds_read_b64_tr_b16 v[40:41], v6 offset:34816\n"
"v_mfma_f32_16x16x32_bf16 v[180:183], v[44:47], a[212:215], v[180:183]\n"
"ds_read_b64_tr_b16 v[42:43], v6 offset:35328\n"
"v_mfma_f32_16x16x32_bf16 v[184:187], v[44:47], a[216:219], v[184:187]\n"
"buffer_load_dwordx4 v1, s[24:27], s21 offen offset:2048 sc1 lds\n"
"v_mfma_f32_16x16x32_bf16 v[188:191], v[44:47], a[220:223], v[188:191]\n"
"v_mfma_f32_16x16x32_bf16 v[192:195], v[12:15], a[208:211], v[192:195]\n"
"ds_read_b64_tr_b16 v[44:45], v6 offset:35840\n"
"v_mfma_f32_16x16x32_bf16 v[196:199], v[12:15], a[212:215], v[196:199]\n"
"ds_read_b64_tr_b16 v[46:47], v6 offset:36352\n"
"v_mfma_f32_16x16x32_bf16 v[200:203], v[12:15], a[216:219], v[200:203]\n"
"buffer_load_dwordx4 v1, s[24:27], s21 offen offset:3072 sc1 lds\n"
"v_mfma_f32_16x16x32_bf16 v[204:207], v[12:15], a[220:223], v[204:207]\n"
"s_waitcnt lgkmcnt(12)\n"
"v_mfma_f32_16x16x32_bf16 v[176:179], v[16:19], a[224:227], v[176:179]\n"
"v_mfma_f32_16x16x32_bf16 v[180:183], v[16:19], a[228:231], v[180:183]\n"
"v_mfma_f32_16x16x32_bf16 v[184:187], v[16:19], a[232:235], v[184:187]\n"
"v_mfma_f32_16x16x32_bf16 v[188:191], v[16:19], a[236:239], v[188:191]\n"
"v_mfma_f32_16x16x32_bf16 v[192:195], v[20:23], a[224:227], v[192:195]\n"
"v_mfma_f32_16x16x32_bf16 v[196:199], v[20:23], a[228:231], v[196:199]\n"
"v_mfma_f32_16x16x32_bf16 v[200:203], v[20:23], a[232:235], v[200:203]\n"
"v_mfma_f32_16x16x32_bf16 v[204:207], v[20:23], a[236:239], v[204:207]\n"
"s_waitcnt lgkmcnt(8)\n"
"v_mfma_f32_16x16x32_bf16 v[176:179], v[24:27], a[240:243], v[176:179]\n"
"v_mfma_f32_16x16x32_bf16 v[180:183], v[24:27], a[244:247], v[180:183]\n"
"v_mfma_f32_16x16x32_bf16 v[184:187], v[24:27], a[248:251], v[184:187]\n"
"v_mfma_f32_16x16x32_bf16 v[188:191], v[24:27], a[252:255], v[188:191]\n"
"v_mfma_f32_16x16x32_bf16 v[192:195], v[28:31], a[240:243], v[192:195]\n"
"v_mfma_f32_16x16x32_bf16 v[196:199], v[28:31], a[244:247], v[196:199]\n"
"v_mfma_f32_16x16x32_bf16 v[200:203], v[28:31], a[248:251], v[200:203]\n"
"v_mfma_f32_16x16x32_bf16 v[204:207], v[28:31], a[252:255], v[204:207]\n"
"s_waitcnt lgkmcnt(4)\n"
"v_mfma_f32_16x16x32_bf16 v[176:179], v[32:35], v[48:51], v[176:179]\n"
"v_mfma_f32_16x16x32_bf16 v[180:183], v[32:35], v[52:55], v[180:183]\n"
"v_mfma_f32_16x16x32_bf16 v[184:187], v[32:35], v[56:59], v[184:187]\n"
"v_mfma_f32_16x16x32_bf16 v[188:191], v[32:35], v[60:63], v[188:191]\n"
"v_mfma_f32_16x16x32_bf16 v[192:195], v[36:39], v[48:51], v[192:195]\n"
"v_mfma_f32_16x16x32_bf16 v[196:199], v[36:39], v[52:55], v[196:199]\n"
"v_mfma_f32_16x16x32_bf16 v[200:203], v[36:39], v[56:59], v[200:203]\n"
"v_mfma_f32_16x16x32_bf16 v[204:207], v[36:39], v[60:63], v[204:207]\n"
"s_waitcnt lgkmcnt(0)\n"
"v_mfma_f32_16x16x32_bf16 v[176:179], v[40:43], v[64:67], v[176:179]\n"
"v_mfma_f32_16x16x32_bf16 v[180:183], v[40:43], v[68:71], v[180:183]\n"
"v_mfma_f32_16x16x32_bf16 v[184:187], v[40:43], v[72:75], v[184:187]\n"
"v_mfma_f32_16x16x32_bf16 v[188:191], v[40:43], v[76:79], v[188:191]\n"
"v_mfma_f32_16x16x32_bf16 v[192:195], v[44:47], v[64:67], v[192:195]\n"
"v_mfma_f32_16x16x32_bf16 v[196:199], v[44:47], v[68:71], v[196:199]\n"
"v_mfma_f32_16x16x32_bf16 v[200:203], v[44:47], v[72:75], v[200:203]\n"
"v_mfma_f32_16x16x32_bf16 v[204:207], v[44:47], v[76:79], v[204:207]\n"
"s_nop 7\n"
"v_max_f32_e32 v176, 0, v176\n"
"v_max_f32_e32 v177, 0, v177\n"
"v_max_f32_e32 v178, 0, v178\n"
"v_max_f32_e32 v179, 0, v179\n"
"v_mul_f32_e32 v192, v176, v192\n"
"v_mul_f32_e32 v193, v177, v193\n"
"v_mul_f32_e32 v194, v178, v194\n"
"v_mul_f32_e32 v195, v179, v195\n"
"v_cvt_pk_bf16_f32 v146, v192, v193\n"
"v_cvt_pk_bf16_f32 v147, v194, v195\n"
"v_max_f32_e32 v180, 0, v180\n"
"v_max_f32_e32 v181, 0, v181\n"
"v_max_f32_e32 v182, 0, v182\n"
"v_max_f32_e32 v183, 0, v183\n"
"v_mul_f32_e32 v196, v180, v196\n"
"v_mul_f32_e32 v197, v181, v197\n"
"v_mul_f32_e32 v198, v182, v198\n"
"v_mul_f32_e32 v199, v183, v199\n"
"v_cvt_pk_bf16_f32 v150, v196, v197\n"
"v_cvt_pk_bf16_f32 v151, v198, v199\n"
"v_max_f32_e32 v184, 0, v184\n"
"v_max_f32_e32 v185, 0, v185\n"
"v_max_f32_e32 v186, 0, v186\n"
"v_max_f32_e32 v187, 0, v187\n"
"v_mul_f32_e32 v200, v184, v200\n"
"v_mul_f32_e32 v201, v185, v201\n"
"v_mul_f32_e32 v202, v186, v202\n"
"v_mul_f32_e32 v203, v187, v203\n"
"v_cvt_pk_bf16_f32 v154, v200, v201\n"
"v_cvt_pk_bf16_f32 v155, v202, v203\n"
"v_max_f32_e32 v188, 0, v188\n"
"v_max_f32_e32 v189, 0, v189\n"
"v_max_f32_e32 v190, 0, v190\n"
"v_max_f32_e32 v191, 0, v191\n"
"v_mul_f32_e32 v204, v188, v204\n"
"v_mul_f32_e32 v205, v189, v205\n"
"v_mul_f32_e32 v206, v190, v206\n"
"v_mul_f32_e32 v207, v191, v207\n"
"v_cvt_pk_bf16_f32 v158, v204, v205\n"
"v_cvt_pk_bf16_f32 v159, v206, v207\n"
"s_mov_b32 s22, 0\n"
"buffer_store_dwordx4 v[144:147], v11, s[16:19], s22 offen sc1\n"
"s_mov_b32 s22, 147456\n"
"buffer_store_dwordx4 v[148:151], v11, s[16:19], s22 offen sc1\n"
"s_mov_b32 s22, 294912\n"
"buffer_store_dwordx4 v[152:155], v11, s[16:19], s22 offen sc1\n"
"s_mov_b32 s22, 442368\n"
"buffer_store_dwordx4 v[156:159], v11, s[16:19], s22 offen sc1\n"
"s_waitcnt vmcnt(0)\n"
"s_waitcnt lgkmcnt(0)\n"
      :: GEMM_IN(a1) : "v12","v13","v14","v15","v16","v17","v18","v19","v20","v21","v22","v23","v24","v25","v26","v27","v28","v29","v30","v31","v32","v33","v34","v35","v36","v37","v38","v39","v40","v41","v42","v43","v44","v45","v46","v47","v48","v49","v50","v51","v52","v53","v54","v55","v56","v57","v58","v59","v60","v61","v62","v63","v64","v65","v66","v67","v68","v69","v70","v71","v72","v73","v74","v75","v76","v77","v78","v79","v80","v81","v82","v83","v84","v85","v86","v87","v88","v89","v90","v91","v92","v93","v94","v95","v96","v97","v98","v99","v100","v101","v102","v103","v104","v105","v106","v107","v108","v109","v110","v111","v112","v113","v114","v115","v116","v117","v118","v119","v120","v121","v122","v123","v124","v125","v126","v127","v128","v129","v130","v131","v132","v133","v134","v135","v136","v137","v138","v139","v140","v141","v142","v143","v144","v145","v146","v147","v148","v149","v150","v151","v152","v153","v154","v155","v156","v157","v158","v159","v160","v161","v162","v163","v164","v165","v166","v167","v168","v169","v170","v171","v172","v173","v174","v175","v176","v177","v178","v179","v180","v181","v182","v183","v184","v185","v186","v187","v188","v189","v190","v191","v192","v193","v194","v195","v196","v197","v198","v199","v200","v201","v202","v203","v204","v205","v206","v207","v208","v209","v210","v211","v212","v213","v214","v215","v216","v217","v218","v219","v220","v221","v222","v223","v224","v225","v226","v227","v228","v229","v230","v231","v232","v233","v234","v235","v236","v237","v238","v239","v240","v241","v242","v243","a0","a1","a2","a3","a4","a5","a6","a7","a8","a9","a10","a11","a12","a13","a14","a15","a16","a17","a18","a19","a20","a21","a22","a23","a24","a25","a26","a27","a28","a29","a30","a31","a32","a33","a34","a35","a36","a37","a38","a39","a40","a41","a42","a43","a44","a45","a46","a47","a48","a49","a50","a51","a52","a53","a54","a55","a56","a57","a58","a59","a60","a61","a62","a63","a64","a65","a66","a67","a68","a69","a70","a71","a72","a73","a74","a75","a76","a77","a78","a79","a80","a81","a82","a83","a84","a85","a86","a87","a88","a89","a90","a91","a92","a93","a94","a95","a96","a97","a98","a99","a100","a101","a102","a103","a104","a105","a106","a107","a108","a109","a110","a111","a112","a113","a114","a115","a116","a117","a118","a119","a120","a121","a122","a123","a124","a125","a126","a127","a128","a129","a130","a131","a132","a133","a134","a135","a136","a137","a138","a139","a140","a141","a142","a143","a144","a145","a146","a147","a148","a149","a150","a151","a152","a153","a154","a155","a156","a157","a158","a159","a160","a161","a162","a163","a164","a165","a166","a167","a168","a169","a170","a171","a172","a173","a174","a175","a176","a177","a178","a179","a180","a181","a182","a183","a184","a185","a186","a187","a188","a189","a190","a191","a192","a193","a194","a195","a196","a197","a198","a199","a200","a201","a202","a203","a204","a205","a206","a207","a208","a209","a210","a211","a212","a213","a214","a215","a216","a217","a218","a219","a220","a221","a222","a223","a224","a225","a226","a227","a228","a229","a230","a231","a232","a233","a234","a235","a236","a237","a238","a239","a240","a241","a242","a243","a244","a245","a246","a247","a248","a249","a250","a251","a252","a253","a254","a255","s21","s22","s53","scc","vcc","memory");

    CODE_LABEL("Lm_E");
    signal_cnt<false>(cnt + (CNT_D + rb2 * 4u + split) * 64u + 32u, wid);
    if (wid < 3u) code_touch(pc, oE, oZ, tsc, wid, lane_id(), 24576u);

  }
  f32x4 x1v[3][2], gb[3];
  {
    const unsigned lane = lane_id();
    GemmArgs a = gemm_args<2>(ws + WS_HHF, ws + WS_WDT + (size_t)(cb * 4u + split) * 331776u, rb * 8u + wid * 2u, split * 36u, ldsb, wid, lane);
    a.srdO = make_srd(ws + WS_SLAB2 + (size_t)(rb * 4u + cb) * 147456u, 147456u);
    a.srdN = a.srdA; a.poll = cnt + (CNT_D + (rb >> 1) * 4u + split) * 64u + 32u; a.sig = a.poll;
    a.vo0 = (wid * 4u + split) * 9216u + lane * 16u; a.vo1 = 0;
    {
      const __amdgpu_buffer_rsrc_t rx = mk_rsrc(ws + WS_X1, NTOK * DM * 4u);
      const unsigned tok16 = rb * 8u + split * 2u;
#pragma unroll
      for (int i = 0; i < 3; ++i) {
        const unsigned nb = (i < 2 || three) ? wid + 4u * i : wid;
#pragma unroll
        for (int tb = 0; tb < 2; ++tb) x1v[i][tb] = ld16_sc1(rx, (((tok16 + tb) * 36u + cb * 9u + nb) * 64u + lane) * 16u);
        gb[i] = *(const f32x4*)(mod + 5 * DM + cb * 144u + 16u * nb + 4u * (lane >> 4));
      }
    }
    wait_cnt(cnt + (CNT_D + (rb >> 1) * 4u + split) * 64u, 8u, wid);

    asm volatile(
"s_nop 4\n"
"s_mov_b32 s22, 0\n"
"buffer_load_dwordx4 a[0:3], v2, s[12:15], s22 offen sc1\n"
"buffer_load_dwordx4 a[4:7], v3, s[12:15], s22 offen sc1\n"
"buffer_load_dwordx4 a[8:11], v2, s[12:15], s22 offen offset:1024 sc1\n"
"buffer_load_dwordx4 a[12:15], v3, s[12:15], s22 offen offset:1024 sc1\n"
"buffer_load_dwordx4 a[16:19], v2, s[12:15], s22 offen offset:2048 sc1\n"
"buffer_load_dwordx4 a[20:23], v3, s[12:15], s22 offen offset:2048 sc1\n"
"buffer_load_dwordx4 a[24:27], v2, s[12:15], s22 offen offset:3072 sc1\n"
"buffer_load_dwordx4 a[28:31], v3, s[12:15], s22 offen offset:3072 sc1\n"
"s_mov_b32 s22, 4096\n"
"buffer_load_dwordx4 a[32:35], v2, s[12:15], s22 offen sc1\n"
"buffer_load_dwordx4 a[36:39], v3, s[12:15], s22 offen sc1\n"
"buffer_load_dwordx4 a[40:43], v2, s[12:15], s22 offen offset:1024 sc1\n"
"buffer_load_dwordx4 a[44:47], v3, s[12:15], s22 offen offset:1024 sc1\n"
"buffer_load_dwordx4 a[48:51], v2, s[12:15], s22 offen offset:2048 sc1\n"
"buffer_load_dwordx4 a[52:55], v3, s[12:15], s22 offen offset:2048 sc1\n"
"buffer_load_dwordx4 a[56:59], v2, s[12:15], s22 offen offset:3072 sc1\n"
"buffer_load_dwordx4 a[60:63], v3, s[12:15], s22 offen offset:3072 sc1\n"
"s_mov_b32 s22, 8192\n"
"buffer_load_dwordx4 a[64:67], v2, s[12:15], s22 offen sc1\n"
"buffer_load_dwordx4 a[68:71], v3, s[12:15], s22 offen sc1\n"
"buffer_load_dwordx4 a[72:75], v2, s[12:15], s22 offen offset:1024 sc1\n"
"buffer_load_dwordx4 a[76:79], v3, s[12:15], s22 offen offset:1024 sc1\n"
"s_add_u32 m0, s20, 16384\n"
"s_mov_b32 s21, 114688\n"
"buffer_load_dwordx4 v1, s[8:11], s21 offen sc1 lds\n"
"buffer_load_dwordx4 v1, s[8:11], s21 offen offset:1024 sc1 lds\n"
"buffer_load_dwordx4 v1, s[8:11], s21 offen offset:2048 sc1 lds\n"
"buffer_load_dwordx4 v1, s[8:11], s21 offen offset:3072 sc1 lds\n"
"ds_read_b64_tr_b16 v[12:13], v6 offset:49152\n"
"ds_read_b64_tr_b16 v[14:15], v6 offset:49664\n"
"ds_read_b64_tr_b16 v[16:17], v6 offset:50176\n"
"ds_read_b64_tr_b16 v[18:19], v6 offset:50688\n"
"ds_read_b64_tr_b16 v[20:21], v6 offset:51200\n"
"ds_read_b64_tr_b16 v[22:23], v6 offset:51712\n"
"ds_read_b64_tr_b16 v[24:25], v6 offset:52224\n"
"ds_read_b64_tr_b16 v[26:27], v6 offset:52736\n"
"ds_read_b64_tr_b16 v[28:29], v6 offset:53248\n"
"ds_read_b64_tr_b16 v[30:31], v6 offset:53760\n"
"ds_read_b64_tr_b16 v[32:33], v6 offset:54272\n"
"ds_read_b64_tr_b16 v[34:35], v6 offset:54784\n"
"ds_read_b64_tr_b16 v[36:37], v6 offset:55296\n"
"ds_read_b64_tr_b16 v[38:39], v6 offset:55808\n"
"ds_read_b64_tr_b16 v[40:41], v6 offset:56320\n"
"ds_read_b64_tr_b16 v[42:43], v6 offset:56832\n"
"s_waitcnt vmcnt(22)\n"
"s_waitcnt lgkmcnt(12)\n"
"v_mfma_f32_16x16x32_bf16 v[80:83], v[12:15], a[0:3], 0\n"
"ds_read_b64_tr_b16 v[44:45], v6 offset:57344\n"
"v_mfma_f32_16x16x32_bf16 v[84:87], v[12:15], a[4:7], 0\n"
"ds_read_b64_tr_b16 v[46:47], v6 offset:57856\n"
"v_mfma_f32_16x16x32_bf16 v[88:91], v[16:19], a[0:3], 0\n"
"ds_read_b64_tr_b16 v[12:13], v6 offset:58368\n"
"v_mfma_f32_16x16x32_bf16 v[92:95], v[16:19], a[4:7], 0\n"
"ds_read_b64_tr_b16 v[14:15], v6 offset:58880\n"
"s_waitcnt lgkmcnt(12)\n"
"v_mfma_f32_16x16x32_bf16 v[96:99], v[20:23], a[0:3], 0\n"
"ds_read_b64_tr_b16 v[16:17], v6 offset:59392\n"
"buffer_load_dwordx4 a[80:83], v2, s[12:15], s22 offen offset:2048 sc1\n"
"v_mfma_f32_16x16x32_bf16 v[100:103], v[20:23], a[4:7], 0\n"
"ds_read_b64_tr_b16 v[18:19], v6 offset:59904\n"
"v_mfma_f32_16x16x32_bf16 v[104:107], v[24:27], a[0:3], 0\n"
"ds_read_b64_tr_b16 v[20:21], v6 offset:60416\n"
"v_mfma_f32_16x16x32_bf16 v[108:111], v[24:27], a[4:7], 0\n"
"ds_read_b64_tr_b16 v[22:23], v6 offset:60928\n"
"s_waitcnt lgkmcnt(12)\n"
"v_mfma_f32_16x16x32_bf16 v[112:115], v[28:31], a[0:3], 0\n"
"ds_read_b64_tr_b16 v[24:25], v6 offset:61440\n"
"v_mfma_f32_16x16x32_bf16 v[116:119], v[28:31], a[4:7], 0\n"
"ds_read_b64_tr_b16 v[26:27], v6 offset:61952\n"
"buffer_load_dwordx4 a[84:87], v3, s[12:15], s22 offen offset:2048 sc1\n"
"v_mfma_f32_16x16x32_bf16 v[120:123], v[32:35], a[0:3], 0\n"
"ds_read_b64_tr_b16 v[28:29], v6 offset:62464\n"
"v_mfma_f32_16x16x32_bf16 v[124:127], v[32:35], a[4:7], 0\n"
"ds_read_b64_tr_b16 v[30:31], v6 offset:62976\n"
"s_waitcnt lgkmcnt(12)\n"
"v_mfma_f32_16x16x32_bf16 v[128:131], v[36:39], a[0:3], 0\n"
"ds_read_b64_tr_b16 v[32:33], v6 offset:63488\n"
"v_mfma_f32_16x16x32_bf16 v[132:135], v[36:39], a[4:7], 0\n"
"ds_read_b64_tr_b16 v[34:35], v6 offset:64000\n"
"s_waitcnt vmcnt(22)\n"
"v_mfma_f32_16x16x32_bf16 v[80:83], v[40:43], a[8:11], v[80:83]\n"
"ds_read_b64_tr_b16 v[36:37], v6 offset:64512\n"
"buffer_load_dwordx4 a[88:91], v2, s[12:15], s22 offen offset:3072 sc1\n"
"v_mfma_f32_16x16x32_bf16 v[84:87], v[40:43], a[12:15], v[84:87]\n"
"ds_read_b64_tr_b16 v[38:39], v6 offset:65024\n"
"s_waitcnt lgkmcnt(12)\n"
"v_mfma_f32_16x16x32_bf16 v[88:91], v[44:47], a[8:11], v[88:91]\n"
"ds_read_b64_tr_b16 v[40:41], v7 offset:0\n"
"v_mfma_f32_16x16x32_bf16 v[92:95], v[44:47], a[12:15], v[92:95]\n"
"ds_read_b64_tr_b16 v[42:43], v7 offset:512\n"
"v_mfma_f32_16x16x32_bf16 v[96:99], v[12:15], a[8:11], v[96:99]\n"
"ds_read_b64_tr_b16 v[44:45], v7 offset:1024\n"
"v_mfma_f32_16x16x32_bf16 v[100:103], v[12:15], a[12:15], v[100:103]\n"
"ds_read_b64_tr_b16 v[46:47], v7 offset:1536\n"
"buffer_load_dwordx4 a[92:95], v3, s[12:15], s22 offen offset:3072 sc1\n"
"s_waitcnt lgkmcnt(12)\n"
"v_mfma_f32_16x16x32_bf16 v[104:107], v[16:19], a[8:11], v[104:107]\n"
"ds_read_b64_tr_b16 v[12:13], v7 offset:2048\n"
"v_mfma_f32_16x16x32_bf16 v[108:111], v[16:19], a[12:15], v[108:111]\n"
"ds_read_b64_tr_b16 v[14:15], v7 offset:2560\n"
"v_mfma_f32_16x16x32_bf16 v[112:115], v[20:23], a[8:11], v[112:115]\n"
"ds_read_b64_tr_b16 v[16:17], v7 offset:3072\n"
"v_mfma_f32_16x16x32_bf16 v[116:119], v[20:23], a[12:15], v[116:119]\n"
"ds_read_b64_tr_b16 v[18:19], v7 offset:3584\n"
"s_waitcnt lgkmcnt(12)\n"
"v_mfma_f32_16x16x32_bf16 v[120:123], v[24:27], a[8:11], v[120:123]\n"
"ds_read_b64_tr_b16 v[20:21], v7 offset:4096\n"
"s_add_u32 m0, s20, 32768\n"
"s_mov_b32 s21, 131072\n"
"buffer_load_dwordx4 v1, s[8:11], s21 offen sc1 lds\n"
"v_mfma_f32_16x16x32_bf16 v[124:127], v[24:27], a[12:15], v[124:127]\n"
"ds_read_b64_tr_b16 v[22:23], v7 offset:4608\n"
"v_mfma_f32_16x16x32_bf16 v[128:131], v[28:31], a[8:11], v[128:131]\n"
"ds_read_b64_tr_b16 v[24:25], v7 offset:5120\n"
"v_mfma_f32_16x16x32_bf16 v[132:135], v[28:31], a[12:15], v[132:135]\n"
"ds_read_b64_tr_b16 v[26:27], v7 offset:5632\n"
"s_waitcnt vmcnt(23)\n"
"s_waitcnt lgkmcnt(12)\n"
"v_mfma_f32_16x16x32_bf16 v[80:83], v[32:35], a[16:19], v[80:83]\n"
"ds_read_b64_tr_b16 v[28:29], v7 offset:6144\n"
"v_mfma_f32_16x16x32_bf16 v[84:87], v[32:35], a[20:23], v[84:87]\n"
"ds_read_b64_tr_b16 v[30:31], v7 offset:6656\n"
"buffer_load_dwordx4 v1, s[8:11], s21 offen offset:1024 sc1 lds\n"
"v_mfma_f32_16x16x32_bf16 v[88:91], v[36:39], a[16:19], v[88:91]\n"
"ds_read_b64_tr_b16 v[32:33], v7 offset:7168\n"
"v_mfma_f32_16x16x32_bf16 v[92:95], v[36:39], a[20:23], v[92:95]\n"
"ds_read_b64_tr_b16 v[34:35], v7 offset:7680\n"
"s_waitcnt lgkmcnt(12)\n"
"v_mfma_f32_16x16x32_bf16 v[96:99], v[40:43], a[16:19], v[96:99]\n"
"ds_read_b64_tr_b16 v[36:37], v7 offset:8192\n"
"v_mfma_f32_16x16x32_bf16 v[100:103], v[40:43], a[20:23], v[100:103]\n"
"ds_read_b64_tr_b16 v[38:39], v7 offset:8704\n"
"v_mfma_f32_16x16x32_bf16 v[104:107], v[44:47], a[16:19], v[104:107]\n"
"ds_read_b64_tr_b16 v[40:41], v7 offset:9216\n"
"buffer_load_dwordx4 v1, s[8:11], s21 offen offset:2048 sc1 lds\n"
"v_mfma_f32_16x16x32_bf16 v[108:111], v[44:47], a[20:23], v[108:111]\n"
"ds_read_b64_tr_b16 v[42:43], v7 offset:9728\n"
"s_waitcnt lgkmcnt(12)\n"
"v_mfma_f32_16x16x32_bf16 v[112:115], v[12:15], a[16:19], v[112:115]\n"
"ds_read_b64_tr_b16 v[44:45], v7 offset:10240\n"
"v_mfma_f32_16x16x32_bf16 v[116:119], v[12:15], a[20:23], v[116:119]\n"
"ds_read_b64_tr_b16 v[46:47], v7 offset:10752\n"
"v_mfma_f32_16x16x32_bf16 v[120:123], v[16:19], a[16:19], v[120:123]\n"
"ds_read_b64_tr_b16 v[12:13], v7 offset:11264\n"
"v_mfma_f32_16x16x32_bf16 v[124:127], v[16:19], a[20:23], v[124:127]\n"
"ds_read_b64_tr_b16 v[14:15], v7 offset:11776\n"
"buffer_load_dwordx4 v1, s[8:11], s21 offen offset:3072 sc1 lds\n"
"s_waitcnt lgkmcnt(12)\n"
"v_mfma_f32_16x16x32_bf16 v[128:131], v[20:23], a[16:19], v[128:131]\n"
"ds_read_b64_tr_b16 v[16:17], v7 offset:12288\n"
"v_mfma_f32_16x16x32_bf16 v[132:135], v[20:23], a[20:23], v[132:135]\n"
"ds_read_b64_tr_b16 v[18:19], v7 offset:12800\n"
"s_waitcnt vmcnt(24)\n"
"v_mfma_f32_16x16x32_bf16 v[80:83], v[24:27], a[24:27], v[80:83]\n"
"ds_read_b64_tr_b16 v[20:21], v7 offset:13312\n"
"v_mfma_f32_16x16x32_bf16 v[84:87], v[24:27], a[28:31], v[84:87]\n"
"ds_read_b64_tr_b16 v[22:23], v7 offset:13824\n"
"s_waitcnt lgkmcnt(12)\n"
"v_mfma_f32_16x16x32_bf16 v[88:91], v[28:31], a[24:27], v[88:91]\n"
"ds_read_b64_tr_b16 v[24:25], v7 offset:14336\n"
"s_mov_b32 s22, 12288\n"
"buffer_load_dwordx4 a[96:99], v2, s[12:15], s22 offen sc1\n"
"v_mfma_f32_16x16x32_bf16 v[92:95], v[28:31], a[28:31], v[92:95]\n"
"ds_read_b64_tr_b16 v[26:27], v7 offset:14848\n"
"v_mfma_f32_16x16x32_bf16 v[96:99], v[32:35], a[24:27], v[96:99]\n"
"ds_read_b64_tr_b16 v[28:29], v7 offset:15360\n"
"v_mfma_f32_16x16x32_bf16 v[100:103], v[32:35], a[28:31], v[100:103]\n"
"ds_read_b64_tr_b16 v[30:31], v7 offset:15872\n"
"s_barrier\n"
"s_waitcnt lgkmcnt(12)\n"
"v_mfma_f32_16x16x32_bf16 v[104:107], v[36:39], a[24:27], v[104:107]\n"
"ds_read_b64_tr_b16 v[32:33], v7 offset:16384\n"
"v_mfma_f32_16x16x32_bf16 v[108:111], v[36:39], a[28:31], v[108:111]\n"
"ds_read_b64_tr_b16 v[34:35], v7 offset:16896\n"
"buffer_load_dwordx4 a[100:103], v3, s[12:15], s22 offen sc1\n"
"v_mfma_f32_16x16x32_bf16 v[112:115], v[40:43], a[24:27], v[112:115]\n"
"ds_read_b64_tr_b16 v[36:37], v7 offset:17408\n"
"v_mfma_f32_16x16x32_bf16 v[116:119], v[40:43], a[28:31], v[116:119]\n"
"ds_read_b64_tr_b16 v[38:39], v7 offset:17920\n"
"s_waitcnt lgkmcnt(12)\n"
"v_mfma_f32_16x16x32_bf16 v[120:123], v[44:47], a[24:27], v[120:123]\n"
"ds_read_b64_tr_b16 v[40:41], v7 offset:18432\n"
"v_mfma_f32_16x16x32_bf16 v[124:127], v[44:47], a[28:31], v[124:127]\n"
"ds_read_b64_tr_b16 v[42:43], v7 offset:18944\n"
"v_mfma_f32_16x16x32_bf16 v[128:131], v[12:15], a[24:27], v[128:131]\n"
"ds_read_b64_tr_b16 v[44:45], v7 offset:19456\n"
"buffer_load_dwordx4 a[104:107], v2, s[12:15], s22 offen offset:1024 sc1\n"
"v_mfma_f32_16x16x32_bf16 v[132:135], v[12:15], a[28:31], v[132:135]\n"
"ds_read_b64_tr_b16 v[46:47], v7 offset:19968\n"
"s_waitcnt vmcnt(25)\n"
"s_waitcnt lgkmcnt(12)\n"
"v_mfma_f32_16x16x32_bf16 v[80:83], v[16:19], a[32:35], v[80:83]\n"
"ds_read_b64_tr_b16 v[12:13], v7 offset:20480\n"
"v_mfma_f32_16x16x32_bf16 v[84:87], v[16:19], a[36:39], v[84:87]\n"
"ds_read_b64_tr_b16 v[14:15], v7 offset:20992\n"
"v_mfma_f32_16x16x32_bf16 v[88:91], v[20:23], a[32:35], v[88:91]\n"
"ds_read_b64_tr_b16 v[16:17], v7 offset:21504\n"
"v_mfma_f32_16x16x32_bf16 v[92:95], v[20:23], a[36:39], v[92:95]\n"
"ds_read_b64_tr_b16 v[18:19], v7 offset:22016\n"
"buffer_load_dwordx4 a[108:111], v3, s[12:15], s22 offen offset:1024 sc1\n"
"s_waitcnt lgkmcnt(12)\n"
"v_mfma_f32_16x16x32_bf16 v[96:99], v[24:27], a[32:35], v[96:99]\n"
"ds_read_b64_tr_b16 v[20:21], v7 offset:22528\n"
"v_mfma_f32_16x16x32_bf16 v[100:103], v[24:27], a[36:39], v[100:103]\n"
"ds_read_b64_tr_b16 v[22:23], v7 offset:23040\n"
"v_mfma_f32_16x16x32_bf16 v[104:107], v[28:31], a[32:35], v[104:107]\n"
"ds_read_b64_tr_b16 v[24:25], v7 offset:23552\n"
"v_mfma_f32_16x16x32_bf16 v[108:111], v[28:31], a[36:39], v[108:111]\n"
"ds_read_b64_tr_b16 v[26:27], v7 offset:24064\n"
"s_waitcnt lgkmcnt(12)\n"
"v_mfma_f32_16x16x32_bf16 v[112:115], v[32:35], a[32:35], v[112:115]\n"
"ds_read_b64_tr_b16 v[28:29], v7 offset:24576\n"
"s_add_u32 m0, s20, 49152\n"
"s_mov_b32 s21, 147456\n"
"buffer_load_dwordx4 v1, s[8:11], s21 offen sc1 lds\n"
"v_mfma_f32_16x16x32_bf16 v[116:119], v[32:35], a[36:39], v[116:119]\n"
"ds_read_b64_tr_b16 v[30:31], v7 offset:25088\n"
"v_mfma_f32_16x16x32_bf16 v[120:123], v[36:39], a[32:35], v[120:123]\n"
"ds_read_b64_tr_b16 v[32:33], v7 offset:25600\n"
"v_mfma_f32_16x16x32_bf16 v[124:127], v[36:39], a[36:39], v[124:127]\n"
"ds_read_b64_tr_b16 v[34:35], v7 offset:26112\n"
"s_waitcnt lgkmcnt(12)\n"
"v_mfma_f32_16x16x32_bf16 v[128:131], v[40:43], a[32:35], v[128:131]\n"
"ds_read_b64_tr_b16 v[36:37], v7 offset:26624\n"
"v_mfma_f32_16x16x32_bf16 v[132:135], v[40:43], a[36:39], v[132:135]\n"
"ds_read_b64_tr_b16 v[38:39], v7 offset:27136\n"
"buffer_load_dwordx4 v1, s[8:11], s21 offen offset:1024 sc1 lds\n"
"s_waitcnt vmcnt(26)\n"
"v_mfma_f32_16x16x32_bf16 v[80:83], v[44:47], a[40:43], v[80:83]\n"
"ds_read_b64_tr_b16 v[40:41], v7 offset:27648\n"
"v_mfma_f32_16x16x32_bf16 v[84:87], v[44:47], a[44:47], v[84:87]\n"
"ds_read_b64_tr_b16 v[42:43], v7 offset:28160\n"
"s_waitcnt lgkmcnt(12)\n"
"v_mfma_f32_16x16x32_bf16 v[88:91], v[12:15], a[40:43], v[88:91]\n"
"ds_read_b64_tr_b16 v[44:45], v7 offset:28672\n"
"v_mfma_f32_16x16x32_bf16 v[92:95], v[12:15], a[44:47], v[92:95]\n"
"ds_read_b64_tr_b16 v[46:47], v7 offset:29184\n"
"v_mfma_f32_16x16x32_bf16 v[96:99], v[16:19], a[40:43], v[96:99]\n"
"ds_read_b64_tr_b16 v[12:13], v7 offset:29696\n"
"buffer_load_dwordx4 v1, s[8:11], s21 offen offset:2048 sc1 lds\n"
"v_mfma_f32_16x16x32_bf16 v[100:103], v[16:19], a[44:47], v[100:103]\n"
"ds_read_b64_tr_b16 v[14:15], v7 offset:30208\n"
"s_waitcnt lgkmcnt(12)\n"
"v_mfma_f32_16x16x32_bf16 v[104:107], v[20:23], a[40:43], v[104:107]\n"
"ds_read_b64_tr_b16 v[16:17], v7 offset:30720\n"
"v_mfma_f32_16x16x32_bf16 v[108:111], v[20:23], a[44:47], v[108:111]\n"
"ds_read_b64_tr_b16 v[18:19], v7 offset:31232\n"
"v_mfma_f32_16x16x32_bf16 v[112:115], v[24:27], a[40:43], v[112:115]\n"
"ds_read_b64_tr_b16 v[20:21], v7 offset:31744\n"
"v_mfma_f32_16x16x32_bf16 v[116:119], v[24:27], a[44:47], v[116:119]\n"
"ds_read_b64_tr_b16 v[22:23], v7 offset:32256\n"
"buffer_load_dwordx4 v1, s[8:11], s21 offen offset:3072 sc1 lds\n"
"s_barrier\n"
"s_waitcnt lgkmcnt(12)\n"
"v_mfma_f32_16x16x32_bf16 v[120:123], v[28:31], a[40:43], v[120:123]\n"
"ds_read_b64_tr_b16 v[24:25], v7 offset:32768\n"
"v_mfma_f32_16x16x32_bf16 v[124:127], v[28:31], a[44:47], v[124:127]\n"
"ds_read_b64_tr_b16 v[26:27], v7 offset:33280\n"
"v_mfma_f32_16x16x32_bf16 v[128:131], v[32:35], a[40:43], v[128:131]\n"
"ds_read_b64_tr_b16 v[28:29], v7 offset:33792\n"
"v_mfma_f32_16x16x32_bf16 v[132:135], v[32:35], a[44:47], v[132:135]\n"
"ds_read_b64_tr_b16 v[30:31], v7 offset:34304\n"
"s_waitcnt vmcnt(26)\n"
"s_waitcnt lgkmcnt(12)\n"
"v_mfma_f32_16x16x32_bf16 v[80:83], v[36:39], a[48:51], v[80:83]\n"
"ds_read_b64_tr_b16 v[32:33], v7 offset:34816\n"
"buffer_load_dwordx4 a[112:115], v2, s[12:15], s22 offen offset:2048 sc1\n"
"v_mfma_f32_16x16x32_bf16 v[84:87], v[36:39], a[52:55], v[84:87]\n"
"ds_read_b64_tr_b16 v[34:35], v7 offset:35328\n"
"v_mfma_f32_16x16x32_bf16 v[88:91], v[40:43], a[48:51], v[88:91]\n"
"ds_read_b64_tr_b16 v[36:37], v7 offset:35840\n"
"v_mfma_f32_16x16x32_bf16 v[92:95], v[40:43], a[52:55], v[92:95]\n"
"ds_read_b64_tr_b16 v[38:39], v7 offset:36352\n"
"s_waitcnt lgkmcnt(12)\n"
"v_mfma_f32_16x16x32_bf16 v[96:99], v[44:47], a[48:51], v[96:99]\n"
"ds_read_b64_tr_b16 v[40:41], v7 offset:36864\n"
"v_mfma_f32_16x16x32_bf16 v[100:103], v[44:47], a[52:55], v[100:103]\n"
"ds_read_b64_tr_b16 v[42:43], v7 offset:37376\n"
"buffer_load_dwordx4 a[116:119], v3, s[12:15], s22 offen offset:2048 sc1\n"
"v_mfma_f32_16x16x32_bf16 v[104:107], v[12:15], a[48:51], v[104:107]\n"
"ds_read_b64_tr_b16 v[44:45], v7 offset:37888\n"
"v_mfma_f32_16x16x32_bf16 v[108:111], v[12:15], a[52:55], v[108:111]\n"
"ds_read_b64_tr_b16 v[46:47], v7 offset:38400\n"
"s_waitcnt lgkmcnt(12)\n"
"v_mfma_f32_16x16x32_bf16 v[112:115], v[16:19], a[48:51], v[112:115]\n"
"ds_read_b64_tr_b16 v[12:13], v7 offset:38912\n"
"v_mfma_f32_16x16x32_bf16 v[116:119], v[16:19], a[52:55], v[116:119]\n"
"ds_read_b64_tr_b16 v[14:15], v7 offset:39424\n"
"v_mfma_f32_16x16x32_bf16 v[120:123], v[20:23], a[48:51], v[120:123]\n"
"ds_read_b64_tr_b16 v[16:17], v7 offset:39936\n"
"buffer_load_dwordx4 a[120:123], v2, s[12:15], s22 offen offset:3072 sc1\n"
"v_mfma_f32_16x16x32_bf16 v[124:127], v[20:23], a[52:55], v[124:127]\n"
"ds_read_b64_tr_b16 v[18:19], v7 offset:40448\n"
"s_waitcnt lgkmcnt(12)\n"
"v_mfma_f32_16x16x32_bf16 v[128:131], v[24:27], a[48:51], v[128:131]\n"
"ds_read_b64_tr_b16 v[20:21], v7 offset:40960\n"
"v_mfma_f32_16x16x32_bf16 v[132:135], v[24:27], a[52:55], v[132:135]\n"
"ds_read_b64_tr_b16 v[22:23], v7 offset:41472\n"
"s_waitcnt vmcnt(27)\n"
"v_mfma_f32_16x16x32_bf16 v[80:83], v[28:31], a[56:59], v[80:83]\n"
"ds_read_b64_tr_b16 v[24:25], v7 offset:41984\n"
"v_mfma_f32_16x16x32_bf16 v[84:87], v[28:31], a[60:63], v[84:87]\n"
"ds_read_b64_tr_b16 v[26:27], v7 offset:42496\n"
"buffer_load_dwordx4 a[124:127], v3, s[12:15], s22 offen offset:3072 sc1\n"
"s_waitcnt lgkmcnt(12)\n"
"v_mfma_f32_16x16x32_bf16 v[88:91], v[32:35], a[56:59], v[88:91]\n"
"ds_read_b64_tr_b16 v[28:29], v7 offset:43008\n"
"v_mfma_f32_16x16x32_bf16 v[92:95], v[32:35], a[60:63], v[92:95]\n"
"ds_read_b64_tr_b16 v[30:31], v7 offset:43520\n"
"v_mfma_f32_16x16x32_bf16 v[96:99], v[36:39], a[56:59], v[96:99]\n"
"ds_read_b64_tr_b16 v[32:33], v7 offset:44032\n"
"v_mfma_f32_16x16x32_bf16 v[100:103], v[36:39], a[60:63], v[100:103]\n"
"ds_read_b64_tr_b16 v[34:35], v7 offset:44544\n"
"s_waitcnt lgkmcnt(12)\n"
"v_mfma_f32_16x16x32_bf16 v[104:107], v[40:43], a[56:59], v[104:107]\n"
"ds_read_b64_tr_b16 v[36:37], v7 offset:45056\n"
"s_mov_b32 s22, 16384\n"
"buffer_load_dwordx4 a[128:131], v2, s[12:15], s22 offen sc1\n"
"v_mfma_f32_16x16x32_bf16 v[108:111], v[40:43], a[60:63], v[108:111]\n"
"ds_read_b64_tr_b16 v[38:39], v7 offset:45568\n"
"v_mfma_f32_16x16x32_bf16 v[112:115], v[44:47], a[56:59], v[112:115]\n"
"ds_read_b64_tr_b16 v[40:41], v7 offset:46080\n"
"v_mfma_f32_16x16x32_bf16 v[116:119], v[44:47], a[60:63], v[116:119]\n"
"ds_read_b64_tr_b16 v[42:43], v7 offset:46592\n"
"s_waitcnt lgkmcnt(12)\n"
"v_mfma_f32_16x16x32_bf16 v[120:123], v[12:15], a[56:59], v[120:123]\n"
"ds_read_b64_tr_b16 v[44:45], v7 offset:47104\n"
"v_mfma_f32_16x16x32_bf16 v[124:127], v[12:15], a[60:63], v[124:127]\n"
"ds_read_b64_tr_b16 v[46:47], v7 offset:47616\n"
"buffer_load_dwordx4 a[132:135], v3, s[12:15], s22 offen sc1\n"
"v_mfma_f32_16x16x32_bf16 v[128:131], v[16:19], a[56:59], v[128:131]\n"
"ds_read_b64_tr_b16 v[12:13], v7 offset:48128\n"
"v_mfma_f32_16x16x32_bf16 v[132:135], v[16:19], a[60:63], v[132:135]\n"
"ds_read_b64_tr_b16 v[14:15], v7 offset:48640\n"
"s_barrier\n"
"s_waitcnt vmcnt(28)\n"
"s_waitcnt lgkmcnt(12)\n"
"v_mfma_f32_16x16x32_bf16 v[80:83], v[20:23], a[64:67], v[80:83]\n"
"ds_read_b64_tr_b16 v[16:17], v7 offset:49152\n"
"v_mfma_f32_16x16x32_bf16 v[84:87], v[20:23], a[68:71], v[84:87]\n"
"ds_read_b64_tr_b16 v[18:19], v7 offset:49664\n"
"v_mfma_f32_16x16x32_bf16 v[88:91], v[24:27], a[64:67], v[88:91]\n"
"ds_read_b64_tr_b16 v[20:21], v7 offset:50176\n"
"s_add_u32 m0, s20, 65536\n"
"s_mov_b32 s21, 163840\n"
"buffer_load_dwordx4 v1, s[8:11], s21 offen sc1 lds\n"
"v_mfma_f32_16x16x32_bf16 v[92:95], v[24:27], a[68:71], v[92:95]\n"
"ds_read_b64_tr_b16 v[22:23], v7 offset:50688\n"
"s_waitcnt lgkmcnt(12)\n"
"v_mfma_f32_16x16x32_bf16 v[96:99], v[28:31], a[64:67], v[96:99]\n"
"ds_read_b64_tr_b16 v[24:25], v7 offset:51200\n"
"v_mfma_f32_16x16x32_bf16 v[100:103], v[28:31], a[68:71], v[100:103]\n"
"ds_read_b64_tr_b16 v[26:27], v7 offset:51712\n"
"v_mfma_f32_16x16x32_bf16 v[104:107], v[32:35], a[64:67], v[104:107]\n"
"ds_read_b64_tr_b16 v[28:29], v7 offset:52224\n"
"v_mfma_f32_16x16x32_bf16 v[108:111], v[32:35], a[68:71], v[108:111]\n"
"ds_read_b64_tr_b16 v[30:31], v7 offset:52736\n"
"buffer_load_dwordx4 v1, s[8:11], s21 offen offset:1024 sc1 lds\n"
"s_waitcnt lgkmcnt(12)\n"
"v_mfma_f32_16x16x32_bf16 v[112:115], v[36:39], a[64:67], v[112:115]\n"
"ds_read_b64_tr_b16 v[32:33], v7 offset:53248\n"
"v_mfma_f32_16x16x32_bf16 v[116:119], v[36:39], a[68:71], v[116:119]\n"
"ds_read_b64_tr_b16 v[34:35], v7 offset:53760\n"
"v_mfma_f32_16x16x32_bf16 v[120:123], v[40:43], a[64:67], v[120:123]\n"
"ds_read_b64_tr_b16 v[36:37], v7 offset:54272\n"
"v_mfma_f32_16x16x32_bf16 v[124:127], v[40:43], a[68:71], v[124:127]\n"
"ds_read_b64_tr_b16 v[38:39], v7 offset:54784\n"
"s_waitcnt lgkmcnt(12)\n"
"v_mfma_f32_16x16x32_bf16 v[128:131], v[44:47], a[64:67], v[128:131]\n"
"ds_read_b64_tr_b16 v[40:41], v7 offset:55296\n"
"buffer_load_dwordx4 v1, s[8:11], s21 offen offset:2048 sc1 lds\n"
"v_mfma_f32_16x16x32_bf16 v[132:135], v[44:47], a[68:71], v[132:135]\n"
"ds_read_b64_tr_b16 v[42:43], v7 offset:55808\n"
"s_waitcnt vmcnt(29)\n"
"v_mfma_f32_16x16x32_bf16 v[80:83], v[12:15], a[72:75], v[80:83]\n"
"ds_read_b64_tr_b16 v[44:45], v7 offset:56320\n"
"v_mfma_f32_16x16x32_bf16 v[84:87], v[12:15], a[76:79], v[84:87]\n"
"ds_read_b64_tr_b16 v[46:47], v7 offset:56832\n"
"s_waitcnt lgkmcnt(12)\n"
"v_mfma_f32_16x16x32_bf16 v[88:91], v[16:19], a[72:75], v[88:91]\n"
"ds_read_b64_tr_b16 v[12:13], v7 offset:57344\n"
"v_mfma_f32_16x16x32_bf16 v[92:95], v[16:19], a[76:79], v[92:95]\n"
"ds_read_b64_tr_b16 v[14:15], v7 offset:57856\n"
"buffer_load_dwordx4 v1, s[8:11], s21 offen offset:3072 sc1 lds\n"
"v_mfma_f32_16x16x32_bf16 v[96:99], v[20:23], a[72:75], v[96:99]\n"
"ds_read_b64_tr_b16 v[16:17], v7 offset:58368\n"
"v_mfma_f32_16x16x32_bf16 v[100:103], v[20:23], a[76:79], v[100:103]\n"
"ds_read_b64_tr_b16 v[18:19], v7 offset:58880\n"
"s_waitcnt lgkmcnt(12)\n"
"v_mfma_f32_16x16x32_bf16 v[104:107], v[24:27], a[72:75], v[104:107]\n"
"ds_read_b64_tr_b16 v[20:21], v7 offset:59392\n"
"v_mfma_f32_16x16x32_bf16 v[108:111], v[24:27], a[76:79], v[108:111]\n"
"ds_read_b64_tr_b16 v[22:23], v7 offset:59904\n"
"v_mfma_f32_16x16x32_bf16 v[112:115], v[28:31], a[72:75], v[112:115]\n"
"ds_read_b64_tr_b16 v[24:25], v7 offset:60416\n"
"buffer_load_dwordx4 a[136:139], v2, s[12:15], s22 offen offset:1024 sc1\n"
"v_mfma_f32_16x16x32_bf16 v[116:119], v[28:31], a[76:79], v[116:119]\n"
"ds_read_b64_tr_b16 v[26:27], v7 offset:60928\n"
"s_waitcnt lgkmcnt(12)\n"
"v_mfma_f32_16x16x32_bf16 v[120:123], v[32:35], a[72:75], v[120:123]\n"
"ds_read_b64_tr_b16 v[28:29], v7 offset:61440\n"
"v_mfma_f32_16x16x32_bf16 v[124:127], v[32:35], a[76:79], v[124:127]\n"
"ds_read_b64_tr_b16 v[30:31], v7 offset:61952\n"
"v_mfma_f32_16x16x32_bf16 v[128:131], v[36:39], a[72:75], v[128:131]\n"
"ds_read_b64_tr_b16 v[32:33], v7 offset:62464\n"
"v_mfma_f32_16x16x32_bf16 v[132:135], v[36:39], a[76:79], v[132:135]\n"
"ds_read_b64_tr_b16 v[34:35], v7 offset:62976\n"
"buffer_load_dwordx4 a[140:143], v3, s[12:15], s22 offen offset:1024 sc1\n"
"s_waitcnt vmcnt(26)\n"
"s_waitcnt lgkmcnt(12)\n"
"v_mfma_f32_16x16x32_bf16 v[80:83], v[40:43], a[80:83], v[80:83]\n"
"ds_read_b64_tr_b16 v[36:37], v7 offset:63488\n"
"v_mfma_f32_16x16x32_bf16 v[84:87], v[40:43], a[84:87], v[84:87]\n"
"ds_read_b64_tr_b16 v[38:39], v7 offset:64000\n"
"v_mfma_f32_16x16x32_bf16 v[88:91], v[44:47], a[80:83], v[88:91]\n"
"ds_read_b64_tr_b16 v[40:41], v7 offset:64512\n"
"v_mfma_f32_16x16x32_bf16 v[92:95], v[44:47], a[84:87], v[92:95]\n"
"ds_read_b64_tr_b16 v[42:43], v7 offset:65024\n"
"s_barrier\n"
"s_waitcnt lgkmcnt(12)\n"
"v_mfma_f32_16x16x32_bf16 v[96:99], v[12:15], a[80:83], v[96:99]\n"
"ds_read_b64_tr_b16 v[44:45], v8 offset:0\n"
"buffer_load_dwordx4 a[144:147], v2, s[12:15], s22 offen offset:2048 sc1\n"
"v_mfma_f32_16x16x32_bf16 v[100:103], v[12:15], a[84:87], v[100:103]\n"
"ds_read_b64_tr_b16 v[46:47], v8 offset:512\n"
"v_mfma_f32_16x16x32_bf16 v[104:107], v[16:19], a[80:83], v[104:107]\n"
"ds_read_b64_tr_b16 v[12:13], v8 offset:1024\n"
"v_mfma_f32_16x16x32_bf16 v[108:111], v[16:19], a[84:87], v[108:111]\n"
"ds_read_b64_tr_b16 v[14:15], v8 offset:1536\n"
"s_waitcnt lgkmcnt(12)\n"
"v_mfma_f32_16x16x32_bf16 v[112:115], v[20:23], a[80:83], v[112:115]\n"
"ds_read_b64_tr_b16 v[16:17], v8 offset:2048\n"
"v_mfma_f32_16x16x32_bf16 v[116:119], v[20:23], a[84:87], v[116:119]\n"
"ds_read_b64_tr_b16 v[18:19], v8 offset:2560\n"
"buffer_load_dwordx4 a[148:151], v3, s[12:15], s22 offen offset:2048 sc1\n"
"v_mfma_f32_16x16x32_bf16 v[120:123], v[24:27], a[80:83], v[120:123]\n"
"ds_read_b64_tr_b16 v[20:21], v8 offset:3072\n"
"v_mfma_f32_16x16x32_bf16 v[124:127], v[24:27], a[84:87], v[124:127]\n"
"ds_read_b64_tr_b16 v[22:23], v8 offset:3584\n"
"s_waitcnt lgkmcnt(12)\n"
"v_mfma_f32_16x16x32_bf16 v[128:131], v[28:31], a[80:83], v[128:131]\n"
"ds_read_b64_tr_b16 v[24:25], v8 offset:4096\n"
"v_mfma_f32_16x16x32_bf16 v[132:135], v[28:31], a[84:87], v[132:135]\n"
"ds_read_b64_tr_b16 v[26:27], v8 offset:4608\n"
"s_waitcnt vmcnt(26)\n"
"v_mfma_f32_16x16x32_bf16 v[80:83], v[32:35], a[88:91], v[80:83]\n"
"ds_read_b64_tr_b16 v[28:29], v8 offset:5120\n"
"s_add_u32 m0, s20, 81920\n"
"s_mov_b32 s21, 180224\n"
"buffer_load_dwordx4 v1, s[8:11], s21 offen sc1 lds\n"
"v_mfma_f32_16x16x32_bf16 v[84:87], v[32:35], a[92:95], v[84:87]\n"
"ds_read_b64_tr_b16 v[30:31], v8 offset:5632\n"
"s_waitcnt lgkmcnt(12)\n"
"v_mfma_f32_16x16x32_bf16 v[88:91], v[36:39], a[88:91], v[88:91]\n"
"ds_read_b64_tr_b16 v[32:33], v8 offset:6144\n"
"v_mfma_f32_16x16x32_bf16 v[92:95], v[36:39], a[92:95], v[92:95]\n"
"ds_read_b64_tr_b16 v[34:35], v8 offset:6656\n"
"v_mfma_f32_16x16x32_bf16 v[96:99], v[40:43], a[88:91], v[96:99]\n"
"ds_read_b64_tr_b16 v[36:37], v8 offset:7168\n"
"v_mfma_f32_16x16x32_bf16 v[100:103], v[40:43], a[92:95], v[100:103]\n"
"ds_read_b64_tr_b16 v[38:39], v8 offset:7680\n"
"buffer_load_dwordx4 v1, s[8:11], s21 offen offset:1024 sc1 lds\n"
"s_waitcnt lgkmcnt(12)\n"
"v_mfma_f32_16x16x32_bf16 v[104:107], v[44:47], a[88:91], v[104:107]\n"
"ds_read_b64_tr_b16 v[40:41], v8 offset:8192\n"
"v_mfma_f32_16x16x32_bf16 v[108:111], v[44:47], a[92:95], v[108:111]\n"
"ds_read_b64_tr_b16 v[42:43], v8 offset:8704\n"
"v_mfma_f32_16x16x32_bf16 v[112:115], v[12:15], a[88:91], v[112:115]\n"
"ds_read_b64_tr_b16 v[44:45], v8 offset:9216\n"
"v_mfma_f32_16x16x32_bf16 v[116:119], v[12:15], a[92:95], v[116:119]\n"
"ds_read_b64_tr_b16 v[46:47], v8 offset:9728\n"
"s_waitcnt lgkmcnt(12)\n"
"v_mfma_f32_16x16x32_bf16 v[120:123], v[16:19], a[88:91], v[120:123]\n"
"ds_read_b64_tr_b16 v[12:13], v8 offset:10240\n"
"buffer_load_dwordx4 v1, s[8:11], s21 offen offset:2048 sc1 lds\n"
"v_mfma_f32_16x16x32_bf16 v[124:127], v[16:19], a[92:95], v[124:127]\n"
"ds_read_b64_tr_b16 v[14:15], v8 offset:10752\n"
"v_mfma_f32_16x16x32_bf16 v[128:131], v[20:23], a[88:91], v[128:131]\n"
"ds_read_b64_tr_b16 v[16:17], v8 offset:11264\n"
"v_mfma_f32_16x16x32_bf16 v[132:135], v[20:23], a[92:95], v[132:135]\n"
"ds_read_b64_tr_b16 v[18:19], v8 offset:11776\n"
"s_waitcnt vmcnt(23)\n"
"s_waitcnt lgkmcnt(12)\n"
"v_mfma_f32_16x16x32_bf16 v[80:83], v[24:27], a[96:99], v[80:83]\n"
"ds_read_b64_tr_b16 v[20:21], v8 offset:12288\n"
"v_mfma_f32_16x16x32_bf16 v[84:87], v[24:27], a[100:103], v[84:87]\n"
"ds_read_b64_tr_b16 v[22:23], v8 offset:12800\n"
"buffer_load_dwordx4 v1, s[8:11], s21 offen offset:3072 sc1 lds\n"
"v_mfma_f32_16x16x32_bf16 v[88:91], v[28:31], a[96:99], v[88:91]\n"
"ds_read_b64_tr_b16 v[24:25], v8 offset:13312\n"
"v_mfma_f32_16x16x32_bf16 v[92:95], v[28:31], a[100:103], v[92:95]\n"
"ds_read_b64_tr_b16 v[26:27], v8 offset:13824\n"
"s_waitcnt lgkmcnt(12)\n"
"v_mfma_f32_16x16x32_bf16 v[96:99], v[32:35], a[96:99], v[96:99]\n"
"ds_read_b64_tr_b16 v[28:29], v8 offset:14336\n"
"v_mfma_f32_16x16x32_bf16 v[100:103], v[32:35], a[100:103], v[100:103]\n"
"ds_read_b64_tr_b16 v[30:31], v8 offset:14848\n"
"v_mfma_f32_16x16x32_bf16 v[104:107], v[36:39], a[96:99], v[104:107]\n"
"ds_read_b64_tr_b16 v[32:33], v8 offset:15360\n"
"buffer_load_dwordx4 a[152:155], v2, s[12:15], s22 offen offset:3072 sc1\n"
"v_mfma_f32_16x16x32_bf16 v[108:111], v[36:39], a[100:103], v[108:111]\n"
"ds_read_b64_tr_b16 v[34:35], v8 offset:15872\n"
"s_barrier\n"
"s_waitcnt lgkmcnt(12)\n"
"v_mfma_f32_16x16x32_bf16 v[112:115], v[40:43], a[96:99], v[112:115]\n"
"ds_read_b64_tr_b16 v[36:37], v6 offset:0\n"
"v_mfma_f32_16x16x32_bf16 v[116:119], v[40:43], a[100:103], v[116:119]\n"
"ds_read_b64_tr_b16 v[38:39], v6 offset:512\n"
"v_mfma_f32_16x16x32_bf16 v[120:123], v[44:47], a[96:99], v[120:123]\n"
"ds_read_b64_tr_b16 v[40:41], v6 offset:1024\n"
"v_mfma_f32_16x16x32_bf16 v[124:127], v[44:47], a[100:103], v[124:127]\n"
"ds_read_b64_tr_b16 v[42:43], v6 offset:1536\n"
"buffer_load_dwordx4 a[156:159], v3, s[12:15], s22 offen offset:3072 sc1\n"
"s_waitcnt lgkmcnt(12)\n"
"v_mfma_f32_16x16x32_bf16 v[128:131], v[12:15], a[96:99], v[128:131]\n"
"ds_read_b64_tr_b16 v[44:45], v6 offset:2048\n"
"v_mfma_f32_16x16x32_bf16 v[132:135], v[12:15], a[100:103], v[132:135]\n"
"ds_read_b64_tr_b16 v[46:47], v6 offset:2560\n"
"s_waitcnt vmcnt(24)\n"
"v_mfma_f32_16x16x32_bf16 v[80:83], v[16:19], a[104:107], v[80:83]\n"
"ds_read_b64_tr_b16 v[12:13], v6 offset:3072\n"
"v_mfma_f32_16x16x32_bf16 v[84:87], v[16:19], a[108:111], v[84:87]\n"
"ds_read_b64_tr_b16 v[14:15], v6 offset:3584\n"
"s_waitcnt lgkmcnt(12)\n"
"v_mfma_f32_16x16x32_bf16 v[88:91], v[20:23], a[104:107], v[88:91]\n"
"ds_read_b64_tr_b16 v[16:17], v6 offset:4096\n"
"s_mov_b32 s22, 20480\n"
"buffer_load_dwordx4 a[160:163], v2, s[12:15], s22 offen sc1\n"
"v_mfma_f32_16x16x32_bf16 v[92:95], v[20:23], a[108:111], v[92:95]\n"
"ds_read_b64_tr_b16 v[18:19], v6 offset:4608\n"
"v_mfma_f32_16x16x32_bf16 v[96:99], v[24:27], a[104:107], v[96:99]\n"
"ds_read_b64_tr_b16 v[20:21], v6 offset:5120\n"
"v_mfma_f32_16x16x32_bf16 v[100:103], v[24:27], a[108:111], v[100:103]\n"
"ds_read_b64_tr_b16 v[22:23], v6 offset:5632\n"
"s_waitcnt lgkmcnt(12)\n"
"v_mfma_f32_16x16x32_bf16 v[104:107], v[28:31], a[104:107], v[104:107]\n"
"ds_read_b64_tr_b16 v[24:25], v6 offset:6144\n"
"v_mfma_f32_16x16x32_bf16 v[108:111], v[28:31], a[108:111], v[108:111]\n"
"ds_read_b64_tr_b16 v[26:27], v6 offset:6656\n"
"buffer_load_dwordx4 a[164:167], v3, s[12:15], s22 offen sc1\n"
"v_mfma_f32_16x16x32_bf16 v[112:115], v[32:35], a[104:107], v[112:115]\n"
"ds_read_b64_tr_b16 v[28:29], v6 offset:7168\n"
"v_mfma_f32_16x16x32_bf16 v[116:119], v[32:35], a[108:111], v[116:119]\n"
"ds_read_b64_tr_b16 v[30:31], v6 offset:7680\n"
"s_waitcnt lgkmcnt(12)\n"
"v_mfma_f32_16x16x32_bf16 v[120:123], v[36:39], a[104:107], v[120:123]\n"
"ds_read_b64_tr_b16 v[32:33], v6 offset:8192\n"
"v_mfma_f32_16x16x32_bf16 v[124:127], v[36:39], a[108:111], v[124:127]\n"
"ds_read_b64_tr_b16 v[34:35], v6 offset:8704\n"
"v_mfma_f32_16x16x32_bf16 v[128:131], v[40:43], a[104:107], v[128:131]\n"
"ds_read_b64_tr_b16 v[36:37], v6 offset:9216\n"
"s_add_u32 m0, s20, 98304\n"
"s_mov_b32 s21, 196608\n"
"buffer_load_dwordx4 v1, s[8:11], s21 offen sc1 lds\n"
"v_mfma_f32_16x16x32_bf16 v[132:135], v[40:43], a[108:111], v[132:135]\n"
"ds_read_b64_tr_b16 v[38:39], v6 offset:9728\n"
"s_waitcnt vmcnt(21)\n"
"s_waitcnt lgkmcnt(12)\n"
"v_mfma_f32_16x16x32_bf16 v[80:83], v[44:47], a[112:115], v[80:83]\n"
"ds_read_b64_tr_b16 v[40:41], v6 offset:10240\n"
"v_mfma_f32_16x16x32_bf16 v[84:87], v[44:47], a[116:119], v[84:87]\n"
"ds_read_b64_tr_b16 v[42:43], v6 offset:10752\n"
"v_mfma_f32_16x16x32_bf16 v[88:91], v[12:15], a[112:115], v[88:91]\n"
"ds_read_b64_tr_b16 v[44:45], v6 offset:11264\n"
"v_mfma_f32_16x16x32_bf16 v[92:95], v[12:15], a[116:119], v[92:95]\n"
"ds_read_b64_tr_b16 v[46:47], v6 offset:11776\n"
"buffer_load_dwordx4 v1, s[8:11], s21 offen offset:1024 sc1 lds\n"
"s_waitcnt lgkmcnt(12)\n"
"v_mfma_f32_16x16x32_bf16 v[96:99], v[16:19], a[112:115], v[96:99]\n"
"ds_read_b64_tr_b16 v[12:13], v6 offset:12288\n"
"v_mfma_f32_16x16x32_bf16 v[100:103], v[16:19], a[116:119], v[100:103]\n"
"ds_read_b64_tr_b16 v[14:15], v6 offset:12800\n"
"v_mfma_f32_16x16x32_bf16 v[104:107], v[20:23], a[112:115], v[104:107]\n"
"ds_read_b64_tr_b16 v[16:17], v6 offset:13312\n"
"v_mfma_f32_16x16x32_bf16 v[108:111], v[20:23], a[116:119], v[108:111]\n"
"ds_read_b64_tr_b16 v[18:19], v6 offset:13824\n"
"s_waitcnt lgkmcnt(12)\n"
"v_mfma_f32_16x16x32_bf16 v[112:115], v[24:27], a[112:115], v[112:115]\n"
"ds_read_b64_tr_b16 v[20:21], v6 offset:14336\n"
"buffer_load_dwordx4 v1, s[8:11], s21 offen offset:2048 sc1 lds\n"
"v_mfma_f32_16x16x32_bf16 v[116:119], v[24:27], a[116:119], v[116:119]\n"
"ds_read_b64_tr_b16 v[22:23], v6 offset:14848\n"
"v_mfma_f32_16x16x32_bf16 v[120:123], v[28:31], a[112:115], v[120:123]\n"
"ds_read_b64_tr_b16 v[24:25], v6 offset:15360\n"
"v_mfma_f32_16x16x32_bf16 v[124:127], v[28:31], a[116:119], v[124:127]\n"
"ds_read_b64_tr_b16 v[26:27], v6 offset:15872\n"
"s_barrier\n"
"s_waitcnt lgkmcnt(12)\n"
"v_mfma_f32_16x16x32_bf16 v[128:131], v[32:35], a[112:115], v[128:131]\n"
"ds_read_b64_tr_b16 v[28:29], v6 offset:16384\n"
"v_mfma_f32_16x16x32_bf16 v[132:135], v[32:35], a[116:119], v[132:135]\n"
"ds_read_b64_tr_b16 v[30:31], v6 offset:16896\n"
"buffer_load_dwordx4 v1, s[8:11], s21 offen offset:3072 sc1 lds\n"
"s_waitcnt vmcnt(22)\n"
"v_mfma_f32_16x16x32_bf16 v[80:83], v[36:39], a[120:123], v[80:83]\n"
"ds_read_b64_tr_b16 v[32:33], v6 offset:17408\n"
"v_mfma_f32_16x16x32_bf16 v[84:87], v[36:39], a[124:127], v[84:87]\n"
"ds_read_b64_tr_b16 v[34:35], v6 offset:17920\n"
"s_waitcnt lgkmcnt(12)\n"
"v_mfma_f32_16x16x32_bf16 v[88:91], v[40:43], a[120:123], v[88:91]\n"
"ds_read_b64_tr_b16 v[36:37], v6 offset:18432\n"
"v_mfma_f32_16x16x32_bf16 v[92:95], v[40:43], a[124:127], v[92:95]\n"
"ds_read_b64_tr_b16 v[38:39], v6 offset:18944\n"
"v_mfma_f32_16x16x32_bf16 v[96:99], v[44:47], a[120:123], v[96:99]\n"
"ds_read_b64_tr_b16 v[40:41], v6 offset:19456\n"
"buffer_load_dwordx4 a[168:171], v2, s[12:15], s22 offen offset:1024 sc1\n"
"v_mfma_f32_16x16x32_bf16 v[100:103], v[44:47], a[124:127], v[100:103]\n"
"ds_read_b64_tr_b16 v[42:43], v6 offset:19968\n"
"s_waitcnt lgkmcnt(12)\n"
"v_mfma_f32_16x16x32_bf16 v[104:107], v[12:15], a[120:123], v[104:107]\n"
"ds_read_b64_tr_b16 v[44:45], v6 offset:20480\n"
"v_mfma_f32_16x16x32_bf16 v[108:111], v[12:15], a[124:127], v[108:111]\n"
"ds_read_b64_tr_b16 v[46:47], v6 offset:20992\n"
"v_mfma_f32_16x16x32_bf16 v[112:115], v[16:19], a[120:123], v[112:115]\n"
"ds_read_b64_tr_b16 v[12:13], v6 offset:21504\n"
"v_mfma_f32_16x16x32_bf16 v[116:119], v[16:19], a[124:127], v[116:119]\n"
"ds_read_b64_tr_b16 v[14:15], v6 offset:22016\n"
"buffer_load_dwordx4 a[172:175], v3, s[12:15], s22 offen offset:1024 sc1\n"
"s_waitcnt lgkmcnt(12)\n"
"v_mfma_f32_16x16x32_bf16 v[120:123], v[20:23], a[120:123], v[120:123]\n"
"ds_read_b64_tr_b16 v[16:17], v6 offset:22528\n"
"v_mfma_f32_16x16x32_bf16 v[124:127], v[20:23], a[124:127], v[124:127]\n"
"ds_read_b64_tr_b16 v[18:19], v6 offset:23040\n"
"v_mfma_f32_16x16x32_bf16 v[128:131], v[24:27], a[120:123], v[128:131]\n"
"ds_read_b64_tr_b16 v[20:21], v6 offset:23552\n"
"v_mfma_f32_16x16x32_bf16 v[132:135], v[24:27], a[124:127], v[132:135]\n"
"ds_read_b64_tr_b16 v[22:23], v6 offset:24064\n"
"s_waitcnt vmcnt(22)\n"
"s_waitcnt lgkmcnt(12)\n"
"v_mfma_f32_16x16x32_bf16 v[80:83], v[28:31], a[128:131], v[80:83]\n"
"ds_read_b64_tr_b16 v[24:25], v6 offset:24576\n"
"v_mov_b32_e32 v154, 0\n"
"global_load_dword v155, v154, s[50:51] sc1\n"
"v_mfma_f32_16x16x32_bf16 v[84:87], v[28:31], a[132:135], v[84:87]\n"
"ds_read_b64_tr_b16 v[26:27], v6 offset:25088\n"
"v_mfma_f32_16x16x32_bf16 v[88:91], v[32:35], a[128:131], v[88:91]\n"
"ds_read_b64_tr_b16 v[28:29], v6 offset:25600\n"
"v_mfma_f32_16x16x32_bf16 v[92:95], v[32:35], a[132:135], v[92:95]\n"
"ds_read_b64_tr_b16 v[30:31], v6 offset:26112\n"
"s_waitcnt lgkmcnt(12)\n"
"v_mfma_f32_16x16x32_bf16 v[96:99], v[36:39], a[128:131], v[96:99]\n"
"ds_read_b64_tr_b16 v[32:33], v6 offset:26624\n"
"v_mfma_f32_16x16x32_bf16 v[100:103], v[36:39], a[132:135], v[100:103]\n"
"ds_read_b64_tr_b16 v[34:35], v6 offset:27136\n"
"buffer_load_dwordx4 a[176:179], v2, s[12:15], s22 offen offset:2048 sc1\n"
"v_mfma_f32_16x16x32_bf16 v[104:107], v[40:43], a[128:131], v[104:107]\n"
"ds_read_b64_tr_b16 v[36:37], v6 offset:27648\n"
"v_mfma_f32_16x16x32_bf16 v[108:111], v[40:43], a[132:135], v[108:111]\n"
"ds_read_b64_tr_b16 v[38:39], v6 offset:28160\n"
"s_waitcnt lgkmcnt(12)\n"
"v_mfma_f32_16x16x32_bf16 v[112:115], v[44:47], a[128:131], v[112:115]\n"
"ds_read_b64_tr_b16 v[40:41], v6 offset:28672\n"
"v_mfma_f32_16x16x32_bf16 v[116:119], v[44:47], a[132:135], v[116:119]\n"
"ds_read_b64_tr_b16 v[42:43], v6 offset:29184\n"
"v_mfma_f32_16x16x32_bf16 v[120:123], v[12:15], a[128:131], v[120:123]\n"
"ds_read_b64_tr_b16 v[44:45], v6 offset:29696\n"
"buffer_load_dwordx4 a[180:183], v3, s[12:15], s22 offen offset:2048 sc1\n"
"v_mfma_f32_16x16x32_bf16 v[124:127], v[12:15], a[132:135], v[124:127]\n"
"ds_read_b64_tr_b16 v[46:47], v6 offset:30208\n"
"s_waitcnt lgkmcnt(12)\n"
"v_mfma_f32_16x16x32_bf16 v[128:131], v[16:19], a[128:131], v[128:131]\n"
"ds_read_b64_tr_b16 v[12:13], v6 offset:30720\n"
"v_mfma_f32_16x16x32_bf16 v[132:135], v[16:19], a[132:135], v[132:135]\n"
"ds_read_b64_tr_b16 v[14:15], v6 offset:31232\n"
"s_waitcnt vmcnt(19)\n"
"v_mfma_f32_16x16x32_bf16 v[80:83], v[20:23], a[136:139], v[80:83]\n"
"ds_read_b64_tr_b16 v[16:17], v6 offset:31744\n"
"v_mfma_f32_16x16x32_bf16 v[84:87], v[20:23], a[140:143], v[84:87]\n"
"ds_read_b64_tr_b16 v[18:19], v6 offset:32256\n"
"s_add_u32 m0, s20, 114688\n"
"s_mov_b32 s21, 212992\n"
"buffer_load_dwordx4 v1, s[8:11], s21 offen sc1 lds\n"
"s_barrier\n"
"s_waitcnt lgkmcnt(12)\n"
"v_mfma_f32_16x16x32_bf16 v[88:91], v[24:27], a[136:139], v[88:91]\n"
"ds_read_b64_tr_b16 v[20:21], v6 offset:32768\n"
"v_mfma_f32_16x16x32_bf16 v[92:95], v[24:27], a[140:143], v[92:95]\n"
"ds_read_b64_tr_b16 v[22:23], v6 offset:33280\n"
"v_mfma_f32_16x16x32_bf16 v[96:99], v[28:31], a[136:139], v[96:99]\n"
"ds_read_b64_tr_b16 v[24:25], v6 offset:33792\n"
"v_mfma_f32_16x16x32_bf16 v[100:103], v[28:31], a[140:143], v[100:103]\n"
"ds_read_b64_tr_b16 v[26:27], v6 offset:34304\n"
"s_waitcnt lgkmcnt(12)\n"
"v_mfma_f32_16x16x32_bf16 v[104:107], v[32:35], a[136:139], v[104:107]\n"
"ds_read_b64_tr_b16 v[28:29], v6 offset:34816\n"
"buffer_load_dwordx4 v1, s[8:11], s21 offen offset:1024 sc1 lds\n"
"v_mfma_f32_16x16x32_bf16 v[108:111], v[32:35], a[140:143], v[108:111]\n"
"ds_read_b64_tr_b16 v[30:31], v6 offset:35328\n"
"v_mfma_f32_16x16x32_bf16 v[112:115], v[36:39], a[136:139], v[112:115]\n"
"ds_read_b64_tr_b16 v[32:33], v6 offset:35840\n"
"v_mfma_f32_16x16x32_bf16 v[116:119], v[36:39], a[140:143], v[116:119]\n"
"ds_read_b64_tr_b16 v[34:35], v6 offset:36352\n"
"s_waitcnt lgkmcnt(12)\n"
"v_mfma_f32_16x16x32_bf16 v[120:123], v[40:43], a[136:139], v[120:123]\n"
"ds_read_b64_tr_b16 v[36:37], v6 offset:36864\n"
"v_mfma_f32_16x16x32_bf16 v[124:127], v[40:43], a[140:143], v[124:127]\n"
"ds_read_b64_tr_b16 v[38:39], v6 offset:37376\n"
"buffer_load_dwordx4 v1, s[8:11], s21 offen offset:2048 sc1 lds\n"
"v_mfma_f32_16x16x32_bf16 v[128:131], v[44:47], a[136:139], v[128:131]\n"
"ds_read_b64_tr_b16 v[40:41], v6 offset:37888\n"
"v_mfma_f32_16x16x32_bf16 v[132:135], v[44:47], a[140:143], v[132:135]\n"
"ds_read_b64_tr_b16 v[42:43], v6 offset:38400\n"
"s_waitcnt vmcnt(20)\n"
"s_waitcnt lgkmcnt(12)\n"
"v_mfma_f32_16x16x32_bf16 v[80:83], v[12:15], a[144:147], v[80:83]\n"
"ds_read_b64_tr_b16 v[44:45], v6 offset:38912\n"
"v_mfma_f32_16x16x32_bf16 v[84:87], v[12:15], a[148:151], v[84:87]\n"
"ds_read_b64_tr_b16 v[46:47], v6 offset:39424\n"
"v_mfma_f32_16x16x32_bf16 v[88:91], v[16:19], a[144:147], v[88:91]\n"
"ds_read_b64_tr_b16 v[12:13], v6 offset:39936\n"
"buffer_load_dwordx4 v1, s[8:11], s21 offen offset:3072 sc1 lds\n"
"v_mfma_f32_16x16x32_bf16 v[92:95], v[16:19], a[148:151], v[92:95]\n"
"ds_read_b64_tr_b16 v[14:15], v6 offset:40448\n"
"s_waitcnt lgkmcnt(12)\n"
"v_mfma_f32_16x16x32_bf16 v[96:99], v[20:23], a[144:147], v[96:99]\n"
"ds_read_b64_tr_b16 v[16:17], v6 offset:40960\n"
"v_mfma_f32_16x16x32_bf16 v[100:103], v[20:23], a[148:151], v[100:103]\n"
"ds_read_b64_tr_b16 v[18:19], v6 offset:41472\n"
"v_mfma_f32_16x16x32_bf16 v[104:107], v[24:27], a[144:147], v[104:107]\n"
"ds_read_b64_tr_b16 v[20:21], v6 offset:41984\n"
"v_mfma_f32_16x16x32_bf16 v[108:111], v[24:27], a[148:151], v[108:111]\n"
"ds_read_b64_tr_b16 v[22:23], v6 offset:42496\n"
"buffer_load_dwordx4 a[184:187], v2, s[12:15], s22 offen offset:3072 sc1\n"
"s_waitcnt lgkmcnt(12)\n"
"v_mfma_f32_16x16x32_bf16 v[112:115], v[28:31], a[144:147], v[112:115]\n"
"ds_read_b64_tr_b16 v[24:25], v6 offset:43008\n"
"v_mfma_f32_16x16x32_bf16 v[116:119], v[28:31], a[148:151], v[116:119]\n"
"ds_read_b64_tr_b16 v[26:27], v6 offset:43520\n"
"v_mfma_f32_16x16x32_bf16 v[120:123], v[32:35], a[144:147], v[120:123]\n"
"ds_read_b64_tr_b16 v[28:29], v6 offset:44032\n"
"v_mfma_f32_16x16x32_bf16 v[124:127], v[32:35], a[148:151], v[124:127]\n"
"ds_read_b64_tr_b16 v[30:31], v6 offset:44544\n"
"s_waitcnt lgkmcnt(12)\n"
"v_mfma_f32_16x16x32_bf16 v[128:131], v[36:39], a[144:147], v[128:131]\n"
"ds_read_b64_tr_b16 v[32:33], v6 offset:45056\n"
"buffer_load_dwordx4 a[188:191], v3, s[12:15], s22 offen offset:3072 sc1\n"
"v_mfma_f32_16x16x32_bf16 v[132:135], v[36:39], a[148:151], v[132:135]\n"
"ds_read_b64_tr_b16 v[34:35], v6 offset:45568\n"
"s_waitcnt vmcnt(17)\n"
"v_mfma_f32_16x16x32_bf16 v[80:83], v[40:43], a[152:155], v[80:83]\n"
"ds_read_b64_tr_b16 v[36:37], v6 offset:46080\n"
"v_mfma_f32_16x16x32_bf16 v[84:87], v[40:43], a[156:159], v[84:87]\n"
"ds_read_b64_tr_b16 v[38:39], v6 offset:46592\n"
"s_waitcnt lgkmcnt(12)\n"
"v_mfma_f32_16x16x32_bf16 v[88:91], v[44:47], a[152:155], v[88:91]\n"
"ds_read_b64_tr_b16 v[40:41], v6 offset:47104\n"
"v_mfma_f32_16x16x32_bf16 v[92:95], v[44:47], a[156:159], v[92:95]\n"
"ds_read_b64_tr_b16 v[42:43], v6 offset:47616\n"
"s_mov_b32 s22, 24576\n"
"buffer_load_dwordx4 a[192:195], v2, s[12:15], s22 offen sc1\n"
"v_mfma_f32_16x16x32_bf16 v[96:99], v[12:15], a[152:155], v[96:99]\n"
"ds_read_b64_tr_b16 v[44:45], v6 offset:48128\n"
"v_mfma_f32_16x16x32_bf16 v[100:103], v[12:15], a[156:159], v[100:103]\n"
"ds_read_b64_tr_b16 v[46:47], v6 offset:48640\n"
"s_barrier\n"
"s_waitcnt lgkmcnt(12)\n"
"v_mfma_f32_16x16x32_bf16 v[104:107], v[16:19], a[152:155], v[104:107]\n"
"ds_read_b64_tr_b16 v[12:13], v6 offset:49152\n"
"v_mfma_f32_16x16x32_bf16 v[108:111], v[16:19], a[156:159], v[108:111]\n"
"ds_read_b64_tr_b16 v[14:15], v6 offset:49664\n"
"v_mfma_f32_16x16x32_bf16 v[112:115], v[20:23], a[152:155], v[112:115]\n"
"ds_read_b64_tr_b16 v[16:17], v6 offset:50176\n"
"buffer_load_dwordx4 a[196:199], v3, s[12:15], s22 offen sc1\n"
"v_mfma_f32_16x16x32_bf16 v[116:119], v[20:23], a[156:159], v[116:119]\n"
"ds_read_b64_tr_b16 v[18:19], v6 offset:50688\n"
"s_waitcnt lgkmcnt(12)\n"
"v_mfma_f32_16x16x32_bf16 v[120:123], v[24:27], a[152:155], v[120:123]\n"
"ds_read_b64_tr_b16 v[20:21], v6 offset:51200\n"
"v_mfma_f32_16x16x32_bf16 v[124:127], v[24:27], a[156:159], v[124:127]\n"
"ds_read_b64_tr_b16 v[22:23], v6 offset:51712\n"
"v_mfma_f32_16x16x32_bf16 v[128:131], v[28:31], a[152:155], v[128:131]\n"
"ds_read_b64_tr_b16 v[24:25], v6 offset:52224\n"
"v_mfma_f32_16x16x32_bf16 v[132:135], v[28:31], a[156:159], v[132:135]\n"
"ds_read_b64_tr_b16 v[26:27], v6 offset:52736\n"
"buffer_load_dwordx4 a[200:203], v2, s[12:15], s22 offen offset:1024 sc1\n"
"s_waitcnt vmcnt(18)\n"
"s_waitcnt lgkmcnt(12)\n"
"v_mfma_f32_16x16x32_bf16 v[80:83], v[32:35], a[160:163], v[80:83]\n"
"ds_read_b64_tr_b16 v[28:29], v6 offset:53248\n"
"v_mfma_f32_16x16x32_bf16 v[84:87], v[32:35], a[164:167], v[84:87]\n"
"ds_read_b64_tr_b16 v[30:31], v6 offset:53760\n"
"v_mfma_f32_16x16x32_bf16 v[88:91], v[36:39], a[160:163], v[88:91]\n"
"ds_read_b64_tr_b16 v[32:33], v6 offset:54272\n"
"v_mfma_f32_16x16x32_bf16 v[92:95], v[36:39], a[164:167], v[92:95]\n"
"ds_read_b64_tr_b16 v[34:35], v6 offset:54784\n"
"s_waitcnt lgkmcnt(12)\n"
"v_mfma_f32_16x16x32_bf16 v[96:99], v[40:43], a[160:163], v[96:99]\n"
"ds_read_b64_tr_b16 v[36:37], v6 offset:55296\n"
"buffer_load_dwordx4 a[204:207], v3, s[12:15], s22 offen offset:1024 sc1\n"
"v_mfma_f32_16x16x32_bf16 v[100:103], v[40:43], a[164:167], v[100:103]\n"
"ds_read_b64_tr_b16 v[38:39], v6 offset:55808\n"
"v_mfma_f32_16x16x32_bf16 v[104:107], v[44:47], a[160:163], v[104:107]\n"
"ds_read_b64_tr_b16 v[40:41], v6 offset:56320\n"
"v_mfma_f32_16x16x32_bf16 v[108:111], v[44:47], a[164:167], v[108:111]\n"
"ds_read_b64_tr_b16 v[42:43], v6 offset:56832\n"
"s_waitcnt lgkmcnt(12)\n"
"v_mfma_f32_16x16x32_bf16 v[112:115], v[12:15], a[160:163], v[112:115]\n"
"ds_read_b64_tr_b16 v[44:45], v6 offset:57344\n"
"v_mfma_f32_16x16x32_bf16 v[116:119], v[12:15], a[164:167], v[116:119]\n"
"ds_read_b64_tr_b16 v[46:47], v6 offset:57856\n"
"s_add_u32 m0, s20, 131072\n"
"s_mov_b32 s21, 229376\n"
"buffer_load_dwordx4 v1, s[8:11], s21 offen sc1 lds\n"
"v_mfma_f32_16x16x32_bf16 v[120:123], v[16:19], a[160:163], v[120:123]\n"
"ds_read_b64_tr_b16 v[12:13], v6 offset:58368\n"
"v_mfma_f32_16x16x32_bf16 v[124:127], v[16:19], a[164:167], v[124:127]\n"
"ds_read_b64_tr_b16 v[14:15], v6 offset:58880\n"
"s_waitcnt lgkmcnt(12)\n"
"v_mfma_f32_16x16x32_bf16 v[128:131], v[20:23], a[160:163], v[128:131]\n"
"ds_read_b64_tr_b16 v[16:17], v6 offset:59392\n"
"v_mfma_f32_16x16x32_bf16 v[132:135], v[20:23], a[164:167], v[132:135]\n"
"ds_read_b64_tr_b16 v[18:19], v6 offset:59904\n"
"s_waitcnt vmcnt(14)\n"
"v_mfma_f32_16x16x32_bf16 v[80:83], v[24:27], a[168:171], v[80:83]\n"
"ds_read_b64_tr_b16 v[20:21], v6 offset:60416\n"
"buffer_load_dwordx4 v1, s[8:11], s21 offen offset:1024 sc1 lds\n"
"v_mfma_f32_16x16x32_bf16 v[84:87], v[24:27], a[172:175], v[84:87]\n"
"ds_read_b64_tr_b16 v[22:23], v6 offset:60928\n"
"s_waitcnt lgkmcnt(12)\n"
"v_mfma_f32_16x16x32_bf16 v[88:91], v[28:31], a[168:171], v[88:91]\n"
"ds_read_b64_tr_b16 v[24:25], v6 offset:61440\n"
"v_mfma_f32_16x16x32_bf16 v[92:95], v[28:31], a[172:175], v[92:95]\n"
"ds_read_b64_tr_b16 v[26:27], v6 offset:61952\n"
"v_mfma_f32_16x16x32_bf16 v[96:99], v[32:35], a[168:171], v[96:99]\n"
"ds_read_b64_tr_b16 v[28:29], v6 offset:62464\n"
"v_mfma_f32_16x16x32_bf16 v[100:103], v[32:35], a[172:175], v[100:103]\n"
"ds_read_b64_tr_b16 v[30:31], v6 offset:62976\n"
"buffer_load_dwordx4 v1, s[8:11], s21 offen offset:2048 sc1 lds\n"
"s_waitcnt lgkmcnt(12)\n"
"v_mfma_f32_16x16x32_bf16 v[104:107], v[36:39], a[168:171], v[104:107]\n"
"ds_read_b64_tr_b16 v[32:33], v6 offset:63488\n"
"v_mfma_f32_16x16x32_bf16 v[108:111], v[36:39], a[172:175], v[108:111]\n"
"ds_read_b64_tr_b16 v[34:35], v6 offset:64000\n"
"v_mfma_f32_16x16x32_bf16 v[112:115], v[40:43], a[168:171], v[112:115]\n"
"ds_read_b64_tr_b16 v[36:37], v6 offset:64512\n"
"v_mfma_f32_16x16x32_bf16 v[116:119], v[40:43], a[172:175], v[116:119]\n"
"ds_read_b64_tr_b16 v[38:39], v6 offset:65024\n"
"s_barrier\n"
"s_waitcnt lgkmcnt(12)\n"
"v_mfma_f32_16x16x32_bf16 v[120:123], v[44:47], a[168:171], v[120:123]\n"
"ds_read_b64_tr_b16 v[40:41], v7 offset:0\n"
"buffer_load_dwordx4 v1, s[8:11], s21 offen offset:3072 sc1 lds\n"
"v_mfma_f32_16x16x32_bf16 v[124:127], v[44:47], a[172:175], v[124:127]\n"
"ds_read_b64_tr_b16 v[42:43], v7 offset:512\n"
"v_mfma_f32_16x16x32_bf16 v[128:131], v[12:15], a[168:171], v[128:131]\n"
"ds_read_b64_tr_b16 v[44:45], v7 offset:1024\n"
"v_mfma_f32_16x16x32_bf16 v[132:135], v[12:15], a[172:175], v[132:135]\n"
"ds_read_b64_tr_b16 v[46:47], v7 offset:1536\n"
"s_waitcnt vmcnt(14)\n"
"s_waitcnt lgkmcnt(12)\n"
"v_mfma_f32_16x16x32_bf16 v[80:83], v[16:19], a[176:179], v[80:83]\n"
"ds_read_b64_tr_b16 v[12:13], v7 offset:2048\n"
"v_mfma_f32_16x16x32_bf16 v[84:87], v[16:19], a[180:183], v[84:87]\n"
"ds_read_b64_tr_b16 v[14:15], v7 offset:2560\n"
"buffer_load_dwordx4 a[208:211], v2, s[12:15], s22 offen offset:2048 sc1\n"
"v_mfma_f32_16x16x32_bf16 v[88:91], v[20:23], a[176:179], v[88:91]\n"
"ds_read_b64_tr_b16 v[16:17], v7 offset:3072\n"
"v_mfma_f32_16x16x32_bf16 v[92:95], v[20:23], a[180:183], v[92:95]\n"
"ds_read_b64_tr_b16 v[18:19], v7 offset:3584\n"
"s_waitcnt lgkmcnt(12)\n"
"v_mfma_f32_16x16x32_bf16 v[96:99], v[24:27], a[176:179], v[96:99]\n"
"ds_read_b64_tr_b16 v[20:21], v7 offset:4096\n"
"v_mfma_f32_16x16x32_bf16 v[100:103], v[24:27], a[180:183], v[100:103]\n"
"ds_read_b64_tr_b16 v[22:23], v7 offset:4608\n"
"v_mfma_f32_16x16x32_bf16 v[104:107], v[28:31], a[176:179], v[104:107]\n"
"ds_read_b64_tr_b16 v[24:25], v7 offset:5120\n"
"buffer_load_dwordx4 a[212:215], v3, s[12:15], s22 offen offset:2048 sc1\n"
"v_mfma_f32_16x16x32_bf16 v[108:111], v[28:31], a[180:183], v[108:111]\n"
"ds_read_b64_tr_b16 v[26:27], v7 offset:5632\n"
"s_waitcnt lgkmcnt(12)\n"
"v_mfma_f32_16x16x32_bf16 v[112:115], v[32:35], a[176:179], v[112:115]\n"
"ds_read_b64_tr_b16 v[28:29], v7 offset:6144\n"
"v_mfma_f32_16x16x32_bf16 v[116:119], v[32:35], a[180:183], v[116:119]\n"
"ds_read_b64_tr_b16 v[30:31], v7 offset:6656\n"
"v_mfma_f32_16x16x32_bf16 v[120:123], v[36:39], a[176:179], v[120:123]\n"
"ds_read_b64_tr_b16 v[32:33], v7 offset:7168\n"
"v_mfma_f32_16x16x32_bf16 v[124:127], v[36:39], a[180:183], v[124:127]\n"
"ds_read_b64_tr_b16 v[34:35], v7 offset:7680\n"
"buffer_load_dwordx4 a[216:219], v2, s[12:15], s22 offen offset:3072 sc1\n"
"s_waitcnt lgkmcnt(12)\n"
"v_mfma_f32_16x16x32_bf16 v[128:131], v[40:43], a[176:179], v[128:131]\n"
"ds_read_b64_tr_b16 v[36:37], v7 offset:8192\n"
"v_mfma_f32_16x16x32_bf16 v[132:135], v[40:43], a[180:183], v[132:135]\n"
"ds_read_b64_tr_b16 v[38:39], v7 offset:8704\n"
"s_waitcnt vmcnt(11)\n"
"v_mfma_f32_16x16x32_bf16 v[80:83], v[44:47], a[184:187], v[80:83]\n"
"ds_read_b64_tr_b16 v[40:41], v7 offset:9216\n"
"v_mfma_f32_16x16x32_bf16 v[84:87], v[44:47], a[188:191], v[84:87]\n"
"ds_read_b64_tr_b16 v[42:43], v7 offset:9728\n"
"s_waitcnt lgkmcnt(12)\n"
"v_mfma_f32_16x16x32_bf16 v[88:91], v[12:15], a[184:187], v[88:91]\n"
"ds_read_b64_tr_b16 v[44:45], v7 offset:10240\n"
"buffer_load_dwordx4 a[220:223], v3, s[12:15], s22 offen offset:3072 sc1\n"
"v_mfma_f32_16x16x32_bf16 v[92:95], v[12:15], a[188:191], v[92:95]\n"
"ds_read_b64_tr_b16 v[46:47], v7 offset:10752\n"
"v_mfma_f32_16x16x32_bf16 v[96:99], v[16:19], a[184:187], v[96:99]\n"
"ds_read_b64_tr_b16 v[12:13], v7 offset:11264\n"
"v_mfma_f32_16x16x32_bf16 v[100:103], v[16:19], a[188:191], v[100:103]\n"
"ds_read_b64_tr_b16 v[14:15], v7 offset:11776\n"
"s_waitcnt lgkmcnt(12)\n"
"v_mfma_f32_16x16x32_bf16 v[104:107], v[20:23], a[184:187], v[104:107]\n"
"ds_read_b64_tr_b16 v[16:17], v7 offset:12288\n"
"v_mfma_f32_16x16x32_bf16 v[108:111], v[20:23], a[188:191], v[108:111]\n"
"ds_read_b64_tr_b16 v[18:19], v7 offset:12800\n"
"s_add_u32 m0, s20, 0\n"
"s_mov_b32 s21, 245760\n"
"buffer_load_dwordx4 v1, s[8:11], s21 offen sc1 lds\n"
"v_mfma_f32_16x16x32_bf16 v[112:115], v[24:27], a[184:187], v[112:115]\n"
"ds_read_b64_tr_b16 v[20:21], v7 offset:13312\n"
"v_mfma_f32_16x16x32_bf16 v[116:119], v[24:27], a[188:191], v[116:119]\n"
"ds_read_b64_tr_b16 v[22:23], v7 offset:13824\n"
"s_waitcnt lgkmcnt(12)\n"
"v_mfma_f32_16x16x32_bf16 v[120:123], v[28:31], a[184:187], v[120:123]\n"
"ds_read_b64_tr_b16 v[24:25], v7 offset:14336\n"
"v_mfma_f32_16x16x32_bf16 v[124:127], v[28:31], a[188:191], v[124:127]\n"
"ds_read_b64_tr_b16 v[26:27], v7 offset:14848\n"
"v_mfma_f32_16x16x32_bf16 v[128:131], v[32:35], a[184:187], v[128:131]\n"
"ds_read_b64_tr_b16 v[28:29], v7 offset:15360\n"
"buffer_load_dwordx4 v1, s[8:11], s21 offen offset:1024 sc1 lds\n"
"v_mfma_f32_16x16x32_bf16 v[132:135], v[32:35], a[188:191], v[132:135]\n"
"ds_read_b64_tr_b16 v[30:31], v7 offset:15872\n"
"s_barrier\n"
"s_waitcnt vmcnt(12)\n"
"s_waitcnt lgkmcnt(12)\n"
"v_mfma_f32_16x16x32_bf16 v[80:83], v[36:39], a[192:195], v[80:83]\n"
"ds_read_b64_tr_b16 v[32:33], v7 offset:16384\n"
"v_mfma_f32_16x16x32_bf16 v[84:87], v[36:39], a[196:199], v[84:87]\n"
"ds_read_b64_tr_b16 v[34:35], v7 offset:16896\n"
"v_mfma_f32_16x16x32_bf16 v[88:91], v[40:43], a[192:195], v[88:91]\n"
"ds_read_b64_tr_b16 v[36:37], v7 offset:17408\n"
"v_mfma_f32_16x16x32_bf16 v[92:95], v[40:43], a[196:199], v[92:95]\n"
"ds_read_b64_tr_b16 v[38:39], v7 offset:17920\n"
"buffer_load_dwordx4 v1, s[8:11], s21 offen offset:2048 sc1 lds\n"
"s_waitcnt lgkmcnt(12)\n"
"v_mfma_f32_16x16x32_bf16 v[96:99], v[44:47], a[192:195], v[96:99]\n"
"ds_read_b64_tr_b16 v[40:41], v7 offset:18432\n"
"v_mfma_f32_16x16x32_bf16 v[100:103], v[44:47], a[196:199], v[100:103]\n"
"ds_read_b64_tr_b16 v[42:43], v7 offset:18944\n"
"v_mfma_f32_16x16x32_bf16 v[104:107], v[12:15], a[192:195], v[104:107]\n"
"ds_read_b64_tr_b16 v[44:45], v7 offset:19456\n"
"v_mfma_f32_16x16x32_bf16 v[108:111], v[12:15], a[196:199], v[108:111]\n"
"ds_read_b64_tr_b16 v[46:47], v7 offset:19968\n"
"s_waitcnt lgkmcnt(12)\n"
"v_mfma_f32_16x16x32_bf16 v[112:115], v[16:19], a[192:195], v[112:115]\n"
"ds_read_b64_tr_b16 v[12:13], v7 offset:20480\n"
"buffer_load_dwordx4 v1, s[8:11], s21 offen offset:3072 sc1 lds\n"
"v_mfma_f32_16x16x32_bf16 v[116:119], v[16:19], a[196:199], v[116:119]\n"
"ds_read_b64_tr_b16 v[14:15], v7 offset:20992\n"
"v_mfma_f32_16x16x32_bf16 v[120:123], v[20:23], a[192:195], v[120:123]\n"
"ds_read_b64_tr_b16 v[16:17], v7 offset:21504\n"
"v_mfma_f32_16x16x32_bf16 v[124:127], v[20:23], a[196:199], v[124:127]\n"
"ds_read_b64_tr_b16 v[18:19], v7 offset:22016\n"
"s_waitcnt lgkmcnt(12)\n"
"v_mfma_f32_16x16x32_bf16 v[128:131], v[24:27], a[192:195], v[128:131]\n"
"ds_read_b64_tr_b16 v[20:21], v7 offset:22528\n"
"v_mfma_f32_16x16x32_bf16 v[132:135], v[24:27], a[196:199], v[132:135]\n"
"ds_read_b64_tr_b16 v[22:23], v7 offset:23040\n"
"s_mov_b32 s53, 2097152\n"
"Lg2_poll_%=:\n"
"v_cmp_gt_u32_e32 vcc, s52, v155\n"
"s_cmp_eq_u64 vcc, 0\n"
"s_cbranch_scc1 Lg2_polld_%=\n"
"global_load_dword v155, v154, s[50:51] sc1\n"
"s_waitcnt vmcnt(0)\n"
"s_sub_u32 s53, s53, 1\n"
"s_cmp_lg_u32 s53, 0\n"
"s_cbranch_scc1 Lg2_poll_%=\n"
"Lg2_polld_%=:\n"
"s_mov_b32 s22, 28672\n"
"buffer_load_dwordx4 a[224:227], v2, s[12:15], s22 offen sc1\n"
"s_waitcnt vmcnt(13)\n"
"v_mfma_f32_16x16x32_bf16 v[80:83], v[28:31], a[200:203], v[80:83]\n"
"ds_read_b64_tr_b16 v[24:25], v7 offset:23552\n"
"v_mfma_f32_16x16x32_bf16 v[84:87], v[28:31], a[204:207], v[84:87]\n"
"ds_read_b64_tr_b16 v[26:27], v7 offset:24064\n"
"s_waitcnt lgkmcnt(12)\n"
"v_mfma_f32_16x16x32_bf16 v[88:91], v[32:35], a[200:203], v[88:91]\n"
"ds_read_b64_tr_b16 v[28:29], v7 offset:24576\n"
"v_mfma_f32_16x16x32_bf16 v[92:95], v[32:35], a[204:207], v[92:95]\n"
"ds_read_b64_tr_b16 v[30:31], v7 offset:25088\n"
"v_mfma_f32_16x16x32_bf16 v[96:99], v[36:39], a[200:203], v[96:99]\n"
"ds_read_b64_tr_b16 v[32:33], v7 offset:25600\n"
"buffer_load_dwordx4 a[228:231], v3, s[12:15], s22 offen sc1\n"
"v_mfma_f32_16x16x32_bf16 v[100:103], v[36:39], a[204:207], v[100:103]\n"
"ds_read_b64_tr_b16 v[34:35], v7 offset:26112\n"
"s_waitcnt lgkmcnt(12)\n"
"v_mfma_f32_16x16x32_bf16 v[104:107], v[40:43], a[200:203], v[104:107]\n"
"ds_read_b64_tr_b16 v[36:37], v7 offset:26624\n"
"v_mfma_f32_16x16x32_bf16 v[108:111], v[40:43], a[204:207], v[108:111]\n"
"ds_read_b64_tr_b16 v[38:39], v7 offset:27136\n"
"v_mfma_f32_16x16x32_bf16 v[112:115], v[44:47], a[200:203], v[112:115]\n"
"ds_read_b64_tr_b16 v[40:41], v7 offset:27648\n"
"v_mfma_f32_16x16x32_bf16 v[116:119], v[44:47], a[204:207], v[116:119]\n"
"ds_read_b64_tr_b16 v[42:43], v7 offset:28160\n"
"buffer_load_dwordx4 a[232:235], v2, s[12:15], s22 offen offset:1024 sc1\n"
"s_waitcnt lgkmcnt(12)\n"
"v_mfma_f32_16x16x32_bf16 v[120:123], v[12:15], a[200:203], v[120:123]\n"
"ds_read_b64_tr_b16 v[44:45], v7 offset:28672\n"
"v_mfma_f32_16x16x32_bf16 v[124:127], v[12:15], a[204:207], v[124:127]\n"
"ds_read_b64_tr_b16 v[46:47], v7 offset:29184\n"
"v_mfma_f32_16x16x32_bf16 v[128:131], v[16:19], a[200:203], v[128:131]\n"
"ds_read_b64_tr_b16 v[12:13], v7 offset:29696\n"
"v_mfma_f32_16x16x32_bf16 v[132:135], v[16:19], a[204:207], v[132:135]\n"
"ds_read_b64_tr_b16 v[14:15], v7 offset:30208\n"
"s_waitcnt vmcnt(9)\n"
"s_waitcnt lgkmcnt(12)\n"
"v_mfma_f32_16x16x32_bf16 v[80:83], v[20:23], a[208:211], v[80:83]\n"
"ds_read_b64_tr_b16 v[16:17], v7 offset:30720\n"
"buffer_load_dwordx4 a[236:239], v3, s[12:15], s22 offen offset:1024 sc1\n"
"v_mfma_f32_16x16x32_bf16 v[84:87], v[20:23], a[212:215], v[84:87]\n"
"ds_read_b64_tr_b16 v[18:19], v7 offset:31232\n"
"v_mfma_f32_16x16x32_bf16 v[88:91], v[24:27], a[208:211], v[88:91]\n"
"ds_read_b64_tr_b16 v[20:21], v7 offset:31744\n"
"v_mfma_f32_16x16x32_bf16 v[92:95], v[24:27], a[212:215], v[92:95]\n"
"ds_read_b64_tr_b16 v[22:23], v7 offset:32256\n"
"s_barrier\n"
"s_waitcnt lgkmcnt(12)\n"
"v_mfma_f32_16x16x32_bf16 v[96:99], v[28:31], a[208:211], v[96:99]\n"
"ds_read_b64_tr_b16 v[24:25], v7 offset:32768\n"
"v_mfma_f32_16x16x32_bf16 v[100:103], v[28:31], a[212:215], v[100:103]\n"
"ds_read_b64_tr_b16 v[26:27], v7 offset:33280\n"
"s_add_u32 m0, s20, 16384\n"
"s_mov_b32 s21, 262144\n"
"buffer_load_dwordx4 v1, s[8:11], s21 offen sc1 lds\n"
"v_mfma_f32_16x16x32_bf16 v[104:107], v[32:35], a[208:211], v[104:107]\n"
"ds_read_b64_tr_b16 v[28:29], v7 offset:33792\n"
"v_mfma_f32_16x16x32_bf16 v[108:111], v[32:35], a[212:215], v[108:111]\n"
"ds_read_b64_tr_b16 v[30:31], v7 offset:34304\n"
"s_waitcnt lgkmcnt(12)\n"
"v_mfma_f32_16x16x32_bf16 v[112:115], v[36:39], a[208:211], v[112:115]\n"
"ds_read_b64_tr_b16 v[32:33], v7 offset:34816\n"
"v_mfma_f32_16x16x32_bf16 v[116:119], v[36:39], a[212:215], v[116:119]\n"
"ds_read_b64_tr_b16 v[34:35], v7 offset:35328\n"
"v_mfma_f32_16x16x32_bf16 v[120:123], v[40:43], a[208:211], v[120:123]\n"
"ds_read_b64_tr_b16 v[36:37], v7 offset:35840\n"
"buffer_load_dwordx4 v1, s[8:11], s21 offen offset:1024 sc1 lds\n"
"v_mfma_f32_16x16x32_bf16 v[124:127], v[40:43], a[212:215], v[124:127]\n"
"ds_read_b64_tr_b16 v[38:39], v7 offset:36352\n"
"s_waitcnt lgkmcnt(12)\n"
"v_mfma_f32_16x16x32_bf16 v[128:131], v[44:47], a[208:211], v[128:131]\n"
"ds_read_b64_tr_b16 v[40:41], v7 offset:36864\n"
"v_mfma_f32_16x16x32_bf16 v[132:135], v[44:47], a[212:215], v[132:135]\n"
"ds_read_b64_tr_b16 v[42:43], v7 offset:37376\n"
"s_waitcnt vmcnt(10)\n"
"v_mfma_f32_16x16x32_bf16 v[80:83], v[12:15], a[216:219], v[80:83]\n"
"ds_read_b64_tr_b16 v[44:45], v7 offset:37888\n"
"v_mfma_f32_16x16x32_bf16 v[84:87], v[12:15], a[220:223], v[84:87]\n"
"ds_read_b64_tr_b16 v[46:47], v7 offset:38400\n"
"buffer_load_dwordx4 v1, s[8:11], s21 offen offset:2048 sc1 lds\n"
"s_waitcnt lgkmcnt(12)\n"
"v_mfma_f32_16x16x32_bf16 v[88:91], v[16:19], a[216:219], v[88:91]\n"
"ds_read_b64_tr_b16 v[12:13], v7 offset:38912\n"
"v_mfma_f32_16x16x32_bf16 v[92:95], v[16:19], a[220:223], v[92:95]\n"
"ds_read_b64_tr_b16 v[14:15], v7 offset:39424\n"
"v_mfma_f32_16x16x32_bf16 v[96:99], v[20:23], a[216:219], v[96:99]\n"
"ds_read_b64_tr_b16 v[16:17], v7 offset:39936\n"
"v_mfma_f32_16x16x32_bf16 v[100:103], v[20:23], a[220:223], v[100:103]\n"
"ds_read_b64_tr_b16 v[18:19], v7 offset:40448\n"
"s_waitcnt lgkmcnt(12)\n"
"v_mfma_f32_16x16x32_bf16 v[104:107], v[24:27], a[216:219], v[104:107]\n"
"ds_read_b64_tr_b16 v[20:21], v7 offset:40960\n"
"buffer_load_dwordx4 v1, s[8:11], s21 offen offset:3072 sc1 lds\n"
"v_mfma_f32_16x16x32_bf16 v[108:111], v[24:27], a[220:223], v[108:111]\n"
"ds_read_b64_tr_b16 v[22:23], v7 offset:41472\n"
"v_mfma_f32_16x16x32_bf16 v[112:115], v[28:31], a[216:219], v[112:115]\n"
"ds_read_b64_tr_b16 v[24:25], v7 offset:41984\n"
"v_mfma_f32_16x16x32_bf16 v[116:119], v[28:31], a[220:223], v[116:119]\n"
"ds_read_b64_tr_b16 v[26:27], v7 offset:42496\n"
"s_waitcnt lgkmcnt(12)\n"
"v_mfma_f32_16x16x32_bf16 v[120:123], v[32:35], a[216:219], v[120:123]\n"
"ds_read_b64_tr_b16 v[28:29], v7 offset:43008\n"
"v_mfma_f32_16x16x32_bf16 v[124:127], v[32:35], a[220:223], v[124:127]\n"
"ds_read_b64_tr_b16 v[30:31], v7 offset:43520\n"
"buffer_load_dwordx4 a[240:243], v2, s[12:15], s22 offen offset:2048 sc1\n"
"v_mfma_f32_16x16x32_bf16 v[128:131], v[36:39], a[216:219], v[128:131]\n"
"ds_read_b64_tr_b16 v[32:33], v7 offset:44032\n"
"v_mfma_f32_16x16x32_bf16 v[132:135], v[36:39], a[220:223], v[132:135]\n"
"ds_read_b64_tr_b16 v[34:35], v7 offset:44544\n"
"s_waitcnt vmcnt(7)\n"
"s_waitcnt lgkmcnt(12)\n"
"v_mfma_f32_16x16x32_bf16 v[80:83], v[40:43], a[224:227], v[80:83]\n"
"ds_read_b64_tr_b16 v[36:37], v7 offset:45056\n"
"v_mfma_f32_16x16x32_bf16 v[84:87], v[40:43], a[228:231], v[84:87]\n"
"ds_read_b64_tr_b16 v[38:39], v7 offset:45568\n"
"v_mfma_f32_16x16x32_bf16 v[88:91], v[44:47], a[224:227], v[88:91]\n"
"ds_read_b64_tr_b16 v[40:41], v7 offset:46080\n"
"buffer_load_dwordx4 a[244:247], v3, s[12:15], s22 offen offset:2048 sc1\n"
"v_mfma_f32_16x16x32_bf16 v[92:95], v[44:47], a[228:231], v[92:95]\n"
"ds_read_b64_tr_b16 v[42:43], v7 offset:46592\n"
"s_waitcnt lgkmcnt(12)\n"
"v_mfma_f32_16x16x32_bf16 v[96:99], v[12:15], a[224:227], v[96:99]\n"
"ds_read_b64_tr_b16 v[44:45], v7 offset:47104\n"
"v_mfma_f32_16x16x32_bf16 v[100:103], v[12:15], a[228:231], v[100:103]\n"
"ds_read_b64_tr_b16 v[46:47], v7 offset:47616\n"
"v_mfma_f32_16x16x32_bf16 v[104:107], v[16:19], a[224:227], v[104:107]\n"
"ds_read_b64_tr_b16 v[12:13], v7 offset:48128\n"
"v_mfma_f32_16x16x32_bf16 v[108:111], v[16:19], a[228:231], v[108:111]\n"
"ds_read_b64_tr_b16 v[14:15], v7 offset:48640\n"
"buffer_load_dwordx4 a[248:251], v2, s[12:15], s22 offen offset:3072 sc1\n"
"s_barrier\n"
"s_waitcnt lgkmcnt(12)\n"
"v_mfma_f32_16x16x32_bf16 v[112:115], v[20:23], a[224:227], v[112:115]\n"
"ds_read_b64_tr_b16 v[16:17], v7 offset:49152\n"
"v_mfma_f32_16x16x32_bf16 v[116:119], v[20:23], a[228:231], v[116:119]\n"
"ds_read_b64_tr_b16 v[18:19], v7 offset:49664\n"
"v_mfma_f32_16x16x32_bf16 v[120:123], v[24:27], a[224:227], v[120:123]\n"
"ds_read_b64_tr_b16 v[20:21], v7 offset:50176\n"
"v_mfma_f32_16x16x32_bf16 v[124:127], v[24:27], a[228:231], v[124:127]\n"
"ds_read_b64_tr_b16 v[22:23], v7 offset:50688\n"
"s_waitcnt lgkmcnt(12)\n"
"v_mfma_f32_16x16x32_bf16 v[128:131], v[28:31], a[224:227], v[128:131]\n"
"ds_read_b64_tr_b16 v[24:25], v7 offset:51200\n"
"buffer_load_dwordx4 a[252:255], v3, s[12:15], s22 offen offset:3072 sc1\n"
"v_mfma_f32_16x16x32_bf16 v[132:135], v[28:31], a[228:231], v[132:135]\n"
"ds_read_b64_tr_b16 v[26:27], v7 offset:51712\n"
"s_waitcnt vmcnt(8)\n"
"v_mfma_f32_16x16x32_bf16 v[80:83], v[32:35], a[232:235], v[80:83]\n"
"ds_read_b64_tr_b16 v[28:29], v7 offset:52224\n"
"v_mfma_f32_16x16x32_bf16 v[84:87], v[32:35], a[236:239], v[84:87]\n"
"ds_read_b64_tr_b16 v[30:31], v7 offset:52736\n"
"s_waitcnt lgkmcnt(12)\n"
"v_mfma_f32_16x16x32_bf16 v[88:91], v[36:39], a[232:235], v[88:91]\n"
"ds_read_b64_tr_b16 v[32:33], v7 offset:53248\n"
"v_mfma_f32_16x16x32_bf16 v[92:95], v[36:39], a[236:239], v[92:95]\n"
"ds_read_b64_tr_b16 v[34:35], v7 offset:53760\n"
"s_mov_b32 s22, 32768\n"
"buffer_load_dwordx4 v[48:51], v2, s[12:15], s22 offen sc1\n"
"v_mfma_f32_16x16x32_bf16 v[96:99], v[40:43], a[232:235], v[96:99]\n"
"ds_read_b64_tr_b16 v[36:37], v7 offset:54272\n"
"v_mfma_f32_16x16x32_bf16 v[100:103], v[40:43], a[236:239], v[100:103]\n"
"ds_read_b64_tr_b16 v[38:39], v7 offset:54784\n"
"s_waitcnt lgkmcnt(12)\n"
"v_mfma_f32_16x16x32_bf16 v[104:107], v[44:47], a[232:235], v[104:107]\n"
"ds_read_b64_tr_b16 v[40:41], v7 offset:55296\n"
"v_mfma_f32_16x16x32_bf16 v[108:111], v[44:47], a[236:239], v[108:111]\n"
"ds_read_b64_tr_b16 v[42:43], v7 offset:55808\n"
"v_mfma_f32_16x16x32_bf16 v[112:115], v[12:15], a[232:235], v[112:115]\n"
"ds_read_b64_tr_b16 v[44:45], v7 offset:56320\n"
"buffer_load_dwordx4 v[52:55], v3, s[12:15], s22 offen sc1\n"
"v_mfma_f32_16x16x32_bf16 v[116:119], v[12:15], a[236:239], v[116:119]\n"
"ds_read_b64_tr_b16 v[46:47], v7 offset:56832\n"
"s_waitcnt lgkmcnt(12)\n"
"v_mfma_f32_16x16x32_bf16 v[120:123], v[16:19], a[232:235], v[120:123]\n"
"ds_read_b64_tr_b16 v[12:13], v7 offset:57344\n"
"v_mfma_f32_16x16x32_bf16 v[124:127], v[16:19], a[236:239], v[124:127]\n"
"ds_read_b64_tr_b16 v[14:15], v7 offset:57856\n"
"v_mfma_f32_16x16x32_bf16 v[128:131], v[20:23], a[232:235], v[128:131]\n"
"ds_read_b64_tr_b16 v[16:17], v7 offset:58368\n"
"v_mfma_f32_16x16x32_bf16 v[132:135], v[20:23], a[236:239], v[132:135]\n"
"ds_read_b64_tr_b16 v[18:19], v7 offset:58880\n"
"s_add_u32 m0, s20, 32768\n"
"s_mov_b32 s21, 278528\n"
"buffer_load_dwordx4 v1, s[8:11], s21 offen sc1 lds\n"
"s_waitcnt vmcnt(5)\n"
"s_waitcnt lgkmcnt(12)\n"
"v_mfma_f32_16x16x32_bf16 v[80:83], v[24:27], a[240:243], v[80:83]\n"
"ds_read_b64_tr_b16 v[20:21], v7 offset:59392\n"
"v_mfma_f32_16x16x32_bf16 v[84:87], v[24:27], a[244:247], v[84:87]\n"
"ds_read_b64_tr_b16 v[22:23], v7 offset:59904\n"
"v_mfma_f32_16x16x32_bf16 v[88:91], v[28:31], a[240:243], v[88:91]\n"
"ds_read_b64_tr_b16 v[24:25], v7 offset:60416\n"
"v_mfma_f32_16x16x32_bf16 v[92:95], v[28:31], a[244:247], v[92:95]\n"
"ds_read_b64_tr_b16 v[26:27], v7 offset:60928\n"
"s_waitcnt lgkmcnt(12)\n"
"v_mfma_f32_16x16x32_bf16 v[96:99], v[32:35], a[240:243], v[96:99]\n"
"ds_read_b64_tr_b16 v[28:29], v7 offset:61440\n"
"buffer_load_dwordx4 v1, s[8:11], s21 offen offset:1024 sc1 lds\n"
"v_mfma_f32_16x16x32_bf16 v[100:103], v[32:35], a[244:247], v[100:103]\n"
"ds_read_b64_tr_b16 v[30:31], v7 offset:61952\n"
"v_mfma_f32_16x16x32_bf16 v[104:107], v[36:39], a[240:243], v[104:107]\n"
"ds_read_b64_tr_b16 v[32:33], v7 offset:62464\n"
"v_mfma_f32_16x16x32_bf16 v[108:111], v[36:39], a[244:247], v[108:111]\n"
"ds_read_b64_tr_b16 v[34:35], v7 offset:62976\n"
"s_waitcnt lgkmcnt(12)\n"
"v_mfma_f32_16x16x32_bf16 v[112:115], v[40:43], a[240:243], v[112:115]\n"
"ds_read_b64_tr_b16 v[36:37], v7 offset:63488\n"
"v_mfma_f32_16x16x32_bf16 v[116:119], v[40:43], a[244:247], v[116:119]\n"
"ds_read_b64_tr_b16 v[38:39], v7 offset:64000\n"
"buffer_load_dwordx4 v1, s[8:11], s21 offen offset:2048 sc1 lds\n"
"v_mfma_f32_16x16x32_bf16 v[120:123], v[44:47], a[240:243], v[120:123]\n"
"ds_read_b64_tr_b16 v[40:41], v7 offset:64512\n"
"v_mfma_f32_16x16x32_bf16 v[124:127], v[44:47], a[244:247], v[124:127]\n"
"ds_read_b64_tr_b16 v[42:43], v7 offset:65024\n"
"s_barrier\n"
"s_waitcnt lgkmcnt(12)\n"
"v_mfma_f32_16x16x32_bf16 v[128:131], v[12:15], a[240:243], v[128:131]\n"
"ds_read_b64_tr_b16 v[44:45], v8 offset:0\n"
"v_mfma_f32_16x16x32_bf16 v[132:135], v[12:15], a[244:247], v[132:135]\n"
"ds_read_b64_tr_b16 v[46:47], v8 offset:512\n"
"s_waitcnt vmcnt(5)\n"
"v_mfma_f32_16x16x32_bf16 v[80:83], v[16:19], a[248:251], v[80:83]\n"
"ds_read_b64_tr_b16 v[12:13], v8 offset:1024\n"
"buffer_load_dwordx4 v1, s[8:11], s21 offen offset:3072 sc1 lds\n"
"v_mfma_f32_16x16x32_bf16 v[84:87], v[16:19], a[252:255], v[84:87]\n"
"ds_read_b64_tr_b16 v[14:15], v8 offset:1536\n"
"s_waitcnt lgkmcnt(12)\n"
"v_mfma_f32_16x16x32_bf16 v[88:91], v[20:23], a[248:251], v[88:91]\n"
"ds_read_b64_tr_b16 v[16:17], v8 offset:2048\n"
"v_mfma_f32_16x16x32_bf16 v[92:95], v[20:23], a[252:255], v[92:95]\n"
"ds_read_b64_tr_b16 v[18:19], v8 offset:2560\n"
"v_mfma_f32_16x16x32_bf16 v[96:99], v[24:27], a[248:251], v[96:99]\n"
"ds_read_b64_tr_b16 v[20:21], v8 offset:3072\n"
"v_mfma_f32_16x16x32_bf16 v[100:103], v[24:27], a[252:255], v[100:103]\n"
"ds_read_b64_tr_b16 v[22:23], v8 offset:3584\n"
"buffer_load_dwordx4 v[56:59], v2, s[12:15], s22 offen offset:1024 sc1\n"
"s_waitcnt lgkmcnt(12)\n"
"v_mfma_f32_16x16x32_bf16 v[104:107], v[28:31], a[248:251], v[104:107]\n"
"ds_read_b64_tr_b16 v[24:25], v8 offset:4096\n"
"v_mfma_f32_16x16x32_bf16 v[108:111], v[28:31], a[252:255], v[108:111]\n"
"ds_read_b64_tr_b16 v[26:27], v8 offset:4608\n"
"v_mfma_f32_16x16x32_bf16 v[112:115], v[32:35], a[248:251], v[112:115]\n"
"ds_read_b64_tr_b16 v[28:29], v8 offset:5120\n"
"v_mfma_f32_16x16x32_bf16 v[116:119], v[32:35], a[252:255], v[116:119]\n"
"ds_read_b64_tr_b16 v[30:31], v8 offset:5632\n"
"s_waitcnt lgkmcnt(12)\n"
"v_mfma_f32_16x16x32_bf16 v[120:123], v[36:39], a[248:251], v[120:123]\n"
"ds_read_b64_tr_b16 v[32:33], v8 offset:6144\n"
"buffer_load_dwordx4 v[60:63], v3, s[12:15], s22 offen offset:1024 sc1\n"
"v_mfma_f32_16x16x32_bf16 v[124:127], v[36:39], a[252:255], v[124:127]\n"
"ds_read_b64_tr_b16 v[34:35], v8 offset:6656\n"
"v_mfma_f32_16x16x32_bf16 v[128:131], v[40:43], a[248:251], v[128:131]\n"
"ds_read_b64_tr_b16 v[36:37], v8 offset:7168\n"
"v_mfma_f32_16x16x32_bf16 v[132:135], v[40:43], a[252:255], v[132:135]\n"
"ds_read_b64_tr_b16 v[38:39], v8 offset:7680\n"
"s_waitcnt vmcnt(6)\n"
"s_waitcnt lgkmcnt(12)\n"
"v_mfma_f32_16x16x32_bf16 v[80:83], v[44:47], v[48:51], v[80:83]\n"
"ds_read_b64_tr_b16 v[40:41], v8 offset:8192\n"
"v_mfma_f32_16x16x32_bf16 v[84:87], v[44:47], v[52:55], v[84:87]\n"
"ds_read_b64_tr_b16 v[42:43], v8 offset:8704\n"
"buffer_load_dwordx4 v[64:67], v2, s[12:15], s22 offen offset:2048 sc1\n"
"v_mfma_f32_16x16x32_bf16 v[88:91], v[12:15], v[48:51], v[88:91]\n"
"ds_read_b64_tr_b16 v[44:45], v8 offset:9216\n"
"v_mfma_f32_16x16x32_bf16 v[92:95], v[12:15], v[52:55], v[92:95]\n"
"ds_read_b64_tr_b16 v[46:47], v8 offset:9728\n"
"s_waitcnt lgkmcnt(12)\n"
"v_mfma_f32_16x16x32_bf16 v[96:99], v[16:19], v[48:51], v[96:99]\n"
"ds_read_b64_tr_b16 v[12:13], v8 offset:10240\n"
"v_mfma_f32_16x16x32_bf16 v[100:103], v[16:19], v[52:55], v[100:103]\n"
"ds_read_b64_tr_b16 v[14:15], v8 offset:10752\n"
"v_mfma_f32_16x16x32_bf16 v[104:107], v[20:23], v[48:51], v[104:107]\n"
"ds_read_b64_tr_b16 v[16:17], v8 offset:11264\n"
"buffer_load_dwordx4 v[68:71], v3, s[12:15], s22 offen offset:2048 sc1\n"
"v_mfma_f32_16x16x32_bf16 v[108:111], v[20:23], v[52:55], v[108:111]\n"
"ds_read_b64_tr_b16 v[18:19], v8 offset:11776\n"
"s_waitcnt lgkmcnt(12)\n"
"v_mfma_f32_16x16x32_bf16 v[112:115], v[24:27], v[48:51], v[112:115]\n"
"ds_read_b64_tr_b16 v[20:21], v8 offset:12288\n"
"v_mfma_f32_16x16x32_bf16 v[116:119], v[24:27], v[52:55], v[116:119]\n"
"ds_read_b64_tr_b16 v[22:23], v8 offset:12800\n"
"v_mfma_f32_16x16x32_bf16 v[120:123], v[28:31], v[48:51], v[120:123]\n"
"ds_read_b64_tr_b16 v[24:25], v8 offset:13312\n"
"v_mfma_f32_16x16x32_bf16 v[124:127], v[28:31], v[52:55], v[124:127]\n"
"ds_read_b64_tr_b16 v[26:27], v8 offset:13824\n"
"s_add_u32 m0, s20, 49152\n"
"s_mov_b32 s21, 294912\n"
"buffer_load_dwordx4 v1, s[8:11], s21 offen sc1 lds\n"
"s_waitcnt lgkmcnt(12)\n"
"v_mfma_f32_16x16x32_bf16 v[128:131], v[32:35], v[48:51], v[128:131]\n"
"ds_read_b64_tr_b16 v[28:29], v8 offset:14336\n"
"v_mfma_f32_16x16x32_bf16 v[132:135], v[32:35], v[52:55], v[132:135]\n"
"ds_read_b64_tr_b16 v[30:31], v8 offset:14848\n"
"s_waitcnt vmcnt(3)\n"
"v_mfma_f32_16x16x32_bf16 v[80:83], v[36:39], v[56:59], v[80:83]\n"
"ds_read_b64_tr_b16 v[32:33], v8 offset:15360\n"
"v_mfma_f32_16x16x32_bf16 v[84:87], v[36:39], v[60:63], v[84:87]\n"
"ds_read_b64_tr_b16 v[34:35], v8 offset:15872\n"
"s_barrier\n"
"s_waitcnt lgkmcnt(12)\n"
"v_mfma_f32_16x16x32_bf16 v[88:91], v[40:43], v[56:59], v[88:91]\n"
"ds_read_b64_tr_b16 v[36:37], v6 offset:0\n"
"buffer_load_dwordx4 v1, s[8:11], s21 offen offset:1024 sc1 lds\n"
"v_mfma_f32_16x16x32_bf16 v[92:95], v[40:43], v[60:63], v[92:95]\n"
"ds_read_b64_tr_b16 v[38:39], v6 offset:512\n"
"v_mfma_f32_16x16x32_bf16 v[96:99], v[44:47], v[56:59], v[96:99]\n"
"ds_read_b64_tr_b16 v[40:41], v6 offset:1024\n"
"v_mfma_f32_16x16x32_bf16 v[100:103], v[44:47], v[60:63], v[100:103]\n"
"ds_read_b64_tr_b16 v[42:43], v6 offset:1536\n"
"s_waitcnt lgkmcnt(12)\n"
"v_mfma_f32_16x16x32_bf16 v[104:107], v[12:15], v[56:59], v[104:107]\n"
"ds_read_b64_tr_b16 v[44:45], v6 offset:2048\n"
"v_mfma_f32_16x16x32_bf16 v[108:111], v[12:15], v[60:63], v[108:111]\n"
"ds_read_b64_tr_b16 v[46:47], v6 offset:2560\n"
"buffer_load_dwordx4 v1, s[8:11], s21 offen offset:2048 sc1 lds\n"
"v_mfma_f32_16x16x32_bf16 v[112:115], v[16:19], v[56:59], v[112:115]\n"
"ds_read_b64_tr_b16 v[12:13], v6 offset:3072\n"
"v_mfma_f32_16x16x32_bf16 v[116:119], v[16:19], v[60:63], v[116:119]\n"
"ds_read_b64_tr_b16 v[14:15], v6 offset:3584\n"
"s_waitcnt lgkmcnt(12)\n"
"v_mfma_f32_16x16x32_bf16 v[120:123], v[20:23], v[56:59], v[120:123]\n"
"ds_read_b64_tr_b16 v[16:17], v6 offset:4096\n"
"v_mfma_f32_16x16x32_bf16 v[124:127], v[20:23], v[60:63], v[124:127]\n"
"ds_read_b64_tr_b16 v[18:19], v6 offset:4608\n"
"v_mfma_f32_16x16x32_bf16 v[128:131], v[24:27], v[56:59], v[128:131]\n"
"ds_read_b64_tr_b16 v[20:21], v6 offset:5120\n"
"buffer_load_dwordx4 v1, s[8:11], s21 offen offset:3072 sc1 lds\n"
"v_mfma_f32_16x16x32_bf16 v[132:135], v[24:27], v[60:63], v[132:135]\n"
"ds_read_b64_tr_b16 v[22:23], v6 offset:5632\n"
"s_waitcnt vmcnt(4)\n"
"s_waitcnt lgkmcnt(12)\n"
"v_mfma_f32_16x16x32_bf16 v[80:83], v[28:31], v[64:67], v[80:83]\n"
"ds_read_b64_tr_b16 v[24:25], v6 offset:6144\n"
"v_mfma_f32_16x16x32_bf16 v[84:87], v[28:31], v[68:71], v[84:87]\n"
"ds_read_b64_tr_b16 v[26:27], v6 offset:6656\n"
"v_mfma_f32_16x16x32_bf16 v[88:91], v[32:35], v[64:67], v[88:91]\n"
"ds_read_b64_tr_b16 v[28:29], v6 offset:7168\n"
"v_mfma_f32_16x16x32_bf16 v[92:95], v[32:35], v[68:71], v[92:95]\n"
"ds_read_b64_tr_b16 v[30:31], v6 offset:7680\n"
"buffer_load_dwordx4 v[72:75], v2, s[12:15], s22 offen offset:3072 sc1\n"
"s_waitcnt lgkmcnt(12)\n"
"v_mfma_f32_16x16x32_bf16 v[96:99], v[36:39], v[64:67], v[96:99]\n"
"ds_read_b64_tr_b16 v[32:33], v6 offset:8192\n"
"v_mfma_f32_16x16x32_bf16 v[100:103], v[36:39], v[68:71], v[100:103]\n"
"ds_read_b64_tr_b16 v[34:35], v6 offset:8704\n"
"v_mfma_f32_16x16x32_bf16 v[104:107], v[40:43], v[64:67], v[104:107]\n"
"ds_read_b64_tr_b16 v[36:37], v6 offset:9216\n"
"v_mfma_f32_16x16x32_bf16 v[108:111], v[40:43], v[68:71], v[108:111]\n"
"ds_read_b64_tr_b16 v[38:39], v6 offset:9728\n"
"s_waitcnt lgkmcnt(12)\n"
"v_mfma_f32_16x16x32_bf16 v[112:115], v[44:47], v[64:67], v[112:115]\n"
"ds_read_b64_tr_b16 v[40:41], v6 offset:10240\n"
"buffer_load_dwordx4 v[76:79], v3, s[12:15], s22 offen offset:3072 sc1\n"
"v_mfma_f32_16x16x32_bf16 v[116:119], v[44:47], v[68:71], v[116:119]\n"
"ds_read_b64_tr_b16 v[42:43], v6 offset:10752\n"
"v_mfma_f32_16x16x32_bf16 v[120:123], v[12:15], v[64:67], v[120:123]\n"
"ds_read_b64_tr_b16 v[44:45], v6 offset:11264\n"
"v_mfma_f32_16x16x32_bf16 v[124:127], v[12:15], v[68:71], v[124:127]\n"
"ds_read_b64_tr_b16 v[46:47], v6 offset:11776\n"
"s_waitcnt lgkmcnt(12)\n"
"v_mfma_f32_16x16x32_bf16 v[128:131], v[16:19], v[64:67], v[128:131]\n"
"ds_read_b64_tr_b16 v[12:13], v6 offset:12288\n"
"v_mfma_f32_16x16x32_bf16 v[132:135], v[16:19], v[68:71], v[132:135]\n"
"ds_read_b64_tr_b16 v[14:15], v6 offset:12800\n"
"s_add_u32 m0, s20, 65536\n"
"s_mov_b32 s21, 311296\n"
"buffer_load_dwordx4 v1, s[8:11], s21 offen sc1 lds\n"
"s_waitcnt vmcnt(1)\n"
"v_mfma_f32_16x16x32_bf16 v[80:83], v[20:23], v[72:75], v[80:83]\n"
"ds_read_b64_tr_b16 v[16:17], v6 offset:13312\n"
"v_mfma_f32_16x16x32_bf16 v[84:87], v[20:23], v[76:79], v[84:87]\n"
"ds_read_b64_tr_b16 v[18:19], v6 offset:13824\n"
"s_waitcnt lgkmcnt(12)\n"
"v_mfma_f32_16x16x32_bf16 v[88:91], v[24:27], v[72:75], v[88:91]\n"
"ds_read_b64_tr_b16 v[20:21], v6 offset:14336\n"
"v_mfma_f32_16x16x32_bf16 v[92:95], v[24:27], v[76:79], v[92:95]\n"
"ds_read_b64_tr_b16 v[22:23], v6 offset:14848\n"
"v_mfma_f32_16x16x32_bf16 v[96:99], v[28:31], v[72:75], v[96:99]\n"
"ds_read_b64_tr_b16 v[24:25], v6 offset:15360\n"
"buffer_load_dwordx4 v1, s[8:11], s21 offen offset:1024 sc1 lds\n"
"v_mfma_f32_16x16x32_bf16 v[100:103], v[28:31], v[76:79], v[100:103]\n"
"ds_read_b64_tr_b16 v[26:27], v6 offset:15872\n"
"s_barrier\n"
"s_waitcnt lgkmcnt(12)\n"
"v_mfma_f32_16x16x32_bf16 v[104:107], v[32:35], v[72:75], v[104:107]\n"
"ds_read_b64_tr_b16 v[28:29], v6 offset:16384\n"
"v_mfma_f32_16x16x32_bf16 v[108:111], v[32:35], v[76:79], v[108:111]\n"
"ds_read_b64_tr_b16 v[30:31], v6 offset:16896\n"
"v_cvt_pk_bf16_f32 v80, v80, v81\n"
"v_mfma_f32_16x16x32_bf16 v[112:115], v[36:39], v[72:75], v[112:115]\n"
"ds_read_b64_tr_b16 v[32:33], v6 offset:17408\n"
"v_mfma_f32_16x16x32_bf16 v[116:119], v[36:39], v[76:79], v[116:119]\n"
"ds_read_b64_tr_b16 v[34:35], v6 offset:17920\n"
"buffer_load_dwordx4 v1, s[8:11], s21 offen offset:2048 sc1 lds\n"
"s_waitcnt lgkmcnt(12)\n"
"v_mfma_f32_16x16x32_bf16 v[120:123], v[40:43], v[72:75], v[120:123]\n"
"ds_read_b64_tr_b16 v[36:37], v6 offset:18432\n"
"v_mfma_f32_16x16x32_bf16 v[124:127], v[40:43], v[76:79], v[124:127]\n"
"ds_read_b64_tr_b16 v[38:39], v6 offset:18944\n"
"v_cvt_pk_bf16_f32 v81, v82, v83\n"
"v_mfma_f32_16x16x32_bf16 v[128:131], v[44:47], v[72:75], v[128:131]\n"
"ds_read_b64_tr_b16 v[40:41], v6 offset:19456\n"
"v_mfma_f32_16x16x32_bf16 v[132:135], v[44:47], v[76:79], v[132:135]\n"
"ds_read_b64_tr_b16 v[42:43], v6 offset:19968\n"
"v_cvt_pk_bf16_f32 v82, v84, v85\n"
"s_waitcnt lgkmcnt(12)\n"
"v_mfma_f32_16x16x32_bf16 v[136:139], v[12:15], a[0:3], 0\n"
"ds_read_b64_tr_b16 v[44:45], v6 offset:20480\n"
"buffer_load_dwordx4 v1, s[8:11], s21 offen offset:3072 sc1 lds\n"
"v_mfma_f32_16x16x32_bf16 v[140:143], v[12:15], a[4:7], 0\n"
"ds_read_b64_tr_b16 v[46:47], v6 offset:20992\n"
"v_cvt_pk_bf16_f32 v83, v86, v87\n"
"v_mfma_f32_16x16x32_bf16 v[136:139], v[16:19], a[8:11], v[136:139]\n"
"ds_read_b64_tr_b16 v[12:13], v6 offset:21504\n"
"v_mfma_f32_16x16x32_bf16 v[140:143], v[16:19], a[12:15], v[140:143]\n"
"ds_read_b64_tr_b16 v[14:15], v6 offset:22016\n"
"buffer_store_dwordx4 v[80:83], v9, s[16:19], 0 offen sc1\n"
"s_waitcnt lgkmcnt(12)\n"
"v_mfma_f32_16x16x32_bf16 v[136:139], v[20:23], a[16:19], v[136:139]\n"
"ds_read_b64_tr_b16 v[16:17], v6 offset:22528\n"
"v_mfma_f32_16x16x32_bf16 v[140:143], v[20:23], a[20:23], v[140:143]\n"
"ds_read_b64_tr_b16 v[18:19], v6 offset:23040\n"
"s_add_u32 m0, s20, 81920\n"
"s_mov_b32 s21, 327680\n"
"buffer_load_dwordx4 v1, s[8:11], s21 offen sc1 lds\n"
"v_mfma_f32_16x16x32_bf16 v[136:139], v[24:27], a[24:27], v[136:139]\n"
"ds_read_b64_tr_b16 v[20:21], v6 offset:23552\n"
"v_mfma_f32_16x16x32_bf16 v[140:143], v[24:27], a[28:31], v[140:143]\n"
"ds_read_b64_tr_b16 v[22:23], v6 offset:24064\n"
"v_cvt_pk_bf16_f32 v88, v88, v89\n"
"s_waitcnt lgkmcnt(12)\n"
"v_mfma_f32_16x16x32_bf16 v[136:139], v[28:31], a[32:35], v[136:139]\n"
"ds_read_b64_tr_b16 v[24:25], v6 offset:24576\n"
"v_mfma_f32_16x16x32_bf16 v[140:143], v[28:31], a[36:39], v[140:143]\n"
"ds_read_b64_tr_b16 v[26:27], v6 offset:25088\n"
"v_cvt_pk_bf16_f32 v89, v90, v91\n"
"v_mfma_f32_16x16x32_bf16 v[136:139], v[32:35], a[40:43], v[136:139]\n"
"ds_read_b64_tr_b16 v[28:29], v6 offset:25600\n"
"buffer_load_dwordx4 v1, s[8:11], s21 offen offset:1024 sc1 lds\n"
"v_mfma_f32_16x16x32_bf16 v[140:143], v[32:35], a[44:47], v[140:143]\n"
"ds_read_b64_tr_b16 v[30:31], v6 offset:26112\n"
"v_cvt_pk_bf16_f32 v90, v92, v93\n"
"s_waitcnt lgkmcnt(12)\n"
"v_mfma_f32_16x16x32_bf16 v[136:139], v[36:39], a[48:51], v[136:139]\n"
"ds_read_b64_tr_b16 v[32:33], v6 offset:26624\n"
"v_mfma_f32_16x16x32_bf16 v[140:143], v[36:39], a[52:55], v[140:143]\n"
"ds_read_b64_tr_b16 v[34:35], v6 offset:27136\n"
"v_cvt_pk_bf16_f32 v91, v94, v95\n"
"v_mfma_f32_16x16x32_bf16 v[136:139], v[40:43], a[56:59], v[136:139]\n"
"ds_read_b64_tr_b16 v[36:37], v6 offset:27648\n"
"v_mfma_f32_16x16x32_bf16 v[140:143], v[40:43], a[60:63], v[140:143]\n"
"ds_read_b64_tr_b16 v[38:39], v6 offset:28160\n"
"buffer_load_dwordx4 v1, s[8:11], s21 offen offset:2048 sc1 lds\n"
"s_waitcnt lgkmcnt(12)\n"
"v_mfma_f32_16x16x32_bf16 v[136:139], v[44:47], a[64:67], v[136:139]\n"
"ds_read_b64_tr_b16 v[40:41], v6 offset:28672\n"
"v_mfma_f32_16x16x32_bf16 v[140:143], v[44:47], a[68:71], v[140:143]\n"
"ds_read_b64_tr_b16 v[42:43], v6 offset:29184\n"
"v_mfma_f32_16x16x32_bf16 v[136:139], v[12:15], a[72:75], v[136:139]\n"
"ds_read_b64_tr_b16 v[44:45], v6 offset:29696\n"
"v_mfma_f32_16x16x32_bf16 v[140:143], v[12:15], a[76:79], v[140:143]\n"
"ds_read_b64_tr_b16 v[46:47], v6 offset:30208\n"
"v_cvt_pk_bf16_f32 v96, v96, v97\n"
"buffer_store_dwordx4 v[88:91], v9, s[16:19], 0 offen offset:1024 sc1\n"
"s_waitcnt lgkmcnt(12)\n"
"v_mfma_f32_16x16x32_bf16 v[136:139], v[16:19], a[80:83], v[136:139]\n"
"ds_read_b64_tr_b16 v[12:13], v6 offset:30720\n"
"buffer_load_dwordx4 v1, s[8:11], s21 offen offset:3072 sc1 lds\n"
"v_mfma_f32_16x16x32_bf16 v[140:143], v[16:19], a[84:87], v[140:143]\n"
"ds_read_b64_tr_b16 v[14:15], v6 offset:31232\n"
"v_cvt_pk_bf16_f32 v97, v98, v99\n"
"v_mfma_f32_16x16x32_bf16 v[136:139], v[20:23], a[88:91], v[136:139]\n"
"ds_read_b64_tr_b16 v[16:17], v6 offset:31744\n"
"v_mfma_f32_16x16x32_bf16 v[140:143], v[20:23], a[92:95], v[140:143]\n"
"ds_read_b64_tr_b16 v[18:19], v6 offset:32256\n"
"v_cvt_pk_bf16_f32 v98, v100, v101\n"
"s_barrier\n"
"s_waitcnt lgkmcnt(12)\n"
"v_mfma_f32_16x16x32_bf16 v[136:139], v[24:27], a[96:99], v[136:139]\n"
"ds_read_b64_tr_b16 v[20:21], v6 offset:32768\n"
"v_mfma_f32_16x16x32_bf16 v[140:143], v[24:27], a[100:103], v[140:143]\n"
"ds_read_b64_tr_b16 v[22:23], v6 offset:33280\n"
"v_cvt_pk_bf16_f32 v99, v102, v103\n"
"v_mfma_f32_16x16x32_bf16 v[136:139], v[28:31], a[104:107], v[136:139]\n"
"ds_read_b64_tr_b16 v[24:25], v6 offset:33792\n"
"v_mfma_f32_16x16x32_bf16 v[140:143], v[28:31], a[108:111], v[140:143]\n"
"ds_read_b64_tr_b16 v[26:27], v6 offset:34304\n"
"s_waitcnt lgkmcnt(12)\n"
"v_mfma_f32_16x16x32_bf16 v[136:139], v[32:35], a[112:115], v[136:139]\n"
"ds_read_b64_tr_b16 v[28:29], v6 offset:34816\n"
"v_mfma_f32_16x16x32_bf16 v[140:143], v[32:35], a[116:119], v[140:143]\n"
"ds_read_b64_tr_b16 v[30:31], v6 offset:35328\n"
"v_cvt_pk_bf16_f32 v104, v104, v105\n"
"v_mfma_f32_16x16x32_bf16 v[136:139], v[36:39], a[120:123], v[136:139]\n"
"ds_read_b64_tr_b16 v[32:33], v6 offset:35840\n"
"v_mfma_f32_16x16x32_bf16 v[140:143], v[36:39], a[124:127], v[140:143]\n"
"ds_read_b64_tr_b16 v[34:35], v6 offset:36352\n"
"v_cvt_pk_bf16_f32 v105, v106, v107\n"
"s_waitcnt lgkmcnt(12)\n"
"v_mfma_f32_16x16x32_bf16 v[136:139], v[40:43], a[128:131], v[136:139]\n"
"ds_read_b64_tr_b16 v[36:37], v6 offset:36864\n"
"v_mfma_f32_16x16x32_bf16 v[140:143], v[40:43], a[132:135], v[140:143]\n"
"ds_read_b64_tr_b16 v[38:39], v6 offset:37376\n"
"v_cvt_pk_bf16_f32 v106, v108, v109\n"
"v_mfma_f32_16x16x32_bf16 v[136:139], v[44:47], a[136:139], v[136:139]\n"
"ds_read_b64_tr_b16 v[40:41], v6 offset:37888\n"
"v_mfma_f32_16x16x32_bf16 v[140:143], v[44:47], a[140:143], v[140:143]\n"
"ds_read_b64_tr_b16 v[42:43], v6 offset:38400\n"
"v_cvt_pk_bf16_f32 v107, v110, v111\n"
"buffer_store_dwordx4 v[96:99], v9, s[16:19], 0 offen offset:2048 sc1\n"
"s_waitcnt lgkmcnt(12)\n"
"v_mfma_f32_16x16x32_bf16 v[136:139], v[12:15], a[144:147], v[136:139]\n"
"ds_read_b64_tr_b16 v[44:45], v6 offset:38912\n"
"v_mfma_f32_16x16x32_bf16 v[140:143], v[12:15], a[148:151], v[140:143]\n"
"ds_read_b64_tr_b16 v[46:47], v6 offset:39424\n"
"v_mfma_f32_16x16x32_bf16 v[136:139], v[16:19], a[152:155], v[136:139]\n"
"ds_read_b64_tr_b16 v[12:13], v6 offset:39936\n"
"v_mfma_f32_16x16x32_bf16 v[140:143], v[16:19], a[156:159], v[140:143]\n"
"ds_read_b64_tr_b16 v[14:15], v6 offset:40448\n"
"v_cvt_pk_bf16_f32 v112, v112, v113\n"
"s_waitcnt lgkmcnt(12)\n"
"v_mfma_f32_16x16x32_bf16 v[136:139], v[20:23], a[160:163], v[136:139]\n"
"ds_read_b64_tr_b16 v[16:17], v6 offset:40960\n"
"v_mfma_f32_16x16x32_bf16 v[140:143], v[20:23], a[164:167], v[140:143]\n"
"ds_read_b64_tr_b16 v[18:19], v6 offset:41472\n"
"v_cvt_pk_bf16_f32 v113, v114, v115\n"
"v_mfma_f32_16x16x32_bf16 v[136:139], v[24:27], a[168:171], v[136:139]\n"
"ds_read_b64_tr_b16 v[20:21], v6 offset:41984\n"
"v_mfma_f32_16x16x32_bf16 v[140:143], v[24:27], a[172:175], v[140:143]\n"
"ds_read_b64_tr_b16 v[22:23], v6 offset:42496\n"
"v_cvt_pk_bf16_f32 v114, v116, v117\n"
"s_waitcnt lgkmcnt(12)\n"
"v_mfma_f32_16x16x32_bf16 v[136:139], v[28:31], a[176:179], v[136:139]\n"
"ds_read_b64_tr_b16 v[24:25], v6 offset:43008\n"
"v_mfma_f32_16x16x32_bf16 v[140:143], v[28:31], a[180:183], v[140:143]\n"
"ds_read_b64_tr_b16 v[26:27], v6 offset:43520\n"
"v_cvt_pk_bf16_f32 v115, v118, v119\n"
"v_mfma_f32_16x16x32_bf16 v[136:139], v[32:35], a[184:187], v[136:139]\n"
"ds_read_b64_tr_b16 v[28:29], v6 offset:44032\n"
"v_mfma_f32_16x16x32_bf16 v[140:143], v[32:35], a[188:191], v[140:143]\n"
"ds_read_b64_tr_b16 v[30:31], v6 offset:44544\n"
"s_waitcnt lgkmcnt(12)\n"
"v_mfma_f32_16x16x32_bf16 v[136:139], v[36:39], a[192:195], v[136:139]\n"
"ds_read_b64_tr_b16 v[32:33], v6 offset:45056\n"
"v_mfma_f32_16x16x32_bf16 v[140:143], v[36:39], a[196:199], v[140:143]\n"
"ds_read_b64_tr_b16 v[34:35], v6 offset:45568\n"
"v_cvt_pk_bf16_f32 v120, v120, v121\n"
"v_mfma_f32_16x16x32_bf16 v[136:139], v[40:43], a[200:203], v[136:139]\n"
"ds_read_b64_tr_b16 v[36:37], v6 offset:46080\n"
"v_mfma_f32_16x16x32_bf16 v[140:143], v[40:43], a[204:207], v[140:143]\n"
"ds_read_b64_tr_b16 v[38:39], v6 offset:46592\n"
"v_cvt_pk_bf16_f32 v121, v122, v123\n"
"buffer_store_dwordx4 v[104:107], v9, s[16:19], 0 offen offset:3072 sc1\n"
"s_waitcnt lgkmcnt(12)\n"
"v_mfma_f32_16x16x32_bf16 v[136:139], v[44:47], a[208:211], v[136:139]\n"
"ds_read_b64_tr_b16 v[40:41], v6 offset:47104\n"
"v_mfma_f32_16x16x32_bf16 v[140:143], v[44:47], a[212:215], v[140:143]\n"
"ds_read_b64_tr_b16 v[42:43], v6 offset:47616\n"
"v_cvt_pk_bf16_f32 v122, v124, v125\n"
"v_mfma_f32_16x16x32_bf16 v[136:139], v[12:15], a[216:219], v[136:139]\n"
"ds_read_b64_tr_b16 v[44:45], v6 offset:48128\n"
"v_mfma_f32_16x16x32_bf16 v[140:143], v[12:15], a[220:223], v[140:143]\n"
"ds_read_b64_tr_b16 v[46:47], v6 offset:48640\n"
"v_cvt_pk_bf16_f32 v123, v126, v127\n"
"s_barrier\n"
"s_waitcnt lgkmcnt(12)\n"
"v_mfma_f32_16x16x32_bf16 v[136:139], v[16:19], a[224:227], v[136:139]\n"
"ds_read_b64_tr_b16 v[12:13], v6 offset:49152\n"
"v_mfma_f32_16x16x32_bf16 v[140:143], v[16:19], a[228:231], v[140:143]\n"
"ds_read_b64_tr_b16 v[14:15], v6 offset:49664\n"
"v_mfma_f32_16x16x32_bf16 v[136:139], v[20:23], a[232:235], v[136:139]\n"
"ds_read_b64_tr_b16 v[16:17], v6 offset:50176\n"
"v_mfma_f32_16x16x32_bf16 v[140:143], v[20:23], a[236:239], v[140:143]\n"
"ds_read_b64_tr_b16 v[18:19], v6 offset:50688\n"
"v_cvt_pk_bf16_f32 v128, v128, v129\n"
"s_waitcnt lgkmcnt(12)\n"
"v_mfma_f32_16x16x32_bf16 v[136:139], v[24:27], a[240:243], v[136:139]\n"
"ds_read_b64_tr_b16 v[20:21], v6 offset:51200\n"
"v_mfma_f32_16x16x32_bf16 v[140:143], v[24:27], a[244:247], v[140:143]\n"
"ds_read_b64_tr_b16 v[22:23], v6 offset:51712\n"
"v_cvt_pk_bf16_f32 v129, v130, v131\n"
"v_mfma_f32_16x16x32_bf16 v[136:139], v[28:31], a[248:251], v[136:139]\n"
"ds_read_b64_tr_b16 v[24:25], v6 offset:52224\n"
"v_mfma_f32_16x16x32_bf16 v[140:143], v[28:31], a[252:255], v[140:143]\n"
"ds_read_b64_tr_b16 v[26:27], v6 offset:52736\n"
"v_cvt_pk_bf16_f32 v130, v132, v133\n"
"s_waitcnt lgkmcnt(12)\n"
"v_mfma_f32_16x16x32_bf16 v[136:139], v[32:35], v[48:51], v[136:139]\n"
"ds_read_b64_tr_b16 v[28:29], v6 offset:53248\n"
"v_mfma_f32_16x16x32_bf16 v[140:143], v[32:35], v[52:55], v[140:143]\n"
"ds_read_b64_tr_b16 v[30:31], v6 offset:53760\n"
"v_cvt_pk_bf16_f32 v131, v134, v135\n"
"v_mfma_f32_16x16x32_bf16 v[136:139], v[36:39], v[56:59], v[136:139]\n"
"ds_read_b64_tr_b16 v[32:33], v6 offset:54272\n"
"v_mfma_f32_16x16x32_bf16 v[140:143], v[36:39], v[60:63], v[140:143]\n"
"ds_read_b64_tr_b16 v[34:35], v6 offset:54784\n"
"s_mov_b32 s22, 4096\n"
"buffer_store_dwordx4 v[112:115], v9, s[16:19], s22 offen sc1\n"
"s_waitcnt lgkmcnt(12)\n"
"v_mfma_f32_16x16x32_bf16 v[136:139], v[40:43], v[64:67], v[136:139]\n"
"ds_read_b64_tr_b16 v[36:37], v6 offset:55296\n"
"v_mfma_f32_16x16x32_bf16 v[140:143], v[40:43], v[68:71], v[140:143]\n"
"ds_read_b64_tr_b16 v[38:39], v6 offset:55808\n"
"v_mfma_f32_16x16x32_bf16 v[136:139], v[44:47], v[72:75], v[136:139]\n"
"ds_read_b64_tr_b16 v[40:41], v6 offset:56320\n"
"v_mfma_f32_16x16x32_bf16 v[140:143], v[44:47], v[76:79], v[140:143]\n"
"ds_read_b64_tr_b16 v[42:43], v6 offset:56832\n"
"s_waitcnt lgkmcnt(12)\n"
"v_mfma_f32_16x16x32_bf16 v[144:147], v[12:15], a[0:3], 0\n"
"ds_read_b64_tr_b16 v[44:45], v6 offset:57344\n"
"v_mfma_f32_16x16x32_bf16 v[148:151], v[12:15], a[4:7], 0\n"
"ds_read_b64_tr_b16 v[46:47], v6 offset:57856\n"
"v_mfma_f32_16x16x32_bf16 v[144:147], v[16:19], a[8:11], v[144:147]\n"
"ds_read_b64_tr_b16 v[12:13], v6 offset:58368\n"
"v_mfma_f32_16x16x32_bf16 v[148:151], v[16:19], a[12:15], v[148:151]\n"
"ds_read_b64_tr_b16 v[14:15], v6 offset:58880\n"
"s_waitcnt lgkmcnt(12)\n"
"v_mfma_f32_16x16x32_bf16 v[144:147], v[20:23], a[16:19], v[144:147]\n"
"ds_read_b64_tr_b16 v[16:17], v6 offset:59392\n"
"v_mfma_f32_16x16x32_bf16 v[148:151], v[20:23], a[20:23], v[148:151]\n"
"ds_read_b64_tr_b16 v[18:19], v6 offset:59904\n"
"v_cvt_pk_bf16_f32 v136, v136, v137\n"
"v_mfma_f32_16x16x32_bf16 v[144:147], v[24:27], a[24:27], v[144:147]\n"
"ds_read_b64_tr_b16 v[20:21], v6 offset:60416\n"
"v_mfma_f32_16x16x32_bf16 v[148:151], v[24:27], a[28:31], v[148:151]\n"
"ds_read_b64_tr_b16 v[22:23], v6 offset:60928\n"
"v_cvt_pk_bf16_f32 v137, v138, v139\n"
"s_waitcnt lgkmcnt(12)\n"
"v_mfma_f32_16x16x32_bf16 v[144:147], v[28:31], a[32:35], v[144:147]\n"
"ds_read_b64_tr_b16 v[24:25], v6 offset:61440\n"
"v_mfma_f32_16x16x32_bf16 v[148:151], v[28:31], a[36:39], v[148:151]\n"
"ds_read_b64_tr_b16 v[26:27], v6 offset:61952\n"
"v_cvt_pk_bf16_f32 v138, v140, v141\n"
"v_mfma_f32_16x16x32_bf16 v[144:147], v[32:35], a[40:43], v[144:147]\n"
"ds_read_b64_tr_b16 v[28:29], v6 offset:62464\n"
"v_mfma_f32_16x16x32_bf16 v[148:151], v[32:35], a[44:47], v[148:151]\n"
"ds_read_b64_tr_b16 v[30:31], v6 offset:62976\n"
"v_cvt_pk_bf16_f32 v139, v142, v143\n"
"s_mov_b32 s22, 5120\n"
"buffer_store_dwordx4 v[120:123], v9, s[16:19], s22 offen sc1\n"
"s_waitcnt lgkmcnt(12)\n"
"v_mfma_f32_16x16x32_bf16 v[144:147], v[36:39], a[48:51], v[144:147]\n"
"ds_read_b64_tr_b16 v[32:33], v6 offset:63488\n"
"v_mfma_f32_16x16x32_bf16 v[148:151], v[36:39], a[52:55], v[148:151]\n"
"ds_read_b64_tr_b16 v[34:35], v6 offset:64000\n"
"v_mfma_f32_16x16x32_bf16 v[144:147], v[40:43], a[56:59], v[144:147]\n"
"ds_read_b64_tr_b16 v[36:37], v6 offset:64512\n"
"v_mfma_f32_16x16x32_bf16 v[148:151], v[40:43], a[60:63], v[148:151]\n"
"ds_read_b64_tr_b16 v[38:39], v6 offset:65024\n"
"s_waitcnt vmcnt(10)\n"
"s_barrier\n"
"s_waitcnt lgkmcnt(12)\n"
"v_mfma_f32_16x16x32_bf16 v[144:147], v[44:47], a[64:67], v[144:147]\n"
"ds_read_b64_tr_b16 v[40:41], v7 offset:0\n"
"v_mfma_f32_16x16x32_bf16 v[148:151], v[44:47], a[68:71], v[148:151]\n"
"ds_read_b64_tr_b16 v[42:43], v7 offset:512\n"
"v_mfma_f32_16x16x32_bf16 v[144:147], v[12:15], a[72:75], v[144:147]\n"
"ds_read_b64_tr_b16 v[44:45], v7 offset:1024\n"
"v_mfma_f32_16x16x32_bf16 v[148:151], v[12:15], a[76:79], v[148:151]\n"
"ds_read_b64_tr_b16 v[46:47], v7 offset:1536\n"
"s_mov_b32 s22, 6144\n"
"buffer_store_dwordx4 v[128:131], v9, s[16:19], s22 offen sc1\n"
"s_waitcnt lgkmcnt(12)\n"
"v_mfma_f32_16x16x32_bf16 v[144:147], v[16:19], a[80:83], v[144:147]\n"
"ds_read_b64_tr_b16 v[12:13], v7 offset:2048\n"
"v_mfma_f32_16x16x32_bf16 v[148:151], v[16:19], a[84:87], v[148:151]\n"
"ds_read_b64_tr_b16 v[14:15], v7 offset:2560\n"
"v_mfma_f32_16x16x32_bf16 v[144:147], v[20:23], a[88:91], v[144:147]\n"
"ds_read_b64_tr_b16 v[16:17], v7 offset:3072\n"
"v_mfma_f32_16x16x32_bf16 v[148:151], v[20:23], a[92:95], v[148:151]\n"
"ds_read_b64_tr_b16 v[18:19], v7 offset:3584\n"
"s_waitcnt lgkmcnt(12)\n"
"v_mfma_f32_16x16x32_bf16 v[144:147], v[24:27], a[96:99], v[144:147]\n"
"ds_read_b64_tr_b16 v[20:21], v7 offset:4096\n"
"v_mfma_f32_16x16x32_bf16 v[148:151], v[24:27], a[100:103], v[148:151]\n"
"ds_read_b64_tr_b16 v[22:23], v7 offset:4608\n"
"v_mfma_f32_16x16x32_bf16 v[144:147], v[28:31], a[104:107], v[144:147]\n"
"ds_read_b64_tr_b16 v[24:25], v7 offset:5120\n"
"v_mfma_f32_16x16x32_bf16 v[148:151], v[28:31], a[108:111], v[148:151]\n"
"ds_read_b64_tr_b16 v[26:27], v7 offset:5632\n"
"s_mov_b32 s22, 7168\n"
"buffer_store_dwordx4 v[136:139], v9, s[16:19], s22 offen sc1\n"
"s_waitcnt lgkmcnt(12)\n"
"v_mfma_f32_16x16x32_bf16 v[144:147], v[32:35], a[112:115], v[144:147]\n"
"ds_read_b64_tr_b16 v[28:29], v7 offset:6144\n"
"v_mfma_f32_16x16x32_bf16 v[148:151], v[32:35], a[116:119], v[148:151]\n"
"ds_read_b64_tr_b16 v[30:31], v7 offset:6656\n"
"v_mfma_f32_16x16x32_bf16 v[144:147], v[36:39], a[120:123], v[144:147]\n"
"ds_read_b64_tr_b16 v[32:33], v7 offset:7168\n"
"v_mfma_f32_16x16x32_bf16 v[148:151], v[36:39], a[124:127], v[148:151]\n"
"ds_read_b64_tr_b16 v[34:35], v7 offset:7680\n"
"s_waitcnt lgkmcnt(12)\n"
"v_mfma_f32_16x16x32_bf16 v[144:147], v[40:43], a[128:131], v[144:147]\n"
"ds_read_b64_tr_b16 v[36:37], v7 offset:8192\n"
"v_mfma_f32_16x16x32_bf16 v[148:151], v[40:43], a[132:135], v[148:151]\n"
"ds_read_b64_tr_b16 v[38:39], v7 offset:8704\n"
"v_mfma_f32_16x16x32_bf16 v[144:147], v[44:47], a[136:139], v[144:147]\n"
"ds_read_b64_tr_b16 v[40:41], v7 offset:9216\n"
"v_mfma_f32_16x16x32_bf16 v[148:151], v[44:47], a[140:143], v[148:151]\n"
"ds_read_b64_tr_b16 v[42:43], v7 offset:9728\n"
"s_waitcnt lgkmcnt(12)\n"
"v_mfma_f32_16x16x32_bf16 v[144:147], v[12:15], a[144:147], v[144:147]\n"
"ds_read_b64_tr_b16 v[44:45], v7 offset:10240\n"
"v_mfma_f32_16x16x32_bf16 v[148:151], v[12:15], a[148:151], v[148:151]\n"
"ds_read_b64_tr_b16 v[46:47], v7 offset:10752\n"
"v_mfma_f32_16x16x32_bf16 v[144:147], v[16:19], a[152:155], v[144:147]\n"
"ds_read_b64_tr_b16 v[12:13], v7 offset:11264\n"
"v_mfma_f32_16x16x32_bf16 v[148:151], v[16:19], a[156:159], v[148:151]\n"
"ds_read_b64_tr_b16 v[14:15], v7 offset:11776\n"
"s_waitcnt lgkmcnt(12)\n"
"v_mfma_f32_16x16x32_bf16 v[144:147], v[20:23], a[160:163], v[144:147]\n"
"ds_read_b64_tr_b16 v[16:17], v7 offset:12288\n"
"v_mfma_f32_16x16x32_bf16 v[148:151], v[20:23], a[164:167], v[148:151]\n"
"ds_read_b64_tr_b16 v[18:19], v7 offset:12800\n"
"v_mfma_f32_16x16x32_bf16 v[144:147], v[24:27], a[168:171], v[144:147]\n"
"ds_read_b64_tr_b16 v[20:21], v7 offset:13312\n"
"v_mfma_f32_16x16x32_bf16 v[148:151], v[24:27], a[172:175], v[148:151]\n"
"ds_read_b64_tr_b16 v[22:23], v7 offset:13824\n"
"s_waitcnt lgkmcnt(12)\n"
"v_mfma_f32_16x16x32_bf16 v[144:147], v[28:31], a[176:179], v[144:147]\n"
"ds_read_b64_tr_b16 v[24:25], v7 offset:14336\n"
"v_mfma_f32_16x16x32_bf16 v[148:151], v[28:31], a[180:183], v[148:151]\n"
"ds_read_b64_tr_b16 v[26:27], v7 offset:14848\n"
"v_mfma_f32_16x16x32_bf16 v[144:147], v[32:35], a[184:187], v[144:147]\n"
"ds_read_b64_tr_b16 v[28:29], v7 offset:15360\n"
"v_mfma_f32_16x16x32_bf16 v[148:151], v[32:35], a[188:191], v[148:151]\n"
"ds_read_b64_tr_b16 v[30:31], v7 offset:15872\n"
"s_waitcnt vmcnt(6)\n"
"s_barrier\n"
"s_waitcnt lgkmcnt(12)\n"
"v_mfma_f32_16x16x32_bf16 v[144:147], v[36:39], a[192:195], v[144:147]\n"
"ds_read_b64_tr_b16 v[32:33], v7 offset:16384\n"
"v_mfma_f32_16x16x32_bf16 v[148:151], v[36:39], a[196:199], v[148:151]\n"
"ds_read_b64_tr_b16 v[34:35], v7 offset:16896\n"
"v_mfma_f32_16x16x32_bf16 v[144:147], v[40:43], a[200:203], v[144:147]\n"
"ds_read_b64_tr_b16 v[36:37], v7 offset:17408\n"
"v_mfma_f32_16x16x32_bf16 v[148:151], v[40:43], a[204:207], v[148:151]\n"
"ds_read_b64_tr_b16 v[38:39], v7 offset:17920\n"
"s_waitcnt lgkmcnt(12)\n"
"v_mfma_f32_16x16x32_bf16 v[144:147], v[44:47], a[208:211], v[144:147]\n"
"ds_read_b64_tr_b16 v[40:41], v7 offset:18432\n"
"v_mfma_f32_16x16x32_bf16 v[148:151], v[44:47], a[212:215], v[148:151]\n"
"ds_read_b64_tr_b16 v[42:43], v7 offset:18944\n"
"v_mfma_f32_16x16x32_bf16 v[144:147], v[12:15], a[216:219], v[144:147]\n"
"ds_read_b64_tr_b16 v[44:45], v7 offset:19456\n"
"v_mfma_f32_16x16x32_bf16 v[148:151], v[12:15], a[220:223], v[148:151]\n"
"ds_read_b64_tr_b16 v[46:47], v7 offset:19968\n"
"s_waitcnt lgkmcnt(12)\n"
"v_mfma_f32_16x16x32_bf16 v[144:147], v[16:19], a[224:227], v[144:147]\n"
"v_mfma_f32_16x16x32_bf16 v[148:151], v[16:19], a[228:231], v[148:151]\n"
"v_mfma_f32_16x16x32_bf16 v[144:147], v[20:23], a[232:235], v[144:147]\n"
"v_mfma_f32_16x16x32_bf16 v[148:151], v[20:23], a[236:239], v[148:151]\n"
"s_waitcnt lgkmcnt(8)\n"
"v_mfma_f32_16x16x32_bf16 v[144:147], v[24:27], a[240:243], v[144:147]\n"
"v_mfma_f32_16x16x32_bf16 v[148:151], v[24:27], a[244:247], v[148:151]\n"
"v_mfma_f32_16x16x32_bf16 v[144:147], v[28:31], a[248:251], v[144:147]\n"
"v_mfma_f32_16x16x32_bf16 v[148:151], v[28:31], a[252:255], v[148:151]\n"
"s_waitcnt lgkmcnt(4)\n"
"v_mfma_f32_16x16x32_bf16 v[144:147], v[32:35], v[48:51], v[144:147]\n"
"v_mfma_f32_16x16x32_bf16 v[148:151], v[32:35], v[52:55], v[148:151]\n"
"v_mfma_f32_16x16x32_bf16 v[144:147], v[36:39], v[56:59], v[144:147]\n"
"v_mfma_f32_16x16x32_bf16 v[148:151], v[36:39], v[60:63], v[148:151]\n"
"s_waitcnt lgkmcnt(0)\n"
"v_mfma_f32_16x16x32_bf16 v[144:147], v[40:43], v[64:67], v[144:147]\n"
"v_mfma_f32_16x16x32_bf16 v[148:151], v[40:43], v[68:71], v[148:151]\n"
"v_mfma_f32_16x16x32_bf16 v[144:147], v[44:47], v[72:75], v[144:147]\n"
"v_mfma_f32_16x16x32_bf16 v[148:151], v[44:47], v[76:79], v[148:151]\n"
"s_nop 7\n"
"v_cvt_pk_bf16_f32 v144, v144, v145\n"
"v_cvt_pk_bf16_f32 v145, v146, v147\n"
"v_cvt_pk_bf16_f32 v146, v148, v149\n"
"v_cvt_pk_bf16_f32 v147, v150, v151\n"
"s_mov_b32 s22, 8192\n"
"buffer_store_dwordx4 v[144:147], v9, s[16:19], s22 offen sc1\n"
"s_waitcnt vmcnt(0)\n"
"s_waitcnt lgkmcnt(0)\n"
      :: GEMM_IN(a) : "v12","v13","v14","v15","v16","v17","v18","v19","v20","v21","v22","v23","v24","v25","v26","v27","v28","v29","v30","v31","v32","v33","v34","v35","v36","v37","v38","v39","v40","v41","v42","v43","v44","v45","v46","v47","v48","v49","v50","v51","v52","v53","v54","v55","v56","v57","v58","v59","v60","v61","v62","v63","v64","v65","v66","v67","v68","v69","v70","v71","v72","v73","v74","v75","v76","v77","v78","v79","v80","v81","v82","v83","v84","v85","v86","v87","v88","v89","v90","v91","v92","v93","v94","v95","v96","v97","v98","v99","v100","v101","v102","v103","v104","v105","v106","v107","v108","v109","v110","v111","v112","v113","v114","v115","v116","v117","v118","v119","v120","v121","v122","v123","v124","v125","v126","v127","v128","v129","v130","v131","v132","v133","v134","v135","v136","v137","v138","v139","v140","v141","v142","v143","v144","v145","v146","v147","v148","v149","v150","v151","v152","v153","v154","v155","v156","v157","v158","v159","a0","a1","a2","a3","a4","a5","a6","a7","a8","a9","a10","a11","a12","a13","a14","a15","a16","a17","a18","a19","a20","a21","a22","a23","a24","a25","a26","a27","a28","a29","a30","a31","a32","a33","a34","a35","a36","a37","a38","a39","a40","a41","a42","a43","a44","a45","a46","a47","a48","a49","a50","a51","a52","a53","a54","a55","a56","a57","a58","a59","a60","a61","a62","a63","a64","a65","a66","a67","a68","a69","a70","a71","a72","a73","a74","a75","a76","a77","a78","a79","a80","a81","a82","a83","a84","a85","a86","a87","a88","a89","a90","a91","a92","a93","a94","a95","a96","a97","a98","a99","a100","a101","a102","a103","a104","a105","a106","a107","a108","a109","a110","a111","a112","a113","a114","a115","a116","a117","a118","a119","a120","a121","a122","a123","a124","a125","a126","a127","a128","a129","a130","a131","a132","a133","a134","a135","a136","a137","a138","a139","a140","a141","a142","a143","a144","a145","a146","a147","a148","a149","a150","a151","a152","a153","a154","a155","a156","a157","a158","a159","a160","a161","a162","a163","a164","a165","a166","a167","a168","a169","a170","a171","a172","a173","a174","a175","a176","a177","a178","a179","a180","a181","a182","a183","a184","a185","a186","a187","a188","a189","a190","a191","a192","a193","a194","a195","a196","a197","a198","a199","a200","a201","a202","a203","a204","a205","a206","a207","a208","a209","a210","a211","a212","a213","a214","a215","a216","a217","a218","a219","a220","a221","a222","a223","a224","a225","a226","a227","a228","a229","a230","a231","a232","a233","a234","a235","a236","a237","a238","a239","a240","a241","a242","a243","a244","a245","a246","a247","a248","a249","a250","a251","a252","a253","a254","a255","s21","s22","s53","scc","vcc","memory");

    signal_cnt<false>(cnt + (CNT_E + rb * 4u + cb) * 64u, wid);

  }
  {
    const unsigned lane = lane_id(), g = lane >> 4, r = lane & 15u;
    const unsigned tok16 = rb * 8u + split * 2u;
    wait_cnt(cnt + (CNT_E + rb * 4u + cb) * 64u, 4u, wid);

    const __amdgpu_buffer_rsrc_t rs = mk_rsrc(ws + WS_SLAB2 + (size_t)(rb * 4u + cb) * 147456u + split * 36864u, 36864u);
    u32x4 pw[3][4];
#pragma unroll
    for (int i = 0; i < 3; ++i) {
      const unsigned nb = (i < 2 || three) ? wid + 4u * i : wid;
#pragma unroll
      for (int sp = 0; sp < 4; ++sp) pw[i][sp] = ldu16_sc1(rs, sp * 9216u + nb * 1024u + lane * 16u);
    }
#pragma unroll
    for (int i = 0; i < 3; ++i) {
      const unsigned nb = (i < 2 || three) ? wid + 4u * i : wid, n = cb * 144u + 16u * nb + 4u * g;
#pragma unroll
      for (int tb = 0; tb < 2; ++tb) {
        float s[4] = {0.f, 0.f, 0.f, 0.f};
#pragma unroll
        for (int sp = 0; sp < 4; ++sp) unpack4(s, pw[i][sp][2 * tb], pw[i][sp][2 * tb + 1]);
        const u32x2 o = {cvtpk(x1v[i][tb][0] + gb[i][0] * s[0], x1v[i][tb][1] + gb[i][1] * s[1]), cvtpk(x1v[i][tb][2] + gb[i][2] * s[2], x1v[i][tb][3] + gb[i][3] * s[3])};
        if (i < 2 || three) *(u32x2*)(out + (size_t)((tok16 + tb) * 16u + r) * DM + n) = o;
      }
    }
  }
  CODE_LABEL("Lm_Z");
}

extern "C" void kernel_launch(void* const* d_in, const int* in_sizes, int n_in, void* d_out, int out_size, void* d_ws, size_t ws_size, hipStream_t stream) {
  const u16* x    = (const u16*)d_in[0];
  const u16* kc   = (const u16*)d_in[1];
  const u16* vc   = (const u16*)d_in[2];
  const u16* wq   = (const u16*)d_in[3];
  const u16* wo   = (const u16*)d_in[4];
  const u16* wg   = (const u16*)d_in[6];
  const u16* wu   = (const u16*)d_in[7];
  const u16* wd   = (const u16*)d_in[8];
  const u16* cc   = (const u16*)d_in[9];
  const u16* wmod = (const u16*)d_in[10];
  char* ws = (char*)d_ws;
  (void)hipFuncSetAttribute((const void*)k_main, hipFuncAttributeMaxDynamicSharedMemorySize, (int)SHM_ATTN);
  k_prep<<<NMOD + 512 + 48, 256, 0, stream>>>(cc, wmod, x, ws, wq);
  k_main<<<256, 256, SHM_ATTN, stream>>>(wq, kc, vc, wo, wg, wu, wd, ws, x, (u16*)d_out);
}
```
